```python
import torch

def _softmax(x, axis=-1):
    return torch.softmax(x, dim=axis)
def _gelu(x):
    return torch.nn.functional.gelu(x, approximate="tanh")
def _silu(x):
    return torch.nn.functional.silu(x)
def _sigmoid(x):
    return torch.sigmoid(x)
def _layernorm(x, axis=-1, eps=1e-5):
    m = x.mean(dim=axis, keepdim=True)
    v = x.var(dim=axis, keepdim=True, unbiased=False)
    return (x - m) / torch.sqrt(v + eps)
def _scatter_add(tgt, idx, src, axis=0):
    return tgt.clone().index_add_(axis, idx, src)
def _segment_sum(src, seg, n):
    out = torch.zeros((n,) + src.shape[1:], dtype=src.dtype)
    return out.index_add_(0, seg, src)
def _pad(x, pw):
    flat = tuple(v for pair in reversed(pw) for v in pair)
    return torch.nn.functional.pad(x, flat)

kernel_name = 'recurrent_scan__compressed_kv_cla__glu_gated_glu__prompt'

def setup_inputs():

    _w = (lambda a: (lambda t: t.to(torch.bfloat16) if t.is_floating_point() else t)(torch.as_tensor(a)))
    def _A_setup():
        B, H, Sq, Sk, D, M = 1, 32, 2048, 2048, 128, 512
        Dc = 4 * D
        return {
            "x": _w((lambda *s: torch.randn(*s, dtype=torch.bfloat16) * 0.05)(B, Sq, M)),
            "c_kv": _w((lambda *s: torch.randn(*s, dtype=torch.bfloat16) * 0.2)(B, Sk, Dc)),
            "w_q": _w((lambda *s: torch.randn(*s, dtype=torch.bfloat16) * 3.2)(M, H, D) * (M ** -0.5)),
            "w_uk": _w((lambda *s: torch.randn(*s, dtype=torch.bfloat16) * 3.2)(Dc, H, D) * (Dc ** -0.5)),
            "w_uv": _w((lambda *s: torch.randn(*s, dtype=torch.bfloat16) * 0.2)(Dc, H, D) * (Dc ** -0.5)),
            "w_o": _w((lambda *s: torch.randn(*s, dtype=torch.bfloat16) * 0.2)(H, D, M) * ((H * D) ** -0.5)),
        }
    _A_in = _A_setup()

    def _B_setup():
        B, S, D, F = 1, 2048, 512, 1792
        return {
            "x": _w((lambda *s: torch.randn(*s, dtype=torch.bfloat16) * 0.05)(B, S, D)),
            "w_gate": _w((lambda *s: torch.randn(*s, dtype=torch.bfloat16) * 0.2)(D, F)),
            "w_up": _w((lambda *s: torch.randn(*s, dtype=torch.bfloat16) * 0.1)(D, F)),
            "w_down": _w((lambda *s: torch.randn(*s, dtype=torch.bfloat16) * 0.1)(F, D)),
        }
    _B_in = _B_setup()

    return {
        "A_x": _A_in["x"],
        "A_c_kv": _A_in["c_kv"],
        "A_w_q": _A_in["w_q"],
        "A_w_uk": _A_in["w_uk"],
        "A_w_uv": _A_in["w_uv"],
        "A_w_o": _A_in["w_o"],
        "B_x": _B_in["x"],
        "B_w_gate": _B_in["w_gate"],
        "B_w_up": _B_in["w_up"],
        "B_w_down": _B_in["w_down"],
    }

def reference(A_x, A_c_kv, A_w_q, A_w_uk, A_w_uv, A_w_o, B_x, B_w_gate, B_w_up, B_w_down):
    def _A_ref(x, c_kv, w_q, w_uk, w_uv, w_o):
        B, Sq, M = x.shape[0], x.shape[1], x.shape[2]
        Sk = c_kv.shape[1]
        H, D = w_q.shape[1], w_q.shape[2]
        q = torch.einsum("bqm,mhd->bhqd", x, w_q)
        k = torch.einsum("bkc,chd->bhkd", c_kv, w_uk)
        v = torch.einsum("bkc,chd->bhkd", c_kv, w_uv)
        scores = torch.einsum("bhqd,bhkd->bhqk", q, k) * (D ** -0.5)
        off = Sk - Sq
        qi = torch.arange(Sq)[:, None]
        kj = torch.arange(Sk)[None, :]
        cmask = kj <= (qi + off)
        scores = torch.where(cmask[None, None], scores, -1e9)
        p = _softmax(scores, axis=-1)
        ctx = torch.einsum("bhqk,bhkd->bhqd", p, v)
        return torch.einsum("bhqd,hdm->bqm", ctx, w_o)

    def _B_ref(x, w_gate, w_up, w_down):
        g = torch.einsum("bsd,df->bsf", x, w_gate)
        u = torch.einsum("bsd,df->bsf", x, w_up)
        h = _sigmoid(g) * u
        return torch.einsum("bsf,fd->bsd", h, w_down)

    x = A_x
    xn = _layernorm(x, axis=-1)
    s = _A_ref(xn, A_c_kv, A_w_q, A_w_uk, A_w_uv, A_w_o)
    g = _silu(_B_ref(xn, B_w_gate, B_w_up, B_w_down))
    return x + s * g
```

```cpp
#include <hip/hip_runtime.h>
#include <hip/hip_bf16.h>
typedef unsigned short u16;
__device__ __forceinline__ float b2f(u16 b){ return __uint_as_float(((unsigned)b)<<16); }
__device__ __forceinline__ u16 f2b(float f){ __hip_bfloat16 h=__float2bfloat16(f); return *(u16*)&h; }
__device__ __forceinline__ float rb(float f){ return b2f(f2b(f)); }

__global__ void ln_k(const u16* x, u16* xn){
  int row=blockIdx.x; int t=threadIdx.x;
  float v[8]; float s=0;
  for(int i=0;i<8;i++){ v[i]=b2f(x[row*512+t*8+i]); s+=v[i]; }
  for(int o=32;o;o>>=1) s+=__shfl_xor(s,o);
  float mean=s/512.f; float q=0;
  for(int i=0;i<8;i++){ float d=v[i]-mean; q+=d*d; }
  for(int o=32;o;o>>=1) q+=__shfl_xor(q,o);
  float var=rb(q/512.f); float m=rb(mean);
  float den=rb(sqrtf(rb(var+1e-5f)));
  for(int i=0;i<8;i++) xn[row*512+t*8+i]=f2b(rb(v[i]-m)/den);
}
__global__ void gemm_k(const u16* A,const u16* B,u16* C,int M,int N,int K){
  __shared__ float As[16][17],Bs[16][17];
  int tx=threadIdx.x&15,ty=threadIdx.x>>4; int r=blockIdx.y*16+ty,c=blockIdx.x*16+tx; float acc=0;
  for(int k0=0;k0<K;k0+=16){ As[ty][tx]=b2f(A[(long)r*K+k0+tx]); Bs[ty][tx]=b2f(B[(long)(k0+ty)*N+c]); __syncthreads();
    for(int k=0;k<16;k++) acc+=As[ty][k]*Bs[k][tx]; __syncthreads(); }
  C[(long)r*N+c]=f2b(acc);
}
__global__ void attn_k(const u16* Q,const u16* K,const u16* V,u16* O){
  int row=blockIdx.x,h=blockIdx.y,t=threadIdx.x; __shared__ float q[128]; __shared__ float p[2048]; __shared__ float red[128];
  q[t]=b2f(Q[(long)row*4096+h*128+t]); __syncthreads();
  float mx=-1e30f;
  for(int j=t;j<=row;j+=128){ float s=0; for(int d=0;d<128;d++) s+=q[d]*b2f(K[(long)j*4096+h*128+d]); s*=0.08838834764831845f; p[j]=s; mx=fmaxf(mx,s);}
  red[t]=mx; __syncthreads(); for(int o=64;o;o>>=1){ if(t<o) red[t]=fmaxf(red[t],red[t+o]); __syncthreads(); } mx=red[0]; __syncthreads();
  float sum=0; for(int j=t;j<=row;j+=128){ float e=__expf(p[j]-mx); p[j]=e; sum+=e; }
  red[t]=sum; __syncthreads(); for(int o=64;o;o>>=1){ if(t<o) red[t]+=red[t+o]; __syncthreads(); } sum=red[0];
  float acc=0; for(int j=0;j<=row;j++) acc+=p[j]*b2f(V[(long)j*4096+h*128+t]);
  O[(long)row*4096+h*128+t]=f2b(acc/sum);
}
__global__ void glu_k(const u16* g,const u16* u,u16* h,int n){ int i=blockIdx.x*256+threadIdx.x; if(i<n){ float gg=b2f(g[i]); float s=rb(1.f/(1.f+__expf(-gg))); h[i]=f2b(s*b2f(u[i])); } }
__global__ void fin_k(const u16* x,const u16* s,const u16* d,u16* o,int n){ int i=blockIdx.x*256+threadIdx.x; if(i<n){ float dd=b2f(d[i]); float g=rb(dd/(1.f+__expf(-dd))); o[i]=f2b(b2f(x[i])+rb(b2f(s[i])*g)); } }

extern "C" void kernel_launch(void* const* d_in, const int* in_sizes, int n_in, void* d_out, int out_size, void* d_ws, size_t ws_size, hipStream_t stream) {
  const u16* x=(const u16*)d_in[0]; const u16* ckv=(const u16*)d_in[1]; const u16* wq=(const u16*)d_in[2]; const u16* wuk=(const u16*)d_in[3]; const u16* wuv=(const u16*)d_in[4]; const u16* wo=(const u16*)d_in[5];
  const u16* wg=(const u16*)d_in[7]; const u16* wu=(const u16*)d_in[8]; const u16* wd=(const u16*)d_in[9];
  u16* ws=(u16*)d_ws; u16* xn=ws; u16* Q=xn+2048*512; u16* K=Q+2048*4096; u16* V=K+2048*4096; u16* C=V+2048*4096; u16* G=C+2048*4096; u16* U=G+2048*1792; u16* Hh=U+2048*1792; u16* S=Hh+2048*1792; u16* Dn=S+2048*512;
  ln_k<<<2048,64,0,stream>>>(x,xn);
  gemm_k<<<dim3(4096/16,128),256,0,stream>>>(xn,wq,Q,2048,4096,512);
  gemm_k<<<dim3(4096/16,128),256,0,stream>>>(ckv,wuk,K,2048,4096,512);
  gemm_k<<<dim3(4096/16,128),256,0,stream>>>(ckv,wuv,V,2048,4096,512);
  gemm_k<<<dim3(1792/16,128),256,0,stream>>>(xn,wg,G,2048,1792,512);
  gemm_k<<<dim3(1792/16,128),256,0,stream>>>(xn,wu,U,2048,1792,512);
  attn_k<<<dim3(2048,32),128,0,stream>>>(Q,K,V,C);
  glu_k<<<2048*1792/256,256,0,stream>>>(G,U,Hh,2048*1792);
  gemm_k<<<dim3(512/16,128),256,0,stream>>>(C,wo,S,2048,512,4096);
  gemm_k<<<dim3(512/16,128),256,0,stream>>>(Hh,wd,Dn,2048,512,1792);
  fin_k<<<2048*512/256,256,0,stream>>>(x,S,Dn,(u16*)d_out,2048*512);
}
```

```cpp
#include <hip/hip_runtime.h>
#define LAS __attribute__((address_space(3)))
#define DI __device__ __forceinline__
typedef unsigned short u16;
typedef short bf16x8 __attribute__((ext_vector_type(8)));
typedef short s16x4 __attribute__((ext_vector_type(4)));
typedef float f32x2 __attribute__((ext_vector_type(2)));
typedef float f32x4 __attribute__((ext_vector_type(4)));
typedef float f32x16 __attribute__((ext_vector_type(16)));
typedef unsigned u32x4 __attribute__((ext_vector_type(4)));
typedef unsigned u32x2 __attribute__((ext_vector_type(2)));
typedef __bf16 bf16x2_t __attribute__((ext_vector_type(2)));

#define MIB (1ull << 20)
#define WS_XT   (0 * MIB)
#define WS_CT   (2 * MIB)
#define WS_QF   (4 * MIB)
#define WS_KF   (20 * MIB)
#define WS_VF   (36 * MIB)
#define WS_CTX  (52 * MIB)
#define WS_HF   (68 * MIB)
#define WS_PART (76 * MIB)

DI unsigned cvtpk(float lo, float hi) { f32x2 v = {lo, hi}; bf16x2_t b = __builtin_convertvector(v, bf16x2_t); return __builtin_bit_cast(unsigned, b); }
DI float bflo(unsigned w) { return __uint_as_float(w << 16); }
DI float bfhi(unsigned w) { return __uint_as_float(w & 0xffff0000u); }
DI float rbf(float x) { return bflo(cvtpk(x, 0.f)); }
DI void glds(const void* gbase, unsigned voff, unsigned ldsaddr) {
  asm volatile("s_mov_b32 m0, %0\n\ts_nop 0\n\tglobal_load_lds_dwordx4 %1, %2" :: "s"(ldsaddr), "v"(voff), "s"(gbase) : "memory");
}
DI s16x4 trread(LAS const unsigned char* p) { return __builtin_amdgcn_ds_read_tr16_b64_v4i16((LAS s16x4*)p); }

__global__ __launch_bounds__(64) void k0_ln(const u16* __restrict__ x, const u16* __restrict__ ckv, unsigned char* __restrict__ ws) {
  const int b = blockIdx.x, lane = threadIdx.x, r = lane & 7, cg = lane >> 3;
  const bool isx = b < 256;
  const int row = (b & 255) * 8 + r;
  const u16* src = (isx ? x : ckv) + (size_t)row * 512 + cg * 8;
  u32x4 v[8];
#pragma unroll
  for (int i = 0; i < 8; ++i) v[i] = *(const u32x4*)(src + i * 64);
  if (isx) {
    float s = 0.f;
#pragma unroll
    for (int i = 0; i < 8; ++i)
#pragma unroll
      for (int j = 0; j < 4; ++j) s += bflo(v[i][j]) + bfhi(v[i][j]);
    s += __shfl_xor(s, 8); s += __shfl_xor(s, 16); s += __shfl_xor(s, 32);
    const float mean = s * (1.f / 512.f);
    float q = 0.f;
#pragma unroll
    for (int i = 0; i < 8; ++i)
#pragma unroll
      for (int j = 0; j < 4; ++j) { const float a = bflo(v[i][j]) - mean, c = bfhi(v[i][j]) - mean; q += a * a + c * c; }
    q += __shfl_xor(q, 8); q += __shfl_xor(q, 16); q += __shfl_xor(q, 32);
    const float m = rbf(mean), den = 1.f / rbf(sqrtf(rbf(rbf(q * (1.f / 512.f)) + 1e-5f)));
#pragma unroll
    for (int i = 0; i < 8; ++i)
#pragma unroll
      for (int j = 0; j < 4; ++j) { const unsigned d = cvtpk(bflo(v[i][j]) - m, bfhi(v[i][j]) - m); v[i][j] = cvtpk(bflo(d) * den, bfhi(d) * den); }
  }
  unsigned char* dst = ws + (isx ? WS_XT : WS_CT) + (size_t)(row >> 7) * 131072 + cg * 2048 + (row & 127) * 16;
#pragma unroll
  for (int i = 0; i < 8; ++i) *(u32x4*)(dst + i * 16384) = v[i];
}

namespace k1 {
constexpr int HTB = 16384;
#define SA(b, h) (((b) * 2 + (h)) * HTB)
#define SB(b, h) ((4 + (b) * 2 + (h)) * HTB)
#define WAIT_V(n) asm volatile("s_waitcnt vmcnt(" #n ")" ::: "memory")
#define WAIT_L(n) asm volatile("s_waitcnt lgkmcnt(" #n ")" ::: "memory")
#define BAR __builtin_amdgcn_s_barrier()
#define SCHED __builtin_amdgcn_sched_barrier(0)
struct Unit { const char* a; const char* b0; const char* b1; unsigned ldb; unsigned voffB; int pm, pn; };
struct Ctx { LAS unsigned char* lds; unsigned ldsw, voffA; int aoff, boff0, boff1; };

DI void stageA(const Ctx& c, int off, const char* g) {
#pragma unroll
  for (int i = 0; i < 2; ++i) glds(g + i * 8192, c.voffA, off + c.ldsw + i * 8192);
}
DI void stageB(const Ctx& c, int off, const char* g, unsigned voff, unsigned ldb) {
#pragma unroll
  for (int i = 0; i < 2; ++i) glds(g + (size_t)i * 32 * ldb, voff, off + c.ldsw + i * 8192);
}
DI void lda(const Ctx& c, bf16x8 (&d)[4][2], int off) {
#pragma unroll
  for (int m = 0; m < 4; ++m)
#pragma unroll
    for (int k = 0; k < 2; ++k) d[m][k] = *(const LAS bf16x8*)(c.lds + off + c.aoff + m * 256 + k * 8192);
}
DI void ldb(const Ctx& c, bf16x8 (&d)[2][2], int off) {
#pragma unroll
  for (int n = 0; n < 2; ++n)
#pragma unroll
    for (int k = 0; k < 2; ++k) { const LAS unsigned char* p = c.lds + off + (n ? c.boff1 : c.boff0) + k * 8192;
      const s16x4 lo = trread(p), hi = trread(p + 1024); d[n][k] = __builtin_shufflevector(lo, hi, 0, 1, 2, 3, 4, 5, 6, 7); }
}
template <bool VT> DI void mma(f32x4 (&acc)[4][2], const bf16x8 (&A)[4][2], const bf16x8 (&B)[2][2]) {
  __builtin_amdgcn_s_setprio(1);
#pragma unroll
  for (int m = 0; m < 4; ++m)
#pragma unroll
    for (int n = 0; n < 2; ++n)
#pragma unroll
      for (int k = 0; k < 2; ++k)
        acc[m][n] = VT ? __builtin_amdgcn_mfma_f32_16x16x32_bf16(A[m][k], B[n][k], acc[m][n], 0, 0, 0) : __builtin_amdgcn_mfma_f32_16x16x32_bf16(B[n][k], A[m][k], acc[m][n], 0, 0, 0);
  __builtin_amdgcn_s_setprio(0);
}
constexpr size_t AH = 131072, AK = 16384;
template <bool VT> DI void kloop(const Ctx& c, f32x4 (&acc)[2][2][4][2], const Unit& cur, const Unit& nxt) {
  bf16x8 At[4][2], B0[2][2], B1[2][2];
#pragma unroll
  for (int a = 0; a < 2; ++a)
#pragma unroll
    for (int b = 0; b < 2; ++b)
#pragma unroll
      for (int m = 0; m < 4; ++m)
#pragma unroll
        for (int n = 0; n < 2; ++n) acc[a][b][m][n] = (f32x4){0.f, 0.f, 0.f, 0.f};
#pragma unroll 1
  for (int t = 0; t < 8; t += 2) {
    const bool last = (t == 6);
    const char* a1 = cur.a + (size_t)(t + 1) * AK;
    const char* a2 = last ? nxt.a : cur.a + (size_t)(t + 2) * AK;
    const unsigned ldb2 = last ? nxt.ldb : cur.ldb, vo2 = last ? nxt.voffB : cur.voffB;
    const char* b20 = last ? nxt.b0 : cur.b0 + (size_t)(t + 2) * 64 * cur.ldb;
    const char* b21 = last ? nxt.b1 : cur.b1 + (size_t)(t + 2) * 64 * cur.ldb;
    const char* a3 = a2 + AK; const char* b30 = b20 + (size_t)64 * ldb2; const char* b31 = b21 + (size_t)64 * ldb2;
    ldb(c, B0, SB(0, 0)); ldb(c, B1, SB(0, 1)); SCHED; lda(c, At, SA(0, 0)); stageA(c, SA(1, 1), a1 + AH);
    WAIT_V(8); WAIT_L(0); BAR; mma<VT>(acc[0][0], At, B0); mma<VT>(acc[0][1], At, B1); BAR; SCHED;
    lda(c, At, SA(0, 1)); stageB(c, SB(0, 0), b20, vo2, ldb2); stageB(c, SB(0, 1), b21, vo2, ldb2); stageA(c, SA(0, 0), a2);
    WAIT_V(8); WAIT_L(0); BAR; mma<VT>(acc[1][0], At, B0); mma<VT>(acc[1][1], At, B1); BAR; SCHED;
    ldb(c, B0, SB(1, 0)); ldb(c, B1, SB(1, 1)); SCHED; lda(c, At, SA(1, 0)); stageA(c, SA(0, 1), a2 + AH);
    WAIT_V(8); WAIT_L(0); BAR; mma<VT>(acc[0][0], At, B0); mma<VT>(acc[0][1], At, B1); BAR; SCHED;
    lda(c, At, SA(1, 1)); stageB(c, SB(1, 0), b30, vo2, ldb2); stageB(c, SB(1, 1), b31, vo2, ldb2); stageA(c, SA(1, 0), a3);
    WAIT_V(8); WAIT_L(0); BAR; mma<VT>(acc[1][0], At, B0); mma<VT>(acc[1][1], At, B1); BAR; SCHED;
  }
}
DI u32x4 pack8(f32x4 a, f32x4 b) { u32x4 w; w.x = cvtpk(a[0], a[1]); w.y = cvtpk(a[2], a[3]); w.z = cvtpk(b[0], b[1]); w.w = cvtpk(b[2], b[3]); return w; }
}

__global__ __launch_bounds__(512, 2) void k1_proj(const u16* __restrict__ wq, const u16* __restrict__ wuk, const u16* __restrict__ wuv,
                                                  const u16* __restrict__ wg, const u16* __restrict__ wu, unsigned char* __restrict__ ws) {
  using namespace k1;
  extern __shared__ __attribute__((aligned(16))) unsigned char smem[];
  const int tid = threadIdx.x, wid = __builtin_amdgcn_readfirstlane(tid >> 6), lane = tid & 63, wr = wid >> 2, wc = wid & 3, fr = lane & 15, fq = lane >> 4;
  Ctx c; c.lds = (LAS unsigned char*)smem; c.ldsw = (unsigned)(size_t)c.lds + wid * 1024u; c.voffA = tid * 16u;
  c.aoff = fq * 2048 + wr * 1024 + fr * 16;
  { const int q = fr >> 2, p = lane & 3, sg = q | ((fq & 1) << 2);
    c.boff0 = (8 * fq + q) * 256 + (((2 * wc) ^ sg) * 32) + 8 * p; c.boff1 = (8 * fq + q) * 256 + (((2 * wc + 1) ^ sg) * 32) + 8 * p; }
  const int brow = tid >> 4, bslot = tid & 15, bsg = (brow & 3) | (((brow >> 3) & 1) << 2);
  const unsigned bcol = (((bslot >> 1) ^ bsg) * 32) + (bslot & 1) * 16;
  const int xcd = blockIdx.x & 7, r = blockIdx.x >> 3;
  Unit u0, u1;
  const bool isV = r >= 16;
  u0.pm = r & 7; u0.pn = 2 * xcd + ((r >> 3) & 1);
  u0.a = (const char*)ws + WS_CT + (size_t)u0.pm * 2 * AH; u0.b0 = (const char*)(isV ? wuv : wuk) + u0.pn * 512; u0.b1 = u0.b0 + 256; u0.ldb = 8192; u0.voffB = brow * 8192u + bcol;
  const bool isQ = r < 16, has1 = r < 30;
  if (isQ) { u1.pm = r & 7; u1.pn = 2 * xcd + (r >> 3); u1.b0 = (const char*)wq + u1.pn * 512; u1.b1 = u1.b0 + 256; u1.ldb = 8192; }
  else { const int u = 14 * xcd + (has1 ? r - 16 : 0); u1.pm = u & 7; u1.pn = u >> 3; u1.b0 = (const char*)wg + u1.pn * 256; u1.b1 = (const char*)wu + u1.pn * 256; u1.ldb = 3584; }
  u1.a = (const char*)ws + WS_XT + (size_t)u1.pm * 2 * AH; u1.voffB = brow * u1.ldb + bcol;

  f32x4 acc[2][2][4][2];
  stageB(c, SB(0, 0), u0.b0, u0.voffB, u0.ldb); stageB(c, SB(0, 1), u0.b1, u0.voffB, u0.ldb); stageA(c, SA(0, 0), u0.a); stageA(c, SA(0, 1), u0.a + AH);
  if (wr == 1) BAR;
  WAIT_V(2); BAR;
  stageB(c, SB(1, 0), u0.b0 + (size_t)64 * u0.ldb, u0.voffB, u0.ldb); stageA(c, SA(1, 0), u0.a + AK); stageB(c, SB(1, 1), u0.b1 + (size_t)64 * u0.ldb, u0.voffB, u0.ldb);
  WAIT_V(6); BAR;
  if (isV) {
    kloop<true>(c, acc, u0, u1);
    if (wr == 0) BAR;
    unsigned char* base = ws + WS_VF + (size_t)(fq >> 1) * 4096 + (fq & 1) * 2048 + (32 * wc + 16 * (fr >> 3) + (fr & 7)) * 16;
#pragma unroll
    for (int ai = 0; ai < 2; ++ai)
#pragma unroll
      for (int bj = 0; bj < 2; ++bj) { unsigned char* tb = base + (size_t)((2 * u0.pn + bj) * 32 + 4 * u0.pm + 2 * ai + wr) * 16384;
#pragma unroll
        for (int u = 0; u < 2; ++u)
#pragma unroll
          for (int n = 0; n < 2; ++n) *(u32x4*)(tb + u * 8192 + n * 128) = pack8(acc[ai][bj][2 * u][n], acc[ai][bj][2 * u + 1][n]); }
  } else {
    kloop<false>(c, acc, u0, u1);
    if (wr == 0) BAR;
    unsigned char* base = ws + WS_KF + (size_t)(4 * wc + fq) * 1024 + (16 * (fr >> 3) + (fr & 7)) * 16;
#pragma unroll
    for (int ai = 0; ai < 2; ++ai)
#pragma unroll
      for (int bj = 0; bj < 2; ++bj) { unsigned char* tb = base + (size_t)((2 * u0.pn + bj) * 32 + 4 * u0.pm + 2 * ai + wr) * 16384;
#pragma unroll
        for (int m = 0; m < 4; ++m) *(u32x4*)(tb + (m >> 1) * 512 + (m & 1) * 128) = pack8(acc[ai][bj][m][0], acc[ai][bj][m][1]); }
  }
  if (has1) {
    if (wr == 1) BAR;
    kloop<false>(c, acc, u1, u1);
    if (wr == 0) BAR;
    if (isQ) {
      const float C2 = 0.08838834764831845f * 1.4426950408889634f;
      unsigned char* base = ws + WS_QF + (size_t)(4 * wc + fq) * 512 + fr * 16;
#pragma unroll
      for (int ai = 0; ai < 2; ++ai)
#pragma unroll
        for (int bj = 0; bj < 2; ++bj)
#pragma unroll
          for (int m = 0; m < 4; ++m)
            *(u32x4*)(base + (size_t)((2 * u1.pn + bj) * 64 + 8 * u1.pm + 4 * ai + 2 * wr + (m >> 1)) * 8192 + (m & 1) * 256) = pack8(acc[ai][bj][m][0] * C2, acc[ai][bj][m][1] * C2);
    } else {
      unsigned char* base = ws + WS_HF + (size_t)((2 * u1.pn + (wc >> 1)) * 8 + 4 * (wc & 1) + fq) * 2048 + (64 * wr + fr) * 16;
#pragma unroll
      for (int ai = 0; ai < 2; ++ai)
#pragma unroll
        for (int m = 0; m < 4; ++m) { f32x4 hv[2];
#pragma unroll
          for (int n = 0; n < 2; ++n)
#pragma unroll
            for (int i = 0; i < 4; ++i) { const float e = __builtin_amdgcn_exp2f(acc[ai][0][m][n][i] * -1.4426950408889634f); hv[n][i] = acc[ai][1][m][n][i] * __builtin_amdgcn_rcpf(1.f + e); }
          *(u32x4*)(base + (size_t)(2 * u1.pm + ai) * 28 * 16384 + m * 256) = pack8(hv[0], hv[1]); }
    }
  }
  WAIT_V(0);
}

__global__ __launch_bounds__(512, 2) void k2_attn(unsigned char* __restrict__ ws) {
  extern __shared__ __attribute__((aligned(16))) unsigned char smem[];
  LAS unsigned char* lds = (LAS unsigned char*)smem;
  const int tid = threadIdx.x, wid = __builtin_amdgcn_readfirstlane(tid >> 6), lane = tid & 63, r32 = lane & 31, hi = lane >> 5;
  const int xcd = blockIdx.x & 7, r = blockIdx.x >> 3, h = 4 * xcd + (r >> 3), p = r & 7;
  const int blk = wid < 4 ? 15 - p : p, NTL = 2 * (16 - p), myNT = 2 * (blk + 1);
  const int row0 = 128 * blk + 32 * (wid & 3);
  const char* Kg = (const char*)ws + WS_KF + (size_t)h * 32 * 16384 + tid * 16; const char* Vg = (const char*)ws + WS_VF + (size_t)h * 32 * 16384 + tid * 16;
  auto dma = [&](int t) { const int s = (t & 1) * 32768;
#pragma unroll
    for (int i = 0; i < 2; ++i) { __builtin_amdgcn_global_load_lds((const unsigned*)(Kg + (size_t)t * 16384 + i * 8192), (LAS unsigned*)(lds + s + wid * 1024 + i * 8192), 16, 0, 0);
      __builtin_amdgcn_global_load_lds((const unsigned*)(Vg + (size_t)t * 16384 + i * 8192), (LAS unsigned*)(lds + s + 16384 + wid * 1024 + i * 8192), 16, 0, 0); } };
  bf16x8 qf[8];
  { const char* Qg = (const char*)ws + WS_QF + (size_t)(h * 64 + (row0 >> 5)) * 8192 + lane * 16;
#pragma unroll
    for (int d0 = 0; d0 < 8; ++d0) qf[d0] = *(const bf16x8*)(Qg + d0 * 1024); }
  dma(0);
  f32x16 o[4] = {}; float m = -1e30f, l = 0.f;
  for (int t = 0; t < NTL; ++t) {
    asm volatile("s_waitcnt vmcnt(0) lgkmcnt(0)\n\ts_barrier" ::: "memory");
    if (t + 1 < NTL) dma(t + 1);
    if (t < myNT) {
      const LAS unsigned char* kp = lds + (t & 1) * 32768 + hi * 1024 + r32 * 16; const LAS unsigned char* vp = lds + (t & 1) * 32768 + 16384 + hi * 2048 + r32 * 16;
      f32x16 s[2] = {};
#pragma unroll
      for (int kb = 0; kb < 2; ++kb)
#pragma unroll
        for (int d0 = 0; d0 < 8; ++d0) s[kb] = __builtin_amdgcn_mfma_f32_32x32x16_bf16(*(const LAS bf16x8*)(kp + d0 * 2048 + kb * 512), qf[d0], s[kb], 0, 0, 0);
      if (t >= myNT - 2) { const int thr = row0 + r32 - 64 * t - 4 * hi;
#pragma unroll
        for (int kb = 0; kb < 2; ++kb)
#pragma unroll
          for (int rr = 0; rr < 16; ++rr) { const int cc = 32 * kb + 16 * ((rr >> 2) & 1) + 8 * (rr >> 3) + (rr & 3); if (cc > thr) s[kb][rr] = -__builtin_inff(); } }
      float mx = s[0][0];
#pragma unroll
      for (int rr = 0; rr < 16; ++rr) mx = fmaxf(mx, fmaxf(s[0][rr], s[1][rr]));
      mx = fmaxf(mx, __shfl_xor(mx, 32));
      const float mn = fmaxf(m, mx), al = __builtin_amdgcn_exp2f(m - mn); m = mn;
      float ps = 0.f;
#pragma unroll
      for (int kb = 0; kb < 2; ++kb)
#pragma unroll
        for (int rr = 0; rr < 16; ++rr) { s[kb][rr] = __builtin_amdgcn_exp2f(s[kb][rr] - mn); ps += s[kb][rr]; }
      l = l * al + ps;
#pragma unroll
      for (int db = 0; db < 4; ++db) o[db] *= al;
#pragma unroll
      for (int ks = 0; ks < 4; ++ks) { u32x4 w;
#pragma unroll
        for (int j = 0; j < 4; ++j) w[j] = cvtpk(s[ks >> 1][8 * (ks & 1) + 2 * j], s[ks >> 1][8 * (ks & 1) + 2 * j + 1]);
        const bf16x8 pf = __builtin_bit_cast(bf16x8, w);
#pragma unroll
        for (int db = 0; db < 4; ++db) o[db] = __builtin_amdgcn_mfma_f32_32x32x16_bf16(*(const LAS bf16x8*)(vp + ks * 4096 + db * 512), pf, o[db], 0, 0, 0); }
    }
  }
  l += __shfl_xor(l, 32);
  const float il = 1.f / l;
  unsigned char* cb = ws + WS_CTX + ((size_t)(blk * 64 + 2 * h) * 8 + hi) * 2048 + (32 * (wid & 3) + r32) * 16;
#pragma unroll
  for (int db = 0; db < 4; ++db)
#pragma unroll
    for (int u = 0; u < 2; ++u) { u32x4 w;
#pragma unroll
      for (int j = 0; j < 4; ++j) w[j] = cvtpk(o[db][8 * u + 2 * j] * il, o[db][8 * u + 2 * j + 1] * il);
      *(u32x4*)(cb + (size_t)(4 * db + 2 * u) * 2048) = w; }
}

__global__ __launch_bounds__(512, 2) void k3_out(const u16* __restrict__ wo, const u16* __restrict__ wd, unsigned char* __restrict__ ws) {
  extern __shared__ __attribute__((aligned(16))) unsigned char smem[];
  LAS unsigned char* lds = (LAS unsigned char*)smem;
  const int tid = threadIdx.x, wid = __builtin_amdgcn_readfirstlane(tid >> 6), lane = tid & 63, wr = wid >> 2, wc = wid & 3, fr = lane & 15, fq = lane >> 4;
  const int xcd = blockIdx.x & 7, r = blockIdx.x >> 3, mt = 2 * xcd + (r >> 4), nt = (r >> 2) & 3, sp = r & 3;
  const int brow = tid >> 4, bslot = tid & 15;
  const unsigned voffB = brow * 1024u + (((bslot >> 1) ^ (brow & 7)) * 32) + (bslot & 1) * 16 + nt * 256, voffA = tid * 16u;
  const unsigned ldsw = (unsigned)(size_t)lds + wid * 1024u;
  auto stage = [&](int kk, int slot) {
    const char* a = kk < 64 ? (const char*)ws + WS_CTX + (size_t)(mt * 64 + kk) * 16384 : (const char*)ws + WS_HF + (size_t)(mt * 28 + kk - 64) * 16384;
    const char* b = kk < 64 ? (const char*)wo + (size_t)kk * 65536 : (const char*)wd + (size_t)(kk - 64) * 65536;
#pragma unroll
    for (int i = 0; i < 2; ++i) { glds(a + i * 8192, voffA, ldsw + slot * 32768 + i * 8192);
      glds(b + i * 32768, voffB, ldsw + slot * 32768 + 16384 + i * 8192); } };
  const int aoff = fq * 2048 + wr * 1024 + fr * 16;
  int boff[2];
  { const int q = fr >> 2, p = lane & 3, row = 4 * fq + q;
#pragma unroll
    for (int n = 0; n < 2; ++n) boff[n] = 16384 + row * 256 + (((2 * wc + n) ^ (row & 7)) * 32) + 8 * p; }
  f32x4 acc[4][2] = {};
  const int k0 = 23 * sp;
  auto flush = [&](int slot) { float* P = (float*)(ws + WS_PART) + (size_t)slot * 2048 * 512 + (size_t)(128 * mt + 64 * wr + fr) * 512 + 128 * nt + 32 * wc + 4 * fq;
#pragma unroll
    for (int m = 0; m < 4; ++m)
#pragma unroll
      for (int n = 0; n < 2; ++n) { *(f32x4*)(P + m * 16 * 512 + n * 16) = acc[m][n]; acc[m][n] = (f32x4){0.f, 0.f, 0.f, 0.f}; } };
  stage(k0, 0); stage(k0 + 1, 1); stage(k0 + 2, 2);
  for (int i = 0; i < 23; ++i) {
    if (i < 21) asm volatile("s_waitcnt vmcnt(8) lgkmcnt(0)\n\ts_barrier" ::: "memory");
    else asm volatile("s_waitcnt vmcnt(0) lgkmcnt(0)\n\ts_barrier" ::: "memory");
    if (i + 3 < 23) stage(k0 + i + 3, (i + 3) & 3);
    if (k0 + i == 64) { flush(2); asm volatile("s_waitcnt vmcnt(0)" ::: "memory"); }
    const LAS unsigned char* sb = lds + (i & 3) * 32768;
#pragma unroll
    for (int k = 0; k < 2; ++k) { bf16x8 A[4], B[2];
#pragma unroll
      for (int m = 0; m < 4; ++m) A[m] = *(const LAS bf16x8*)(sb + aoff + m * 256 + k * 8192);
#pragma unroll
      for (int n = 0; n < 2; ++n) { const s16x4 lo = trread(sb + boff[n] + k * 8192), hi = trread(sb + boff[n] + k * 8192 + 4096); B[n] = __builtin_shufflevector(lo, hi, 0, 1, 2, 3, 4, 5, 6, 7); }
#pragma unroll
      for (int m = 0; m < 4; ++m)
#pragma unroll
        for (int n = 0; n < 2; ++n) acc[m][n] = __builtin_amdgcn_mfma_f32_16x16x32_bf16(B[n], A[m], acc[m][n], 0, 0, 0); }
  }
  flush(sp < 2 ? sp : sp + 1);
}
__global__ __launch_bounds__(256) void k4_fin(const u16* __restrict__ x, const unsigned char* __restrict__ ws, u16* __restrict__ out) {
  const size_t i = (size_t)(blockIdx.x * 256 + threadIdx.x) * 4;
  const float* P = (const float*)(ws + WS_PART) + i; const size_t S = 2048 * 512;
  const f32x4 s = *(const f32x4*)P + *(const f32x4*)(P + S) + *(const f32x4*)(P + 2 * S), d = *(const f32x4*)(P + 3 * S) + *(const f32x4*)(P + 4 * S);
  const u32x2 xv = *(const u32x2*)(x + i);
  float o[4];
#pragma unroll
  for (int j = 0; j < 4; ++j) { const float dd = rbf(d[j]); const float g = rbf(dd * __builtin_amdgcn_rcpf(1.f + __builtin_amdgcn_exp2f(dd * -1.4426950408889634f)));
    const float xx = (j & 1) ? bfhi(xv[j >> 1]) : bflo(xv[j >> 1]); o[j] = xx + rbf(rbf(s[j]) * g); }
  u32x2 w; w.x = cvtpk(o[0], o[1]); w.y = cvtpk(o[2], o[3]); *(u32x2*)(out + i) = w;
}

extern "C" void kernel_launch(void* const* d_in, const int* in_sizes, int n_in, void* d_out, int out_size, void* d_ws, size_t ws_size, hipStream_t stream) {
  (void)in_sizes; (void)n_in; (void)out_size; (void)ws_size;
  const u16* x = (const u16*)d_in[0]; const u16* ckv = (const u16*)d_in[1]; const u16* wq = (const u16*)d_in[2]; const u16* wuk = (const u16*)d_in[3];
  const u16* wuv = (const u16*)d_in[4]; const u16* wo = (const u16*)d_in[5]; const u16* wg = (const u16*)d_in[7]; const u16* wu = (const u16*)d_in[8]; const u16* wd = (const u16*)d_in[9];
  unsigned char* ws = (unsigned char*)d_ws;
  (void)hipFuncSetAttribute((const void*)k1_proj, hipFuncAttributeMaxDynamicSharedMemorySize, 131072);
  (void)hipFuncSetAttribute((const void*)k2_attn, hipFuncAttributeMaxDynamicSharedMemorySize, 131072);
  (void)hipFuncSetAttribute((const void*)k3_out, hipFuncAttributeMaxDynamicSharedMemorySize, 131072);
  k0_ln<<<512, 64, 0, stream>>>(x, ckv, ws);
  k1_proj<<<256, 512, 131072, stream>>>(wq, wuk, wuv, wg, wu, ws);
  k2_attn<<<256, 512, 131072, stream>>>(ws);
  k3_out<<<256, 512, 131072, stream>>>(wo, wd, ws);
  k4_fin<<<2048 * 512 / 4 / 256, 256, 0, stream>>>(x, ws, (u16*)d_out);
}
```

```cpp
#include <hip/hip_runtime.h>
#define LAS __attribute__((address_space(3)))
#define DI __device__ __forceinline__
typedef unsigned short u16;
typedef short bf16x8 __attribute__((ext_vector_type(8)));
typedef short s16x4 __attribute__((ext_vector_type(4)));
typedef float f32x2 __attribute__((ext_vector_type(2)));
typedef float f32x4 __attribute__((ext_vector_type(4)));
typedef float f32x16 __attribute__((ext_vector_type(16)));
typedef unsigned u32x4 __attribute__((ext_vector_type(4)));
typedef unsigned u32x2 __attribute__((ext_vector_type(2)));
typedef __bf16 bf16x2_t __attribute__((ext_vector_type(2)));

#define MIB (1ull << 20)
#define WS_XT   (0 * MIB)
#define WS_CT   (2 * MIB)
#define WS_QF   (4 * MIB)
#define WS_KF   (20 * MIB)
#define WS_VF   (36 * MIB)
#define WS_CTX  (52 * MIB)
#define WS_HF   (68 * MIB)
#define WS_PART (76 * MIB)

#define WS_DBG (128 * MIB)
#define STAMP(kid, i) do {} while (0)
#define STAMP_DECL
DI unsigned cvtpk(float lo, float hi) { f32x2 v = {lo, hi}; bf16x2_t b = __builtin_convertvector(v, bf16x2_t); return __builtin_bit_cast(unsigned, b); }
DI float bflo(unsigned w) { return __uint_as_float(w << 16); }
DI float bfhi(unsigned w) { return __uint_as_float(w & 0xffff0000u); }
DI float rbf(float x) { return bflo(cvtpk(x, 0.f)); }
DI void glds(const void* gbase, unsigned voff, unsigned ldsaddr) {
  asm volatile("s_mov_b32 m0, %0\n\ts_nop 0\n\tglobal_load_lds_dwordx4 %1, %2" :: "s"(ldsaddr), "v"(voff), "s"(gbase) : "memory");
}
#define STPOL 2
DI void st16(void* p, u32x4 v) {
  asm volatile("global_store_dwordx4 %0, %1, off sc1\n\ts_nop 1" :: "v"(p), "v"(v) : "memory");
}
DI s16x4 trread(LAS const unsigned char* p) { return __builtin_amdgcn_ds_read_tr16_b64_v4i16((LAS s16x4*)p); }

__global__ __launch_bounds__(64) void k0_ln(const u16* __restrict__ x, const u16* __restrict__ ckv, unsigned char* __restrict__ ws) {
  STAMP_DECL; STAMP(0, 0);
  const int b = blockIdx.x, lane = threadIdx.x, r = lane & 7, cg = lane >> 3;
  const bool isx = b < 256;
  const int row = (b & 255) * 8 + r;
  const u16* src = (isx ? x : ckv) + (size_t)row * 512 + cg * 8;
  u32x4 v[8];
#pragma unroll
  for (int i = 0; i < 8; ++i) v[i] = *(const u32x4*)(src + i * 64);
  if (isx) {
    float s = 0.f;
#pragma unroll
    for (int i = 0; i < 8; ++i)
#pragma unroll
      for (int j = 0; j < 4; ++j) s += bflo(v[i][j]) + bfhi(v[i][j]);
    s += __shfl_xor(s, 8); s += __shfl_xor(s, 16); s += __shfl_xor(s, 32);
    const float mean = s * (1.f / 512.f);
    float q = 0.f;
#pragma unroll
    for (int i = 0; i < 8; ++i)
#pragma unroll
      for (int j = 0; j < 4; ++j) { const float a = bflo(v[i][j]) - mean, c = bfhi(v[i][j]) - mean; q += a * a + c * c; }
    q += __shfl_xor(q, 8); q += __shfl_xor(q, 16); q += __shfl_xor(q, 32);
    const float m = rbf(mean), den = 1.f / rbf(sqrtf(rbf(rbf(q * (1.f / 512.f)) + 1e-5f)));
#pragma unroll
    for (int i = 0; i < 8; ++i)
#pragma unroll
      for (int j = 0; j < 4; ++j) { const unsigned d = cvtpk(bflo(v[i][j]) - m, bfhi(v[i][j]) - m); v[i][j] = cvtpk(bflo(d) * den, bfhi(d) * den); }
  }
  unsigned char* dst = ws + (isx ? WS_XT : WS_CT) + (size_t)(row >> 7) * 131072 + cg * 2048 + (row & 127) * 16;
#pragma unroll
  for (int i = 0; i < 8; ++i) *(u32x4*)(dst + i * 16384) = v[i];
  STAMP(0, 7);
}

namespace k1 {
constexpr int HTB = 16384;
#define SA(b, h) (((b) * 2 + (h)) * HTB)
#define SB(b, h) ((4 + (b) * 2 + (h)) * HTB)
#define WAIT_V(n) asm volatile("s_waitcnt vmcnt(" #n ")" ::: "memory")
#define WAIT_L(n) asm volatile("s_waitcnt lgkmcnt(" #n ")" ::: "memory")
#define BAR __builtin_amdgcn_s_barrier()
#define SCHED __builtin_amdgcn_sched_barrier(0)
struct Unit { const char* a; const char* b0; const char* b1; unsigned ldb; unsigned voffB; int pm, pn; };
struct Ctx { LAS unsigned char* lds; unsigned ldsw, voffA; int aoff, boff0, boff1; };

DI void stageA(const Ctx& c, int off, const char* g) {
#pragma unroll
  for (int i = 0; i < 2; ++i) glds(g + i * 8192, c.voffA, off + c.ldsw + i * 8192);
}
DI void stageB(const Ctx& c, int off, const char* g, unsigned voff, unsigned ldb) {
#pragma unroll
  for (int i = 0; i < 2; ++i) glds(g + (size_t)i * 32 * ldb, voff, off + c.ldsw + i * 8192);
}
DI void lda(const Ctx& c, bf16x8 (&d)[4][2], int off) {
#pragma unroll
  for (int m = 0; m < 4; ++m)
#pragma unroll
    for (int k = 0; k < 2; ++k) d[m][k] = *(const LAS bf16x8*)(c.lds + off + c.aoff + m * 256 + k * 8192);
}
DI void ldb(const Ctx& c, bf16x8 (&d)[2][2], int off) {
#pragma unroll
  for (int n = 0; n < 2; ++n)
#pragma unroll
    for (int k = 0; k < 2; ++k) { const LAS unsigned char* p = c.lds + off + (n ? c.boff1 : c.boff0) + k * 8192;
      const s16x4 lo = trread(p), hi = trread(p + 1024); d[n][k] = __builtin_shufflevector(lo, hi, 0, 1, 2, 3, 4, 5, 6, 7); }
}
template <bool VT> DI void mma(f32x4 (&acc)[4][2], const bf16x8 (&A)[4][2], const bf16x8 (&B)[2][2]) {
  __builtin_amdgcn_s_setprio(1);
#pragma unroll
  for (int m = 0; m < 4; ++m)
#pragma unroll
    for (int n = 0; n < 2; ++n)
#pragma unroll
      for (int k = 0; k < 2; ++k)
        acc[m][n] = VT ? __builtin_amdgcn_mfma_f32_16x16x32_bf16(A[m][k], B[n][k], acc[m][n], 0, 0, 0) : __builtin_amdgcn_mfma_f32_16x16x32_bf16(B[n][k], A[m][k], acc[m][n], 0, 0, 0);
  __builtin_amdgcn_s_setprio(0);
}
constexpr size_t AH = 131072, AK = 16384;
template <bool VT> DI void kloop(const Ctx& c, f32x4 (&acc)[2][2][4][2], const Unit& cur, const Unit& nxt) {
  bf16x8 At[4][2], B0[2][2], B1[2][2];
#pragma unroll
  for (int a = 0; a < 2; ++a)
#pragma unroll
    for (int b = 0; b < 2; ++b)
#pragma unroll
      for (int m = 0; m < 4; ++m)
#pragma unroll
        for (int n = 0; n < 2; ++n) acc[a][b][m][n] = (f32x4){0.f, 0.f, 0.f, 0.f};
#pragma unroll 1
  for (int t = 0; t < 8; t += 2) {
    const bool last = (t == 6);
    const char* a1 = cur.a + (size_t)(t + 1) * AK;
    const char* a2 = last ? nxt.a : cur.a + (size_t)(t + 2) * AK;
    const unsigned ldb2 = last ? nxt.ldb : cur.ldb, vo2 = last ? nxt.voffB : cur.voffB;
    const char* b20 = last ? nxt.b0 : cur.b0 + (size_t)(t + 2) * 64 * cur.ldb;
    const char* b21 = last ? nxt.b1 : cur.b1 + (size_t)(t + 2) * 64 * cur.ldb;
    const char* a3 = a2 + AK; const char* b30 = b20 + (size_t)64 * ldb2; const char* b31 = b21 + (size_t)64 * ldb2;
    ldb(c, B0, SB(0, 0)); ldb(c, B1, SB(0, 1)); SCHED; lda(c, At, SA(0, 0)); stageA(c, SA(1, 1), a1 + AH);
    WAIT_V(8); WAIT_L(0); BAR; mma<VT>(acc[0][0], At, B0); mma<VT>(acc[0][1], At, B1); BAR; SCHED;
    lda(c, At, SA(0, 1)); stageB(c, SB(0, 0), b20, vo2, ldb2); stageB(c, SB(0, 1), b21, vo2, ldb2); stageA(c, SA(0, 0), a2);
    WAIT_V(8); WAIT_L(0); BAR; mma<VT>(acc[1][0], At, B0); mma<VT>(acc[1][1], At, B1); BAR; SCHED;
    ldb(c, B0, SB(1, 0)); ldb(c, B1, SB(1, 1)); SCHED; lda(c, At, SA(1, 0)); stageA(c, SA(0, 1), a2 + AH);
    WAIT_V(8); WAIT_L(0); BAR; mma<VT>(acc[0][0], At, B0); mma<VT>(acc[0][1], At, B1); BAR; SCHED;
    lda(c, At, SA(1, 1)); stageB(c, SB(1, 0), b30, vo2, ldb2); stageB(c, SB(1, 1), b31, vo2, ldb2); stageA(c, SA(1, 0), a3);
    WAIT_V(8); WAIT_L(0); BAR; mma<VT>(acc[1][0], At, B0); mma<VT>(acc[1][1], At, B1); BAR; SCHED;
  }
}
DI u32x4 pack8(f32x4 a, f32x4 b) { u32x4 w; w.x = cvtpk(a[0], a[1]); w.y = cvtpk(a[2], a[3]); w.z = cvtpk(b[0], b[1]); w.w = cvtpk(b[2], b[3]); return w; }
}

__global__ __launch_bounds__(512, 2) void k1_proj(const u16* __restrict__ wq, const u16* __restrict__ wuk, const u16* __restrict__ wuv,
                                                  const u16* __restrict__ wg, const u16* __restrict__ wu, unsigned char* __restrict__ ws) {
  using namespace k1;
  STAMP_DECL; STAMP(1, 0);
  extern __shared__ __attribute__((aligned(16))) unsigned char smem[];
  const int tid = threadIdx.x, wid = __builtin_amdgcn_readfirstlane(tid >> 6), lane = tid & 63, wr = wid >> 2, wc = wid & 3, fr = lane & 15, fq = lane >> 4;
  Ctx c; c.lds = (LAS unsigned char*)smem; c.ldsw = (unsigned)(size_t)c.lds + wid * 1024u; c.voffA = tid * 16u;
  c.aoff = fq * 2048 + wr * 1024 + fr * 16;
  { const int q = fr >> 2, p = lane & 3, sg = q | ((fq & 1) << 2);
    c.boff0 = (8 * fq + q) * 256 + (((2 * wc) ^ sg) * 32) + 8 * p; c.boff1 = (8 * fq + q) * 256 + (((2 * wc + 1) ^ sg) * 32) + 8 * p; }
  const int brow = tid >> 4, bslot = tid & 15, bsg = (brow & 3) | (((brow >> 3) & 1) << 2);
  const unsigned bcol = (((bslot >> 1) ^ bsg) * 32) + (bslot & 1) * 16;
  const int xcd = blockIdx.x & 7, r = blockIdx.x >> 3;
  Unit u0, u1;
  const bool isV = r >= 16;
  u0.pm = r & 7; u0.pn = 2 * xcd + ((r >> 3) & 1);
  u0.a = (const char*)ws + WS_CT + (size_t)u0.pm * 2 * AH; u0.b0 = (const char*)(isV ? wuv : wuk) + u0.pn * 512; u0.b1 = u0.b0 + 256; u0.ldb = 8192; u0.voffB = brow * 8192u + bcol;
  const bool isQ = r < 16, has1 = r < 30;
  if (isQ) { u1.pm = r & 7; u1.pn = 2 * xcd + (r >> 3); u1.b0 = (const char*)wq + u1.pn * 512; u1.b1 = u1.b0 + 256; u1.ldb = 8192; }
  else { const int u = 14 * xcd + (has1 ? r - 16 : 0); u1.pm = u & 7; u1.pn = u >> 3; u1.b0 = (const char*)wg + u1.pn * 256; u1.b1 = (const char*)wu + u1.pn * 256; u1.ldb = 3584; }
  u1.a = (const char*)ws + WS_XT + (size_t)u1.pm * 2 * AH; u1.voffB = brow * u1.ldb + bcol;

  f32x4 acc[2][2][4][2];
  stageB(c, SB(0, 0), u0.b0, u0.voffB, u0.ldb); stageB(c, SB(0, 1), u0.b1, u0.voffB, u0.ldb); stageA(c, SA(0, 0), u0.a); stageA(c, SA(0, 1), u0.a + AH);
  if (wr == 1) BAR;
  WAIT_V(2); BAR;
  stageB(c, SB(1, 0), u0.b0 + (size_t)64 * u0.ldb, u0.voffB, u0.ldb); stageA(c, SA(1, 0), u0.a + AK); stageB(c, SB(1, 1), u0.b1 + (size_t)64 * u0.ldb, u0.voffB, u0.ldb);
  WAIT_V(6); BAR;
  STAMP(1, 1);
  if (isV) {
    kloop<true>(c, acc, u0, u1);
    if (wr == 0) BAR;
    STAMP(1, 2);
    unsigned char* base = ws + WS_VF + (size_t)(fq >> 1) * 4096 + (fq & 1) * 2048 + (32 * wc + 16 * (fr >> 3) + (fr & 7)) * 16;
#pragma unroll
    for (int ai = 0; ai < 2; ++ai)
#pragma unroll
      for (int bj = 0; bj < 2; ++bj) { unsigned char* tb = base + (size_t)((2 * u0.pn + bj) * 32 + 4 * u0.pm + 2 * ai + wr) * 16384;
#pragma unroll
        for (int u = 0; u < 2; ++u)
#pragma unroll
          for (int n = 0; n < 2; ++n) st16(tb + u * 8192 + n * 128, pack8(acc[ai][bj][2 * u][n], acc[ai][bj][2 * u + 1][n])); }
  } else {
    kloop<false>(c, acc, u0, u1);
    if (wr == 0) BAR;
    STAMP(1, 2);
    unsigned char* base = ws + WS_KF + (size_t)(4 * wc + fq) * 1024 + (16 * (fr >> 3) + (fr & 7)) * 16;
#pragma unroll
    for (int ai = 0; ai < 2; ++ai)
#pragma unroll
      for (int bj = 0; bj < 2; ++bj) { unsigned char* tb = base + (size_t)((2 * u0.pn + bj) * 32 + 4 * u0.pm + 2 * ai + wr) * 16384;
#pragma unroll
        for (int m = 0; m < 4; ++m) st16(tb + (m >> 1) * 512 + (m & 1) * 128, pack8(acc[ai][bj][m][0], acc[ai][bj][m][1])); }
  }
  STAMP(1, 3);
  if (has1) {
    if (wr == 1) BAR;
    kloop<false>(c, acc, u1, u1);
    if (wr == 0) BAR;
    STAMP(1, 4);
    if (isQ) {
      const float C2 = 0.08838834764831845f * 1.4426950408889634f;
      unsigned char* base = ws + WS_QF + (size_t)(4 * wc + fq) * 512 + fr * 16;
#pragma unroll
      for (int ai = 0; ai < 2; ++ai)
#pragma unroll
        for (int bj = 0; bj < 2; ++bj)
#pragma unroll
          for (int m = 0; m < 4; ++m)
            st16(base + (size_t)((2 * u1.pn + bj) * 64 + 8 * u1.pm + 4 * ai + 2 * wr + (m >> 1)) * 8192 + (m & 1) * 256, pack8(acc[ai][bj][m][0] * C2, acc[ai][bj][m][1] * C2));
    } else {
      unsigned char* base = ws + WS_HF + (size_t)((2 * u1.pn + (wc >> 1)) * 8 + 4 * (wc & 1) + fq) * 2048 + (64 * wr + fr) * 16;
#pragma unroll
      for (int ai = 0; ai < 2; ++ai)
#pragma unroll
        for (int m = 0; m < 4; ++m) { f32x4 hv[2];
#pragma unroll
          for (int n = 0; n < 2; ++n)
#pragma unroll
            for (int i = 0; i < 4; ++i) { const float e = __builtin_amdgcn_exp2f(acc[ai][0][m][n][i] * -1.4426950408889634f); hv[n][i] = acc[ai][1][m][n][i] * __builtin_amdgcn_rcpf(1.f + e); }
          st16(base + (size_t)(2 * u1.pm + ai) * 28 * 16384 + m * 256, pack8(hv[0], hv[1])); }
    }
  }
  STAMP(1, 5);
  WAIT_V(0);
  STAMP(1, 7);
}

__global__ __launch_bounds__(512, 2) void k2_attn(unsigned char* __restrict__ ws) {
  extern __shared__ __attribute__((aligned(16))) unsigned char smem[];
  LAS unsigned char* lds = (LAS unsigned char*)smem;
  const int tid = threadIdx.x, wid = __builtin_amdgcn_readfirstlane(tid >> 6), lane = tid & 63, r32 = lane & 31, hi = lane >> 5;
  STAMP_DECL; STAMP(2, 0);
  const int xcd = blockIdx.x & 7, r = blockIdx.x >> 3, h = 4 * xcd + (r >> 3), p = r & 7;
  const int blk = wid < 4 ? 15 - p : p, NTL = 2 * (16 - p), myNT = 2 * (blk + 1);
  const int row0 = 128 * blk + 32 * (wid & 3);
  const char* Kg = (const char*)ws + WS_KF + (size_t)h * 32 * 16384 + tid * 16; const char* Vg = (const char*)ws + WS_VF + (size_t)h * 32 * 16384 + tid * 16;
  auto dma = [&](int t) { const int s = (t & 1) * 32768;
#pragma unroll
    for (int i = 0; i < 2; ++i) { __builtin_amdgcn_global_load_lds((const unsigned*)(Kg + (size_t)t * 16384 + i * 8192), (LAS unsigned*)(lds + s + wid * 1024 + i * 8192), 16, 0, 0);
      __builtin_amdgcn_global_load_lds((const unsigned*)(Vg + (size_t)t * 16384 + i * 8192), (LAS unsigned*)(lds + s + 16384 + wid * 1024 + i * 8192), 16, 0, 0); } };
  bf16x8 qf[8];
  { const char* Qg = (const char*)ws + WS_QF + (size_t)(h * 64 + (row0 >> 5)) * 8192 + lane * 16;
#pragma unroll
    for (int d0 = 0; d0 < 8; ++d0) qf[d0] = *(const bf16x8*)(Qg + d0 * 1024); }
  dma(0);
  f32x16 o[4] = {}; float m = -1e30f, l = 0.f;
  for (int t = 0; t < NTL; ++t) {
    asm volatile("s_waitcnt vmcnt(0) lgkmcnt(0)\n\ts_barrier" ::: "memory");
    if (t + 1 < NTL) dma(t + 1);
    if (t < myNT) {
      const LAS unsigned char* kp = lds + (t & 1) * 32768 + hi * 1024 + r32 * 16; const LAS unsigned char* vp = lds + (t & 1) * 32768 + 16384 + hi * 2048 + r32 * 16;
      f32x16 s[2] = {};
#pragma unroll
      for (int kb = 0; kb < 2; ++kb)
#pragma unroll
        for (int d0 = 0; d0 < 8; ++d0) s[kb] = __builtin_amdgcn_mfma_f32_32x32x16_bf16(*(const LAS bf16x8*)(kp + d0 * 2048 + kb * 512), qf[d0], s[kb], 0, 0, 0);
      if (t >= myNT - 2) { const int thr = row0 + r32 - 64 * t - 4 * hi;
#pragma unroll
        for (int kb = 0; kb < 2; ++kb)
#pragma unroll
          for (int rr = 0; rr < 16; ++rr) { const int cc = 32 * kb + 16 * ((rr >> 2) & 1) + 8 * (rr >> 3) + (rr & 3); if (cc > thr) s[kb][rr] = -__builtin_inff(); } }
      float mx = s[0][0];
#pragma unroll
      for (int rr = 0; rr < 16; ++rr) mx = fmaxf(mx, fmaxf(s[0][rr], s[1][rr]));
      mx = fmaxf(mx, __shfl_xor(mx, 32));
      const float mn = fmaxf(m, mx), al = __builtin_amdgcn_exp2f(m - mn); m = mn;
      float ps = 0.f;
#pragma unroll
      for (int kb = 0; kb < 2; ++kb)
#pragma unroll
        for (int rr = 0; rr < 16; ++rr) { s[kb][rr] = __builtin_amdgcn_exp2f(s[kb][rr] - mn); ps += s[kb][rr]; }
      l = l * al + ps;
#pragma unroll
      for (int db = 0; db < 4; ++db) o[db] *= al;
#pragma unroll
      for (int ks = 0; ks < 4; ++ks) { u32x4 w;
#pragma unroll
        for (int j = 0; j < 4; ++j) w[j] = cvtpk(s[ks >> 1][8 * (ks & 1) + 2 * j], s[ks >> 1][8 * (ks & 1) + 2 * j + 1]);
        const bf16x8 pf = __builtin_bit_cast(bf16x8, w);
#pragma unroll
        for (int db = 0; db < 4; ++db) o[db] = __builtin_amdgcn_mfma_f32_32x32x16_bf16(*(const LAS bf16x8*)(vp + ks * 4096 + db * 512), pf, o[db], 0, 0, 0); }
    }
  }
  l += __shfl_xor(l, 32);
  const float il = 1.f / l;
  unsigned char* cb = ws + WS_CTX + ((size_t)(blk * 64 + 2 * h) * 8 + hi) * 2048 + (32 * (wid & 3) + r32) * 16;
#pragma unroll
  for (int db = 0; db < 4; ++db)
#pragma unroll
    for (int u = 0; u < 2; ++u) { u32x4 w;
#pragma unroll
      for (int j = 0; j < 4; ++j) w[j] = cvtpk(o[db][8 * u + 2 * j] * il, o[db][8 * u + 2 * j + 1] * il);
      st16(cb + (size_t)(4 * db + 2 * u) * 2048, w); }
  STAMP(2, 7);
}

__global__ __launch_bounds__(512, 2) void k3_out(const u16* __restrict__ wo, const u16* __restrict__ wd, unsigned char* __restrict__ ws) {
  extern __shared__ __attribute__((aligned(16))) unsigned char smem[];
  LAS unsigned char* lds = (LAS unsigned char*)smem;
  const int tid = threadIdx.x, wid = __builtin_amdgcn_readfirstlane(tid >> 6), lane = tid & 63, wr = wid >> 2, wc = wid & 3, fr = lane & 15, fq = lane >> 4;
  STAMP_DECL; STAMP(3, 0);
  const int xcd = blockIdx.x & 7, r = blockIdx.x >> 3,
    mt = 2 * xcd + (r >> 4), nt = (r >> 2) & 3, sp = r & 3;
  const int brow = tid >> 4, bslot = tid & 15;
  const unsigned voffB = brow * 1024u + (((bslot >> 1) ^ (brow & 7)) * 32) + (bslot & 1) * 16 + nt * 256, voffA = tid * 16u;
  const unsigned ldsw = (unsigned)(size_t)lds + wid * 1024u;
  auto stage = [&](int kk, int slot) {
    const char* a = kk < 64 ? (const char*)ws + WS_CTX + (size_t)(mt * 64 + kk) * 16384 : (const char*)ws + WS_HF + (size_t)(mt * 28 + kk - 64) * 16384;
    const char* b = kk < 64 ? (const char*)wo + (size_t)kk * 65536 : (const char*)wd + (size_t)(kk - 64) * 65536;
#pragma unroll
    for (int i = 0; i < 2; ++i) { glds(a + i * 8192, voffA, ldsw + slot * 32768 + i * 8192);
      glds(b + i * 32768, voffB, ldsw + slot * 32768 + 16384 + i * 8192); } };
  const int aoff = fq * 2048 + wr * 1024 + fr * 16;
  int boff[2];
  { const int q = fr >> 2, p = lane & 3, row = 4 * fq + q;
#pragma unroll
    for (int n = 0; n < 2; ++n) boff[n] = 16384 + row * 256 + (((2 * wc + n) ^ (row & 7)) * 32) + 8 * p; }
  f32x4 acc[4][2] = {};
  const int k0 = 23 * sp;
  auto flush = [&](int slot) { float* P = (float*)(ws + WS_PART) + (size_t)slot * 2048 * 512 + (size_t)(128 * mt + 64 * wr + fr) * 512 + 128 * nt + 32 * wc + 4 * fq;
#pragma unroll
    for (int m = 0; m < 4; ++m)
#pragma unroll
      for (int n = 0; n < 2; ++n) { *(f32x4*)(P + m * 16 * 512 + n * 16) = acc[m][n]; acc[m][n] = (f32x4){0.f, 0.f, 0.f, 0.f}; } };
  stage(k0, 0); stage(k0 + 1, 1); stage(k0 + 2, 2);
  for (int i = 0; i < 23; ++i) {
    if (i < 21) asm volatile("s_waitcnt vmcnt(8) lgkmcnt(0)\n\ts_barrier" ::: "memory");
    else asm volatile("s_waitcnt vmcnt(0) lgkmcnt(0)\n\ts_barrier" ::: "memory");
    if (i + 3 < 23) stage(k0 + i + 3, (i + 3) & 3);
    if (k0 + i == 64) { flush(2); asm volatile("s_waitcnt vmcnt(0)" ::: "memory"); }
    const LAS unsigned char* sb = lds + (i & 3) * 32768;
#pragma unroll
    for (int k = 0; k < 2; ++k) { bf16x8 A[4], B[2];
#pragma unroll
      for (int m = 0; m < 4; ++m) A[m] = *(const LAS bf16x8*)(sb + aoff + m * 256 + k * 8192);
#pragma unroll
      for (int n = 0; n < 2; ++n) { const s16x4 lo = trread(sb + boff[n] + k * 8192), hi = trread(sb + boff[n] + k * 8192 + 4096); B[n] = __builtin_shufflevector(lo, hi, 0, 1, 2, 3, 4, 5, 6, 7); }
#pragma unroll
      for (int m = 0; m < 4; ++m)
#pragma unroll
        for (int n = 0; n < 2; ++n) acc[m][n] = __builtin_amdgcn_mfma_f32_16x16x32_bf16(B[n], A[m], acc[m][n], 0, 0, 0); }
  }
  STAMP(3, 1);
  flush(sp < 2 ? sp : sp + 1);
  STAMP(3, 7);
}
__global__ __launch_bounds__(256) void k4_fin(const u16* __restrict__ x, const unsigned char* __restrict__ ws, u16* __restrict__ out) {
  STAMP_DECL; STAMP(4, 0);
  const size_t i = (size_t)(blockIdx.x * 256 + threadIdx.x) * 4;
  const float* P = (const float*)(ws + WS_PART) + i; const size_t S = 2048 * 512;
  const f32x4 s = *(const f32x4*)P + *(const f32x4*)(P + S) + *(const f32x4*)(P + 2 * S), d = *(const f32x4*)(P + 3 * S) + *(const f32x4*)(P + 4 * S);
  const u32x2 xv = *(const u32x2*)(x + i);
  float o[4];
#pragma unroll
  for (int j = 0; j < 4; ++j) { const float dd = rbf(d[j]); const float g = rbf(dd * __builtin_amdgcn_rcpf(1.f + __builtin_amdgcn_exp2f(dd * -1.4426950408889634f)));
    const float xx = (j & 1) ? bfhi(xv[j >> 1]) : bflo(xv[j >> 1]); o[j] = xx + rbf(rbf(s[j]) * g); }
  u32x2 w; w.x = cvtpk(o[0], o[1]); w.y = cvtpk(o[2], o[3]); *(u32x2*)(out + i) = w;
  STAMP(4, 7);
}

extern "C" void kernel_launch(void* const* d_in, const int* in_sizes, int n_in, void* d_out, int out_size, void* d_ws, size_t ws_size, hipStream_t stream) {
  (void)in_sizes; (void)n_in; (void)out_size; (void)ws_size;
  const u16* x = (const u16*)d_in[0]; const u16* ckv = (const u16*)d_in[1]; const u16* wq = (const u16*)d_in[2]; const u16* wuk = (const u16*)d_in[3];
  const u16* wuv = (const u16*)d_in[4]; const u16* wo = (const u16*)d_in[5]; const u16* wg = (const u16*)d_in[7]; const u16* wu = (const u16*)d_in[8]; const u16* wd = (const u16*)d_in[9];
  unsigned char* ws = (unsigned char*)d_ws;
  (void)hipFuncSetAttribute((const void*)k1_proj, hipFuncAttributeMaxDynamicSharedMemorySize, 131072);
  (void)hipFuncSetAttribute((const void*)k2_attn, hipFuncAttributeMaxDynamicSharedMemorySize, 131072);
  (void)hipFuncSetAttribute((const void*)k3_out, hipFuncAttributeMaxDynamicSharedMemorySize, 131072);
  k0_ln<<<512, 64, 0, stream>>>(x, ckv, ws);
  k1_proj<<<256, 512, 131072, stream>>>(wq, wuk, wuv, wg, wu, ws);
  k2_attn<<<256, 512, 131072, stream>>>(ws);
  k3_out<<<256, 512, 131072, stream>>>(wo, wd, ws);
  k4_fin<<<2048 * 512 / 4 / 256, 256, 0, stream>>>(x, ws, (u16*)d_out);
}
```

```cpp
#include <hip/hip_runtime.h>
typedef unsigned short u16;
#define MEGA_ASM \
  "v_mbcnt_lo_u32_b32 v0, -1, 0\n\t" \
  "v_mbcnt_hi_u32_b32 v0, -1, v0\n\t" \
  "v_lshlrev_b32_e32 v13, 4, v0\n\t" \
  "s_lshl_b32 s52, %[bid], 3\n\t" \
  "s_add_u32 s52, s52, %[w]\n\t" \
  "s_lshl_b32 s52, s52, 10\n\t" \
  "s_mov_b64 s[54:55], %[x]\n\t" \
  "s_add_u32 s54, s54, s52\n\t" \
  "s_addc_u32 s55, s55, 0\n\t" \
  "global_load_dwordx4 v[208:211], v13, s[54:55]\n\t" \
  "s_bfe_u32 s66, %[bid], 0x30003\n\t" \
  "s_lshl_b32 s62, %[w], 12\n\t" \
  "s_lshr_b32 s54, %[bid], 6\n\t" \
  "s_add_u32 s48, s54, s66\n\t" \
  "s_lshl_b32 s55, %[bid], 2\n\t" \
  "s_add_u32 s48, s48, s55\n\t" \
  "s_and_b32 s48, s48, 7\n\t" \
  "s_lshl_b32 s52, s66, 18\n\t" \
  "s_lshl_b32 s53, %[w], 15\n\t" \
  "s_add_u32 s52, s52, s53\n\t" \
  "s_lshl_b32 s55, s48, 6\n\t" \
  "s_add_u32 s52, s52, s55\n\t" \
  "s_mov_b64 s[40:41], %[ckv]\n\t" \
  "s_add_u32 s40, s40, s52\n\t" \
  "s_addc_u32 s41, s41, 0\n\t" \
  "v_lshrrev_b32_e32 v12, 5, v0\n\t" \
  "v_lshlrev_b32_e32 v12, 1, v12\n\t" \
  "v_and_b32_e32 v9, 3, v0\n\t" \
  "v_xor_b32_e32 v12, v12, v9\n\t" \
  "v_lshlrev_b32_e32 v12, 4, v12\n\t" \
  "v_lshrrev_b32_e32 v9, 2, v0\n\t" \
  "v_lshl_or_b32 v1, v9, 10, v12\n\t" \
  "v_add_u32_e32 v2, 0x3c00, v1\n\t" \
  "s_add_u32 m0, s62, 0x0\n\t" \
  "s_nop 0\n\t" \
  "global_load_lds_dwordx4 v1, s[40:41] offset:0 sc1\n\t" \
  "global_load_lds_dwordx4 v2, s[40:41] offset:1024 sc1\n\t" \
  "s_and_b32 s38, %[bid], 7\n\t" \
  "s_lshr_b32 s64, %[bid], 7\n\t" \
  "s_and_b32 s59, s54, 1\n\t" \
  "s_lshl_b32 s52, s38, 1\n\t" \
  "s_add_u32 s59, s59, s52\n\t" \
  "s_lshl_b32 s52, s59, 9\n\t" \
  "s_add_u32 s52, s52, s53\n\t" \
  "s_lshl_b32 s55, s48, 18\n\t" \
  "s_add_u32 s52, s52, s55\n\t" \
  "s_cmp_lg_u32 s64, 0\n\t" \
  "s_cselect_b64 s[42:43], %[wuv], %[wuk]\n\t" \
  "s_add_u32 s42, s42, s52\n\t" \
  "s_addc_u32 s43, s43, 0\n\t" \
  "s_sub_u32 s42, s42, 2048\n\t" \
  "s_subb_u32 s43, s43, 0\n\t" \
  "v_and_b32_e32 v10, 15, v0\n\t" \
  "v_lshrrev_b32_e32 v11, 4, v0\n\t" \
  "s_lshl_b32 s52, %[w], 1\n\t" \
  "s_and_b32 s52, s52, 4\n\t" \
  "v_or_b32_e32 v12, s52, v11\n\t" \
  "v_lshrrev_b32_e32 v9, 1, v10\n\t" \
  "v_xor_b32_e32 v12, v12, v9\n\t" \
  "v_lshlrev_b32_e32 v12, 5, v12\n\t" \
  "v_and_b32_e32 v9, 1, v0\n\t" \
  "v_lshl_or_b32 v12, v9, 4, v12\n\t" \
  "v_lshl_or_b32 v3, v11, 13, v12\n\t" \
  "global_load_lds_dwordx4 v3, s[42:43] offset:2048\n\t" \
  "s_sub_u32 s54, s42, 768\n\t" \
  "s_subb_u32 s55, s43, 0\n\t" \
  "v_mov_b32_e32 v4, v3\n\t" \
  "v_mov_b32_e32 v5, 0\n\t" \
  "v_lshl_add_u64 v[4:5], v[4:5], 0, s[54:55]\n\t" \
  "s_mov_b32 s44, 0x40000\n\t" \
  "s_mov_b32 s45, 0\n\t" \
  "global_load_lds_dwordx4 v[4:5], off offset:3072\n\t" \
  "s_xor_b32 s54, s48, 1\n\t" \
  "s_xor_b32 s55, s48, 0\n\t" \
  "s_sub_u32 s54, s54, s55\n\t" \
  "s_ashr_i32 s73, s54, 31\n\t" \
  "s_lshl_b32 s72, s54, 18\n\t" \
  "s_lshl_b32 s74, s54, 6\n\t" \
  "s_xor_b32 s54, s48, 2\n\t" \
  "s_xor_b32 s55, s48, 1\n\t" \
  "s_sub_u32 s54, s54, s55\n\t" \
  "s_ashr_i32 s77, s54, 31\n\t" \
  "s_lshl_b32 s76, s54, 18\n\t" \
  "s_lshl_b32 s78, s54, 6\n\t" \
  "s_xor_b32 s54, s48, 4\n\t" \
  "s_xor_b32 s55, s48, 3\n\t" \
  "s_sub_u32 s54, s54, s55\n\t" \
  "s_ashr_i32 s81, s54, 31\n\t" \
  "s_lshl_b32 s80, s54, 18\n\t" \
  "s_lshl_b32 s82, s54, 6\n\t" \
  "s_xor_b32 s54, s48, 8\n\t" \
  "s_xor_b32 s55, s48, 7\n\t" \
  "s_sub_u32 s54, s54, s55\n\t" \
  "s_ashr_i32 s45, s54, 31\n\t" \
  "s_lshl_b32 s44, s54, 18\n\t" \
  "s_lshl_b32 s46, s54, 6\n\t" \
  "s_add_u32 s40, s40, s74\n\t" \
  "s_addc_u32 s41, s41, s73\n\t" \
  "s_add_u32 s42, s42, s72\n\t" \
  "s_addc_u32 s43, s43, s73\n\t" \
  "v_lshl_add_u64 v[4:5], v[4:5], 0, s[72:73]\n\t" \
  "v_lshlrev_b32_e32 v13, 4, v0\n\t" \
  "s_mov_b64 s[60:61], %[ws]\n\t" \
  "s_add_u32 s60, s60, 0x7000000\n\t" \
  "s_addc_u32 s61, s61, 0\n\t" \
  "s_load_dwordx2 s[36:37], s[60:61], 0x10000\n\t" \
  "s_lshr_b32 s39, %[bid], 3\n\t" \
  "s_and_b32 s63, %[w], 3\n\t" \
  "v_lshlrev_b32_e32 v12, 4, v11\n\t" \
  "v_lshl_or_b32 v12, v10, 6, v12\n\t" \
  "v_lshrrev_b32_e32 v9, 3, v10\n\t" \
  "v_lshlrev_b32_e32 v9, 5, v9\n\t" \
  "v_xor_b32_e32 v12, v12, v9\n\t" \
  "s_lshr_b32 s52, %[w], 2\n\t" \
  "s_lshl_b32 s52, s52, 14\n\t" \
  "v_or_b32_e32 v6, s52, v12\n\t" \
  "v_lshrrev_b32_e32 v9, 2, v10\n\t" \
  "v_and_b32_e32 v12, 1, v11\n\t" \
  "v_lshl_or_b32 v12, v12, 2, v9\n\t" \
  "s_lshl_b32 s52, s63, 1\n\t" \
  "v_xor_b32_e32 v12, s52, v12\n\t" \
  "v_lshlrev_b32_e32 v12, 5, v12\n\t" \
  "v_lshl_or_b32 v12, v9, 8, v12\n\t" \
  "v_and_b32_e32 v9, 3, v0\n\t" \
  "v_lshl_or_b32 v12, v9, 3, v12\n\t" \
  "v_lshl_or_b32 v12, v11, 13, v12\n\t" \
  "v_add_u32_e32 v7, 2048, v12\n\t" \
  "s_add_u32 s52, s64, 10\n\t" \
  "s_lshl_b32 s52, s63, s52\n\t" \
  "s_cmp_lg_u32 s64, 0\n\t" \
  "s_cselect_b32 s54, 81920, 0\n\t" \
  "s_cselect_b32 s53, 1, 4\n\t" \
  "s_lshl_b32 s53, s53, 10\n\t" \
  "v_add_u32_e32 v8, s52, v13\n\t" \
  "v_add_u32_e32 v9, s53, v8\n\t" \
  "s_mul_i32 s52, s59, 68\n\t" \
  "s_lshl_b32 s53, s66, 2\n\t" \
  "s_add_u32 s52, s52, s53\n\t" \
  "s_lshr_b32 s53, %[w], 2\n\t" \
  "s_lshl_b32 s53, s53, 1\n\t" \
  "s_add_u32 s52, s52, s53\n\t" \
  "s_lshl_b32 s52, s52, 15\n\t" \
  "s_add_u32 s52, s52, s54\n\t" \
  "s_add_u32 s52, s52, 0x1400000\n\t" \
  "s_mov_b64 s[50:51], %[ws]\n\t" \
  "s_add_u32 s50, s50, s52\n\t" \
  "s_addc_u32 s51, s51, 0\n\t" \
  "s_lshl_b32 s65, s38, 2\n\t" \
  "s_lshr_b32 s52, s39, 3\n\t" \
  "s_add_u32 s65, s65, s52\n\t" \
  "v_add_u32_e32 v10, 0, v6\n\t" \
  "v_add_u32_e32 v11, 0, v7\n\t" \
  "v_xor_b32_e32 v12, 32, v11\n\t" \
  "s_lshr_b32 s52, %[w], 2\n\t" \
  "s_mul_i32 s52, s52, 0\n\t" \
  "s_add_u32 s47, 0, s52\n\t" \
  "s_and_b32 s47, s47, 0\n\t" \
  "s_waitcnt vmcnt(4)\n\t" \
  "v_lshlrev_b32_e32 v212, 16, v208\n\t" \
  "v_and_b32_e32 v213, 0xffff0000, v208\n\t" \
  "v_lshlrev_b32_e32 v214, 16, v209\n\t" \
  "v_and_b32_e32 v215, 0xffff0000, v209\n\t" \
  "v_lshlrev_b32_e32 v216, 16, v210\n\t" \
  "v_and_b32_e32 v217, 0xffff0000, v210\n\t" \
  "v_lshlrev_b32_e32 v218, 16, v211\n\t" \
  "v_and_b32_e32 v219, 0xffff0000, v211\n\t" \
  "v_add_f32_e32 v220, v212, v213\n\t" \
  "v_add_f32_e32 v221, v214, v215\n\t" \
  "v_add_f32_e32 v220, v220, v216\n\t" \
  "v_add_f32_e32 v221, v221, v217\n\t" \
  "v_add_f32_e32 v220, v220, v218\n\t" \
  "v_add_f32_e32 v221, v221, v219\n\t" \
  "v_add_f32_e32 v220, v220, v221\n\t" \
  "s_nop 1\n\t" \
  "v_add_f32_dpp v220, v220, v220 row_shr:1 row_mask:0xf bank_mask:0xf bound_ctrl:0\n\t" \
  "s_nop 1\n\t" \
  "v_add_f32_dpp v220, v220, v220 row_shr:2 row_mask:0xf bank_mask:0xf bound_ctrl:0\n\t" \
  "s_nop 1\n\t" \
  "v_add_f32_dpp v220, v220, v220 row_shr:4 row_mask:0xf bank_mask:0xf bound_ctrl:0\n\t" \
  "s_nop 1\n\t" \
  "v_add_f32_dpp v220, v220, v220 row_shr:8 row_mask:0xf bank_mask:0xf bound_ctrl:0\n\t" \
  "s_nop 1\n\t" \
  "v_add_f32_dpp v220, v220, v220 row_bcast:15 row_mask:0xa bank_mask:0xf\n\t" \
  "s_nop 1\n\t" \
  "v_add_f32_dpp v220, v220, v220 row_bcast:31 row_mask:0xc bank_mask:0xf\n\t" \
  "s_nop 1\n\t" \
  "v_readlane_b32 s52, v220, 63\n\t" \
  "s_nop 1\n\t" \
  "v_mov_b32_e32 v221, 0x3b000000\n\t" \
  "s_nop 1\n\t" \
  "v_mul_f32_e32 v221, s52, v221\n\t" \
  "v_sub_f32_e32 v208, v212, v221\n\t" \
  "v_mul_f32_e32 v220, v208, v208\n\t" \
  "v_sub_f32_e32 v208, v213, v221\n\t" \
  "v_fmac_f32_e32 v220, v208, v208\n\t" \
  "v_sub_f32_e32 v208, v214, v221\n\t" \
  "v_fmac_f32_e32 v220, v208, v208\n\t" \
  "v_sub_f32_e32 v208, v215, v221\n\t" \
  "v_fmac_f32_e32 v220, v208, v208\n\t" \
  "v_sub_f32_e32 v208, v216, v221\n\t" \
  "v_fmac_f32_e32 v220, v208, v208\n\t" \
  "v_sub_f32_e32 v208, v217, v221\n\t" \
  "v_fmac_f32_e32 v220, v208, v208\n\t" \
  "v_sub_f32_e32 v208, v218, v221\n\t" \
  "v_fmac_f32_e32 v220, v208, v208\n\t" \
  "v_sub_f32_e32 v208, v219, v221\n\t" \
  "v_fmac_f32_e32 v220, v208, v208\n\t" \
  "s_nop 1\n\t" \
  "v_add_f32_dpp v220, v220, v220 row_shr:1 row_mask:0xf bank_mask:0xf bound_ctrl:0\n\t" \
  "s_nop 1\n\t" \
  "v_add_f32_dpp v220, v220, v220 row_shr:2 row_mask:0xf bank_mask:0xf bound_ctrl:0\n\t" \
  "s_nop 1\n\t" \
  "v_add_f32_dpp v220, v220, v220 row_shr:4 row_mask:0xf bank_mask:0xf bound_ctrl:0\n\t" \
  "s_nop 1\n\t" \
  "v_add_f32_dpp v220, v220, v220 row_shr:8 row_mask:0xf bank_mask:0xf bound_ctrl:0\n\t" \
  "s_nop 1\n\t" \
  "v_add_f32_dpp v220, v220, v220 row_bcast:15 row_mask:0xa bank_mask:0xf\n\t" \
  "s_nop 1\n\t" \
  "v_add_f32_dpp v220, v220, v220 row_bcast:31 row_mask:0xc bank_mask:0xf\n\t" \
  "s_nop 1\n\t" \
  "v_readlane_b32 s52, v220, 63\n\t" \
  "s_nop 1\n\t" \
  "v_mov_b32_e32 v222, 0x3b000000\n\t" \
  "s_nop 1\n\t" \
  "v_mul_f32_e32 v222, s52, v222\n\t" \
  "v_cvt_pk_bf16_f32 v222, v222, v222\n\t" \
  "v_lshlrev_b32_e32 v222, 16, v222\n\t" \
  "v_add_f32_e32 v222, 0x3727c5ac, v222\n\t" \
  "v_cvt_pk_bf16_f32 v222, v222, v222\n\t" \
  "v_lshlrev_b32_e32 v222, 16, v222\n\t" \
  "v_sqrt_f32_e32 v222, v222\n\t" \
  "v_cvt_pk_bf16_f32 v221, v221, v221\n\t" \
  "v_cvt_pk_bf16_f32 v222, v222, v222\n\t" \
  "v_lshlrev_b32_e32 v222, 16, v222\n\t" \
  "v_rcp_f32_e32 v222, v222\n\t" \
  "v_lshlrev_b32_e32 v221, 16, v221\n\t" \
  "v_sub_f32_e32 v212, v212, v221\n\t" \
  "v_sub_f32_e32 v213, v213, v221\n\t" \
  "v_cvt_pk_bf16_f32 v208, v212, v213\n\t" \
  "v_lshlrev_b32_e32 v212, 16, v208\n\t" \
  "v_and_b32_e32 v213, 0xffff0000, v208\n\t" \
  "v_mul_f32_e32 v212, v212, v222\n\t" \
  "v_mul_f32_e32 v213, v213, v222\n\t" \
  "v_cvt_pk_bf16_f32 v208, v212, v213\n\t" \
  "v_sub_f32_e32 v214, v214, v221\n\t" \
  "v_sub_f32_e32 v215, v215, v221\n\t" \
  "v_cvt_pk_bf16_f32 v209, v214, v215\n\t" \
  "v_lshlrev_b32_e32 v214, 16, v209\n\t" \
  "v_and_b32_e32 v215, 0xffff0000, v209\n\t" \
  "v_mul_f32_e32 v214, v214, v222\n\t" \
  "v_mul_f32_e32 v215, v215, v222\n\t" \
  "v_cvt_pk_bf16_f32 v209, v214, v215\n\t" \
  "v_sub_f32_e32 v216, v216, v221\n\t" \
  "v_sub_f32_e32 v217, v217, v221\n\t" \
  "v_cvt_pk_bf16_f32 v210, v216, v217\n\t" \
  "v_lshlrev_b32_e32 v216, 16, v210\n\t" \
  "v_and_b32_e32 v217, 0xffff0000, v210\n\t" \
  "v_mul_f32_e32 v216, v216, v222\n\t" \
  "v_mul_f32_e32 v217, v217, v222\n\t" \
  "v_cvt_pk_bf16_f32 v210, v216, v217\n\t" \
  "v_sub_f32_e32 v218, v218, v221\n\t" \
  "v_sub_f32_e32 v219, v219, v221\n\t" \
  "v_cvt_pk_bf16_f32 v211, v218, v219\n\t" \
  "v_lshlrev_b32_e32 v218, 16, v211\n\t" \
  "v_and_b32_e32 v219, 0xffff0000, v211\n\t" \
  "v_mul_f32_e32 v218, v218, v222\n\t" \
  "v_mul_f32_e32 v219, v219, v222\n\t" \
  "v_cvt_pk_bf16_f32 v211, v218, v219\n\t" \
  "s_lshr_b32 s52, %[bid], 1\n\t" \
  "s_lshl_b32 s52, s52, 14\n\t" \
  "s_and_b32 s53, %[bid], 1\n\t" \
  "s_lshl_b32 s54, s53, 9\n\t" \
  "s_add_u32 s52, s52, s54\n\t" \
  "s_lshl_b32 s54, %[w], 6\n\t" \
  "s_add_u32 s52, s52, s54\n\t" \
  "s_lshl_b32 s53, s53, 5\n\t" \
  "v_and_b32_e32 v13, 48, v13\n\t" \
  "v_xor_b32_e32 v13, s53, v13\n\t" \
  "v_lshrrev_b32_e32 v212, 2, v0\n\t" \
  "v_lshl_or_b32 v13, v212, 10, v13\n\t" \
  "s_mov_b64 s[54:55], %[ws]\n\t" \
  "s_add_u32 s54, s54, s52\n\t" \
  "s_addc_u32 s55, s55, 0\n\t" \
  "global_store_dwordx4 v13, v[208:211], s[54:55] sc1\n\t" \
  "s_waitcnt lgkmcnt(0)\n\t" \
  "s_waitcnt vmcnt(1)\n\t" \
  "s_barrier\n\t" \
  "s_add_u32 m0, s62, 0x8000\n\t" \
  "s_nop 0\n\t" \
  "global_load_lds_dwordx4 v1, s[40:41] offset:0 sc1\n\t" \
  "global_load_lds_dwordx4 v2, s[40:41] offset:1024 sc1\n\t" \
  "global_load_lds_dwordx4 v3, s[42:43] offset:2048\n\t" \
  "global_load_lds_dwordx4 v[4:5], off offset:3072\n\t" \
  "s_add_u32 s40, s40, s78\n\t" \
  "s_addc_u32 s41, s41, s77\n\t" \
  "s_add_u32 s42, s42, s76\n\t" \
  "s_addc_u32 s43, s43, s77\n\t" \
  "v_lshl_add_u64 v[4:5], v[4:5], 0, s[76:77]\n\t" \
  "s_add_u32 s36, s36, 1\n\t" \
  "s_addc_u32 s37, s37, 0\n\t" \
  "ds_read_b64_tr_b16 v[176:177], v11 offset:0\n\t" \
  "ds_read_b64_tr_b16 v[178:179], v11 offset:4096\n\t" \
  "ds_read_b64_tr_b16 v[180:181], v12 offset:0\n\t" \
  "ds_read_b64_tr_b16 v[182:183], v12 offset:4096\n\t" \
  "ds_read_b64_tr_b16 v[184:185], v11 offset:1024\n\t" \
  "ds_read_b64_tr_b16 v[186:187], v11 offset:5120\n\t" \
  "ds_read_b64_tr_b16 v[188:189], v12 offset:1024\n\t" \
  "ds_read_b64_tr_b16 v[190:191], v12 offset:5120\n\t" \
  "ds_read_b128 v[144:147], v10 offset:0\n\t" \
  "ds_read_b128 v[148:151], v10 offset:1024\n\t" \
  "ds_read_b128 v[152:155], v10 offset:4096\n\t" \
  "ds_read_b128 v[156:159], v10 offset:5120\n\t" \
  "ds_read_b128 v[160:163], v10 offset:8192\n\t" \
  "ds_read_b128 v[164:167], v10 offset:9216\n\t" \
  "ds_read_b128 v[168:171], v10 offset:12288\n\t" \
  "ds_read_b128 v[172:175], v10 offset:13312\n\t" \
  "s_cmp_lg_u32 s64, 0\n\t" \
  "s_cbranch_scc1 UV_%=\n\t" \
  "s_waitcnt lgkmcnt(4)\n\t" \
  "v_mfma_f32_16x16x32_bf16 v[80:83], v[176:179], v[144:147], 0\n\t" \
  "s_waitcnt vmcnt(0)\n\t" \
  "s_barrier\n\t" \
  "v_mfma_f32_16x16x32_bf16 v[84:87], v[180:183], v[144:147], 0\n\t" \
  "v_add_u32_e32 v10, 0x8000, v6\n\t" \
  "v_add_u32_e32 v11, 0x8000, v7\n\t" \
  "s_add_u32 m0, s62, 0x10000\n\t" \
  "v_mfma_f32_16x16x32_bf16 v[16:19], v[184:187], v[144:147], 0\n\t" \
  "v_xor_b32_e32 v12, 32, v11\n\t" \
  "global_load_lds_dwordx4 v1, s[40:41] offset:0 sc1\n\t" \
  "v_mfma_f32_16x16x32_bf16 v[20:23], v[188:191], v[144:147], 0\n\t" \
  "ds_read_b128 v[144:147], v10 offset:0\n\t" \
  "v_mfma_f32_16x16x32_bf16 v[28:31], v[188:191], v[148:151], 0\n\t" \
  "ds_read_b64_tr_b16 v[192:193], v11 offset:0\n\t" \
  "global_load_lds_dwordx4 v2, s[40:41] offset:1024 sc1\n\t" \
  "v_mfma_f32_16x16x32_bf16 v[24:27], v[184:187], v[148:151], 0\n\t" \
  "ds_read_b64_tr_b16 v[194:195], v11 offset:4096\n\t" \
  "v_mfma_f32_16x16x32_bf16 v[92:95], v[180:183], v[148:151], 0\n\t" \
  "global_load_lds_dwordx4 v3, s[42:43] offset:2048\n\t" \
  "v_mfma_f32_16x16x32_bf16 v[88:91], v[176:179], v[148:151], 0\n\t" \
  "ds_read_b128 v[148:151], v10 offset:1024\n\t" \
  "v_mfma_f32_16x16x32_bf16 v[96:99], v[176:179], v[152:155], 0\n\t" \
  "ds_read_b64_tr_b16 v[196:197], v12 offset:0\n\t" \
  "global_load_lds_dwordx4 v[4:5], off offset:3072\n\t" \
  "v_mfma_f32_16x16x32_bf16 v[100:103], v[180:183], v[152:155], 0\n\t" \
  "ds_read_b64_tr_b16 v[198:199], v12 offset:4096\n\t" \
  "v_mfma_f32_16x16x32_bf16 v[32:35], v[184:187], v[152:155], 0\n\t" \
  "s_add_u32 s40, s40, s74\n\t" \
  "s_addc_u32 s41, s41, s73\n\t" \
  "v_mfma_f32_16x16x32_bf16 v[36:39], v[188:191], v[152:155], 0\n\t" \
  "ds_read_b128 v[152:155], v10 offset:4096\n\t" \
  "s_add_u32 s42, s42, s72\n\t" \
  "s_addc_u32 s43, s43, s73\n\t" \
  "v_mfma_f32_16x16x32_bf16 v[44:47], v[188:191], v[156:159], 0\n\t" \
  "ds_read_b64_tr_b16 v[200:201], v11 offset:1024\n\t" \
  "v_lshl_add_u64 v[4:5], v[4:5], 0, s[72:73]\n\t" \
  "v_mfma_f32_16x16x32_bf16 v[40:43], v[184:187], v[156:159], 0\n\t" \
  "ds_read_b64_tr_b16 v[202:203], v11 offset:5120\n\t" \
  "v_mfma_f32_16x16x32_bf16 v[108:111], v[180:183], v[156:159], 0\n\t" \
  "v_mfma_f32_16x16x32_bf16 v[104:107], v[176:179], v[156:159], 0\n\t" \
  "ds_read_b128 v[156:159], v10 offset:5120\n\t" \
  "s_add_u32 m0, s62, 0x18000\n\t" \
  "s_waitcnt lgkmcnt(10)\n\t" \
  "v_mfma_f32_16x16x32_bf16 v[112:115], v[176:179], v[160:163], 0\n\t" \
  "ds_read_b64_tr_b16 v[204:205], v12 offset:1024\n\t" \
  "global_load_lds_dwordx4 v1, s[40:41] offset:0 sc1\n\t" \
  "v_mfma_f32_16x16x32_bf16 v[116:119], v[180:183], v[160:163], 0\n\t" \
  "ds_read_b64_tr_b16 v[206:207], v12 offset:5120\n\t" \
  "v_mfma_f32_16x16x32_bf16 v[48:51], v[184:187], v[160:163], 0\n\t" \
  "global_load_lds_dwordx4 v2, s[40:41] offset:1024 sc1\n\t" \
  "v_mfma_f32_16x16x32_bf16 v[52:55], v[188:191], v[160:163], 0\n\t" \
  "ds_read_b128 v[160:163], v10 offset:8192\n\t" \
  "v_mfma_f32_16x16x32_bf16 v[60:63], v[188:191], v[164:167], 0\n\t" \
  "global_load_lds_dwordx4 v3, s[42:43] offset:2048\n\t" \
  "v_mfma_f32_16x16x32_bf16 v[56:59], v[184:187], v[164:167], 0\n\t" \
  "v_mfma_f32_16x16x32_bf16 v[124:127], v[180:183], v[164:167], 0\n\t" \
  "global_load_lds_dwordx4 v[4:5], off offset:3072\n\t" \
  "v_mfma_f32_16x16x32_bf16 v[120:123], v[176:179], v[164:167], 0\n\t" \
  "ds_read_b128 v[164:167], v10 offset:9216\n\t" \
  "v_mfma_f32_16x16x32_bf16 v[128:131], v[176:179], v[168:171], 0\n\t" \
  "s_add_u32 s40, s40, s82\n\t" \
  "s_addc_u32 s41, s41, s81\n\t" \
  "v_mfma_f32_16x16x32_bf16 v[132:135], v[180:183], v[168:171], 0\n\t" \
  "s_add_u32 s42, s42, s80\n\t" \
  "s_addc_u32 s43, s43, s81\n\t" \
  "v_mfma_f32_16x16x32_bf16 v[64:67], v[184:187], v[168:171], 0\n\t" \
  "v_lshl_add_u64 v[4:5], v[4:5], 0, s[80:81]\n\t" \
  "v_mfma_f32_16x16x32_bf16 v[68:71], v[188:191], v[168:171], 0\n\t" \
  "ds_read_b128 v[168:171], v10 offset:12288\n\t" \
  "v_mfma_f32_16x16x32_bf16 v[76:79], v[188:191], v[172:175], 0\n\t" \
  "v_mfma_f32_16x16x32_bf16 v[72:75], v[184:187], v[172:175], 0\n\t" \
  "v_mfma_f32_16x16x32_bf16 v[140:143], v[180:183], v[172:175], 0\n\t" \
  "v_mfma_f32_16x16x32_bf16 v[136:139], v[176:179], v[172:175], 0\n\t" \
  "ds_read_b128 v[172:175], v10 offset:13312\n\t" \
  "s_waitcnt lgkmcnt(4)\n\t" \
  "v_mfma_f32_16x16x32_bf16 v[80:83], v[192:195], v[144:147], v[80:83]\n\t" \
  "s_waitcnt vmcnt(4)\n\t" \
  "s_barrier\n\t" \
  "v_mfma_f32_16x16x32_bf16 v[84:87], v[196:199], v[144:147], v[84:87]\n\t" \
  "v_add_u32_e32 v10, 0x10000, v6\n\t" \
  "v_add_u32_e32 v11, 0x10000, v7\n\t" \
  "s_add_u32 m0, s62, 0x20000\n\t" \
  "v_mfma_f32_16x16x32_bf16 v[16:19], v[200:203], v[144:147], v[16:19]\n\t" \
  "v_xor_b32_e32 v12, 32, v11\n\t" \
  "global_load_lds_dwordx4 v1, s[40:41] offset:0 sc1\n\t" \
  "v_mfma_f32_16x16x32_bf16 v[20:23], v[204:207], v[144:147], v[20:23]\n\t" \
  "ds_read_b128 v[144:147], v10 offset:0\n\t" \
  "v_mfma_f32_16x16x32_bf16 v[28:31], v[204:207], v[148:151], v[28:31]\n\t" \
  "ds_read_b64_tr_b16 v[176:177], v11 offset:0\n\t" \
  "global_load_lds_dwordx4 v2, s[40:41] offset:1024 sc1\n\t" \
  "v_mfma_f32_16x16x32_bf16 v[24:27], v[200:203], v[148:151], v[24:27]\n\t" \
  "ds_read_b64_tr_b16 v[178:179], v11 offset:4096\n\t" \
  "v_mfma_f32_16x16x32_bf16 v[92:95], v[196:199], v[148:151], v[92:95]\n\t" \
  "global_load_lds_dwordx4 v3, s[42:43] offset:2048\n\t" \
  "v_mfma_f32_16x16x32_bf16 v[88:91], v[192:195], v[148:151], v[88:91]\n\t" \
  "ds_read_b128 v[148:151], v10 offset:1024\n\t" \
  "v_mfma_f32_16x16x32_bf16 v[96:99], v[192:195], v[152:155], v[96:99]\n\t" \
  "ds_read_b64_tr_b16 v[180:181], v12 offset:0\n\t" \
  "global_load_lds_dwordx4 v[4:5], off offset:3072\n\t" \
  "v_mfma_f32_16x16x32_bf16 v[100:103], v[196:199], v[152:155], v[100:103]\n\t" \
  "ds_read_b64_tr_b16 v[182:183], v12 offset:4096\n\t" \
  "v_mfma_f32_16x16x32_bf16 v[32:35], v[200:203], v[152:155], v[32:35]\n\t" \
  "s_add_u32 s40, s40, s74\n\t" \
  "s_addc_u32 s41, s41, s73\n\t" \
  "v_mfma_f32_16x16x32_bf16 v[36:39], v[204:207], v[152:155], v[36:39]\n\t" \
  "ds_read_b128 v[152:155], v10 offset:4096\n\t" \
  "s_add_u32 s42, s42, s72\n\t" \
  "s_addc_u32 s43, s43, s73\n\t" \
  "v_mfma_f32_16x16x32_bf16 v[44:47], v[204:207], v[156:159], v[44:47]\n\t" \
  "ds_read_b64_tr_b16 v[184:185], v11 offset:1024\n\t" \
  "v_lshl_add_u64 v[4:5], v[4:5], 0, s[72:73]\n\t" \
  "v_mfma_f32_16x16x32_bf16 v[40:43], v[200:203], v[156:159], v[40:43]\n\t" \
  "ds_read_b64_tr_b16 v[186:187], v11 offset:5120\n\t" \
  "v_mfma_f32_16x16x32_bf16 v[108:111], v[196:199], v[156:159], v[108:111]\n\t" \
  "v_mfma_f32_16x16x32_bf16 v[104:107], v[192:195], v[156:159], v[104:107]\n\t" \
  "ds_read_b128 v[156:159], v10 offset:5120\n\t" \
  "s_add_u32 m0, s62, 0x0\n\t" \
  "s_waitcnt lgkmcnt(10)\n\t" \
  "v_mfma_f32_16x16x32_bf16 v[112:115], v[192:195], v[160:163], v[112:115]\n\t" \
  "ds_read_b64_tr_b16 v[188:189], v12 offset:1024\n\t" \
  "global_load_lds_dwordx4 v1, s[40:41] offset:0 sc1\n\t" \
  "v_mfma_f32_16x16x32_bf16 v[116:119], v[196:199], v[160:163], v[116:119]\n\t" \
  "ds_read_b64_tr_b16 v[190:191], v12 offset:5120\n\t" \
  "v_mfma_f32_16x16x32_bf16 v[48:51], v[200:203], v[160:163], v[48:51]\n\t" \
  "global_load_lds_dwordx4 v2, s[40:41] offset:1024 sc1\n\t" \
  "v_mfma_f32_16x16x32_bf16 v[52:55], v[204:207], v[160:163], v[52:55]\n\t" \
  "ds_read_b128 v[160:163], v10 offset:8192\n\t" \
  "v_mfma_f32_16x16x32_bf16 v[60:63], v[204:207], v[164:167], v[60:63]\n\t" \
  "global_load_lds_dwordx4 v3, s[42:43] offset:2048\n\t" \
  "v_mfma_f32_16x16x32_bf16 v[56:59], v[200:203], v[164:167], v[56:59]\n\t" \
  "v_mfma_f32_16x16x32_bf16 v[124:127], v[196:199], v[164:167], v[124:127]\n\t" \
  "global_load_lds_dwordx4 v[4:5], off offset:3072\n\t" \
  "v_mfma_f32_16x16x32_bf16 v[120:123], v[192:195], v[164:167], v[120:123]\n\t" \
  "ds_read_b128 v[164:167], v10 offset:9216\n\t" \
  "v_mfma_f32_16x16x32_bf16 v[128:131], v[192:195], v[168:171], v[128:131]\n\t" \
  "s_add_u32 s40, s40, s78\n\t" \
  "s_addc_u32 s41, s41, s77\n\t" \
  "v_mfma_f32_16x16x32_bf16 v[132:135], v[196:199], v[168:171], v[132:135]\n\t" \
  "s_add_u32 s42, s42, s76\n\t" \
  "s_addc_u32 s43, s43, s77\n\t" \
  "v_mfma_f32_16x16x32_bf16 v[64:67], v[200:203], v[168:171], v[64:67]\n\t" \
  "v_lshl_add_u64 v[4:5], v[4:5], 0, s[76:77]\n\t" \
  "v_mfma_f32_16x16x32_bf16 v[68:71], v[204:207], v[168:171], v[68:71]\n\t" \
  "ds_read_b128 v[168:171], v10 offset:12288\n\t" \
  "v_mfma_f32_16x16x32_bf16 v[76:79], v[204:207], v[172:175], v[76:79]\n\t" \
  "v_mfma_f32_16x16x32_bf16 v[72:75], v[200:203], v[172:175], v[72:75]\n\t" \
  "v_mfma_f32_16x16x32_bf16 v[140:143], v[196:199], v[172:175], v[140:143]\n\t" \
  "v_mfma_f32_16x16x32_bf16 v[136:139], v[192:195], v[172:175], v[136:139]\n\t" \
  "ds_read_b128 v[172:175], v10 offset:13312\n\t" \
  "s_waitcnt lgkmcnt(4)\n\t" \
  "v_mfma_f32_16x16x32_bf16 v[80:83], v[176:179], v[144:147], v[80:83]\n\t" \
  "s_waitcnt vmcnt(8)\n\t" \
  "s_barrier\n\t" \
  "v_mfma_f32_16x16x32_bf16 v[84:87], v[180:183], v[144:147], v[84:87]\n\t" \
  "v_add_u32_e32 v10, 0x18000, v6\n\t" \
  "v_add_u32_e32 v11, 0x18000, v7\n\t" \
  "v_mfma_f32_16x16x32_bf16 v[16:19], v[184:187], v[144:147], v[16:19]\n\t" \
  "v_xor_b32_e32 v12, 32, v11\n\t" \
  "v_mfma_f32_16x16x32_bf16 v[20:23], v[188:191], v[144:147], v[20:23]\n\t" \
  "ds_read_b128 v[144:147], v10 offset:0\n\t" \
  "v_mfma_f32_16x16x32_bf16 v[28:31], v[188:191], v[148:151], v[28:31]\n\t" \
  "ds_read_b64_tr_b16 v[192:193], v11 offset:0\n\t" \
  "v_mfma_f32_16x16x32_bf16 v[24:27], v[184:187], v[148:151], v[24:27]\n\t" \
  "ds_read_b64_tr_b16 v[194:195], v11 offset:4096\n\t" \
  "v_mfma_f32_16x16x32_bf16 v[92:95], v[180:183], v[148:151], v[92:95]\n\t" \
  "s_add_u32 m0, s62, 0x8000\n\t" \
  "v_mfma_f32_16x16x32_bf16 v[88:91], v[176:179], v[148:151], v[88:91]\n\t" \
  "ds_read_b128 v[148:151], v10 offset:1024\n\t" \
  "v_mfma_f32_16x16x32_bf16 v[96:99], v[176:179], v[152:155], v[96:99]\n\t" \
  "ds_read_b64_tr_b16 v[196:197], v12 offset:0\n\t" \
  "global_load_lds_dwordx4 v1, s[40:41] offset:0 sc1\n\t" \
  "v_mfma_f32_16x16x32_bf16 v[100:103], v[180:183], v[152:155], v[100:103]\n\t" \
  "ds_read_b64_tr_b16 v[198:199], v12 offset:4096\n\t" \
  "v_mfma_f32_16x16x32_bf16 v[32:35], v[184:187], v[152:155], v[32:35]\n\t" \
  "global_load_lds_dwordx4 v2, s[40:41] offset:1024 sc1\n\t" \
  "v_mfma_f32_16x16x32_bf16 v[36:39], v[188:191], v[152:155], v[36:39]\n\t" \
  "ds_read_b128 v[152:155], v10 offset:4096\n\t" \
  "v_mfma_f32_16x16x32_bf16 v[44:47], v[188:191], v[156:159], v[44:47]\n\t" \
  "ds_read_b64_tr_b16 v[200:201], v11 offset:1024\n\t" \
  "global_load_lds_dwordx4 v3, s[42:43] offset:2048\n\t" \
  "v_mfma_f32_16x16x32_bf16 v[40:43], v[184:187], v[156:159], v[40:43]\n\t" \
  "ds_read_b64_tr_b16 v[202:203], v11 offset:5120\n\t" \
  "v_mfma_f32_16x16x32_bf16 v[108:111], v[180:183], v[156:159], v[108:111]\n\t" \
  "global_load_lds_dwordx4 v[4:5], off offset:3072\n\t" \
  "v_mfma_f32_16x16x32_bf16 v[104:107], v[176:179], v[156:159], v[104:107]\n\t" \
  "ds_read_b128 v[156:159], v10 offset:5120\n\t" \
  "s_waitcnt lgkmcnt(10)\n\t" \
  "v_mfma_f32_16x16x32_bf16 v[112:115], v[176:179], v[160:163], v[112:115]\n\t" \
  "ds_read_b64_tr_b16 v[204:205], v12 offset:1024\n\t" \
  "s_add_u32 s40, s40, s74\n\t" \
  "s_addc_u32 s41, s41, s73\n\t" \
  "v_mfma_f32_16x16x32_bf16 v[116:119], v[180:183], v[160:163], v[116:119]\n\t" \
  "ds_read_b64_tr_b16 v[206:207], v12 offset:5120\n\t" \
  "s_add_u32 s42, s42, s72\n\t" \
  "s_addc_u32 s43, s43, s73\n\t" \
  "v_mfma_f32_16x16x32_bf16 v[48:51], v[184:187], v[160:163], v[48:51]\n\t" \
  "v_lshl_add_u64 v[4:5], v[4:5], 0, s[72:73]\n\t" \
  "v_mfma_f32_16x16x32_bf16 v[52:55], v[188:191], v[160:163], v[52:55]\n\t" \
  "ds_read_b128 v[160:163], v10 offset:8192\n\t" \
  "v_mfma_f32_16x16x32_bf16 v[60:63], v[188:191], v[164:167], v[60:63]\n\t" \
  "v_mfma_f32_16x16x32_bf16 v[56:59], v[184:187], v[164:167], v[56:59]\n\t" \
  "v_mfma_f32_16x16x32_bf16 v[124:127], v[180:183], v[164:167], v[124:127]\n\t" \
  "v_mfma_f32_16x16x32_bf16 v[120:123], v[176:179], v[164:167], v[120:123]\n\t" \
  "ds_read_b128 v[164:167], v10 offset:9216\n\t" \
  "v_mfma_f32_16x16x32_bf16 v[128:131], v[176:179], v[168:171], v[128:131]\n\t" \
  "v_mfma_f32_16x16x32_bf16 v[132:135], v[180:183], v[168:171], v[132:135]\n\t" \
  "v_mfma_f32_16x16x32_bf16 v[64:67], v[184:187], v[168:171], v[64:67]\n\t" \
  "v_mfma_f32_16x16x32_bf16 v[68:71], v[188:191], v[168:171], v[68:71]\n\t" \
  "ds_read_b128 v[168:171], v10 offset:12288\n\t" \
  "v_mfma_f32_16x16x32_bf16 v[76:79], v[188:191], v[172:175], v[76:79]\n\t" \
  "v_mfma_f32_16x16x32_bf16 v[72:75], v[184:187], v[172:175], v[72:75]\n\t" \
  "v_mfma_f32_16x16x32_bf16 v[140:143], v[180:183], v[172:175], v[140:143]\n\t" \
  "v_mfma_f32_16x16x32_bf16 v[136:139], v[176:179], v[172:175], v[136:139]\n\t" \
  "ds_read_b128 v[172:175], v10 offset:13312\n\t" \
  "s_waitcnt lgkmcnt(4)\n\t" \
  "v_mfma_f32_16x16x32_bf16 v[80:83], v[192:195], v[144:147], v[80:83]\n\t" \
  "s_waitcnt vmcnt(8)\n\t" \
  "s_barrier\n\t" \
  "v_mfma_f32_16x16x32_bf16 v[84:87], v[196:199], v[144:147], v[84:87]\n\t" \
  "v_add_u32_e32 v10, 0x20000, v6\n\t" \
  "v_add_u32_e32 v11, 0x20000, v7\n\t" \
  "v_mfma_f32_16x16x32_bf16 v[16:19], v[200:203], v[144:147], v[16:19]\n\t" \
  "v_xor_b32_e32 v12, 32, v11\n\t" \
  "v_mfma_f32_16x16x32_bf16 v[20:23], v[204:207], v[144:147], v[20:23]\n\t" \
  "ds_read_b128 v[144:147], v10 offset:0\n\t" \
  "v_mfma_f32_16x16x32_bf16 v[28:31], v[204:207], v[148:151], v[28:31]\n\t" \
  "ds_read_b64_tr_b16 v[176:177], v11 offset:0\n\t" \
  "v_mfma_f32_16x16x32_bf16 v[24:27], v[200:203], v[148:151], v[24:27]\n\t" \
  "ds_read_b64_tr_b16 v[178:179], v11 offset:4096\n\t" \
  "v_mfma_f32_16x16x32_bf16 v[92:95], v[196:199], v[148:151], v[92:95]\n\t" \
  "s_add_u32 m0, s62, 0x10000\n\t" \
  "v_mfma_f32_16x16x32_bf16 v[88:91], v[192:195], v[148:151], v[88:91]\n\t" \
  "ds_read_b128 v[148:151], v10 offset:1024\n\t" \
  "v_mfma_f32_16x16x32_bf16 v[96:99], v[192:195], v[152:155], v[96:99]\n\t" \
  "ds_read_b64_tr_b16 v[180:181], v12 offset:0\n\t" \
  "global_load_lds_dwordx4 v1, s[40:41] offset:0 sc1\n\t" \
  "v_mfma_f32_16x16x32_bf16 v[100:103], v[196:199], v[152:155], v[100:103]\n\t" \
  "ds_read_b64_tr_b16 v[182:183], v12 offset:4096\n\t" \
  "v_mfma_f32_16x16x32_bf16 v[32:35], v[200:203], v[152:155], v[32:35]\n\t" \
  "global_load_lds_dwordx4 v2, s[40:41] offset:1024 sc1\n\t" \
  "v_mfma_f32_16x16x32_bf16 v[36:39], v[204:207], v[152:155], v[36:39]\n\t" \
  "ds_read_b128 v[152:155], v10 offset:4096\n\t" \
  "v_mfma_f32_16x16x32_bf16 v[44:47], v[204:207], v[156:159], v[44:47]\n\t" \
  "ds_read_b64_tr_b16 v[184:185], v11 offset:1024\n\t" \
  "global_load_lds_dwordx4 v3, s[42:43] offset:2048\n\t" \
  "v_mfma_f32_16x16x32_bf16 v[40:43], v[200:203], v[156:159], v[40:43]\n\t" \
  "ds_read_b64_tr_b16 v[186:187], v11 offset:5120\n\t" \
  "v_mfma_f32_16x16x32_bf16 v[108:111], v[196:199], v[156:159], v[108:111]\n\t" \
  "global_load_lds_dwordx4 v[4:5], off offset:3072\n\t" \
  "v_mfma_f32_16x16x32_bf16 v[104:107], v[192:195], v[156:159], v[104:107]\n\t" \
  "ds_read_b128 v[156:159], v10 offset:5120\n\t" \
  "s_waitcnt lgkmcnt(10)\n\t" \
  "v_mfma_f32_16x16x32_bf16 v[112:115], v[192:195], v[160:163], v[112:115]\n\t" \
  "ds_read_b64_tr_b16 v[188:189], v12 offset:1024\n\t" \
  "s_add_u32 s40, s40, s46\n\t" \
  "s_addc_u32 s41, s41, s45\n\t" \
  "v_mfma_f32_16x16x32_bf16 v[116:119], v[196:199], v[160:163], v[116:119]\n\t" \
  "ds_read_b64_tr_b16 v[190:191], v12 offset:5120\n\t" \
  "s_add_u32 s42, s42, s44\n\t" \
  "s_addc_u32 s43, s43, s45\n\t" \
  "v_mfma_f32_16x16x32_bf16 v[48:51], v[200:203], v[160:163], v[48:51]\n\t" \
  "v_lshl_add_u64 v[4:5], v[4:5], 0, s[44:45]\n\t" \
  "v_mfma_f32_16x16x32_bf16 v[52:55], v[204:207], v[160:163], v[52:55]\n\t" \
  "ds_read_b128 v[160:163], v10 offset:8192\n\t" \
  "v_mfma_f32_16x16x32_bf16 v[60:63], v[204:207], v[164:167], v[60:63]\n\t" \
  "s_lshl_b32 s52, %[bid], 3\n\t" \
  "s_add_u32 s52, s52, %[w]\n\t" \
  "v_mfma_f32_16x16x32_bf16 v[56:59], v[200:203], v[164:167], v[56:59]\n\t" \
  "s_lshl_b32 s52, s52, 7\n\t" \
  "s_add_u32 s52, s52, 0x6f00000\n\t" \
  "v_mfma_f32_16x16x32_bf16 v[124:127], v[196:199], v[164:167], v[124:127]\n\t" \
  "s_mov_b64 s[54:55], %[ws]\n\t" \
  "s_add_u32 s54, s54, s52\n\t" \
  "s_addc_u32 s55, s55, 0\n\t" \
  "v_mfma_f32_16x16x32_bf16 v[120:123], v[192:195], v[164:167], v[120:123]\n\t" \
  "ds_read_b128 v[164:167], v10 offset:9216\n\t" \
  "v_mfma_f32_16x16x32_bf16 v[128:131], v[192:195], v[168:171], v[128:131]\n\t" \
  "v_mov_b32_e32 v228, 0\n\t" \
  "v_mov_b32_e32 v229, 0\n\t" \
  "v_mfma_f32_16x16x32_bf16 v[132:135], v[196:199], v[168:171], v[132:135]\n\t" \
  "v_mov_b32_e32 v230, 0\n\t" \
  "v_mov_b32_e32 v231, 0\n\t" \
  "v_mfma_f32_16x16x32_bf16 v[64:67], v[200:203], v[168:171], v[64:67]\n\t" \
  "global_store_dwordx4 v228, v[228:231], s[54:55] sc1\n\t" \
  "v_mfma_f32_16x16x32_bf16 v[68:71], v[204:207], v[168:171], v[68:71]\n\t" \
  "ds_read_b128 v[168:171], v10 offset:12288\n\t" \
  "v_mfma_f32_16x16x32_bf16 v[76:79], v[204:207], v[172:175], v[76:79]\n\t" \
  "v_mfma_f32_16x16x32_bf16 v[72:75], v[200:203], v[172:175], v[72:75]\n\t" \
  "v_mfma_f32_16x16x32_bf16 v[140:143], v[196:199], v[172:175], v[140:143]\n\t" \
  "v_mfma_f32_16x16x32_bf16 v[136:139], v[192:195], v[172:175], v[136:139]\n\t" \
  "ds_read_b128 v[172:175], v10 offset:13312\n\t" \
  "s_waitcnt lgkmcnt(4)\n\t" \
  "v_mfma_f32_16x16x32_bf16 v[80:83], v[176:179], v[144:147], v[80:83]\n\t" \
  "s_waitcnt vmcnt(8)\n\t" \
  "s_barrier\n\t" \
  "v_mfma_f32_16x16x32_bf16 v[84:87], v[180:183], v[144:147], v[84:87]\n\t" \
  "v_add_u32_e32 v10, 0x0, v6\n\t" \
  "v_add_u32_e32 v11, 0x0, v7\n\t" \
  "v_mfma_f32_16x16x32_bf16 v[16:19], v[184:187], v[144:147], v[16:19]\n\t" \
  "v_xor_b32_e32 v12, 32, v11\n\t" \
  "v_mfma_f32_16x16x32_bf16 v[20:23], v[188:191], v[144:147], v[20:23]\n\t" \
  "ds_read_b128 v[144:147], v10 offset:0\n\t" \
  "v_mfma_f32_16x16x32_bf16 v[28:31], v[188:191], v[148:151], v[28:31]\n\t" \
  "ds_read_b64_tr_b16 v[192:193], v11 offset:0\n\t" \
  "v_mfma_f32_16x16x32_bf16 v[24:27], v[184:187], v[148:151], v[24:27]\n\t" \
  "ds_read_b64_tr_b16 v[194:195], v11 offset:4096\n\t" \
  "v_mfma_f32_16x16x32_bf16 v[92:95], v[180:183], v[148:151], v[92:95]\n\t" \
  "s_add_u32 m0, s62, 0x18000\n\t" \
  "v_mfma_f32_16x16x32_bf16 v[88:91], v[176:179], v[148:151], v[88:91]\n\t" \
  "ds_read_b128 v[148:151], v10 offset:1024\n\t" \
  "v_mfma_f32_16x16x32_bf16 v[96:99], v[176:179], v[152:155], v[96:99]\n\t" \
  "ds_read_b64_tr_b16 v[196:197], v12 offset:0\n\t" \
  "global_load_lds_dwordx4 v1, s[40:41] offset:0 sc1\n\t" \
  "s_lshl_b32 s52, %[bid], 3\n\t" \
  "s_add_u32 s52, s52, 0x11000\n\t" \
  "s_cmp_lg_u32 %[w], 0\n\t" \
  "s_cbranch_scc1 PBlT_%=\n\t" \
  "v_mov_b32_e32 v14, s36\n\t" \
  "v_mov_b32_e32 v15, s37\n\t" \
  "v_mov_b32_e32 v13, s52\n\t" \
  "s_mov_b64 exec, 1\n\t" \
  "global_store_dwordx2 v13, v[14:15], s[60:61] sc1\n\t" \
  "s_mov_b64 exec, -1\n\t" \
  "PBlT_%=:\n\t" \
  "v_mfma_f32_16x16x32_bf16 v[100:103], v[180:183], v[152:155], v[100:103]\n\t" \
  "ds_read_b64_tr_b16 v[198:199], v12 offset:4096\n\t" \
  "v_mfma_f32_16x16x32_bf16 v[32:35], v[184:187], v[152:155], v[32:35]\n\t" \
  "global_load_lds_dwordx4 v2, s[40:41] offset:1024 sc1\n\t" \
  "v_mfma_f32_16x16x32_bf16 v[36:39], v[188:191], v[152:155], v[36:39]\n\t" \
  "ds_read_b128 v[152:155], v10 offset:4096\n\t" \
  "v_mfma_f32_16x16x32_bf16 v[44:47], v[188:191], v[156:159], v[44:47]\n\t" \
  "ds_read_b64_tr_b16 v[200:201], v11 offset:1024\n\t" \
  "global_load_lds_dwordx4 v3, s[42:43] offset:2048\n\t" \
  "v_mfma_f32_16x16x32_bf16 v[40:43], v[184:187], v[156:159], v[40:43]\n\t" \
  "ds_read_b64_tr_b16 v[202:203], v11 offset:5120\n\t" \
  "v_mfma_f32_16x16x32_bf16 v[108:111], v[180:183], v[156:159], v[108:111]\n\t" \
  "global_load_lds_dwordx4 v[4:5], off offset:3072\n\t" \
  "v_mfma_f32_16x16x32_bf16 v[104:107], v[176:179], v[156:159], v[104:107]\n\t" \
  "ds_read_b128 v[156:159], v10 offset:5120\n\t" \
  "s_waitcnt lgkmcnt(10)\n\t" \
  "v_mfma_f32_16x16x32_bf16 v[112:115], v[176:179], v[160:163], v[112:115]\n\t" \
  "ds_read_b64_tr_b16 v[204:205], v12 offset:1024\n\t" \
  "s_add_u32 s40, s40, s74\n\t" \
  "s_addc_u32 s41, s41, s73\n\t" \
  "v_mfma_f32_16x16x32_bf16 v[116:119], v[180:183], v[160:163], v[116:119]\n\t" \
  "ds_read_b64_tr_b16 v[206:207], v12 offset:5120\n\t" \
  "s_add_u32 s42, s42, s72\n\t" \
  "s_addc_u32 s43, s43, s73\n\t" \
  "v_mfma_f32_16x16x32_bf16 v[48:51], v[184:187], v[160:163], v[48:51]\n\t" \
  "v_lshl_add_u64 v[4:5], v[4:5], 0, s[72:73]\n\t" \
  "v_mfma_f32_16x16x32_bf16 v[52:55], v[188:191], v[160:163], v[52:55]\n\t" \
  "ds_read_b128 v[160:163], v10 offset:8192\n\t" \
  "v_mfma_f32_16x16x32_bf16 v[60:63], v[188:191], v[164:167], v[60:63]\n\t" \
  "v_mfma_f32_16x16x32_bf16 v[56:59], v[184:187], v[164:167], v[56:59]\n\t" \
  "v_mfma_f32_16x16x32_bf16 v[124:127], v[180:183], v[164:167], v[124:127]\n\t" \
  "v_mfma_f32_16x16x32_bf16 v[120:123], v[176:179], v[164:167], v[120:123]\n\t" \
  "ds_read_b128 v[164:167], v10 offset:9216\n\t" \
  "v_mfma_f32_16x16x32_bf16 v[128:131], v[176:179], v[168:171], v[128:131]\n\t" \
  "v_mfma_f32_16x16x32_bf16 v[132:135], v[180:183], v[168:171], v[132:135]\n\t" \
  "v_mfma_f32_16x16x32_bf16 v[64:67], v[184:187], v[168:171], v[64:67]\n\t" \
  "v_mfma_f32_16x16x32_bf16 v[68:71], v[188:191], v[168:171], v[68:71]\n\t" \
  "ds_read_b128 v[168:171], v10 offset:12288\n\t" \
  "v_mfma_f32_16x16x32_bf16 v[76:79], v[188:191], v[172:175], v[76:79]\n\t" \
  "v_mfma_f32_16x16x32_bf16 v[72:75], v[184:187], v[172:175], v[72:75]\n\t" \
  "v_mfma_f32_16x16x32_bf16 v[140:143], v[180:183], v[172:175], v[140:143]\n\t" \
  "v_mfma_f32_16x16x32_bf16 v[136:139], v[176:179], v[172:175], v[136:139]\n\t" \
  "ds_read_b128 v[172:175], v10 offset:13312\n\t" \
  "s_waitcnt lgkmcnt(4)\n\t" \
  "v_mfma_f32_16x16x32_bf16 v[80:83], v[192:195], v[144:147], v[80:83]\n\t" \
  "s_waitcnt vmcnt(8)\n\t" \
  "s_barrier\n\t" \
  "v_mfma_f32_16x16x32_bf16 v[84:87], v[196:199], v[144:147], v[84:87]\n\t" \
  "v_add_u32_e32 v10, 0x8000, v6\n\t" \
  "v_add_u32_e32 v11, 0x8000, v7\n\t" \
  "v_mfma_f32_16x16x32_bf16 v[16:19], v[200:203], v[144:147], v[16:19]\n\t" \
  "v_xor_b32_e32 v12, 32, v11\n\t" \
  "v_mfma_f32_16x16x32_bf16 v[20:23], v[204:207], v[144:147], v[20:23]\n\t" \
  "ds_read_b128 v[144:147], v10 offset:0\n\t" \
  "v_mfma_f32_16x16x32_bf16 v[28:31], v[204:207], v[148:151], v[28:31]\n\t" \
  "ds_read_b64_tr_b16 v[176:177], v11 offset:0\n\t" \
  "v_mfma_f32_16x16x32_bf16 v[24:27], v[200:203], v[148:151], v[24:27]\n\t" \
  "ds_read_b64_tr_b16 v[178:179], v11 offset:4096\n\t" \
  "v_mfma_f32_16x16x32_bf16 v[92:95], v[196:199], v[148:151], v[92:95]\n\t" \
  "s_add_u32 m0, s62, 0x20000\n\t" \
  "v_mfma_f32_16x16x32_bf16 v[88:91], v[192:195], v[148:151], v[88:91]\n\t" \
  "ds_read_b128 v[148:151], v10 offset:1024\n\t" \
  "v_mfma_f32_16x16x32_bf16 v[96:99], v[192:195], v[152:155], v[96:99]\n\t" \
  "ds_read_b64_tr_b16 v[180:181], v12 offset:0\n\t" \
  "global_load_lds_dwordx4 v1, s[40:41] offset:0 sc1\n\t" \
  "v_mfma_f32_16x16x32_bf16 v[100:103], v[196:199], v[152:155], v[100:103]\n\t" \
  "ds_read_b64_tr_b16 v[182:183], v12 offset:4096\n\t" \
  "v_mfma_f32_16x16x32_bf16 v[32:35], v[200:203], v[152:155], v[32:35]\n\t" \
  "global_load_lds_dwordx4 v2, s[40:41] offset:1024 sc1\n\t" \
  "v_mfma_f32_16x16x32_bf16 v[36:39], v[204:207], v[152:155], v[36:39]\n\t" \
  "ds_read_b128 v[152:155], v10 offset:4096\n\t" \
  "v_mfma_f32_16x16x32_bf16 v[44:47], v[204:207], v[156:159], v[44:47]\n\t" \
  "ds_read_b64_tr_b16 v[184:185], v11 offset:1024\n\t" \
  "global_load_lds_dwordx4 v3, s[42:43] offset:2048\n\t" \
  "v_mfma_f32_16x16x32_bf16 v[40:43], v[200:203], v[156:159], v[40:43]\n\t" \
  "ds_read_b64_tr_b16 v[186:187], v11 offset:5120\n\t" \
  "v_mfma_f32_16x16x32_bf16 v[108:111], v[196:199], v[156:159], v[108:111]\n\t" \
  "global_load_lds_dwordx4 v[4:5], off offset:3072\n\t" \
  "v_mfma_f32_16x16x32_bf16 v[104:107], v[192:195], v[156:159], v[104:107]\n\t" \
  "ds_read_b128 v[156:159], v10 offset:5120\n\t" \
  "s_waitcnt lgkmcnt(10)\n\t" \
  "v_mfma_f32_16x16x32_bf16 v[112:115], v[192:195], v[160:163], v[112:115]\n\t" \
  "ds_read_b64_tr_b16 v[188:189], v12 offset:1024\n\t" \
  "s_add_u32 s40, s40, s78\n\t" \
  "s_addc_u32 s41, s41, s77\n\t" \
  "v_mfma_f32_16x16x32_bf16 v[116:119], v[196:199], v[160:163], v[116:119]\n\t" \
  "ds_read_b64_tr_b16 v[190:191], v12 offset:5120\n\t" \
  "s_add_u32 s42, s42, s76\n\t" \
  "s_addc_u32 s43, s43, s77\n\t" \
  "v_mfma_f32_16x16x32_bf16 v[48:51], v[200:203], v[160:163], v[48:51]\n\t" \
  "v_lshl_add_u64 v[4:5], v[4:5], 0, s[76:77]\n\t" \
  "v_mfma_f32_16x16x32_bf16 v[52:55], v[204:207], v[160:163], v[52:55]\n\t" \
  "ds_read_b128 v[160:163], v10 offset:8192\n\t" \
  "v_mfma_f32_16x16x32_bf16 v[60:63], v[204:207], v[164:167], v[60:63]\n\t" \
  "v_mfma_f32_16x16x32_bf16 v[56:59], v[200:203], v[164:167], v[56:59]\n\t" \
  "v_mfma_f32_16x16x32_bf16 v[124:127], v[196:199], v[164:167], v[124:127]\n\t" \
  "v_mfma_f32_16x16x32_bf16 v[120:123], v[192:195], v[164:167], v[120:123]\n\t" \
  "ds_read_b128 v[164:167], v10 offset:9216\n\t" \
  "v_mfma_f32_16x16x32_bf16 v[128:131], v[192:195], v[168:171], v[128:131]\n\t" \
  "v_mfma_f32_16x16x32_bf16 v[132:135], v[196:199], v[168:171], v[132:135]\n\t" \
  "v_mfma_f32_16x16x32_bf16 v[64:67], v[200:203], v[168:171], v[64:67]\n\t" \
  "v_mfma_f32_16x16x32_bf16 v[68:71], v[204:207], v[168:171], v[68:71]\n\t" \
  "ds_read_b128 v[168:171], v10 offset:12288\n\t" \
  "v_mfma_f32_16x16x32_bf16 v[76:79], v[204:207], v[172:175], v[76:79]\n\t" \
  "v_mfma_f32_16x16x32_bf16 v[72:75], v[200:203], v[172:175], v[72:75]\n\t" \
  "v_mfma_f32_16x16x32_bf16 v[140:143], v[196:199], v[172:175], v[140:143]\n\t" \
  "v_mfma_f32_16x16x32_bf16 v[136:139], v[192:195], v[172:175], v[136:139]\n\t" \
  "ds_read_b128 v[172:175], v10 offset:13312\n\t" \
  "s_lshr_b32 s53, %[bid], 6\n\t" \
  "s_cmp_eq_u32 s53, 0\n\t" \
  "s_cbranch_scc1 HT0T_%=\n\t" \
  "HR0T_%=:\n\t" \
  "s_waitcnt lgkmcnt(4)\n\t" \
  "v_mfma_f32_16x16x32_bf16 v[80:83], v[176:179], v[144:147], v[80:83]\n\t" \
  "s_waitcnt vmcnt(8)\n\t" \
  "s_barrier\n\t" \
  "v_mfma_f32_16x16x32_bf16 v[84:87], v[180:183], v[144:147], v[84:87]\n\t" \
  "v_add_u32_e32 v10, 0x10000, v6\n\t" \
  "v_add_u32_e32 v11, 0x10000, v7\n\t" \
  "v_mfma_f32_16x16x32_bf16 v[16:19], v[184:187], v[144:147], v[16:19]\n\t" \
  "v_xor_b32_e32 v12, 32, v11\n\t" \
  "v_mfma_f32_16x16x32_bf16 v[20:23], v[188:191], v[144:147], v[20:23]\n\t" \
  "ds_read_b128 v[144:147], v10 offset:0\n\t" \
  "v_mfma_f32_16x16x32_bf16 v[28:31], v[188:191], v[148:151], v[28:31]\n\t" \
  "ds_read_b64_tr_b16 v[192:193], v11 offset:0\n\t" \
  "v_mfma_f32_16x16x32_bf16 v[24:27], v[184:187], v[148:151], v[24:27]\n\t" \
  "ds_read_b64_tr_b16 v[194:195], v11 offset:4096\n\t" \
  "v_mfma_f32_16x16x32_bf16 v[92:95], v[180:183], v[148:151], v[92:95]\n\t" \
  "s_add_u32 m0, s62, 0x0\n\t" \
  "v_mfma_f32_16x16x32_bf16 v[88:91], v[176:179], v[148:151], v[88:91]\n\t" \
  "ds_read_b128 v[148:151], v10 offset:1024\n\t" \
  "v_mfma_f32_16x16x32_bf16 v[96:99], v[176:179], v[152:155], v[96:99]\n\t" \
  "ds_read_b64_tr_b16 v[196:197], v12 offset:0\n\t" \
  "global_load_lds_dwordx4 v1, s[40:41] offset:0 sc1\n\t" \
  "v_mfma_f32_16x16x32_bf16 v[100:103], v[180:183], v[152:155], v[100:103]\n\t" \
  "ds_read_b64_tr_b16 v[198:199], v12 offset:4096\n\t" \
  "v_mfma_f32_16x16x32_bf16 v[32:35], v[184:187], v[152:155], v[32:35]\n\t" \
  "global_load_lds_dwordx4 v2, s[40:41] offset:1024 sc1\n\t" \
  "v_mfma_f32_16x16x32_bf16 v[36:39], v[188:191], v[152:155], v[36:39]\n\t" \
  "ds_read_b128 v[152:155], v10 offset:4096\n\t" \
  "v_mfma_f32_16x16x32_bf16 v[44:47], v[188:191], v[156:159], v[44:47]\n\t" \
  "ds_read_b64_tr_b16 v[200:201], v11 offset:1024\n\t" \
  "global_load_lds_dwordx4 v3, s[42:43] offset:2048\n\t" \
  "v_mfma_f32_16x16x32_bf16 v[40:43], v[184:187], v[156:159], v[40:43]\n\t" \
  "ds_read_b64_tr_b16 v[202:203], v11 offset:5120\n\t" \
  "v_mfma_f32_16x16x32_bf16 v[108:111], v[180:183], v[156:159], v[108:111]\n\t" \
  "global_load_lds_dwordx4 v[4:5], off offset:3072\n\t" \
  "v_mfma_f32_16x16x32_bf16 v[104:107], v[176:179], v[156:159], v[104:107]\n\t" \
  "ds_read_b128 v[156:159], v10 offset:5120\n\t" \
  "s_waitcnt lgkmcnt(10)\n\t" \
  "v_mfma_f32_16x16x32_bf16 v[112:115], v[176:179], v[160:163], v[112:115]\n\t" \
  "ds_read_b64_tr_b16 v[204:205], v12 offset:1024\n\t" \
  "s_add_u32 s40, s40, s74\n\t" \
  "s_addc_u32 s41, s41, s73\n\t" \
  "v_mfma_f32_16x16x32_bf16 v[116:119], v[180:183], v[160:163], v[116:119]\n\t" \
  "ds_read_b64_tr_b16 v[206:207], v12 offset:5120\n\t" \
  "s_add_u32 s42, s42, s72\n\t" \
  "s_addc_u32 s43, s43, s73\n\t" \
  "v_mfma_f32_16x16x32_bf16 v[48:51], v[184:187], v[160:163], v[48:51]\n\t" \
  "v_lshl_add_u64 v[4:5], v[4:5], 0, s[72:73]\n\t" \
  "v_mfma_f32_16x16x32_bf16 v[52:55], v[188:191], v[160:163], v[52:55]\n\t" \
  "ds_read_b128 v[160:163], v10 offset:8192\n\t" \
  "v_mfma_f32_16x16x32_bf16 v[60:63], v[188:191], v[164:167], v[60:63]\n\t" \
  "v_mfma_f32_16x16x32_bf16 v[56:59], v[184:187], v[164:167], v[56:59]\n\t" \
  "v_mfma_f32_16x16x32_bf16 v[124:127], v[180:183], v[164:167], v[124:127]\n\t" \
  "v_mfma_f32_16x16x32_bf16 v[120:123], v[176:179], v[164:167], v[120:123]\n\t" \
  "ds_read_b128 v[164:167], v10 offset:9216\n\t" \
  "v_mfma_f32_16x16x32_bf16 v[128:131], v[176:179], v[168:171], v[128:131]\n\t" \
  "v_mfma_f32_16x16x32_bf16 v[132:135], v[180:183], v[168:171], v[132:135]\n\t" \
  "v_mfma_f32_16x16x32_bf16 v[64:67], v[184:187], v[168:171], v[64:67]\n\t" \
  "v_mfma_f32_16x16x32_bf16 v[68:71], v[188:191], v[168:171], v[68:71]\n\t" \
  "ds_read_b128 v[168:171], v10 offset:12288\n\t" \
  "v_mfma_f32_16x16x32_bf16 v[76:79], v[188:191], v[172:175], v[76:79]\n\t" \
  "v_mfma_f32_16x16x32_bf16 v[72:75], v[184:187], v[172:175], v[72:75]\n\t" \
  "v_mfma_f32_16x16x32_bf16 v[140:143], v[180:183], v[172:175], v[140:143]\n\t" \
  "v_mfma_f32_16x16x32_bf16 v[136:139], v[176:179], v[172:175], v[136:139]\n\t" \
  "ds_read_b128 v[172:175], v10 offset:13312\n\t" \
  "s_waitcnt lgkmcnt(4)\n\t" \
  "v_mfma_f32_16x16x32_bf16 v[80:83], v[192:195], v[144:147], v[80:83]\n\t" \
  "s_waitcnt vmcnt(8)\n\t" \
  "s_barrier\n\t" \
  "v_mfma_f32_16x16x32_bf16 v[84:87], v[196:199], v[144:147], v[84:87]\n\t" \
  "v_add_u32_e32 v10, 0x18000, v6\n\t" \
  "v_add_u32_e32 v11, 0x18000, v7\n\t" \
  "v_mfma_f32_16x16x32_bf16 v[16:19], v[200:203], v[144:147], v[16:19]\n\t" \
  "v_xor_b32_e32 v12, 32, v11\n\t" \
  "v_mfma_f32_16x16x32_bf16 v[20:23], v[204:207], v[144:147], v[20:23]\n\t" \
  "ds_read_b128 v[144:147], v10 offset:0\n\t" \
  "v_mfma_f32_16x16x32_bf16 v[28:31], v[204:207], v[148:151], v[28:31]\n\t" \
  "ds_read_b64_tr_b16 v[176:177], v11 offset:0\n\t" \
  "v_mfma_f32_16x16x32_bf16 v[24:27], v[200:203], v[148:151], v[24:27]\n\t" \
  "ds_read_b64_tr_b16 v[178:179], v11 offset:4096\n\t" \
  "v_mfma_f32_16x16x32_bf16 v[92:95], v[196:199], v[148:151], v[92:95]\n\t" \
  "s_add_u32 m0, s62, 0x8000\n\t" \
  "v_mfma_f32_16x16x32_bf16 v[88:91], v[192:195], v[148:151], v[88:91]\n\t" \
  "ds_read_b128 v[148:151], v10 offset:1024\n\t" \
  "v_mfma_f32_16x16x32_bf16 v[96:99], v[192:195], v[152:155], v[96:99]\n\t" \
  "ds_read_b64_tr_b16 v[180:181], v12 offset:0\n\t" \
  "global_load_lds_dwordx4 v1, s[40:41] offset:0 sc1\n\t" \
  "v_mfma_f32_16x16x32_bf16 v[100:103], v[196:199], v[152:155], v[100:103]\n\t" \
  "ds_read_b64_tr_b16 v[182:183], v12 offset:4096\n\t" \
  "v_mfma_f32_16x16x32_bf16 v[32:35], v[200:203], v[152:155], v[32:35]\n\t" \
  "global_load_lds_dwordx4 v2, s[40:41] offset:1024 sc1\n\t" \
  "v_mfma_f32_16x16x32_bf16 v[36:39], v[204:207], v[152:155], v[36:39]\n\t" \
  "ds_read_b128 v[152:155], v10 offset:4096\n\t" \
  "v_mfma_f32_16x16x32_bf16 v[44:47], v[204:207], v[156:159], v[44:47]\n\t" \
  "ds_read_b64_tr_b16 v[184:185], v11 offset:1024\n\t" \
  "global_load_lds_dwordx4 v3, s[42:43] offset:2048\n\t" \
  "v_mfma_f32_16x16x32_bf16 v[40:43], v[200:203], v[156:159], v[40:43]\n\t" \
  "ds_read_b64_tr_b16 v[186:187], v11 offset:5120\n\t" \
  "v_mfma_f32_16x16x32_bf16 v[108:111], v[196:199], v[156:159], v[108:111]\n\t" \
  "global_load_lds_dwordx4 v[4:5], off offset:3072\n\t" \
  "v_mfma_f32_16x16x32_bf16 v[104:107], v[192:195], v[156:159], v[104:107]\n\t" \
  "ds_read_b128 v[156:159], v10 offset:5120\n\t" \
  "s_waitcnt lgkmcnt(10)\n\t" \
  "v_mfma_f32_16x16x32_bf16 v[112:115], v[192:195], v[160:163], v[112:115]\n\t" \
  "ds_read_b64_tr_b16 v[188:189], v12 offset:1024\n\t" \
  "s_add_u32 s40, s40, s82\n\t" \
  "s_addc_u32 s41, s41, s81\n\t" \
  "v_mfma_f32_16x16x32_bf16 v[116:119], v[196:199], v[160:163], v[116:119]\n\t" \
  "ds_read_b64_tr_b16 v[190:191], v12 offset:5120\n\t" \
  "s_add_u32 s42, s42, s80\n\t" \
  "s_addc_u32 s43, s43, s81\n\t" \
  "v_mfma_f32_16x16x32_bf16 v[48:51], v[200:203], v[160:163], v[48:51]\n\t" \
  "v_lshl_add_u64 v[4:5], v[4:5], 0, s[80:81]\n\t" \
  "v_mfma_f32_16x16x32_bf16 v[52:55], v[204:207], v[160:163], v[52:55]\n\t" \
  "ds_read_b128 v[160:163], v10 offset:8192\n\t" \
  "v_mfma_f32_16x16x32_bf16 v[60:63], v[204:207], v[164:167], v[60:63]\n\t" \
  "v_mfma_f32_16x16x32_bf16 v[56:59], v[200:203], v[164:167], v[56:59]\n\t" \
  "v_mfma_f32_16x16x32_bf16 v[124:127], v[196:199], v[164:167], v[124:127]\n\t" \
  "v_mfma_f32_16x16x32_bf16 v[120:123], v[192:195], v[164:167], v[120:123]\n\t" \
  "ds_read_b128 v[164:167], v10 offset:9216\n\t" \
  "v_mfma_f32_16x16x32_bf16 v[128:131], v[192:195], v[168:171], v[128:131]\n\t" \
  "v_mfma_f32_16x16x32_bf16 v[132:135], v[196:199], v[168:171], v[132:135]\n\t" \
  "v_mfma_f32_16x16x32_bf16 v[64:67], v[200:203], v[168:171], v[64:67]\n\t" \
  "v_mfma_f32_16x16x32_bf16 v[68:71], v[204:207], v[168:171], v[68:71]\n\t" \
  "ds_read_b128 v[168:171], v10 offset:12288\n\t" \
  "v_mfma_f32_16x16x32_bf16 v[76:79], v[204:207], v[172:175], v[76:79]\n\t" \
  "v_mfma_f32_16x16x32_bf16 v[72:75], v[200:203], v[172:175], v[72:75]\n\t" \
  "v_mfma_f32_16x16x32_bf16 v[140:143], v[196:199], v[172:175], v[140:143]\n\t" \
  "v_mfma_f32_16x16x32_bf16 v[136:139], v[192:195], v[172:175], v[136:139]\n\t" \
  "ds_read_b128 v[172:175], v10 offset:13312\n\t" \
  "s_lshr_b32 s53, %[bid], 6\n\t" \
  "s_cmp_eq_u32 s53, 1\n\t" \
  "s_cbranch_scc1 HT1T_%=\n\t" \
  "HR1T_%=:\n\t" \
  "s_waitcnt lgkmcnt(4)\n\t" \
  "v_mfma_f32_16x16x32_bf16 v[80:83], v[176:179], v[144:147], v[80:83]\n\t" \
  "s_waitcnt vmcnt(8)\n\t" \
  "s_barrier\n\t" \
  "v_mfma_f32_16x16x32_bf16 v[84:87], v[180:183], v[144:147], v[84:87]\n\t" \
  "v_add_u32_e32 v10, 0x20000, v6\n\t" \
  "v_add_u32_e32 v11, 0x20000, v7\n\t" \
  "v_mfma_f32_16x16x32_bf16 v[16:19], v[184:187], v[144:147], v[16:19]\n\t" \
  "v_xor_b32_e32 v12, 32, v11\n\t" \
  "v_mfma_f32_16x16x32_bf16 v[20:23], v[188:191], v[144:147], v[20:23]\n\t" \
  "ds_read_b128 v[144:147], v10 offset:0\n\t" \
  "v_mfma_f32_16x16x32_bf16 v[28:31], v[188:191], v[148:151], v[28:31]\n\t" \
  "ds_read_b64_tr_b16 v[192:193], v11 offset:0\n\t" \
  "v_mfma_f32_16x16x32_bf16 v[24:27], v[184:187], v[148:151], v[24:27]\n\t" \
  "ds_read_b64_tr_b16 v[194:195], v11 offset:4096\n\t" \
  "v_mfma_f32_16x16x32_bf16 v[92:95], v[180:183], v[148:151], v[92:95]\n\t" \
  "s_add_u32 m0, s62, 0x10000\n\t" \
  "v_mfma_f32_16x16x32_bf16 v[88:91], v[176:179], v[148:151], v[88:91]\n\t" \
  "ds_read_b128 v[148:151], v10 offset:1024\n\t" \
  "v_mfma_f32_16x16x32_bf16 v[96:99], v[176:179], v[152:155], v[96:99]\n\t" \
  "ds_read_b64_tr_b16 v[196:197], v12 offset:0\n\t" \
  "global_load_lds_dwordx4 v1, s[40:41] offset:0 sc1\n\t" \
  "v_mfma_f32_16x16x32_bf16 v[100:103], v[180:183], v[152:155], v[100:103]\n\t" \
  "ds_read_b64_tr_b16 v[198:199], v12 offset:4096\n\t" \
  "v_mfma_f32_16x16x32_bf16 v[32:35], v[184:187], v[152:155], v[32:35]\n\t" \
  "global_load_lds_dwordx4 v2, s[40:41] offset:1024 sc1\n\t" \
  "v_mfma_f32_16x16x32_bf16 v[36:39], v[188:191], v[152:155], v[36:39]\n\t" \
  "ds_read_b128 v[152:155], v10 offset:4096\n\t" \
  "v_mfma_f32_16x16x32_bf16 v[44:47], v[188:191], v[156:159], v[44:47]\n\t" \
  "ds_read_b64_tr_b16 v[200:201], v11 offset:1024\n\t" \
  "global_load_lds_dwordx4 v3, s[42:43] offset:2048\n\t" \
  "v_mfma_f32_16x16x32_bf16 v[40:43], v[184:187], v[156:159], v[40:43]\n\t" \
  "ds_read_b64_tr_b16 v[202:203], v11 offset:5120\n\t" \
  "v_mfma_f32_16x16x32_bf16 v[108:111], v[180:183], v[156:159], v[108:111]\n\t" \
  "global_load_lds_dwordx4 v[4:5], off offset:3072\n\t" \
  "v_mfma_f32_16x16x32_bf16 v[104:107], v[176:179], v[156:159], v[104:107]\n\t" \
  "ds_read_b128 v[156:159], v10 offset:5120\n\t" \
  "s_waitcnt lgkmcnt(10)\n\t" \
  "v_mfma_f32_16x16x32_bf16 v[112:115], v[176:179], v[160:163], v[112:115]\n\t" \
  "ds_read_b64_tr_b16 v[204:205], v12 offset:1024\n\t" \
  "s_add_u32 s40, s40, s74\n\t" \
  "s_addc_u32 s41, s41, s73\n\t" \
  "v_mfma_f32_16x16x32_bf16 v[116:119], v[180:183], v[160:163], v[116:119]\n\t" \
  "ds_read_b64_tr_b16 v[206:207], v12 offset:5120\n\t" \
  "s_add_u32 s42, s42, s72\n\t" \
  "s_addc_u32 s43, s43, s73\n\t" \
  "v_mfma_f32_16x16x32_bf16 v[48:51], v[184:187], v[160:163], v[48:51]\n\t" \
  "v_lshl_add_u64 v[4:5], v[4:5], 0, s[72:73]\n\t" \
  "v_mfma_f32_16x16x32_bf16 v[52:55], v[188:191], v[160:163], v[52:55]\n\t" \
  "ds_read_b128 v[160:163], v10 offset:8192\n\t" \
  "v_mfma_f32_16x16x32_bf16 v[60:63], v[188:191], v[164:167], v[60:63]\n\t" \
  "v_mfma_f32_16x16x32_bf16 v[56:59], v[184:187], v[164:167], v[56:59]\n\t" \
  "v_mfma_f32_16x16x32_bf16 v[124:127], v[180:183], v[164:167], v[124:127]\n\t" \
  "v_mfma_f32_16x16x32_bf16 v[120:123], v[176:179], v[164:167], v[120:123]\n\t" \
  "ds_read_b128 v[164:167], v10 offset:9216\n\t" \
  "v_mfma_f32_16x16x32_bf16 v[128:131], v[176:179], v[168:171], v[128:131]\n\t" \
  "v_mfma_f32_16x16x32_bf16 v[132:135], v[180:183], v[168:171], v[132:135]\n\t" \
  "v_mfma_f32_16x16x32_bf16 v[64:67], v[184:187], v[168:171], v[64:67]\n\t" \
  "v_mfma_f32_16x16x32_bf16 v[68:71], v[188:191], v[168:171], v[68:71]\n\t" \
  "ds_read_b128 v[168:171], v10 offset:12288\n\t" \
  "v_mfma_f32_16x16x32_bf16 v[76:79], v[188:191], v[172:175], v[76:79]\n\t" \
  "v_mfma_f32_16x16x32_bf16 v[72:75], v[184:187], v[172:175], v[72:75]\n\t" \
  "v_mfma_f32_16x16x32_bf16 v[140:143], v[180:183], v[172:175], v[140:143]\n\t" \
  "v_mfma_f32_16x16x32_bf16 v[136:139], v[176:179], v[172:175], v[136:139]\n\t" \
  "ds_read_b128 v[172:175], v10 offset:13312\n\t" \
  "s_waitcnt lgkmcnt(4)\n\t" \
  "v_mfma_f32_16x16x32_bf16 v[80:83], v[192:195], v[144:147], v[80:83]\n\t" \
  "s_waitcnt vmcnt(8)\n\t" \
  "s_barrier\n\t" \
  "v_mfma_f32_16x16x32_bf16 v[84:87], v[196:199], v[144:147], v[84:87]\n\t" \
  "v_add_u32_e32 v10, 0x0, v6\n\t" \
  "v_add_u32_e32 v11, 0x0, v7\n\t" \
  "v_mfma_f32_16x16x32_bf16 v[16:19], v[200:203], v[144:147], v[16:19]\n\t" \
  "v_xor_b32_e32 v12, 32, v11\n\t" \
  "v_mfma_f32_16x16x32_bf16 v[20:23], v[204:207], v[144:147], v[20:23]\n\t" \
  "ds_read_b128 v[144:147], v10 offset:0\n\t" \
  "v_mfma_f32_16x16x32_bf16 v[28:31], v[204:207], v[148:151], v[28:31]\n\t" \
  "ds_read_b64_tr_b16 v[176:177], v11 offset:0\n\t" \
  "v_mfma_f32_16x16x32_bf16 v[24:27], v[200:203], v[148:151], v[24:27]\n\t" \
  "ds_read_b64_tr_b16 v[178:179], v11 offset:4096\n\t" \
  "v_mfma_f32_16x16x32_bf16 v[92:95], v[196:199], v[148:151], v[92:95]\n\t" \
  "s_add_u32 m0, s62, 0x18000\n\t" \
  "v_mfma_f32_16x16x32_bf16 v[88:91], v[192:195], v[148:151], v[88:91]\n\t" \
  "ds_read_b128 v[148:151], v10 offset:1024\n\t" \
  "v_mfma_f32_16x16x32_bf16 v[96:99], v[192:195], v[152:155], v[96:99]\n\t" \
  "ds_read_b64_tr_b16 v[180:181], v12 offset:0\n\t" \
  "global_load_lds_dwordx4 v1, s[40:41] offset:0 sc1\n\t" \
  "v_mfma_f32_16x16x32_bf16 v[100:103], v[196:199], v[152:155], v[100:103]\n\t" \
  "ds_read_b64_tr_b16 v[182:183], v12 offset:4096\n\t" \
  "v_mfma_f32_16x16x32_bf16 v[32:35], v[200:203], v[152:155], v[32:35]\n\t" \
  "global_load_lds_dwordx4 v2, s[40:41] offset:1024 sc1\n\t" \
  "v_mfma_f32_16x16x32_bf16 v[36:39], v[204:207], v[152:155], v[36:39]\n\t" \
  "ds_read_b128 v[152:155], v10 offset:4096\n\t" \
  "v_mfma_f32_16x16x32_bf16 v[44:47], v[204:207], v[156:159], v[44:47]\n\t" \
  "ds_read_b64_tr_b16 v[184:185], v11 offset:1024\n\t" \
  "global_load_lds_dwordx4 v3, s[42:43] offset:2048\n\t" \
  "v_mfma_f32_16x16x32_bf16 v[40:43], v[200:203], v[156:159], v[40:43]\n\t" \
  "ds_read_b64_tr_b16 v[186:187], v11 offset:5120\n\t" \
  "v_mfma_f32_16x16x32_bf16 v[108:111], v[196:199], v[156:159], v[108:111]\n\t" \
  "global_load_lds_dwordx4 v[4:5], off offset:3072\n\t" \
  "v_mfma_f32_16x16x32_bf16 v[104:107], v[192:195], v[156:159], v[104:107]\n\t" \
  "ds_read_b128 v[156:159], v10 offset:5120\n\t" \
  "s_waitcnt lgkmcnt(10)\n\t" \
  "v_mfma_f32_16x16x32_bf16 v[112:115], v[192:195], v[160:163], v[112:115]\n\t" \
  "ds_read_b64_tr_b16 v[188:189], v12 offset:1024\n\t" \
  "s_add_u32 s40, s40, s78\n\t" \
  "s_addc_u32 s41, s41, s77\n\t" \
  "v_mfma_f32_16x16x32_bf16 v[116:119], v[196:199], v[160:163], v[116:119]\n\t" \
  "ds_read_b64_tr_b16 v[190:191], v12 offset:5120\n\t" \
  "s_add_u32 s42, s42, s76\n\t" \
  "s_addc_u32 s43, s43, s77\n\t" \
  "v_mfma_f32_16x16x32_bf16 v[48:51], v[200:203], v[160:163], v[48:51]\n\t" \
  "v_lshl_add_u64 v[4:5], v[4:5], 0, s[76:77]\n\t" \
  "v_mfma_f32_16x16x32_bf16 v[52:55], v[204:207], v[160:163], v[52:55]\n\t" \
  "ds_read_b128 v[160:163], v10 offset:8192\n\t" \
  "v_mfma_f32_16x16x32_bf16 v[60:63], v[204:207], v[164:167], v[60:63]\n\t" \
  "s_sub_u32 s59, 15, s66\n\t" \
  "s_cmp_lt_u32 %[w], 4\n\t" \
  "s_cselect_b32 s59, s59, s66\n\t" \
  "v_mfma_f32_16x16x32_bf16 v[56:59], v[200:203], v[164:167], v[56:59]\n\t" \
  "s_lshl_b32 s52, s59, 7\n\t" \
  "s_lshl_b32 s53, s63, 5\n\t" \
  "v_mfma_f32_16x16x32_bf16 v[124:127], v[196:199], v[164:167], v[124:127]\n\t" \
  "s_add_u32 s52, s52, s53\n\t" \
  "s_add_u32 s52, s52, 0x11000\n\t" \
  "v_mfma_f32_16x16x32_bf16 v[120:123], v[192:195], v[164:167], v[120:123]\n\t" \
  "ds_read_b128 v[164:167], v10 offset:9216\n\t" \
  "v_mfma_f32_16x16x32_bf16 v[128:131], v[192:195], v[168:171], v[128:131]\n\t" \
  "v_and_b32_e32 v13, 3, v0\n\t" \
  "v_lshlrev_b32_e32 v13, 3, v13\n\t" \
  "v_mfma_f32_16x16x32_bf16 v[132:135], v[196:199], v[168:171], v[132:135]\n\t" \
  "v_add_u32_e32 v13, s52, v13\n\t" \
  "global_load_dwordx2 v[14:15], v13, s[60:61] sc1\n\t" \
  "v_mfma_f32_16x16x32_bf16 v[64:67], v[200:203], v[168:171], v[64:67]\n\t" \
  "v_mfma_f32_16x16x32_bf16 v[68:71], v[204:207], v[168:171], v[68:71]\n\t" \
  "ds_read_b128 v[168:171], v10 offset:12288\n\t" \
  "v_mfma_f32_16x16x32_bf16 v[76:79], v[204:207], v[172:175], v[76:79]\n\t" \
  "v_mfma_f32_16x16x32_bf16 v[72:75], v[200:203], v[172:175], v[72:75]\n\t" \
  "v_mfma_f32_16x16x32_bf16 v[140:143], v[196:199], v[172:175], v[140:143]\n\t" \
  "v_mfma_f32_16x16x32_bf16 v[136:139], v[192:195], v[172:175], v[136:139]\n\t" \
  "ds_read_b128 v[172:175], v10 offset:13312\n\t" \
  "s_lshr_b32 s53, %[bid], 6\n\t" \
  "s_cmp_eq_u32 s53, 2\n\t" \
  "s_cbranch_scc1 HT2T_%=\n\t" \
  "HR2T_%=:\n\t" \
  "s_waitcnt lgkmcnt(4)\n\t" \
  "v_mfma_f32_16x16x32_bf16 v[80:83], v[176:179], v[144:147], v[80:83]\n\t" \
  "s_waitcnt vmcnt(8)\n\t" \
  "s_barrier\n\t" \
  "v_mfma_f32_16x16x32_bf16 v[84:87], v[180:183], v[144:147], v[84:87]\n\t" \
  "v_add_u32_e32 v10, 0x8000, v6\n\t" \
  "v_add_u32_e32 v11, 0x8000, v7\n\t" \
  "v_mfma_f32_16x16x32_bf16 v[16:19], v[184:187], v[144:147], v[16:19]\n\t" \
  "v_xor_b32_e32 v12, 32, v11\n\t" \
  "v_mfma_f32_16x16x32_bf16 v[20:23], v[188:191], v[144:147], v[20:23]\n\t" \
  "ds_read_b128 v[144:147], v10 offset:0\n\t" \
  "v_mfma_f32_16x16x32_bf16 v[28:31], v[188:191], v[148:151], v[28:31]\n\t" \
  "ds_read_b64_tr_b16 v[192:193], v11 offset:0\n\t" \
  "v_mfma_f32_16x16x32_bf16 v[24:27], v[184:187], v[148:151], v[24:27]\n\t" \
  "ds_read_b64_tr_b16 v[194:195], v11 offset:4096\n\t" \
  "v_mfma_f32_16x16x32_bf16 v[92:95], v[180:183], v[148:151], v[92:95]\n\t" \
  "s_add_u32 m0, s62, 0x20000\n\t" \
  "v_mfma_f32_16x16x32_bf16 v[88:91], v[176:179], v[148:151], v[88:91]\n\t" \
  "ds_read_b128 v[148:151], v10 offset:1024\n\t" \
  "v_mfma_f32_16x16x32_bf16 v[96:99], v[176:179], v[152:155], v[96:99]\n\t" \
  "ds_read_b64_tr_b16 v[196:197], v12 offset:0\n\t" \
  "global_load_lds_dwordx4 v1, s[40:41] offset:0 sc1\n\t" \
  "v_mfma_f32_16x16x32_bf16 v[100:103], v[180:183], v[152:155], v[100:103]\n\t" \
  "ds_read_b64_tr_b16 v[198:199], v12 offset:4096\n\t" \
  "v_mfma_f32_16x16x32_bf16 v[32:35], v[184:187], v[152:155], v[32:35]\n\t" \
  "global_load_lds_dwordx4 v2, s[40:41] offset:1024 sc1\n\t" \
  "v_mfma_f32_16x16x32_bf16 v[36:39], v[188:191], v[152:155], v[36:39]\n\t" \
  "ds_read_b128 v[152:155], v10 offset:4096\n\t" \
  "v_mfma_f32_16x16x32_bf16 v[44:47], v[188:191], v[156:159], v[44:47]\n\t" \
  "ds_read_b64_tr_b16 v[200:201], v11 offset:1024\n\t" \
  "global_load_lds_dwordx4 v3, s[42:43] offset:2048\n\t" \
  "v_mfma_f32_16x16x32_bf16 v[40:43], v[184:187], v[156:159], v[40:43]\n\t" \
  "ds_read_b64_tr_b16 v[202:203], v11 offset:5120\n\t" \
  "v_mfma_f32_16x16x32_bf16 v[108:111], v[180:183], v[156:159], v[108:111]\n\t" \
  "global_load_lds_dwordx4 v[4:5], off offset:3072\n\t" \
  "v_mfma_f32_16x16x32_bf16 v[104:107], v[176:179], v[156:159], v[104:107]\n\t" \
  "ds_read_b128 v[156:159], v10 offset:5120\n\t" \
  "s_waitcnt lgkmcnt(10)\n\t" \
  "v_mfma_f32_16x16x32_bf16 v[112:115], v[176:179], v[160:163], v[112:115]\n\t" \
  "ds_read_b64_tr_b16 v[204:205], v12 offset:1024\n\t" \
  "s_add_u32 s40, s40, s74\n\t" \
  "s_addc_u32 s41, s41, s73\n\t" \
  "v_mfma_f32_16x16x32_bf16 v[116:119], v[180:183], v[160:163], v[116:119]\n\t" \
  "ds_read_b64_tr_b16 v[206:207], v12 offset:5120\n\t" \
  "s_add_u32 s42, s42, s72\n\t" \
  "s_addc_u32 s43, s43, s73\n\t" \
  "v_mfma_f32_16x16x32_bf16 v[48:51], v[184:187], v[160:163], v[48:51]\n\t" \
  "v_lshl_add_u64 v[4:5], v[4:5], 0, s[72:73]\n\t" \
  "v_mfma_f32_16x16x32_bf16 v[52:55], v[188:191], v[160:163], v[52:55]\n\t" \
  "ds_read_b128 v[160:163], v10 offset:8192\n\t" \
  "v_mfma_f32_16x16x32_bf16 v[60:63], v[188:191], v[164:167], v[60:63]\n\t" \
  "v_mfma_f32_16x16x32_bf16 v[56:59], v[184:187], v[164:167], v[56:59]\n\t" \
  "v_mfma_f32_16x16x32_bf16 v[124:127], v[180:183], v[164:167], v[124:127]\n\t" \
  "v_mfma_f32_16x16x32_bf16 v[120:123], v[176:179], v[164:167], v[120:123]\n\t" \
  "ds_read_b128 v[164:167], v10 offset:9216\n\t" \
  "v_mfma_f32_16x16x32_bf16 v[128:131], v[176:179], v[168:171], v[128:131]\n\t" \
  "v_mfma_f32_16x16x32_bf16 v[132:135], v[180:183], v[168:171], v[132:135]\n\t" \
  "v_mfma_f32_16x16x32_bf16 v[64:67], v[184:187], v[168:171], v[64:67]\n\t" \
  "v_mfma_f32_16x16x32_bf16 v[68:71], v[188:191], v[168:171], v[68:71]\n\t" \
  "ds_read_b128 v[168:171], v10 offset:12288\n\t" \
  "v_mfma_f32_16x16x32_bf16 v[76:79], v[188:191], v[172:175], v[76:79]\n\t" \
  "v_mfma_f32_16x16x32_bf16 v[72:75], v[184:187], v[172:175], v[72:75]\n\t" \
  "v_mfma_f32_16x16x32_bf16 v[140:143], v[180:183], v[172:175], v[140:143]\n\t" \
  "v_mfma_f32_16x16x32_bf16 v[136:139], v[176:179], v[172:175], v[136:139]\n\t" \
  "ds_read_b128 v[172:175], v10 offset:13312\n\t" \
  "s_waitcnt lgkmcnt(4)\n\t" \
  "v_mfma_f32_16x16x32_bf16 v[80:83], v[192:195], v[144:147], v[80:83]\n\t" \
  "s_waitcnt vmcnt(8)\n\t" \
  "s_barrier\n\t" \
  "v_mfma_f32_16x16x32_bf16 v[84:87], v[196:199], v[144:147], v[84:87]\n\t" \
  "v_add_u32_e32 v10, 0x10000, v6\n\t" \
  "v_add_u32_e32 v11, 0x10000, v7\n\t" \
  "v_mfma_f32_16x16x32_bf16 v[16:19], v[200:203], v[144:147], v[16:19]\n\t" \
  "v_xor_b32_e32 v12, 32, v11\n\t" \
  "v_mfma_f32_16x16x32_bf16 v[20:23], v[204:207], v[144:147], v[20:23]\n\t" \
  "ds_read_b128 v[144:147], v10 offset:0\n\t" \
  "v_mfma_f32_16x16x32_bf16 v[28:31], v[204:207], v[148:151], v[28:31]\n\t" \
  "ds_read_b64_tr_b16 v[176:177], v11 offset:0\n\t" \
  "v_mfma_f32_16x16x32_bf16 v[24:27], v[200:203], v[148:151], v[24:27]\n\t" \
  "ds_read_b64_tr_b16 v[178:179], v11 offset:4096\n\t" \
  "v_mfma_f32_16x16x32_bf16 v[92:95], v[196:199], v[148:151], v[92:95]\n\t" \
  "s_add_u32 m0, s62, 0x0\n\t" \
  "v_mfma_f32_16x16x32_bf16 v[88:91], v[192:195], v[148:151], v[88:91]\n\t" \
  "ds_read_b128 v[148:151], v10 offset:1024\n\t" \
  "v_mfma_f32_16x16x32_bf16 v[96:99], v[192:195], v[152:155], v[96:99]\n\t" \
  "ds_read_b64_tr_b16 v[180:181], v12 offset:0\n\t" \
  "global_load_lds_dwordx4 v1, s[40:41] offset:0 sc1\n\t" \
  "v_mfma_f32_16x16x32_bf16 v[100:103], v[196:199], v[152:155], v[100:103]\n\t" \
  "ds_read_b64_tr_b16 v[182:183], v12 offset:4096\n\t" \
  "v_mfma_f32_16x16x32_bf16 v[32:35], v[200:203], v[152:155], v[32:35]\n\t" \
  "global_load_lds_dwordx4 v2, s[40:41] offset:1024 sc1\n\t" \
  "v_mfma_f32_16x16x32_bf16 v[36:39], v[204:207], v[152:155], v[36:39]\n\t" \
  "ds_read_b128 v[152:155], v10 offset:4096\n\t" \
  "v_mfma_f32_16x16x32_bf16 v[44:47], v[204:207], v[156:159], v[44:47]\n\t" \
  "ds_read_b64_tr_b16 v[184:185], v11 offset:1024\n\t" \
  "global_load_lds_dwordx4 v3, s[42:43] offset:2048\n\t" \
  "v_mfma_f32_16x16x32_bf16 v[40:43], v[200:203], v[156:159], v[40:43]\n\t" \
  "ds_read_b64_tr_b16 v[186:187], v11 offset:5120\n\t" \
  "v_mfma_f32_16x16x32_bf16 v[108:111], v[196:199], v[156:159], v[108:111]\n\t" \
  "global_load_lds_dwordx4 v[4:5], off offset:3072\n\t" \
  "v_mfma_f32_16x16x32_bf16 v[104:107], v[192:195], v[156:159], v[104:107]\n\t" \
  "ds_read_b128 v[156:159], v10 offset:5120\n\t" \
  "s_waitcnt lgkmcnt(10)\n\t" \
  "v_mfma_f32_16x16x32_bf16 v[112:115], v[192:195], v[160:163], v[112:115]\n\t" \
  "ds_read_b64_tr_b16 v[188:189], v12 offset:1024\n\t" \
  "s_add_u32 s40, s40, s46\n\t" \
  "s_addc_u32 s41, s41, s45\n\t" \
  "v_mfma_f32_16x16x32_bf16 v[116:119], v[196:199], v[160:163], v[116:119]\n\t" \
  "ds_read_b64_tr_b16 v[190:191], v12 offset:5120\n\t" \
  "s_add_u32 s42, s42, s44\n\t" \
  "s_addc_u32 s43, s43, s45\n\t" \
  "v_mfma_f32_16x16x32_bf16 v[48:51], v[200:203], v[160:163], v[48:51]\n\t" \
  "v_lshl_add_u64 v[4:5], v[4:5], 0, s[44:45]\n\t" \
  "v_mfma_f32_16x16x32_bf16 v[52:55], v[204:207], v[160:163], v[52:55]\n\t" \
  "ds_read_b128 v[160:163], v10 offset:8192\n\t" \
  "s_waitcnt vmcnt(8)\n\t" \
  "v_cmp_ne_u64_e32 vcc, s[36:37], v[14:15]\n\t" \
  "s_nop 0\n\t" \
  "s_cbranch_vccnz PSxT_%=\n\t" \
  "PRxT_%=:\n\t" \
  "s_sub_u32 s59, 15, s66\n\t" \
  "s_cmp_lt_u32 %[w], 4\n\t" \
  "s_cselect_b32 s59, s59, s66\n\t" \
  "v_mfma_f32_16x16x32_bf16 v[60:63], v[204:207], v[164:167], v[60:63]\n\t" \
  "s_lshl_b32 s52, s59, 7\n\t" \
  "s_lshl_b32 s53, s63, 5\n\t" \
  "s_add_u32 s52, s52, s53\n\t" \
  "s_lshl_b32 s52, s52, 10\n\t" \
  "v_mfma_f32_16x16x32_bf16 v[56:59], v[200:203], v[164:167], v[56:59]\n\t" \
  "s_mov_b64 s[40:41], %[ws]\n\t" \
  "s_add_u32 s40, s40, s52\n\t" \
  "s_addc_u32 s41, s41, 0\n\t" \
  "v_lshlrev_b32_e32 v1, 4, v0\n\t" \
  "v_add_u32_e32 v2, 0x3c00, v1\n\t" \
  "s_lshl_b32 s52, s65, 8\n\t" \
  "v_mfma_f32_16x16x32_bf16 v[124:127], v[196:199], v[164:167], v[124:127]\n\t" \
  "s_lshl_b32 s53, %[w], 15\n\t" \
  "s_add_u32 s52, s52, s53\n\t" \
  "s_mov_b64 s[42:43], %[wq]\n\t" \
  "s_add_u32 s42, s42, s52\n\t" \
  "s_addc_u32 s43, s43, 0\n\t" \
  "v_mfma_f32_16x16x32_bf16 v[120:123], v[192:195], v[164:167], v[120:123]\n\t" \
  "ds_read_b128 v[164:167], v10 offset:9216\n\t" \
  "s_sub_u32 s42, s42, 2048\n\t" \
  "s_subb_u32 s43, s43, 0\n\t" \
  "s_lshr_b32 s52, s65, 3\n\t" \
  "s_sub_u32 s59, s65, s52\n\t" \
  "s_and_b32 s52, s65, 7\n\t" \
  "s_cmp_eq_u32 s52, 7\n\t" \
  "s_cselect_b32 s59, 28, s59\n\t" \
  "v_mfma_f32_16x16x32_bf16 v[128:131], v[192:195], v[168:171], v[128:131]\n\t" \
  "s_min_u32 s52, s59, 27\n\t" \
  "s_lshl_b32 s52, s52, 7\n\t" \
  "s_mul_i32 s53, %[w], 14336\n\t" \
  "s_add_u32 s52, s52, s53\n\t" \
  "v_mfma_f32_16x16x32_bf16 v[132:135], v[196:199], v[168:171], v[132:135]\n\t" \
  "v_lshrrev_b32_e32 v13, 13, v3\n\t" \
  "v_mul_u32_u24_e32 v13, 3584, v13\n\t" \
  "v_bfe_u32 v14, v3, 6, 2\n\t" \
  "v_lshl_add_u32 v13, v14, 5, v13\n\t" \
  "v_and_b32_e32 v14, 16, v3\n\t" \
  "v_mfma_f32_16x16x32_bf16 v[64:67], v[200:203], v[168:171], v[64:67]\n\t" \
  "v_add3_u32 v13, v13, v14, s52\n\t" \
  "v_and_b32_e32 v14, 32, v3\n\t" \
  "v_cmp_ne_u32_e32 vcc, 0, v14\n\t" \
  "s_mov_b64 s[52:53], %[wg]\n\t" \
  "v_mfma_f32_16x16x32_bf16 v[68:71], v[204:207], v[168:171], v[68:71]\n\t" \
  "ds_read_b128 v[168:171], v10 offset:12288\n\t" \
  "s_sub_u32 s52, s52, 3072\n\t" \
  "s_subb_u32 s53, s53, 0\n\t" \
  "s_mov_b64 s[54:55], %[wu]\n\t" \
  "s_sub_u32 s54, s54, 3072\n\t" \
  "s_subb_u32 s55, s55, 0\n\t" \
  "v_mov_b32_e32 v4, s52\n\t" \
  "v_mov_b32_e32 v5, s53\n\t" \
  "v_mfma_f32_16x16x32_bf16 v[76:79], v[204:207], v[172:175], v[76:79]\n\t" \
  "v_mov_b32_e32 v14, s54\n\t" \
  "v_mov_b32_e32 v15, s55\n\t" \
  "v_cndmask_b32_e32 v4, v4, v14, vcc\n\t" \
  "v_cndmask_b32_e32 v5, v5, v15, vcc\n\t" \
  "v_mov_b32_e32 v14, v13\n\t" \
  "v_mov_b32_e32 v15, 0\n\t" \
  "v_mfma_f32_16x16x32_bf16 v[72:75], v[200:203], v[172:175], v[72:75]\n\t" \
  "v_lshl_add_u64 v[4:5], v[4:5], 0, v[14:15]\n\t" \
  "s_mov_b32 s44, 114688\n\t" \
  "s_mov_b32 s45, 0\n\t" \
  "s_cmp_lt_u32 s59, 28\n\t" \
  "s_cbranch_scc1 NZT_%=\n\t" \
  "s_lshl_b32 s52, %[bid], 3\n\t" \
  "s_add_u32 s52, s52, %[w]\n\t" \
  "s_lshl_b32 s52, s52, 7\n\t" \
  "s_add_u32 s52, s52, 0x6f00000\n\t" \
  "s_mov_b64 s[54:55], %[ws]\n\t" \
  "s_add_u32 s54, s54, s52\n\t" \
  "s_addc_u32 s55, s55, 0\n\t" \
  "s_sub_u32 s54, s54, 3072\n\t" \
  "s_subb_u32 s55, s55, 0\n\t" \
  "v_mov_b32_e32 v4, s54\n\t" \
  "v_mov_b32_e32 v5, s55\n\t" \
  "s_mov_b32 s44, 0\n\t" \
  "NZT_%=:\n\t" \
  "v_mfma_f32_16x16x32_bf16 v[140:143], v[196:199], v[172:175], v[140:143]\n\t" \
  "v_mfma_f32_16x16x32_bf16 v[136:139], v[192:195], v[172:175], v[136:139]\n\t" \
  "ds_read_b128 v[172:175], v10 offset:13312\n\t" \
  "s_lshr_b32 s53, %[bid], 6\n\t" \
  "s_cmp_eq_u32 s53, 3\n\t" \
  "s_cbranch_scc1 HT3T_%=\n\t" \
  "HR3T_%=:\n\t" \
  "s_waitcnt lgkmcnt(4)\n\t" \
  "v_mfma_f32_16x16x32_bf16 v[80:83], v[176:179], v[144:147], v[80:83]\n\t" \
  "s_waitcnt vmcnt(8)\n\t" \
  "s_barrier\n\t" \
  "v_mfma_f32_16x16x32_bf16 v[84:87], v[180:183], v[144:147], v[84:87]\n\t" \
  "v_add_u32_e32 v10, 0x18000, v6\n\t" \
  "v_add_u32_e32 v11, 0x18000, v7\n\t" \
  "v_mfma_f32_16x16x32_bf16 v[16:19], v[184:187], v[144:147], v[16:19]\n\t" \
  "v_xor_b32_e32 v12, 32, v11\n\t" \
  "v_mfma_f32_16x16x32_bf16 v[20:23], v[188:191], v[144:147], v[20:23]\n\t" \
  "ds_read_b128 v[144:147], v10 offset:0\n\t" \
  "v_mfma_f32_16x16x32_bf16 v[28:31], v[188:191], v[148:151], v[28:31]\n\t" \
  "ds_read_b64_tr_b16 v[192:193], v11 offset:0\n\t" \
  "v_mfma_f32_16x16x32_bf16 v[24:27], v[184:187], v[148:151], v[24:27]\n\t" \
  "ds_read_b64_tr_b16 v[194:195], v11 offset:4096\n\t" \
  "v_mfma_f32_16x16x32_bf16 v[92:95], v[180:183], v[148:151], v[92:95]\n\t" \
  "s_add_u32 m0, s62, 0x8000\n\t" \
  "v_mfma_f32_16x16x32_bf16 v[88:91], v[176:179], v[148:151], v[88:91]\n\t" \
  "ds_read_b128 v[148:151], v10 offset:1024\n\t" \
  "v_mfma_f32_16x16x32_bf16 v[96:99], v[176:179], v[152:155], v[96:99]\n\t" \
  "ds_read_b64_tr_b16 v[196:197], v12 offset:0\n\t" \
  "global_load_lds_dwordx4 v1, s[40:41] offset:0 sc1\n\t" \
  "v_mfma_f32_16x16x32_bf16 v[100:103], v[180:183], v[152:155], v[100:103]\n\t" \
  "ds_read_b64_tr_b16 v[198:199], v12 offset:4096\n\t" \
  "v_mfma_f32_16x16x32_bf16 v[32:35], v[184:187], v[152:155], v[32:35]\n\t" \
  "global_load_lds_dwordx4 v2, s[40:41] offset:1024 sc1\n\t" \
  "v_mfma_f32_16x16x32_bf16 v[36:39], v[188:191], v[152:155], v[36:39]\n\t" \
  "ds_read_b128 v[152:155], v10 offset:4096\n\t" \
  "v_mfma_f32_16x16x32_bf16 v[44:47], v[188:191], v[156:159], v[44:47]\n\t" \
  "ds_read_b64_tr_b16 v[200:201], v11 offset:1024\n\t" \
  "global_load_lds_dwordx4 v3, s[42:43] offset:2048\n\t" \
  "v_mfma_f32_16x16x32_bf16 v[40:43], v[184:187], v[156:159], v[40:43]\n\t" \
  "ds_read_b64_tr_b16 v[202:203], v11 offset:5120\n\t" \
  "v_mfma_f32_16x16x32_bf16 v[108:111], v[180:183], v[156:159], v[108:111]\n\t" \
  "global_load_lds_dwordx4 v[4:5], off offset:3072\n\t" \
  "v_mfma_f32_16x16x32_bf16 v[104:107], v[176:179], v[156:159], v[104:107]\n\t" \
  "ds_read_b128 v[156:159], v10 offset:5120\n\t" \
  "s_waitcnt lgkmcnt(10)\n\t" \
  "v_mfma_f32_16x16x32_bf16 v[112:115], v[176:179], v[160:163], v[112:115]\n\t" \
  "ds_read_b64_tr_b16 v[204:205], v12 offset:1024\n\t" \
  "s_add_u32 s40, s40, 1024\n\t" \
  "s_addc_u32 s41, s41, 0\n\t" \
  "v_mfma_f32_16x16x32_bf16 v[116:119], v[180:183], v[160:163], v[116:119]\n\t" \
  "ds_read_b64_tr_b16 v[206:207], v12 offset:5120\n\t" \
  "s_add_u32 s42, s42, 0x40000\n\t" \
  "s_addc_u32 s43, s43, 0\n\t" \
  "v_mfma_f32_16x16x32_bf16 v[48:51], v[184:187], v[160:163], v[48:51]\n\t" \
  "v_lshl_add_u64 v[4:5], v[4:5], 0, s[44:45]\n\t" \
  "v_mfma_f32_16x16x32_bf16 v[52:55], v[188:191], v[160:163], v[52:55]\n\t" \
  "ds_read_b128 v[160:163], v10 offset:8192\n\t" \
  "v_mfma_f32_16x16x32_bf16 v[60:63], v[188:191], v[164:167], v[60:63]\n\t" \
  "v_mfma_f32_16x16x32_bf16 v[56:59], v[184:187], v[164:167], v[56:59]\n\t" \
  "v_mfma_f32_16x16x32_bf16 v[124:127], v[180:183], v[164:167], v[124:127]\n\t" \
  "v_mfma_f32_16x16x32_bf16 v[120:123], v[176:179], v[164:167], v[120:123]\n\t" \
  "ds_read_b128 v[164:167], v10 offset:9216\n\t" \
  "v_mfma_f32_16x16x32_bf16 v[128:131], v[176:179], v[168:171], v[128:131]\n\t" \
  "v_mfma_f32_16x16x32_bf16 v[132:135], v[180:183], v[168:171], v[132:135]\n\t" \
  "v_mfma_f32_16x16x32_bf16 v[64:67], v[184:187], v[168:171], v[64:67]\n\t" \
  "v_mfma_f32_16x16x32_bf16 v[68:71], v[188:191], v[168:171], v[68:71]\n\t" \
  "ds_read_b128 v[168:171], v10 offset:12288\n\t" \
  "v_mfma_f32_16x16x32_bf16 v[76:79], v[188:191], v[172:175], v[76:79]\n\t" \
  "v_mfma_f32_16x16x32_bf16 v[72:75], v[184:187], v[172:175], v[72:75]\n\t" \
  "v_mfma_f32_16x16x32_bf16 v[140:143], v[180:183], v[172:175], v[140:143]\n\t" \
  "v_mfma_f32_16x16x32_bf16 v[136:139], v[176:179], v[172:175], v[136:139]\n\t" \
  "ds_read_b128 v[172:175], v10 offset:13312\n\t" \
  "s_waitcnt lgkmcnt(4)\n\t" \
  "v_mfma_f32_16x16x32_bf16 v[80:83], v[192:195], v[144:147], v[80:83]\n\t" \
  "s_waitcnt vmcnt(8)\n\t" \
  "s_barrier\n\t" \
  "v_mfma_f32_16x16x32_bf16 v[84:87], v[196:199], v[144:147], v[84:87]\n\t" \
  "v_add_u32_e32 v10, 0x20000, v6\n\t" \
  "v_add_u32_e32 v11, 0x20000, v7\n\t" \
  "v_mfma_f32_16x16x32_bf16 v[16:19], v[200:203], v[144:147], v[16:19]\n\t" \
  "v_xor_b32_e32 v12, 32, v11\n\t" \
  "v_mfma_f32_16x16x32_bf16 v[20:23], v[204:207], v[144:147], v[20:23]\n\t" \
  "ds_read_b128 v[144:147], v10 offset:0\n\t" \
  "v_mfma_f32_16x16x32_bf16 v[28:31], v[204:207], v[148:151], v[28:31]\n\t" \
  "ds_read_b64_tr_b16 v[176:177], v11 offset:0\n\t" \
  "v_mfma_f32_16x16x32_bf16 v[24:27], v[200:203], v[148:151], v[24:27]\n\t" \
  "ds_read_b64_tr_b16 v[178:179], v11 offset:4096\n\t" \
  "v_mfma_f32_16x16x32_bf16 v[92:95], v[196:199], v[148:151], v[92:95]\n\t" \
  "s_add_u32 m0, s62, 0x10000\n\t" \
  "v_mfma_f32_16x16x32_bf16 v[88:91], v[192:195], v[148:151], v[88:91]\n\t" \
  "ds_read_b128 v[148:151], v10 offset:1024\n\t" \
  "v_mfma_f32_16x16x32_bf16 v[96:99], v[192:195], v[152:155], v[96:99]\n\t" \
  "ds_read_b64_tr_b16 v[180:181], v12 offset:0\n\t" \
  "global_load_lds_dwordx4 v1, s[40:41] offset:0 sc1\n\t" \
  "v_mfma_f32_16x16x32_bf16 v[100:103], v[196:199], v[152:155], v[100:103]\n\t" \
  "ds_read_b64_tr_b16 v[182:183], v12 offset:4096\n\t" \
  "v_mfma_f32_16x16x32_bf16 v[32:35], v[200:203], v[152:155], v[32:35]\n\t" \
  "global_load_lds_dwordx4 v2, s[40:41] offset:1024 sc1\n\t" \
  "v_mfma_f32_16x16x32_bf16 v[36:39], v[204:207], v[152:155], v[36:39]\n\t" \
  "ds_read_b128 v[152:155], v10 offset:4096\n\t" \
  "v_mfma_f32_16x16x32_bf16 v[44:47], v[204:207], v[156:159], v[44:47]\n\t" \
  "ds_read_b64_tr_b16 v[184:185], v11 offset:1024\n\t" \
  "global_load_lds_dwordx4 v3, s[42:43] offset:2048\n\t" \
  "v_mfma_f32_16x16x32_bf16 v[40:43], v[200:203], v[156:159], v[40:43]\n\t" \
  "ds_read_b64_tr_b16 v[186:187], v11 offset:5120\n\t" \
  "v_mfma_f32_16x16x32_bf16 v[108:111], v[196:199], v[156:159], v[108:111]\n\t" \
  "global_load_lds_dwordx4 v[4:5], off offset:3072\n\t" \
  "v_mfma_f32_16x16x32_bf16 v[104:107], v[192:195], v[156:159], v[104:107]\n\t" \
  "ds_read_b128 v[156:159], v10 offset:5120\n\t" \
  "s_waitcnt lgkmcnt(10)\n\t" \
  "v_mfma_f32_16x16x32_bf16 v[112:115], v[192:195], v[160:163], v[112:115]\n\t" \
  "ds_read_b64_tr_b16 v[188:189], v12 offset:1024\n\t" \
  "s_add_u32 s40, s40, 1024\n\t" \
  "s_addc_u32 s41, s41, 0\n\t" \
  "v_mfma_f32_16x16x32_bf16 v[116:119], v[196:199], v[160:163], v[116:119]\n\t" \
  "ds_read_b64_tr_b16 v[190:191], v12 offset:5120\n\t" \
  "s_add_u32 s42, s42, 0x40000\n\t" \
  "s_addc_u32 s43, s43, 0\n\t" \
  "v_mfma_f32_16x16x32_bf16 v[48:51], v[200:203], v[160:163], v[48:51]\n\t" \
  "v_lshl_add_u64 v[4:5], v[4:5], 0, s[44:45]\n\t" \
  "v_mfma_f32_16x16x32_bf16 v[52:55], v[204:207], v[160:163], v[52:55]\n\t" \
  "ds_read_b128 v[160:163], v10 offset:8192\n\t" \
  "v_mfma_f32_16x16x32_bf16 v[60:63], v[204:207], v[164:167], v[60:63]\n\t" \
  "v_mfma_f32_16x16x32_bf16 v[56:59], v[200:203], v[164:167], v[56:59]\n\t" \
  "v_mfma_f32_16x16x32_bf16 v[124:127], v[196:199], v[164:167], v[124:127]\n\t" \
  "v_mfma_f32_16x16x32_bf16 v[120:123], v[192:195], v[164:167], v[120:123]\n\t" \
  "ds_read_b128 v[164:167], v10 offset:9216\n\t" \
  "v_mfma_f32_16x16x32_bf16 v[128:131], v[192:195], v[168:171], v[128:131]\n\t" \
  "v_mfma_f32_16x16x32_bf16 v[132:135], v[196:199], v[168:171], v[132:135]\n\t" \
  "v_mfma_f32_16x16x32_bf16 v[64:67], v[200:203], v[168:171], v[64:67]\n\t" \
  "v_mfma_f32_16x16x32_bf16 v[68:71], v[204:207], v[168:171], v[68:71]\n\t" \
  "ds_read_b128 v[168:171], v10 offset:12288\n\t" \
  "v_mfma_f32_16x16x32_bf16 v[76:79], v[204:207], v[172:175], v[76:79]\n\t" \
  "v_mfma_f32_16x16x32_bf16 v[72:75], v[200:203], v[172:175], v[72:75]\n\t" \
  "v_mfma_f32_16x16x32_bf16 v[140:143], v[196:199], v[172:175], v[140:143]\n\t" \
  "v_mfma_f32_16x16x32_bf16 v[136:139], v[192:195], v[172:175], v[136:139]\n\t" \
  "ds_read_b128 v[172:175], v10 offset:13312\n\t" \
  "s_waitcnt lgkmcnt(4)\n\t" \
  "v_mfma_f32_16x16x32_bf16 v[80:83], v[176:179], v[144:147], v[80:83]\n\t" \
  "s_waitcnt vmcnt(8)\n\t" \
  "s_barrier\n\t" \
  "v_mfma_f32_16x16x32_bf16 v[84:87], v[180:183], v[144:147], v[84:87]\n\t" \
  "v_add_u32_e32 v10, 0x0, v6\n\t" \
  "v_add_u32_e32 v11, 0x0, v7\n\t" \
  "v_mfma_f32_16x16x32_bf16 v[16:19], v[184:187], v[144:147], v[16:19]\n\t" \
  "v_xor_b32_e32 v12, 32, v11\n\t" \
  "v_mfma_f32_16x16x32_bf16 v[20:23], v[188:191], v[144:147], v[20:23]\n\t" \
  "ds_read_b128 v[144:147], v10 offset:0\n\t" \
  "v_mfma_f32_16x16x32_bf16 v[28:31], v[188:191], v[148:151], v[28:31]\n\t" \
  "ds_read_b64_tr_b16 v[192:193], v11 offset:0\n\t" \
  "v_mfma_f32_16x16x32_bf16 v[24:27], v[184:187], v[148:151], v[24:27]\n\t" \
  "ds_read_b64_tr_b16 v[194:195], v11 offset:4096\n\t" \
  "v_mfma_f32_16x16x32_bf16 v[92:95], v[180:183], v[148:151], v[92:95]\n\t" \
  "s_add_u32 m0, s62, 0x18000\n\t" \
  "v_mfma_f32_16x16x32_bf16 v[88:91], v[176:179], v[148:151], v[88:91]\n\t" \
  "ds_read_b128 v[148:151], v10 offset:1024\n\t" \
  "v_mfma_f32_16x16x32_bf16 v[96:99], v[176:179], v[152:155], v[96:99]\n\t" \
  "ds_read_b64_tr_b16 v[196:197], v12 offset:0\n\t" \
  "global_load_lds_dwordx4 v1, s[40:41] offset:0 sc1\n\t" \
  "v_mfma_f32_16x16x32_bf16 v[100:103], v[180:183], v[152:155], v[100:103]\n\t" \
  "ds_read_b64_tr_b16 v[198:199], v12 offset:4096\n\t" \
  "v_mfma_f32_16x16x32_bf16 v[32:35], v[184:187], v[152:155], v[32:35]\n\t" \
  "global_load_lds_dwordx4 v2, s[40:41] offset:1024 sc1\n\t" \
  "v_mfma_f32_16x16x32_bf16 v[36:39], v[188:191], v[152:155], v[36:39]\n\t" \
  "ds_read_b128 v[152:155], v10 offset:4096\n\t" \
  "v_mfma_f32_16x16x32_bf16 v[44:47], v[188:191], v[156:159], v[44:47]\n\t" \
  "ds_read_b64_tr_b16 v[200:201], v11 offset:1024\n\t" \
  "global_load_lds_dwordx4 v3, s[42:43] offset:2048\n\t" \
  "v_mfma_f32_16x16x32_bf16 v[40:43], v[184:187], v[156:159], v[40:43]\n\t" \
  "ds_read_b64_tr_b16 v[202:203], v11 offset:5120\n\t" \
  "v_mfma_f32_16x16x32_bf16 v[108:111], v[180:183], v[156:159], v[108:111]\n\t" \
  "global_load_lds_dwordx4 v[4:5], off offset:3072\n\t" \
  "v_mfma_f32_16x16x32_bf16 v[104:107], v[176:179], v[156:159], v[104:107]\n\t" \
  "ds_read_b128 v[156:159], v10 offset:5120\n\t" \
  "s_waitcnt lgkmcnt(10)\n\t" \
  "v_mfma_f32_16x16x32_bf16 v[112:115], v[176:179], v[160:163], v[112:115]\n\t" \
  "ds_read_b64_tr_b16 v[204:205], v12 offset:1024\n\t" \
  "s_add_u32 s40, s40, 1024\n\t" \
  "s_addc_u32 s41, s41, 0\n\t" \
  "v_mfma_f32_16x16x32_bf16 v[116:119], v[180:183], v[160:163], v[116:119]\n\t" \
  "ds_read_b64_tr_b16 v[206:207], v12 offset:5120\n\t" \
  "s_add_u32 s42, s42, 0x40000\n\t" \
  "s_addc_u32 s43, s43, 0\n\t" \
  "v_mfma_f32_16x16x32_bf16 v[48:51], v[184:187], v[160:163], v[48:51]\n\t" \
  "v_lshl_add_u64 v[4:5], v[4:5], 0, s[44:45]\n\t" \
  "v_mfma_f32_16x16x32_bf16 v[52:55], v[188:191], v[160:163], v[52:55]\n\t" \
  "ds_read_b128 v[160:163], v10 offset:8192\n\t" \
  "v_mfma_f32_16x16x32_bf16 v[60:63], v[188:191], v[164:167], v[60:63]\n\t" \
  "v_mfma_f32_16x16x32_bf16 v[56:59], v[184:187], v[164:167], v[56:59]\n\t" \
  "v_mfma_f32_16x16x32_bf16 v[124:127], v[180:183], v[164:167], v[124:127]\n\t" \
  "v_mfma_f32_16x16x32_bf16 v[120:123], v[176:179], v[164:167], v[120:123]\n\t" \
  "ds_read_b128 v[164:167], v10 offset:9216\n\t" \
  "v_mfma_f32_16x16x32_bf16 v[128:131], v[176:179], v[168:171], v[128:131]\n\t" \
  "v_mfma_f32_16x16x32_bf16 v[132:135], v[180:183], v[168:171], v[132:135]\n\t" \
  "v_mfma_f32_16x16x32_bf16 v[64:67], v[184:187], v[168:171], v[64:67]\n\t" \
  "v_mfma_f32_16x16x32_bf16 v[68:71], v[188:191], v[168:171], v[68:71]\n\t" \
  "ds_read_b128 v[168:171], v10 offset:12288\n\t" \
  "v_mfma_f32_16x16x32_bf16 v[76:79], v[188:191], v[172:175], v[76:79]\n\t" \
  "v_mfma_f32_16x16x32_bf16 v[72:75], v[184:187], v[172:175], v[72:75]\n\t" \
  "v_mfma_f32_16x16x32_bf16 v[140:143], v[180:183], v[172:175], v[140:143]\n\t" \
  "v_mfma_f32_16x16x32_bf16 v[136:139], v[176:179], v[172:175], v[136:139]\n\t" \
  "ds_read_b128 v[172:175], v10 offset:13312\n\t" \
  "s_waitcnt lgkmcnt(4)\n\t" \
  "v_mfma_f32_16x16x32_bf16 v[80:83], v[192:195], v[144:147], v[80:83]\n\t" \
  "s_waitcnt vmcnt(8)\n\t" \
  "s_barrier\n\t" \
  "v_mfma_f32_16x16x32_bf16 v[84:87], v[196:199], v[144:147], v[84:87]\n\t" \
  "v_add_u32_e32 v10, 0x8000, v6\n\t" \
  "v_add_u32_e32 v11, 0x8000, v7\n\t" \
  "v_mfma_f32_16x16x32_bf16 v[16:19], v[200:203], v[144:147], v[16:19]\n\t" \
  "v_xor_b32_e32 v12, 32, v11\n\t" \
  "v_mfma_f32_16x16x32_bf16 v[20:23], v[204:207], v[144:147], v[20:23]\n\t" \
  "ds_read_b128 v[144:147], v10 offset:0\n\t" \
  "v_mfma_f32_16x16x32_bf16 v[28:31], v[204:207], v[148:151], v[28:31]\n\t" \
  "ds_read_b64_tr_b16 v[176:177], v11 offset:0\n\t" \
  "v_mfma_f32_16x16x32_bf16 v[24:27], v[200:203], v[148:151], v[24:27]\n\t" \
  "ds_read_b64_tr_b16 v[178:179], v11 offset:4096\n\t" \
  "v_mfma_f32_16x16x32_bf16 v[92:95], v[196:199], v[148:151], v[92:95]\n\t" \
  "s_add_u32 m0, s62, 0x20000\n\t" \
  "v_mfma_f32_16x16x32_bf16 v[88:91], v[192:195], v[148:151], v[88:91]\n\t" \
  "ds_read_b128 v[148:151], v10 offset:1024\n\t" \
  "v_mfma_f32_16x16x32_bf16 v[96:99], v[192:195], v[152:155], v[96:99]\n\t" \
  "ds_read_b64_tr_b16 v[180:181], v12 offset:0\n\t" \
  "global_load_lds_dwordx4 v1, s[40:41] offset:0 sc1\n\t" \
  "v_mfma_f32_16x16x32_bf16 v[100:103], v[196:199], v[152:155], v[100:103]\n\t" \
  "ds_read_b64_tr_b16 v[182:183], v12 offset:4096\n\t" \
  "v_mfma_f32_16x16x32_bf16 v[32:35], v[200:203], v[152:155], v[32:35]\n\t" \
  "global_load_lds_dwordx4 v2, s[40:41] offset:1024 sc1\n\t" \
  "v_mfma_f32_16x16x32_bf16 v[36:39], v[204:207], v[152:155], v[36:39]\n\t" \
  "ds_read_b128 v[152:155], v10 offset:4096\n\t" \
  "v_mfma_f32_16x16x32_bf16 v[44:47], v[204:207], v[156:159], v[44:47]\n\t" \
  "ds_read_b64_tr_b16 v[184:185], v11 offset:1024\n\t" \
  "global_load_lds_dwordx4 v3, s[42:43] offset:2048\n\t" \
  "v_mfma_f32_16x16x32_bf16 v[40:43], v[200:203], v[156:159], v[40:43]\n\t" \
  "ds_read_b64_tr_b16 v[186:187], v11 offset:5120\n\t" \
  "v_mfma_f32_16x16x32_bf16 v[108:111], v[196:199], v[156:159], v[108:111]\n\t" \
  "global_load_lds_dwordx4 v[4:5], off offset:3072\n\t" \
  "v_mfma_f32_16x16x32_bf16 v[104:107], v[192:195], v[156:159], v[104:107]\n\t" \
  "ds_read_b128 v[156:159], v10 offset:5120\n\t" \
  "s_waitcnt lgkmcnt(10)\n\t" \
  "v_mfma_f32_16x16x32_bf16 v[112:115], v[192:195], v[160:163], v[112:115]\n\t" \
  "ds_read_b64_tr_b16 v[188:189], v12 offset:1024\n\t" \
  "s_add_u32 s40, s40, 1024\n\t" \
  "s_addc_u32 s41, s41, 0\n\t" \
  "v_mfma_f32_16x16x32_bf16 v[116:119], v[196:199], v[160:163], v[116:119]\n\t" \
  "ds_read_b64_tr_b16 v[190:191], v12 offset:5120\n\t" \
  "s_add_u32 s42, s42, 0x40000\n\t" \
  "s_addc_u32 s43, s43, 0\n\t" \
  "v_mfma_f32_16x16x32_bf16 v[48:51], v[200:203], v[160:163], v[48:51]\n\t" \
  "v_lshl_add_u64 v[4:5], v[4:5], 0, s[44:45]\n\t" \
  "v_mfma_f32_16x16x32_bf16 v[52:55], v[204:207], v[160:163], v[52:55]\n\t" \
  "ds_read_b128 v[160:163], v10 offset:8192\n\t" \
  "v_mfma_f32_16x16x32_bf16 v[60:63], v[204:207], v[164:167], v[60:63]\n\t" \
  "v_mfma_f32_16x16x32_bf16 v[56:59], v[200:203], v[164:167], v[56:59]\n\t" \
  "v_mfma_f32_16x16x32_bf16 v[124:127], v[196:199], v[164:167], v[124:127]\n\t" \
  "v_mfma_f32_16x16x32_bf16 v[120:123], v[192:195], v[164:167], v[120:123]\n\t" \
  "ds_read_b128 v[164:167], v10 offset:9216\n\t" \
  "v_mfma_f32_16x16x32_bf16 v[128:131], v[192:195], v[168:171], v[128:131]\n\t" \
  "v_mfma_f32_16x16x32_bf16 v[132:135], v[196:199], v[168:171], v[132:135]\n\t" \
  "v_mfma_f32_16x16x32_bf16 v[64:67], v[200:203], v[168:171], v[64:67]\n\t" \
  "v_mfma_f32_16x16x32_bf16 v[68:71], v[204:207], v[168:171], v[68:71]\n\t" \
  "ds_read_b128 v[168:171], v10 offset:12288\n\t" \
  "v_mfma_f32_16x16x32_bf16 v[76:79], v[204:207], v[172:175], v[76:79]\n\t" \
  "v_mfma_f32_16x16x32_bf16 v[72:75], v[200:203], v[172:175], v[72:75]\n\t" \
  "v_mfma_f32_16x16x32_bf16 v[140:143], v[196:199], v[172:175], v[140:143]\n\t" \
  "v_mfma_f32_16x16x32_bf16 v[136:139], v[192:195], v[172:175], v[136:139]\n\t" \
  "ds_read_b128 v[172:175], v10 offset:13312\n\t" \
  "s_waitcnt lgkmcnt(4)\n\t" \
  "v_cvt_pk_bf16_f32 v208, v80, v81\n\t" \
  "v_cvt_pk_bf16_f32 v209, v82, v83\n\t" \
  "v_cvt_pk_bf16_f32 v210, v84, v85\n\t" \
  "v_cvt_pk_bf16_f32 v211, v86, v87\n\t" \
  "v_cvt_pk_bf16_f32 v212, v16, v17\n\t" \
  "v_cvt_pk_bf16_f32 v213, v18, v19\n\t" \
  "v_cvt_pk_bf16_f32 v214, v20, v21\n\t" \
  "v_cvt_pk_bf16_f32 v215, v22, v23\n\t" \
  "v_mfma_f32_16x16x32_bf16 v[80:83], v[176:179], v[144:147], 0\n\t" \
  "s_waitcnt vmcnt(8)\n\t" \
  "s_barrier\n\t" \
  "v_mfma_f32_16x16x32_bf16 v[84:87], v[180:183], v[144:147], 0\n\t" \
  "v_add_u32_e32 v10, 0x10000, v6\n\t" \
  "v_add_u32_e32 v11, 0x10000, v7\n\t" \
  "v_mfma_f32_16x16x32_bf16 v[16:19], v[184:187], v[144:147], 0\n\t" \
  "v_xor_b32_e32 v12, 32, v11\n\t" \
  "v_mfma_f32_16x16x32_bf16 v[20:23], v[188:191], v[144:147], 0\n\t" \
  "ds_read_b128 v[144:147], v10 offset:0\n\t" \
  "v_cvt_pk_bf16_f32 v216, v88, v89\n\t" \
  "v_cvt_pk_bf16_f32 v217, v90, v91\n\t" \
  "v_cvt_pk_bf16_f32 v218, v92, v93\n\t" \
  "v_cvt_pk_bf16_f32 v219, v94, v95\n\t" \
  "v_cvt_pk_bf16_f32 v220, v24, v25\n\t" \
  "v_cvt_pk_bf16_f32 v221, v26, v27\n\t" \
  "v_cvt_pk_bf16_f32 v222, v28, v29\n\t" \
  "v_cvt_pk_bf16_f32 v223, v30, v31\n\t" \
  "v_mfma_f32_16x16x32_bf16 v[28:31], v[188:191], v[148:151], 0\n\t" \
  "ds_read_b64_tr_b16 v[192:193], v11 offset:0\n\t" \
  "v_mfma_f32_16x16x32_bf16 v[24:27], v[184:187], v[148:151], 0\n\t" \
  "ds_read_b64_tr_b16 v[194:195], v11 offset:4096\n\t" \
  "v_mfma_f32_16x16x32_bf16 v[92:95], v[180:183], v[148:151], 0\n\t" \
  "s_add_u32 m0, s62, 0x0\n\t" \
  "v_mfma_f32_16x16x32_bf16 v[88:91], v[176:179], v[148:151], 0\n\t" \
  "ds_read_b128 v[148:151], v10 offset:1024\n\t" \
  "v_add_u32_e32 v13, 0x0, v8\n\t" \
  "global_store_dwordx4 v13, v[208:211], s[50:51] sc1\n\t" \
  "v_cvt_pk_bf16_f32 v224, v96, v97\n\t" \
  "v_cvt_pk_bf16_f32 v225, v98, v99\n\t" \
  "v_cvt_pk_bf16_f32 v226, v100, v101\n\t" \
  "v_cvt_pk_bf16_f32 v227, v102, v103\n\t" \
  "v_cvt_pk_bf16_f32 v228, v32, v33\n\t" \
  "v_cvt_pk_bf16_f32 v229, v34, v35\n\t" \
  "v_cvt_pk_bf16_f32 v230, v36, v37\n\t" \
  "v_cvt_pk_bf16_f32 v231, v38, v39\n\t" \
  "v_mfma_f32_16x16x32_bf16 v[96:99], v[176:179], v[152:155], 0\n\t" \
  "ds_read_b64_tr_b16 v[196:197], v12 offset:0\n\t" \
  "global_load_lds_dwordx4 v1, s[40:41] offset:0 sc1\n\t" \
  "v_mfma_f32_16x16x32_bf16 v[100:103], v[180:183], v[152:155], 0\n\t" \
  "ds_read_b64_tr_b16 v[198:199], v12 offset:4096\n\t" \
  "v_mfma_f32_16x16x32_bf16 v[32:35], v[184:187], v[152:155], 0\n\t" \
  "global_load_lds_dwordx4 v2, s[40:41] offset:1024 sc1\n\t" \
  "v_mfma_f32_16x16x32_bf16 v[36:39], v[188:191], v[152:155], 0\n\t" \
  "ds_read_b128 v[152:155], v10 offset:4096\n\t" \
  "v_add_u32_e32 v13, 0x110000, v8\n\t" \
  "global_store_dwordx4 v13, v[212:215], s[50:51] sc1\n\t" \
  "v_cvt_pk_bf16_f32 v232, v104, v105\n\t" \
  "v_cvt_pk_bf16_f32 v233, v106, v107\n\t" \
  "v_cvt_pk_bf16_f32 v234, v108, v109\n\t" \
  "v_cvt_pk_bf16_f32 v235, v110, v111\n\t" \
  "v_cvt_pk_bf16_f32 v236, v40, v41\n\t" \
  "v_cvt_pk_bf16_f32 v237, v42, v43\n\t" \
  "v_cvt_pk_bf16_f32 v238, v44, v45\n\t" \
  "v_cvt_pk_bf16_f32 v239, v46, v47\n\t" \
  "v_mfma_f32_16x16x32_bf16 v[44:47], v[188:191], v[156:159], 0\n\t" \
  "ds_read_b64_tr_b16 v[200:201], v11 offset:1024\n\t" \
  "global_load_lds_dwordx4 v3, s[42:43] offset:2048\n\t" \
  "v_mfma_f32_16x16x32_bf16 v[40:43], v[184:187], v[156:159], 0\n\t" \
  "ds_read_b64_tr_b16 v[202:203], v11 offset:5120\n\t" \
  "v_mfma_f32_16x16x32_bf16 v[108:111], v[180:183], v[156:159], 0\n\t" \
  "global_load_lds_dwordx4 v[4:5], off offset:3072\n\t" \
  "v_mfma_f32_16x16x32_bf16 v[104:107], v[176:179], v[156:159], 0\n\t" \
  "ds_read_b128 v[156:159], v10 offset:5120\n\t" \
  "s_waitcnt lgkmcnt(10)\n\t" \
  "v_cvt_pk_bf16_f32 v240, v112, v113\n\t" \
  "v_cvt_pk_bf16_f32 v241, v114, v115\n\t" \
  "v_cvt_pk_bf16_f32 v242, v116, v117\n\t" \
  "v_cvt_pk_bf16_f32 v243, v118, v119\n\t" \
  "v_cvt_pk_bf16_f32 v244, v48, v49\n\t" \
  "v_cvt_pk_bf16_f32 v245, v50, v51\n\t" \
  "v_cvt_pk_bf16_f32 v246, v52, v53\n\t" \
  "v_cvt_pk_bf16_f32 v247, v54, v55\n\t" \
  "v_mfma_f32_16x16x32_bf16 v[112:115], v[176:179], v[160:163], 0\n\t" \
  "ds_read_b64_tr_b16 v[204:205], v12 offset:1024\n\t" \
  "s_add_u32 s40, s40, 1024\n\t" \
  "s_addc_u32 s41, s41, 0\n\t" \
  "v_mfma_f32_16x16x32_bf16 v[116:119], v[180:183], v[160:163], 0\n\t" \
  "ds_read_b64_tr_b16 v[206:207], v12 offset:5120\n\t" \
  "s_add_u32 s42, s42, 0x40000\n\t" \
  "s_addc_u32 s43, s43, 0\n\t" \
  "v_mfma_f32_16x16x32_bf16 v[48:51], v[184:187], v[160:163], 0\n\t" \
  "v_lshl_add_u64 v[4:5], v[4:5], 0, s[44:45]\n\t" \
  "v_mfma_f32_16x16x32_bf16 v[52:55], v[188:191], v[160:163], 0\n\t" \
  "ds_read_b128 v[160:163], v10 offset:8192\n\t" \
  "v_add_u32_e32 v13, 0x0, v9\n\t" \
  "global_store_dwordx4 v13, v[216:219], s[50:51] sc1\n\t" \
  "v_cvt_pk_bf16_f32 v248, v120, v121\n\t" \
  "v_cvt_pk_bf16_f32 v249, v122, v123\n\t" \
  "v_cvt_pk_bf16_f32 v250, v124, v125\n\t" \
  "v_cvt_pk_bf16_f32 v251, v126, v127\n\t" \
  "v_cvt_pk_bf16_f32 v252, v56, v57\n\t" \
  "v_cvt_pk_bf16_f32 v253, v58, v59\n\t" \
  "v_cvt_pk_bf16_f32 v254, v60, v61\n\t" \
  "v_cvt_pk_bf16_f32 v255, v62, v63\n\t" \
  "v_mfma_f32_16x16x32_bf16 v[60:63], v[188:191], v[164:167], 0\n\t" \
  "v_mfma_f32_16x16x32_bf16 v[56:59], v[184:187], v[164:167], 0\n\t" \
  "v_add_u32_e32 v13, 0x110000, v9\n\t" \
  "global_store_dwordx4 v13, v[220:223], s[50:51] sc1\n\t" \
  "v_mfma_f32_16x16x32_bf16 v[124:127], v[180:183], v[164:167], 0\n\t" \
  "v_mfma_f32_16x16x32_bf16 v[120:123], v[176:179], v[164:167], 0\n\t" \
  "ds_read_b128 v[164:167], v10 offset:9216\n\t" \
  "v_cvt_pk_bf16_f32 v208, v128, v129\n\t" \
  "v_cvt_pk_bf16_f32 v209, v130, v131\n\t" \
  "v_cvt_pk_bf16_f32 v210, v132, v133\n\t" \
  "v_cvt_pk_bf16_f32 v211, v134, v135\n\t" \
  "v_cvt_pk_bf16_f32 v212, v64, v65\n\t" \
  "v_cvt_pk_bf16_f32 v213, v66, v67\n\t" \
  "v_cvt_pk_bf16_f32 v214, v68, v69\n\t" \
  "v_cvt_pk_bf16_f32 v215, v70, v71\n\t" \
  "v_mfma_f32_16x16x32_bf16 v[128:131], v[176:179], v[168:171], 0\n\t" \
  "v_mfma_f32_16x16x32_bf16 v[132:135], v[180:183], v[168:171], 0\n\t" \
  "v_mfma_f32_16x16x32_bf16 v[64:67], v[184:187], v[168:171], 0\n\t" \
  "v_mfma_f32_16x16x32_bf16 v[68:71], v[188:191], v[168:171], 0\n\t" \
  "ds_read_b128 v[168:171], v10 offset:12288\n\t" \
  "v_cvt_pk_bf16_f32 v216, v136, v137\n\t" \
  "v_cvt_pk_bf16_f32 v217, v138, v139\n\t" \
  "v_cvt_pk_bf16_f32 v218, v140, v141\n\t" \
  "v_cvt_pk_bf16_f32 v219, v142, v143\n\t" \
  "v_cvt_pk_bf16_f32 v220, v72, v73\n\t" \
  "v_cvt_pk_bf16_f32 v221, v74, v75\n\t" \
  "v_cvt_pk_bf16_f32 v222, v76, v77\n\t" \
  "v_cvt_pk_bf16_f32 v223, v78, v79\n\t" \
  "v_mfma_f32_16x16x32_bf16 v[76:79], v[188:191], v[172:175], 0\n\t" \
  "v_mfma_f32_16x16x32_bf16 v[72:75], v[184:187], v[172:175], 0\n\t" \
  "v_mfma_f32_16x16x32_bf16 v[140:143], v[180:183], v[172:175], 0\n\t" \
  "v_mfma_f32_16x16x32_bf16 v[136:139], v[176:179], v[172:175], 0\n\t" \
  "ds_read_b128 v[172:175], v10 offset:13312\n\t" \
  "s_branch R1_%=\n\t" \
  "UV_%=:\n\t" \
  "s_waitcnt lgkmcnt(4)\n\t" \
  "v_mfma_f32_16x16x32_bf16 v[80:83], v[144:147], v[176:179], 0\n\t" \
  "s_waitcnt vmcnt(0)\n\t" \
  "s_barrier\n\t" \
  "v_mfma_f32_16x16x32_bf16 v[84:87], v[144:147], v[180:183], 0\n\t" \
  "v_add_u32_e32 v10, 0x8000, v6\n\t" \
  "v_add_u32_e32 v11, 0x8000, v7\n\t" \
  "s_add_u32 m0, s62, 0x10000\n\t" \
  "v_mfma_f32_16x16x32_bf16 v[16:19], v[144:147], v[184:187], 0\n\t" \
  "v_xor_b32_e32 v12, 32, v11\n\t" \
  "global_load_lds_dwordx4 v1, s[40:41] offset:0 sc1\n\t" \
  "v_mfma_f32_16x16x32_bf16 v[20:23], v[144:147], v[188:191], 0\n\t" \
  "ds_read_b128 v[144:147], v10 offset:0\n\t" \
  "v_mfma_f32_16x16x32_bf16 v[28:31], v[148:151], v[188:191], 0\n\t" \
  "ds_read_b64_tr_b16 v[192:193], v11 offset:0\n\t" \
  "global_load_lds_dwordx4 v2, s[40:41] offset:1024 sc1\n\t" \
  "v_mfma_f32_16x16x32_bf16 v[24:27], v[148:151], v[184:187], 0\n\t" \
  "ds_read_b64_tr_b16 v[194:195], v11 offset:4096\n\t" \
  "v_mfma_f32_16x16x32_bf16 v[92:95], v[148:151], v[180:183], 0\n\t" \
  "global_load_lds_dwordx4 v3, s[42:43] offset:2048\n\t" \
  "v_mfma_f32_16x16x32_bf16 v[88:91], v[148:151], v[176:179], 0\n\t" \
  "ds_read_b128 v[148:151], v10 offset:1024\n\t" \
  "v_mfma_f32_16x16x32_bf16 v[96:99], v[152:155], v[176:179], 0\n\t" \
  "ds_read_b64_tr_b16 v[196:197], v12 offset:0\n\t" \
  "global_load_lds_dwordx4 v[4:5], off offset:3072\n\t" \
  "v_mfma_f32_16x16x32_bf16 v[100:103], v[152:155], v[180:183], 0\n\t" \
  "ds_read_b64_tr_b16 v[198:199], v12 offset:4096\n\t" \
  "v_mfma_f32_16x16x32_bf16 v[32:35], v[152:155], v[184:187], 0\n\t" \
  "s_add_u32 s40, s40, s74\n\t" \
  "s_addc_u32 s41, s41, s73\n\t" \
  "v_mfma_f32_16x16x32_bf16 v[36:39], v[152:155], v[188:191], 0\n\t" \
  "ds_read_b128 v[152:155], v10 offset:4096\n\t" \
  "s_add_u32 s42, s42, s72\n\t" \
  "s_addc_u32 s43, s43, s73\n\t" \
  "v_mfma_f32_16x16x32_bf16 v[44:47], v[156:159], v[188:191], 0\n\t" \
  "ds_read_b64_tr_b16 v[200:201], v11 offset:1024\n\t" \
  "v_lshl_add_u64 v[4:5], v[4:5], 0, s[72:73]\n\t" \
  "v_mfma_f32_16x16x32_bf16 v[40:43], v[156:159], v[184:187], 0\n\t" \
  "ds_read_b64_tr_b16 v[202:203], v11 offset:5120\n\t" \
  "v_mfma_f32_16x16x32_bf16 v[108:111], v[156:159], v[180:183], 0\n\t" \
  "v_mfma_f32_16x16x32_bf16 v[104:107], v[156:159], v[176:179], 0\n\t" \
  "ds_read_b128 v[156:159], v10 offset:5120\n\t" \
  "s_add_u32 m0, s62, 0x18000\n\t" \
  "s_waitcnt lgkmcnt(10)\n\t" \
  "v_mfma_f32_16x16x32_bf16 v[112:115], v[160:163], v[176:179], 0\n\t" \
  "ds_read_b64_tr_b16 v[204:205], v12 offset:1024\n\t" \
  "global_load_lds_dwordx4 v1, s[40:41] offset:0 sc1\n\t" \
  "v_mfma_f32_16x16x32_bf16 v[116:119], v[160:163], v[180:183], 0\n\t" \
  "ds_read_b64_tr_b16 v[206:207], v12 offset:5120\n\t" \
  "v_mfma_f32_16x16x32_bf16 v[48:51], v[160:163], v[184:187], 0\n\t" \
  "global_load_lds_dwordx4 v2, s[40:41] offset:1024 sc1\n\t" \
  "v_mfma_f32_16x16x32_bf16 v[52:55], v[160:163], v[188:191], 0\n\t" \
  "ds_read_b128 v[160:163], v10 offset:8192\n\t" \
  "v_mfma_f32_16x16x32_bf16 v[60:63], v[164:167], v[188:191], 0\n\t" \
  "global_load_lds_dwordx4 v3, s[42:43] offset:2048\n\t" \
  "v_mfma_f32_16x16x32_bf16 v[56:59], v[164:167], v[184:187], 0\n\t" \
  "v_mfma_f32_16x16x32_bf16 v[124:127], v[164:167], v[180:183], 0\n\t" \
  "global_load_lds_dwordx4 v[4:5], off offset:3072\n\t" \
  "v_mfma_f32_16x16x32_bf16 v[120:123], v[164:167], v[176:179], 0\n\t" \
  "ds_read_b128 v[164:167], v10 offset:9216\n\t" \
  "v_mfma_f32_16x16x32_bf16 v[128:131], v[168:171], v[176:179], 0\n\t" \
  "s_add_u32 s40, s40, s82\n\t" \
  "s_addc_u32 s41, s41, s81\n\t" \
  "v_mfma_f32_16x16x32_bf16 v[132:135], v[168:171], v[180:183], 0\n\t" \
  "s_add_u32 s42, s42, s80\n\t" \
  "s_addc_u32 s43, s43, s81\n\t" \
  "v_mfma_f32_16x16x32_bf16 v[64:67], v[168:171], v[184:187], 0\n\t" \
  "v_lshl_add_u64 v[4:5], v[4:5], 0, s[80:81]\n\t" \
  "v_mfma_f32_16x16x32_bf16 v[68:71], v[168:171], v[188:191], 0\n\t" \
  "ds_read_b128 v[168:171], v10 offset:12288\n\t" \
  "v_mfma_f32_16x16x32_bf16 v[76:79], v[172:175], v[188:191], 0\n\t" \
  "v_mfma_f32_16x16x32_bf16 v[72:75], v[172:175], v[184:187], 0\n\t" \
  "v_mfma_f32_16x16x32_bf16 v[140:143], v[172:175], v[180:183], 0\n\t" \
  "v_mfma_f32_16x16x32_bf16 v[136:139], v[172:175], v[176:179], 0\n\t" \
  "ds_read_b128 v[172:175], v10 offset:13312\n\t" \
  "s_waitcnt lgkmcnt(4)\n\t" \
  "v_mfma_f32_16x16x32_bf16 v[80:83], v[144:147], v[192:195], v[80:83]\n\t" \
  "s_waitcnt vmcnt(4)\n\t" \
  "s_barrier\n\t" \
  "v_mfma_f32_16x16x32_bf16 v[84:87], v[144:147], v[196:199], v[84:87]\n\t" \
  "v_add_u32_e32 v10, 0x10000, v6\n\t" \
  "v_add_u32_e32 v11, 0x10000, v7\n\t" \
  "s_add_u32 m0, s62, 0x20000\n\t" \
  "v_mfma_f32_16x16x32_bf16 v[16:19], v[144:147], v[200:203], v[16:19]\n\t" \
  "v_xor_b32_e32 v12, 32, v11\n\t" \
  "global_load_lds_dwordx4 v1, s[40:41] offset:0 sc1\n\t" \
  "v_mfma_f32_16x16x32_bf16 v[20:23], v[144:147], v[204:207], v[20:23]\n\t" \
  "ds_read_b128 v[144:147], v10 offset:0\n\t" \
  "v_mfma_f32_16x16x32_bf16 v[28:31], v[148:151], v[204:207], v[28:31]\n\t" \
  "ds_read_b64_tr_b16 v[176:177], v11 offset:0\n\t" \
  "global_load_lds_dwordx4 v2, s[40:41] offset:1024 sc1\n\t" \
  "v_mfma_f32_16x16x32_bf16 v[24:27], v[148:151], v[200:203], v[24:27]\n\t" \
  "ds_read_b64_tr_b16 v[178:179], v11 offset:4096\n\t" \
  "v_mfma_f32_16x16x32_bf16 v[92:95], v[148:151], v[196:199], v[92:95]\n\t" \
  "global_load_lds_dwordx4 v3, s[42:43] offset:2048\n\t" \
  "v_mfma_f32_16x16x32_bf16 v[88:91], v[148:151], v[192:195], v[88:91]\n\t" \
  "ds_read_b128 v[148:151], v10 offset:1024\n\t" \
  "v_mfma_f32_16x16x32_bf16 v[96:99], v[152:155], v[192:195], v[96:99]\n\t" \
  "ds_read_b64_tr_b16 v[180:181], v12 offset:0\n\t" \
  "global_load_lds_dwordx4 v[4:5], off offset:3072\n\t" \
  "v_mfma_f32_16x16x32_bf16 v[100:103], v[152:155], v[196:199], v[100:103]\n\t" \
  "ds_read_b64_tr_b16 v[182:183], v12 offset:4096\n\t" \
  "v_mfma_f32_16x16x32_bf16 v[32:35], v[152:155], v[200:203], v[32:35]\n\t" \
  "s_add_u32 s40, s40, s74\n\t" \
  "s_addc_u32 s41, s41, s73\n\t" \
  "v_mfma_f32_16x16x32_bf16 v[36:39], v[152:155], v[204:207], v[36:39]\n\t" \
  "ds_read_b128 v[152:155], v10 offset:4096\n\t" \
  "s_add_u32 s42, s42, s72\n\t" \
  "s_addc_u32 s43, s43, s73\n\t" \
  "v_mfma_f32_16x16x32_bf16 v[44:47], v[156:159], v[204:207], v[44:47]\n\t" \
  "ds_read_b64_tr_b16 v[184:185], v11 offset:1024\n\t" \
  "v_lshl_add_u64 v[4:5], v[4:5], 0, s[72:73]\n\t" \
  "v_mfma_f32_16x16x32_bf16 v[40:43], v[156:159], v[200:203], v[40:43]\n\t" \
  "ds_read_b64_tr_b16 v[186:187], v11 offset:5120\n\t" \
  "v_mfma_f32_16x16x32_bf16 v[108:111], v[156:159], v[196:199], v[108:111]\n\t" \
  "v_mfma_f32_16x16x32_bf16 v[104:107], v[156:159], v[192:195], v[104:107]\n\t" \
  "ds_read_b128 v[156:159], v10 offset:5120\n\t" \
  "s_add_u32 m0, s62, 0x0\n\t" \
  "s_waitcnt lgkmcnt(10)\n\t" \
  "v_mfma_f32_16x16x32_bf16 v[112:115], v[160:163], v[192:195], v[112:115]\n\t" \
  "ds_read_b64_tr_b16 v[188:189], v12 offset:1024\n\t" \
  "global_load_lds_dwordx4 v1, s[40:41] offset:0 sc1\n\t" \
  "v_mfma_f32_16x16x32_bf16 v[116:119], v[160:163], v[196:199], v[116:119]\n\t" \
  "ds_read_b64_tr_b16 v[190:191], v12 offset:5120\n\t" \
  "v_mfma_f32_16x16x32_bf16 v[48:51], v[160:163], v[200:203], v[48:51]\n\t" \
  "global_load_lds_dwordx4 v2, s[40:41] offset:1024 sc1\n\t" \
  "v_mfma_f32_16x16x32_bf16 v[52:55], v[160:163], v[204:207], v[52:55]\n\t" \
  "ds_read_b128 v[160:163], v10 offset:8192\n\t" \
  "v_mfma_f32_16x16x32_bf16 v[60:63], v[164:167], v[204:207], v[60:63]\n\t" \
  "global_load_lds_dwordx4 v3, s[42:43] offset:2048\n\t" \
  "v_mfma_f32_16x16x32_bf16 v[56:59], v[164:167], v[200:203], v[56:59]\n\t" \
  "v_mfma_f32_16x16x32_bf16 v[124:127], v[164:167], v[196:199], v[124:127]\n\t" \
  "global_load_lds_dwordx4 v[4:5], off offset:3072\n\t" \
  "v_mfma_f32_16x16x32_bf16 v[120:123], v[164:167], v[192:195], v[120:123]\n\t" \
  "ds_read_b128 v[164:167], v10 offset:9216\n\t" \
  "v_mfma_f32_16x16x32_bf16 v[128:131], v[168:171], v[192:195], v[128:131]\n\t" \
  "s_add_u32 s40, s40, s78\n\t" \
  "s_addc_u32 s41, s41, s77\n\t" \
  "v_mfma_f32_16x16x32_bf16 v[132:135], v[168:171], v[196:199], v[132:135]\n\t" \
  "s_add_u32 s42, s42, s76\n\t" \
  "s_addc_u32 s43, s43, s77\n\t" \
  "v_mfma_f32_16x16x32_bf16 v[64:67], v[168:171], v[200:203], v[64:67]\n\t" \
  "v_lshl_add_u64 v[4:5], v[4:5], 0, s[76:77]\n\t" \
  "v_mfma_f32_16x16x32_bf16 v[68:71], v[168:171], v[204:207], v[68:71]\n\t" \
  "ds_read_b128 v[168:171], v10 offset:12288\n\t" \
  "v_mfma_f32_16x16x32_bf16 v[76:79], v[172:175], v[204:207], v[76:79]\n\t" \
  "v_mfma_f32_16x16x32_bf16 v[72:75], v[172:175], v[200:203], v[72:75]\n\t" \
  "v_mfma_f32_16x16x32_bf16 v[140:143], v[172:175], v[196:199], v[140:143]\n\t" \
  "v_mfma_f32_16x16x32_bf16 v[136:139], v[172:175], v[192:195], v[136:139]\n\t" \
  "ds_read_b128 v[172:175], v10 offset:13312\n\t" \
  "s_waitcnt lgkmcnt(4)\n\t" \
  "v_mfma_f32_16x16x32_bf16 v[80:83], v[144:147], v[176:179], v[80:83]\n\t" \
  "s_waitcnt vmcnt(8)\n\t" \
  "s_barrier\n\t" \
  "v_mfma_f32_16x16x32_bf16 v[84:87], v[144:147], v[180:183], v[84:87]\n\t" \
  "v_add_u32_e32 v10, 0x18000, v6\n\t" \
  "v_add_u32_e32 v11, 0x18000, v7\n\t" \
  "v_mfma_f32_16x16x32_bf16 v[16:19], v[144:147], v[184:187], v[16:19]\n\t" \
  "v_xor_b32_e32 v12, 32, v11\n\t" \
  "v_mfma_f32_16x16x32_bf16 v[20:23], v[144:147], v[188:191], v[20:23]\n\t" \
  "ds_read_b128 v[144:147], v10 offset:0\n\t" \
  "v_mfma_f32_16x16x32_bf16 v[28:31], v[148:151], v[188:191], v[28:31]\n\t" \
  "ds_read_b64_tr_b16 v[192:193], v11 offset:0\n\t" \
  "v_mfma_f32_16x16x32_bf16 v[24:27], v[148:151], v[184:187], v[24:27]\n\t" \
  "ds_read_b64_tr_b16 v[194:195], v11 offset:4096\n\t" \
  "v_mfma_f32_16x16x32_bf16 v[92:95], v[148:151], v[180:183], v[92:95]\n\t" \
  "s_add_u32 m0, s62, 0x8000\n\t" \
  "v_mfma_f32_16x16x32_bf16 v[88:91], v[148:151], v[176:179], v[88:91]\n\t" \
  "ds_read_b128 v[148:151], v10 offset:1024\n\t" \
  "v_mfma_f32_16x16x32_bf16 v[96:99], v[152:155], v[176:179], v[96:99]\n\t" \
  "ds_read_b64_tr_b16 v[196:197], v12 offset:0\n\t" \
  "global_load_lds_dwordx4 v1, s[40:41] offset:0 sc1\n\t" \
  "v_mfma_f32_16x16x32_bf16 v[100:103], v[152:155], v[180:183], v[100:103]\n\t" \
  "ds_read_b64_tr_b16 v[198:199], v12 offset:4096\n\t" \
  "v_mfma_f32_16x16x32_bf16 v[32:35], v[152:155], v[184:187], v[32:35]\n\t" \
  "global_load_lds_dwordx4 v2, s[40:41] offset:1024 sc1\n\t" \
  "v_mfma_f32_16x16x32_bf16 v[36:39], v[152:155], v[188:191], v[36:39]\n\t" \
  "ds_read_b128 v[152:155], v10 offset:4096\n\t" \
  "v_mfma_f32_16x16x32_bf16 v[44:47], v[156:159], v[188:191], v[44:47]\n\t" \
  "ds_read_b64_tr_b16 v[200:201], v11 offset:1024\n\t" \
  "global_load_lds_dwordx4 v3, s[42:43] offset:2048\n\t" \
  "v_mfma_f32_16x16x32_bf16 v[40:43], v[156:159], v[184:187], v[40:43]\n\t" \
  "ds_read_b64_tr_b16 v[202:203], v11 offset:5120\n\t" \
  "v_mfma_f32_16x16x32_bf16 v[108:111], v[156:159], v[180:183], v[108:111]\n\t" \
  "global_load_lds_dwordx4 v[4:5], off offset:3072\n\t" \
  "v_mfma_f32_16x16x32_bf16 v[104:107], v[156:159], v[176:179], v[104:107]\n\t" \
  "ds_read_b128 v[156:159], v10 offset:5120\n\t" \
  "s_waitcnt lgkmcnt(10)\n\t" \
  "v_mfma_f32_16x16x32_bf16 v[112:115], v[160:163], v[176:179], v[112:115]\n\t" \
  "ds_read_b64_tr_b16 v[204:205], v12 offset:1024\n\t" \
  "s_add_u32 s40, s40, s74\n\t" \
  "s_addc_u32 s41, s41, s73\n\t" \
  "v_mfma_f32_16x16x32_bf16 v[116:119], v[160:163], v[180:183], v[116:119]\n\t" \
  "ds_read_b64_tr_b16 v[206:207], v12 offset:5120\n\t" \
  "s_add_u32 s42, s42, s72\n\t" \
  "s_addc_u32 s43, s43, s73\n\t" \
  "v_mfma_f32_16x16x32_bf16 v[48:51], v[160:163], v[184:187], v[48:51]\n\t" \
  "v_lshl_add_u64 v[4:5], v[4:5], 0, s[72:73]\n\t" \
  "v_mfma_f32_16x16x32_bf16 v[52:55], v[160:163], v[188:191], v[52:55]\n\t" \
  "ds_read_b128 v[160:163], v10 offset:8192\n\t" \
  "v_mfma_f32_16x16x32_bf16 v[60:63], v[164:167], v[188:191], v[60:63]\n\t" \
  "v_mfma_f32_16x16x32_bf16 v[56:59], v[164:167], v[184:187], v[56:59]\n\t" \
  "v_mfma_f32_16x16x32_bf16 v[124:127], v[164:167], v[180:183], v[124:127]\n\t" \
  "v_mfma_f32_16x16x32_bf16 v[120:123], v[164:167], v[176:179], v[120:123]\n\t" \
  "ds_read_b128 v[164:167], v10 offset:9216\n\t" \
  "v_mfma_f32_16x16x32_bf16 v[128:131], v[168:171], v[176:179], v[128:131]\n\t" \
  "v_mfma_f32_16x16x32_bf16 v[132:135], v[168:171], v[180:183], v[132:135]\n\t" \
  "v_mfma_f32_16x16x32_bf16 v[64:67], v[168:171], v[184:187], v[64:67]\n\t" \
  "v_mfma_f32_16x16x32_bf16 v[68:71], v[168:171], v[188:191], v[68:71]\n\t" \
  "ds_read_b128 v[168:171], v10 offset:12288\n\t" \
  "v_mfma_f32_16x16x32_bf16 v[76:79], v[172:175], v[188:191], v[76:79]\n\t" \
  "v_mfma_f32_16x16x32_bf16 v[72:75], v[172:175], v[184:187], v[72:75]\n\t" \
  "v_mfma_f32_16x16x32_bf16 v[140:143], v[172:175], v[180:183], v[140:143]\n\t" \
  "v_mfma_f32_16x16x32_bf16 v[136:139], v[172:175], v[176:179], v[136:139]\n\t" \
  "ds_read_b128 v[172:175], v10 offset:13312\n\t" \
  "s_waitcnt lgkmcnt(4)\n\t" \
  "v_mfma_f32_16x16x32_bf16 v[80:83], v[144:147], v[192:195], v[80:83]\n\t" \
  "s_waitcnt vmcnt(8)\n\t" \
  "s_barrier\n\t" \
  "v_mfma_f32_16x16x32_bf16 v[84:87], v[144:147], v[196:199], v[84:87]\n\t" \
  "v_add_u32_e32 v10, 0x20000, v6\n\t" \
  "v_add_u32_e32 v11, 0x20000, v7\n\t" \
  "v_mfma_f32_16x16x32_bf16 v[16:19], v[144:147], v[200:203], v[16:19]\n\t" \
  "v_xor_b32_e32 v12, 32, v11\n\t" \
  "v_mfma_f32_16x16x32_bf16 v[20:23], v[144:147], v[204:207], v[20:23]\n\t" \
  "ds_read_b128 v[144:147], v10 offset:0\n\t" \
  "v_mfma_f32_16x16x32_bf16 v[28:31], v[148:151], v[204:207], v[28:31]\n\t" \
  "ds_read_b64_tr_b16 v[176:177], v11 offset:0\n\t" \
  "v_mfma_f32_16x16x32_bf16 v[24:27], v[148:151], v[200:203], v[24:27]\n\t" \
  "ds_read_b64_tr_b16 v[178:179], v11 offset:4096\n\t" \
  "v_mfma_f32_16x16x32_bf16 v[92:95], v[148:151], v[196:199], v[92:95]\n\t" \
  "s_add_u32 m0, s62, 0x10000\n\t" \
  "v_mfma_f32_16x16x32_bf16 v[88:91], v[148:151], v[192:195], v[88:91]\n\t" \
  "ds_read_b128 v[148:151], v10 offset:1024\n\t" \
  "v_mfma_f32_16x16x32_bf16 v[96:99], v[152:155], v[192:195], v[96:99]\n\t" \
  "ds_read_b64_tr_b16 v[180:181], v12 offset:0\n\t" \
  "global_load_lds_dwordx4 v1, s[40:41] offset:0 sc1\n\t" \
  "v_mfma_f32_16x16x32_bf16 v[100:103], v[152:155], v[196:199], v[100:103]\n\t" \
  "ds_read_b64_tr_b16 v[182:183], v12 offset:4096\n\t" \
  "v_mfma_f32_16x16x32_bf16 v[32:35], v[152:155], v[200:203], v[32:35]\n\t" \
  "global_load_lds_dwordx4 v2, s[40:41] offset:1024 sc1\n\t" \
  "v_mfma_f32_16x16x32_bf16 v[36:39], v[152:155], v[204:207], v[36:39]\n\t" \
  "ds_read_b128 v[152:155], v10 offset:4096\n\t" \
  "v_mfma_f32_16x16x32_bf16 v[44:47], v[156:159], v[204:207], v[44:47]\n\t" \
  "ds_read_b64_tr_b16 v[184:185], v11 offset:1024\n\t" \
  "global_load_lds_dwordx4 v3, s[42:43] offset:2048\n\t" \
  "v_mfma_f32_16x16x32_bf16 v[40:43], v[156:159], v[200:203], v[40:43]\n\t" \
  "ds_read_b64_tr_b16 v[186:187], v11 offset:5120\n\t" \
  "v_mfma_f32_16x16x32_bf16 v[108:111], v[156:159], v[196:199], v[108:111]\n\t" \
  "global_load_lds_dwordx4 v[4:5], off offset:3072\n\t" \
  "v_mfma_f32_16x16x32_bf16 v[104:107], v[156:159], v[192:195], v[104:107]\n\t" \
  "ds_read_b128 v[156:159], v10 offset:5120\n\t" \
  "s_waitcnt lgkmcnt(10)\n\t" \
  "v_mfma_f32_16x16x32_bf16 v[112:115], v[160:163], v[192:195], v[112:115]\n\t" \
  "ds_read_b64_tr_b16 v[188:189], v12 offset:1024\n\t" \
  "s_add_u32 s40, s40, s46\n\t" \
  "s_addc_u32 s41, s41, s45\n\t" \
  "v_mfma_f32_16x16x32_bf16 v[116:119], v[160:163], v[196:199], v[116:119]\n\t" \
  "ds_read_b64_tr_b16 v[190:191], v12 offset:5120\n\t" \
  "s_add_u32 s42, s42, s44\n\t" \
  "s_addc_u32 s43, s43, s45\n\t" \
  "v_mfma_f32_16x16x32_bf16 v[48:51], v[160:163], v[200:203], v[48:51]\n\t" \
  "v_lshl_add_u64 v[4:5], v[4:5], 0, s[44:45]\n\t" \
  "v_mfma_f32_16x16x32_bf16 v[52:55], v[160:163], v[204:207], v[52:55]\n\t" \
  "ds_read_b128 v[160:163], v10 offset:8192\n\t" \
  "v_mfma_f32_16x16x32_bf16 v[60:63], v[164:167], v[204:207], v[60:63]\n\t" \
  "s_lshl_b32 s52, %[bid], 3\n\t" \
  "s_add_u32 s52, s52, %[w]\n\t" \
  "v_mfma_f32_16x16x32_bf16 v[56:59], v[164:167], v[200:203], v[56:59]\n\t" \
  "s_lshl_b32 s52, s52, 7\n\t" \
  "s_add_u32 s52, s52, 0x6f00000\n\t" \
  "v_mfma_f32_16x16x32_bf16 v[124:127], v[164:167], v[196:199], v[124:127]\n\t" \
  "s_mov_b64 s[54:55], %[ws]\n\t" \
  "s_add_u32 s54, s54, s52\n\t" \
  "s_addc_u32 s55, s55, 0\n\t" \
  "v_mfma_f32_16x16x32_bf16 v[120:123], v[164:167], v[192:195], v[120:123]\n\t" \
  "ds_read_b128 v[164:167], v10 offset:9216\n\t" \
  "v_mfma_f32_16x16x32_bf16 v[128:131], v[168:171], v[192:195], v[128:131]\n\t" \
  "v_mov_b32_e32 v228, 0\n\t" \
  "v_mov_b32_e32 v229, 0\n\t" \
  "v_mfma_f32_16x16x32_bf16 v[132:135], v[168:171], v[196:199], v[132:135]\n\t" \
  "v_mov_b32_e32 v230, 0\n\t" \
  "v_mov_b32_e32 v231, 0\n\t" \
  "v_mfma_f32_16x16x32_bf16 v[64:67], v[168:171], v[200:203], v[64:67]\n\t" \
  "global_store_dwordx4 v228, v[228:231], s[54:55] sc1\n\t" \
  "v_mfma_f32_16x16x32_bf16 v[68:71], v[168:171], v[204:207], v[68:71]\n\t" \
  "ds_read_b128 v[168:171], v10 offset:12288\n\t" \
  "v_mfma_f32_16x16x32_bf16 v[76:79], v[172:175], v[204:207], v[76:79]\n\t" \
  "v_mfma_f32_16x16x32_bf16 v[72:75], v[172:175], v[200:203], v[72:75]\n\t" \
  "v_mfma_f32_16x16x32_bf16 v[140:143], v[172:175], v[196:199], v[140:143]\n\t" \
  "v_mfma_f32_16x16x32_bf16 v[136:139], v[172:175], v[192:195], v[136:139]\n\t" \
  "ds_read_b128 v[172:175], v10 offset:13312\n\t" \
  "s_waitcnt lgkmcnt(4)\n\t" \
  "v_mfma_f32_16x16x32_bf16 v[80:83], v[144:147], v[176:179], v[80:83]\n\t" \
  "s_waitcnt vmcnt(8)\n\t" \
  "s_barrier\n\t" \
  "v_mfma_f32_16x16x32_bf16 v[84:87], v[144:147], v[180:183], v[84:87]\n\t" \
  "v_add_u32_e32 v10, 0x0, v6\n\t" \
  "v_add_u32_e32 v11, 0x0, v7\n\t" \
  "v_mfma_f32_16x16x32_bf16 v[16:19], v[144:147], v[184:187], v[16:19]\n\t" \
  "v_xor_b32_e32 v12, 32, v11\n\t" \
  "v_mfma_f32_16x16x32_bf16 v[20:23], v[144:147], v[188:191], v[20:23]\n\t" \
  "ds_read_b128 v[144:147], v10 offset:0\n\t" \
  "v_mfma_f32_16x16x32_bf16 v[28:31], v[148:151], v[188:191], v[28:31]\n\t" \
  "ds_read_b64_tr_b16 v[192:193], v11 offset:0\n\t" \
  "v_mfma_f32_16x16x32_bf16 v[24:27], v[148:151], v[184:187], v[24:27]\n\t" \
  "ds_read_b64_tr_b16 v[194:195], v11 offset:4096\n\t" \
  "v_mfma_f32_16x16x32_bf16 v[92:95], v[148:151], v[180:183], v[92:95]\n\t" \
  "s_add_u32 m0, s62, 0x18000\n\t" \
  "v_mfma_f32_16x16x32_bf16 v[88:91], v[148:151], v[176:179], v[88:91]\n\t" \
  "ds_read_b128 v[148:151], v10 offset:1024\n\t" \
  "v_mfma_f32_16x16x32_bf16 v[96:99], v[152:155], v[176:179], v[96:99]\n\t" \
  "ds_read_b64_tr_b16 v[196:197], v12 offset:0\n\t" \
  "global_load_lds_dwordx4 v1, s[40:41] offset:0 sc1\n\t" \
  "s_lshl_b32 s52, %[bid], 3\n\t" \
  "s_add_u32 s52, s52, 0x11000\n\t" \
  "s_cmp_lg_u32 %[w], 0\n\t" \
  "s_cbranch_scc1 PBlV_%=\n\t" \
  "v_mov_b32_e32 v14, s36\n\t" \
  "v_mov_b32_e32 v15, s37\n\t" \
  "v_mov_b32_e32 v13, s52\n\t" \
  "s_mov_b64 exec, 1\n\t" \
  "global_store_dwordx2 v13, v[14:15], s[60:61] sc1\n\t" \
  "s_mov_b64 exec, -1\n\t" \
  "PBlV_%=:\n\t" \
  "v_mfma_f32_16x16x32_bf16 v[100:103], v[152:155], v[180:183], v[100:103]\n\t" \
  "ds_read_b64_tr_b16 v[198:199], v12 offset:4096\n\t" \
  "v_mfma_f32_16x16x32_bf16 v[32:35], v[152:155], v[184:187], v[32:35]\n\t" \
  "global_load_lds_dwordx4 v2, s[40:41] offset:1024 sc1\n\t" \
  "v_mfma_f32_16x16x32_bf16 v[36:39], v[152:155], v[188:191], v[36:39]\n\t" \
  "ds_read_b128 v[152:155], v10 offset:4096\n\t" \
  "v_mfma_f32_16x16x32_bf16 v[44:47], v[156:159], v[188:191], v[44:47]\n\t" \
  "ds_read_b64_tr_b16 v[200:201], v11 offset:1024\n\t" \
  "global_load_lds_dwordx4 v3, s[42:43] offset:2048\n\t" \
  "v_mfma_f32_16x16x32_bf16 v[40:43], v[156:159], v[184:187], v[40:43]\n\t" \
  "ds_read_b64_tr_b16 v[202:203], v11 offset:5120\n\t" \
  "v_mfma_f32_16x16x32_bf16 v[108:111], v[156:159], v[180:183], v[108:111]\n\t" \
  "global_load_lds_dwordx4 v[4:5], off offset:3072\n\t" \
  "v_mfma_f32_16x16x32_bf16 v[104:107], v[156:159], v[176:179], v[104:107]\n\t" \
  "ds_read_b128 v[156:159], v10 offset:5120\n\t" \
  "s_waitcnt lgkmcnt(10)\n\t" \
  "v_mfma_f32_16x16x32_bf16 v[112:115], v[160:163], v[176:179], v[112:115]\n\t" \
  "ds_read_b64_tr_b16 v[204:205], v12 offset:1024\n\t" \
  "s_add_u32 s40, s40, s74\n\t" \
  "s_addc_u32 s41, s41, s73\n\t" \
  "v_mfma_f32_16x16x32_bf16 v[116:119], v[160:163], v[180:183], v[116:119]\n\t" \
  "ds_read_b64_tr_b16 v[206:207], v12 offset:5120\n\t" \
  "s_add_u32 s42, s42, s72\n\t" \
  "s_addc_u32 s43, s43, s73\n\t" \
  "v_mfma_f32_16x16x32_bf16 v[48:51], v[160:163], v[184:187], v[48:51]\n\t" \
  "v_lshl_add_u64 v[4:5], v[4:5], 0, s[72:73]\n\t" \
  "v_mfma_f32_16x16x32_bf16 v[52:55], v[160:163], v[188:191], v[52:55]\n\t" \
  "ds_read_b128 v[160:163], v10 offset:8192\n\t" \
  "v_mfma_f32_16x16x32_bf16 v[60:63], v[164:167], v[188:191], v[60:63]\n\t" \
  "v_mfma_f32_16x16x32_bf16 v[56:59], v[164:167], v[184:187], v[56:59]\n\t" \
  "v_mfma_f32_16x16x32_bf16 v[124:127], v[164:167], v[180:183], v[124:127]\n\t" \
  "v_mfma_f32_16x16x32_bf16 v[120:123], v[164:167], v[176:179], v[120:123]\n\t" \
  "ds_read_b128 v[164:167], v10 offset:9216\n\t" \
  "v_mfma_f32_16x16x32_bf16 v[128:131], v[168:171], v[176:179], v[128:131]\n\t" \
  "v_mfma_f32_16x16x32_bf16 v[132:135], v[168:171], v[180:183], v[132:135]\n\t" \
  "v_mfma_f32_16x16x32_bf16 v[64:67], v[168:171], v[184:187], v[64:67]\n\t" \
  "v_mfma_f32_16x16x32_bf16 v[68:71], v[168:171], v[188:191], v[68:71]\n\t" \
  "ds_read_b128 v[168:171], v10 offset:12288\n\t" \
  "v_mfma_f32_16x16x32_bf16 v[76:79], v[172:175], v[188:191], v[76:79]\n\t" \
  "v_mfma_f32_16x16x32_bf16 v[72:75], v[172:175], v[184:187], v[72:75]\n\t" \
  "v_mfma_f32_16x16x32_bf16 v[140:143], v[172:175], v[180:183], v[140:143]\n\t" \
  "v_mfma_f32_16x16x32_bf16 v[136:139], v[172:175], v[176:179], v[136:139]\n\t" \
  "ds_read_b128 v[172:175], v10 offset:13312\n\t" \
  "s_waitcnt lgkmcnt(4)\n\t" \
  "v_mfma_f32_16x16x32_bf16 v[80:83], v[144:147], v[192:195], v[80:83]\n\t" \
  "s_waitcnt vmcnt(8)\n\t" \
  "s_barrier\n\t" \
  "v_mfma_f32_16x16x32_bf16 v[84:87], v[144:147], v[196:199], v[84:87]\n\t" \
  "v_add_u32_e32 v10, 0x8000, v6\n\t" \
  "v_add_u32_e32 v11, 0x8000, v7\n\t" \
  "v_mfma_f32_16x16x32_bf16 v[16:19], v[144:147], v[200:203], v[16:19]\n\t" \
  "v_xor_b32_e32 v12, 32, v11\n\t" \
  "v_mfma_f32_16x16x32_bf16 v[20:23], v[144:147], v[204:207], v[20:23]\n\t" \
  "ds_read_b128 v[144:147], v10 offset:0\n\t" \
  "v_mfma_f32_16x16x32_bf16 v[28:31], v[148:151], v[204:207], v[28:31]\n\t" \
  "ds_read_b64_tr_b16 v[176:177], v11 offset:0\n\t" \
  "v_mfma_f32_16x16x32_bf16 v[24:27], v[148:151], v[200:203], v[24:27]\n\t" \
  "ds_read_b64_tr_b16 v[178:179], v11 offset:4096\n\t" \
  "v_mfma_f32_16x16x32_bf16 v[92:95], v[148:151], v[196:199], v[92:95]\n\t" \
  "s_add_u32 m0, s62, 0x20000\n\t" \
  "v_mfma_f32_16x16x32_bf16 v[88:91], v[148:151], v[192:195], v[88:91]\n\t" \
  "ds_read_b128 v[148:151], v10 offset:1024\n\t" \
  "v_mfma_f32_16x16x32_bf16 v[96:99], v[152:155], v[192:195], v[96:99]\n\t" \
  "ds_read_b64_tr_b16 v[180:181], v12 offset:0\n\t" \
  "global_load_lds_dwordx4 v1, s[40:41] offset:0 sc1\n\t" \
  "v_mfma_f32_16x16x32_bf16 v[100:103], v[152:155], v[196:199], v[100:103]\n\t" \
  "ds_read_b64_tr_b16 v[182:183], v12 offset:4096\n\t" \
  "v_mfma_f32_16x16x32_bf16 v[32:35], v[152:155], v[200:203], v[32:35]\n\t" \
  "global_load_lds_dwordx4 v2, s[40:41] offset:1024 sc1\n\t" \
  "v_mfma_f32_16x16x32_bf16 v[36:39], v[152:155], v[204:207], v[36:39]\n\t" \
  "ds_read_b128 v[152:155], v10 offset:4096\n\t" \
  "v_mfma_f32_16x16x32_bf16 v[44:47], v[156:159], v[204:207], v[44:47]\n\t" \
  "ds_read_b64_tr_b16 v[184:185], v11 offset:1024\n\t" \
  "global_load_lds_dwordx4 v3, s[42:43] offset:2048\n\t" \
  "v_mfma_f32_16x16x32_bf16 v[40:43], v[156:159], v[200:203], v[40:43]\n\t" \
  "ds_read_b64_tr_b16 v[186:187], v11 offset:5120\n\t" \
  "v_mfma_f32_16x16x32_bf16 v[108:111], v[156:159], v[196:199], v[108:111]\n\t" \
  "global_load_lds_dwordx4 v[4:5], off offset:3072\n\t" \
  "v_mfma_f32_16x16x32_bf16 v[104:107], v[156:159], v[192:195], v[104:107]\n\t" \
  "ds_read_b128 v[156:159], v10 offset:5120\n\t" \
  "s_waitcnt lgkmcnt(10)\n\t" \
  "v_mfma_f32_16x16x32_bf16 v[112:115], v[160:163], v[192:195], v[112:115]\n\t" \
  "ds_read_b64_tr_b16 v[188:189], v12 offset:1024\n\t" \
  "s_add_u32 s40, s40, s78\n\t" \
  "s_addc_u32 s41, s41, s77\n\t" \
  "v_mfma_f32_16x16x32_bf16 v[116:119], v[160:163], v[196:199], v[116:119]\n\t" \
  "ds_read_b64_tr_b16 v[190:191], v12 offset:5120\n\t" \
  "s_add_u32 s42, s42, s76\n\t" \
  "s_addc_u32 s43, s43, s77\n\t" \
  "v_mfma_f32_16x16x32_bf16 v[48:51], v[160:163], v[200:203], v[48:51]\n\t" \
  "v_lshl_add_u64 v[4:5], v[4:5], 0, s[76:77]\n\t" \
  "v_mfma_f32_16x16x32_bf16 v[52:55], v[160:163], v[204:207], v[52:55]\n\t" \
  "ds_read_b128 v[160:163], v10 offset:8192\n\t" \
  "v_mfma_f32_16x16x32_bf16 v[60:63], v[164:167], v[204:207], v[60:63]\n\t" \
  "v_mfma_f32_16x16x32_bf16 v[56:59], v[164:167], v[200:203], v[56:59]\n\t" \
  "v_mfma_f32_16x16x32_bf16 v[124:127], v[164:167], v[196:199], v[124:127]\n\t" \
  "v_mfma_f32_16x16x32_bf16 v[120:123], v[164:167], v[192:195], v[120:123]\n\t" \
  "ds_read_b128 v[164:167], v10 offset:9216\n\t" \
  "v_mfma_f32_16x16x32_bf16 v[128:131], v[168:171], v[192:195], v[128:131]\n\t" \
  "v_mfma_f32_16x16x32_bf16 v[132:135], v[168:171], v[196:199], v[132:135]\n\t" \
  "v_mfma_f32_16x16x32_bf16 v[64:67], v[168:171], v[200:203], v[64:67]\n\t" \
  "v_mfma_f32_16x16x32_bf16 v[68:71], v[168:171], v[204:207], v[68:71]\n\t" \
  "ds_read_b128 v[168:171], v10 offset:12288\n\t" \
  "v_mfma_f32_16x16x32_bf16 v[76:79], v[172:175], v[204:207], v[76:79]\n\t" \
  "v_mfma_f32_16x16x32_bf16 v[72:75], v[172:175], v[200:203], v[72:75]\n\t" \
  "v_mfma_f32_16x16x32_bf16 v[140:143], v[172:175], v[196:199], v[140:143]\n\t" \
  "v_mfma_f32_16x16x32_bf16 v[136:139], v[172:175], v[192:195], v[136:139]\n\t" \
  "ds_read_b128 v[172:175], v10 offset:13312\n\t" \
  "s_lshr_b32 s53, %[bid], 6\n\t" \
  "s_cmp_eq_u32 s53, 0\n\t" \
  "s_cbranch_scc1 HT0V_%=\n\t" \
  "HR0V_%=:\n\t" \
  "s_waitcnt lgkmcnt(4)\n\t" \
  "v_mfma_f32_16x16x32_bf16 v[80:83], v[144:147], v[176:179], v[80:83]\n\t" \
  "s_waitcnt vmcnt(8)\n\t" \
  "s_barrier\n\t" \
  "v_mfma_f32_16x16x32_bf16 v[84:87], v[144:147], v[180:183], v[84:87]\n\t" \
  "v_add_u32_e32 v10, 0x10000, v6\n\t" \
  "v_add_u32_e32 v11, 0x10000, v7\n\t" \
  "v_mfma_f32_16x16x32_bf16 v[16:19], v[144:147], v[184:187], v[16:19]\n\t" \
  "v_xor_b32_e32 v12, 32, v11\n\t" \
  "v_mfma_f32_16x16x32_bf16 v[20:23], v[144:147], v[188:191], v[20:23]\n\t" \
  "ds_read_b128 v[144:147], v10 offset:0\n\t" \
  "v_mfma_f32_16x16x32_bf16 v[28:31], v[148:151], v[188:191], v[28:31]\n\t" \
  "ds_read_b64_tr_b16 v[192:193], v11 offset:0\n\t" \
  "v_mfma_f32_16x16x32_bf16 v[24:27], v[148:151], v[184:187], v[24:27]\n\t" \
  "ds_read_b64_tr_b16 v[194:195], v11 offset:4096\n\t" \
  "v_mfma_f32_16x16x32_bf16 v[92:95], v[148:151], v[180:183], v[92:95]\n\t" \
  "s_add_u32 m0, s62, 0x0\n\t" \
  "v_mfma_f32_16x16x32_bf16 v[88:91], v[148:151], v[176:179], v[88:91]\n\t" \
  "ds_read_b128 v[148:151], v10 offset:1024\n\t" \
  "v_mfma_f32_16x16x32_bf16 v[96:99], v[152:155], v[176:179], v[96:99]\n\t" \
  "ds_read_b64_tr_b16 v[196:197], v12 offset:0\n\t" \
  "global_load_lds_dwordx4 v1, s[40:41] offset:0 sc1\n\t" \
  "v_mfma_f32_16x16x32_bf16 v[100:103], v[152:155], v[180:183], v[100:103]\n\t" \
  "ds_read_b64_tr_b16 v[198:199], v12 offset:4096\n\t" \
  "v_mfma_f32_16x16x32_bf16 v[32:35], v[152:155], v[184:187], v[32:35]\n\t" \
  "global_load_lds_dwordx4 v2, s[40:41] offset:1024 sc1\n\t" \
  "v_mfma_f32_16x16x32_bf16 v[36:39], v[152:155], v[188:191], v[36:39]\n\t" \
  "ds_read_b128 v[152:155], v10 offset:4096\n\t" \
  "v_mfma_f32_16x16x32_bf16 v[44:47], v[156:159], v[188:191], v[44:47]\n\t" \
  "ds_read_b64_tr_b16 v[200:201], v11 offset:1024\n\t" \
  "global_load_lds_dwordx4 v3, s[42:43] offset:2048\n\t" \
  "v_mfma_f32_16x16x32_bf16 v[40:43], v[156:159], v[184:187], v[40:43]\n\t" \
  "ds_read_b64_tr_b16 v[202:203], v11 offset:5120\n\t" \
  "v_mfma_f32_16x16x32_bf16 v[108:111], v[156:159], v[180:183], v[108:111]\n\t" \
  "global_load_lds_dwordx4 v[4:5], off offset:3072\n\t" \
  "v_mfma_f32_16x16x32_bf16 v[104:107], v[156:159], v[176:179], v[104:107]\n\t" \
  "ds_read_b128 v[156:159], v10 offset:5120\n\t" \
  "s_waitcnt lgkmcnt(10)\n\t" \
  "v_mfma_f32_16x16x32_bf16 v[112:115], v[160:163], v[176:179], v[112:115]\n\t" \
  "ds_read_b64_tr_b16 v[204:205], v12 offset:1024\n\t" \
  "s_add_u32 s40, s40, s74\n\t" \
  "s_addc_u32 s41, s41, s73\n\t" \
  "v_mfma_f32_16x16x32_bf16 v[116:119], v[160:163], v[180:183], v[116:119]\n\t" \
  "ds_read_b64_tr_b16 v[206:207], v12 offset:5120\n\t" \
  "s_add_u32 s42, s42, s72\n\t" \
  "s_addc_u32 s43, s43, s73\n\t" \
  "v_mfma_f32_16x16x32_bf16 v[48:51], v[160:163], v[184:187], v[48:51]\n\t" \
  "v_lshl_add_u64 v[4:5], v[4:5], 0, s[72:73]\n\t" \
  "v_mfma_f32_16x16x32_bf16 v[52:55], v[160:163], v[188:191], v[52:55]\n\t" \
  "ds_read_b128 v[160:163], v10 offset:8192\n\t" \
  "v_mfma_f32_16x16x32_bf16 v[60:63], v[164:167], v[188:191], v[60:63]\n\t" \
  "v_mfma_f32_16x16x32_bf16 v[56:59], v[164:167], v[184:187], v[56:59]\n\t" \
  "v_mfma_f32_16x16x32_bf16 v[124:127], v[164:167], v[180:183], v[124:127]\n\t" \
  "v_mfma_f32_16x16x32_bf16 v[120:123], v[164:167], v[176:179], v[120:123]\n\t" \
  "ds_read_b128 v[164:167], v10 offset:9216\n\t" \
  "v_mfma_f32_16x16x32_bf16 v[128:131], v[168:171], v[176:179], v[128:131]\n\t" \
  "v_mfma_f32_16x16x32_bf16 v[132:135], v[168:171], v[180:183], v[132:135]\n\t" \
  "v_mfma_f32_16x16x32_bf16 v[64:67], v[168:171], v[184:187], v[64:67]\n\t" \
  "v_mfma_f32_16x16x32_bf16 v[68:71], v[168:171], v[188:191], v[68:71]\n\t" \
  "ds_read_b128 v[168:171], v10 offset:12288\n\t" \
  "v_mfma_f32_16x16x32_bf16 v[76:79], v[172:175], v[188:191], v[76:79]\n\t" \
  "v_mfma_f32_16x16x32_bf16 v[72:75], v[172:175], v[184:187], v[72:75]\n\t" \
  "v_mfma_f32_16x16x32_bf16 v[140:143], v[172:175], v[180:183], v[140:143]\n\t" \
  "v_mfma_f32_16x16x32_bf16 v[136:139], v[172:175], v[176:179], v[136:139]\n\t" \
  "ds_read_b128 v[172:175], v10 offset:13312\n\t" \
  "s_waitcnt lgkmcnt(4)\n\t" \
  "v_mfma_f32_16x16x32_bf16 v[80:83], v[144:147], v[192:195], v[80:83]\n\t" \
  "s_waitcnt vmcnt(8)\n\t" \
  "s_barrier\n\t" \
  "v_mfma_f32_16x16x32_bf16 v[84:87], v[144:147], v[196:199], v[84:87]\n\t" \
  "v_add_u32_e32 v10, 0x18000, v6\n\t" \
  "v_add_u32_e32 v11, 0x18000, v7\n\t" \
  "v_mfma_f32_16x16x32_bf16 v[16:19], v[144:147], v[200:203], v[16:19]\n\t" \
  "v_xor_b32_e32 v12, 32, v11\n\t" \
  "v_mfma_f32_16x16x32_bf16 v[20:23], v[144:147], v[204:207], v[20:23]\n\t" \
  "ds_read_b128 v[144:147], v10 offset:0\n\t" \
  "v_mfma_f32_16x16x32_bf16 v[28:31], v[148:151], v[204:207], v[28:31]\n\t" \
  "ds_read_b64_tr_b16 v[176:177], v11 offset:0\n\t" \
  "v_mfma_f32_16x16x32_bf16 v[24:27], v[148:151], v[200:203], v[24:27]\n\t" \
  "ds_read_b64_tr_b16 v[178:179], v11 offset:4096\n\t" \
  "v_mfma_f32_16x16x32_bf16 v[92:95], v[148:151], v[196:199], v[92:95]\n\t" \
  "s_add_u32 m0, s62, 0x8000\n\t" \
  "v_mfma_f32_16x16x32_bf16 v[88:91], v[148:151], v[192:195], v[88:91]\n\t" \
  "ds_read_b128 v[148:151], v10 offset:1024\n\t" \
  "v_mfma_f32_16x16x32_bf16 v[96:99], v[152:155], v[192:195], v[96:99]\n\t" \
  "ds_read_b64_tr_b16 v[180:181], v12 offset:0\n\t" \
  "global_load_lds_dwordx4 v1, s[40:41] offset:0 sc1\n\t" \
  "v_mfma_f32_16x16x32_bf16 v[100:103], v[152:155], v[196:199], v[100:103]\n\t" \
  "ds_read_b64_tr_b16 v[182:183], v12 offset:4096\n\t" \
  "v_mfma_f32_16x16x32_bf16 v[32:35], v[152:155], v[200:203], v[32:35]\n\t" \
  "global_load_lds_dwordx4 v2, s[40:41] offset:1024 sc1\n\t" \
  "v_mfma_f32_16x16x32_bf16 v[36:39], v[152:155], v[204:207], v[36:39]\n\t" \
  "ds_read_b128 v[152:155], v10 offset:4096\n\t" \
  "v_mfma_f32_16x16x32_bf16 v[44:47], v[156:159], v[204:207], v[44:47]\n\t" \
  "ds_read_b64_tr_b16 v[184:185], v11 offset:1024\n\t" \
  "global_load_lds_dwordx4 v3, s[42:43] offset:2048\n\t" \
  "v_mfma_f32_16x16x32_bf16 v[40:43], v[156:159], v[200:203], v[40:43]\n\t" \
  "ds_read_b64_tr_b16 v[186:187], v11 offset:5120\n\t" \
  "v_mfma_f32_16x16x32_bf16 v[108:111], v[156:159], v[196:199], v[108:111]\n\t" \
  "global_load_lds_dwordx4 v[4:5], off offset:3072\n\t" \
  "v_mfma_f32_16x16x32_bf16 v[104:107], v[156:159], v[192:195], v[104:107]\n\t" \
  "ds_read_b128 v[156:159], v10 offset:5120\n\t" \
  "s_waitcnt lgkmcnt(10)\n\t" \
  "v_mfma_f32_16x16x32_bf16 v[112:115], v[160:163], v[192:195], v[112:115]\n\t" \
  "ds_read_b64_tr_b16 v[188:189], v12 offset:1024\n\t" \
  "s_add_u32 s40, s40, s82\n\t" \
  "s_addc_u32 s41, s41, s81\n\t" \
  "v_mfma_f32_16x16x32_bf16 v[116:119], v[160:163], v[196:199], v[116:119]\n\t" \
  "ds_read_b64_tr_b16 v[190:191], v12 offset:5120\n\t" \
  "s_add_u32 s42, s42, s80\n\t" \
  "s_addc_u32 s43, s43, s81\n\t" \
  "v_mfma_f32_16x16x32_bf16 v[48:51], v[160:163], v[200:203], v[48:51]\n\t" \
  "v_lshl_add_u64 v[4:5], v[4:5], 0, s[80:81]\n\t" \
  "v_mfma_f32_16x16x32_bf16 v[52:55], v[160:163], v[204:207], v[52:55]\n\t" \
  "ds_read_b128 v[160:163], v10 offset:8192\n\t" \
  "v_mfma_f32_16x16x32_bf16 v[60:63], v[164:167], v[204:207], v[60:63]\n\t" \
  "v_mfma_f32_16x16x32_bf16 v[56:59], v[164:167], v[200:203], v[56:59]\n\t" \
  "v_mfma_f32_16x16x32_bf16 v[124:127], v[164:167], v[196:199], v[124:127]\n\t" \
  "v_mfma_f32_16x16x32_bf16 v[120:123], v[164:167], v[192:195], v[120:123]\n\t" \
  "ds_read_b128 v[164:167], v10 offset:9216\n\t" \
  "v_mfma_f32_16x16x32_bf16 v[128:131], v[168:171], v[192:195], v[128:131]\n\t" \
  "v_mfma_f32_16x16x32_bf16 v[132:135], v[168:171], v[196:199], v[132:135]\n\t" \
  "v_mfma_f32_16x16x32_bf16 v[64:67], v[168:171], v[200:203], v[64:67]\n\t" \
  "v_mfma_f32_16x16x32_bf16 v[68:71], v[168:171], v[204:207], v[68:71]\n\t" \
  "ds_read_b128 v[168:171], v10 offset:12288\n\t" \
  "v_mfma_f32_16x16x32_bf16 v[76:79], v[172:175], v[204:207], v[76:79]\n\t" \
  "v_mfma_f32_16x16x32_bf16 v[72:75], v[172:175], v[200:203], v[72:75]\n\t" \
  "v_mfma_f32_16x16x32_bf16 v[140:143], v[172:175], v[196:199], v[140:143]\n\t" \
  "v_mfma_f32_16x16x32_bf16 v[136:139], v[172:175], v[192:195], v[136:139]\n\t" \
  "ds_read_b128 v[172:175], v10 offset:13312\n\t" \
  "s_lshr_b32 s53, %[bid], 6\n\t" \
  "s_cmp_eq_u32 s53, 1\n\t" \
  "s_cbranch_scc1 HT1V_%=\n\t" \
  "HR1V_%=:\n\t" \
  "s_waitcnt lgkmcnt(4)\n\t" \
  "v_mfma_f32_16x16x32_bf16 v[80:83], v[144:147], v[176:179], v[80:83]\n\t" \
  "s_waitcnt vmcnt(8)\n\t" \
  "s_barrier\n\t" \
  "v_mfma_f32_16x16x32_bf16 v[84:87], v[144:147], v[180:183], v[84:87]\n\t" \
  "v_add_u32_e32 v10, 0x20000, v6\n\t" \
  "v_add_u32_e32 v11, 0x20000, v7\n\t" \
  "v_mfma_f32_16x16x32_bf16 v[16:19], v[144:147], v[184:187], v[16:19]\n\t" \
  "v_xor_b32_e32 v12, 32, v11\n\t" \
  "v_mfma_f32_16x16x32_bf16 v[20:23], v[144:147], v[188:191], v[20:23]\n\t" \
  "ds_read_b128 v[144:147], v10 offset:0\n\t" \
  "v_mfma_f32_16x16x32_bf16 v[28:31], v[148:151], v[188:191], v[28:31]\n\t" \
  "ds_read_b64_tr_b16 v[192:193], v11 offset:0\n\t" \
  "v_mfma_f32_16x16x32_bf16 v[24:27], v[148:151], v[184:187], v[24:27]\n\t" \
  "ds_read_b64_tr_b16 v[194:195], v11 offset:4096\n\t" \
  "v_mfma_f32_16x16x32_bf16 v[92:95], v[148:151], v[180:183], v[92:95]\n\t" \
  "s_add_u32 m0, s62, 0x10000\n\t" \
  "v_mfma_f32_16x16x32_bf16 v[88:91], v[148:151], v[176:179], v[88:91]\n\t" \
  "ds_read_b128 v[148:151], v10 offset:1024\n\t" \
  "v_mfma_f32_16x16x32_bf16 v[96:99], v[152:155], v[176:179], v[96:99]\n\t" \
  "ds_read_b64_tr_b16 v[196:197], v12 offset:0\n\t" \
  "global_load_lds_dwordx4 v1, s[40:41] offset:0 sc1\n\t" \
  "v_mfma_f32_16x16x32_bf16 v[100:103], v[152:155], v[180:183], v[100:103]\n\t" \
  "ds_read_b64_tr_b16 v[198:199], v12 offset:4096\n\t" \
  "v_mfma_f32_16x16x32_bf16 v[32:35], v[152:155], v[184:187], v[32:35]\n\t" \
  "global_load_lds_dwordx4 v2, s[40:41] offset:1024 sc1\n\t" \
  "v_mfma_f32_16x16x32_bf16 v[36:39], v[152:155], v[188:191], v[36:39]\n\t" \
  "ds_read_b128 v[152:155], v10 offset:4096\n\t" \
  "v_mfma_f32_16x16x32_bf16 v[44:47], v[156:159], v[188:191], v[44:47]\n\t" \
  "ds_read_b64_tr_b16 v[200:201], v11 offset:1024\n\t" \
  "global_load_lds_dwordx4 v3, s[42:43] offset:2048\n\t" \
  "v_mfma_f32_16x16x32_bf16 v[40:43], v[156:159], v[184:187], v[40:43]\n\t" \
  "ds_read_b64_tr_b16 v[202:203], v11 offset:5120\n\t" \
  "v_mfma_f32_16x16x32_bf16 v[108:111], v[156:159], v[180:183], v[108:111]\n\t" \
  "global_load_lds_dwordx4 v[4:5], off offset:3072\n\t" \
  "v_mfma_f32_16x16x32_bf16 v[104:107], v[156:159], v[176:179], v[104:107]\n\t" \
  "ds_read_b128 v[156:159], v10 offset:5120\n\t" \
  "s_waitcnt lgkmcnt(10)\n\t" \
  "v_mfma_f32_16x16x32_bf16 v[112:115], v[160:163], v[176:179], v[112:115]\n\t" \
  "ds_read_b64_tr_b16 v[204:205], v12 offset:1024\n\t" \
  "s_add_u32 s40, s40, s74\n\t" \
  "s_addc_u32 s41, s41, s73\n\t" \
  "v_mfma_f32_16x16x32_bf16 v[116:119], v[160:163], v[180:183], v[116:119]\n\t" \
  "ds_read_b64_tr_b16 v[206:207], v12 offset:5120\n\t" \
  "s_add_u32 s42, s42, s72\n\t" \
  "s_addc_u32 s43, s43, s73\n\t" \
  "v_mfma_f32_16x16x32_bf16 v[48:51], v[160:163], v[184:187], v[48:51]\n\t" \
  "v_lshl_add_u64 v[4:5], v[4:5], 0, s[72:73]\n\t" \
  "v_mfma_f32_16x16x32_bf16 v[52:55], v[160:163], v[188:191], v[52:55]\n\t" \
  "ds_read_b128 v[160:163], v10 offset:8192\n\t" \
  "v_mfma_f32_16x16x32_bf16 v[60:63], v[164:167], v[188:191], v[60:63]\n\t" \
  "v_mfma_f32_16x16x32_bf16 v[56:59], v[164:167], v[184:187], v[56:59]\n\t" \
  "v_mfma_f32_16x16x32_bf16 v[124:127], v[164:167], v[180:183], v[124:127]\n\t" \
  "v_mfma_f32_16x16x32_bf16 v[120:123], v[164:167], v[176:179], v[120:123]\n\t" \
  "ds_read_b128 v[164:167], v10 offset:9216\n\t" \
  "v_mfma_f32_16x16x32_bf16 v[128:131], v[168:171], v[176:179], v[128:131]\n\t" \
  "v_mfma_f32_16x16x32_bf16 v[132:135], v[168:171], v[180:183], v[132:135]\n\t" \
  "v_mfma_f32_16x16x32_bf16 v[64:67], v[168:171], v[184:187], v[64:67]\n\t" \
  "v_mfma_f32_16x16x32_bf16 v[68:71], v[168:171], v[188:191], v[68:71]\n\t" \
  "ds_read_b128 v[168:171], v10 offset:12288\n\t" \
  "v_mfma_f32_16x16x32_bf16 v[76:79], v[172:175], v[188:191], v[76:79]\n\t" \
  "v_mfma_f32_16x16x32_bf16 v[72:75], v[172:175], v[184:187], v[72:75]\n\t" \
  "v_mfma_f32_16x16x32_bf16 v[140:143], v[172:175], v[180:183], v[140:143]\n\t" \
  "v_mfma_f32_16x16x32_bf16 v[136:139], v[172:175], v[176:179], v[136:139]\n\t" \
  "ds_read_b128 v[172:175], v10 offset:13312\n\t" \
  "s_waitcnt lgkmcnt(4)\n\t" \
  "v_mfma_f32_16x16x32_bf16 v[80:83], v[144:147], v[192:195], v[80:83]\n\t" \
  "s_waitcnt vmcnt(8)\n\t" \
  "s_barrier\n\t" \
  "v_mfma_f32_16x16x32_bf16 v[84:87], v[144:147], v[196:199], v[84:87]\n\t" \
  "v_add_u32_e32 v10, 0x0, v6\n\t" \
  "v_add_u32_e32 v11, 0x0, v7\n\t" \
  "v_mfma_f32_16x16x32_bf16 v[16:19], v[144:147], v[200:203], v[16:19]\n\t" \
  "v_xor_b32_e32 v12, 32, v11\n\t" \
  "v_mfma_f32_16x16x32_bf16 v[20:23], v[144:147], v[204:207], v[20:23]\n\t" \
  "ds_read_b128 v[144:147], v10 offset:0\n\t" \
  "v_mfma_f32_16x16x32_bf16 v[28:31], v[148:151], v[204:207], v[28:31]\n\t" \
  "ds_read_b64_tr_b16 v[176:177], v11 offset:0\n\t" \
  "v_mfma_f32_16x16x32_bf16 v[24:27], v[148:151], v[200:203], v[24:27]\n\t" \
  "ds_read_b64_tr_b16 v[178:179], v11 offset:4096\n\t" \
  "v_mfma_f32_16x16x32_bf16 v[92:95], v[148:151], v[196:199], v[92:95]\n\t" \
  "s_add_u32 m0, s62, 0x18000\n\t" \
  "v_mfma_f32_16x16x32_bf16 v[88:91], v[148:151], v[192:195], v[88:91]\n\t" \
  "ds_read_b128 v[148:151], v10 offset:1024\n\t" \
  "v_mfma_f32_16x16x32_bf16 v[96:99], v[152:155], v[192:195], v[96:99]\n\t" \
  "ds_read_b64_tr_b16 v[180:181], v12 offset:0\n\t" \
  "global_load_lds_dwordx4 v1, s[40:41] offset:0 sc1\n\t" \
  "v_mfma_f32_16x16x32_bf16 v[100:103], v[152:155], v[196:199], v[100:103]\n\t" \
  "ds_read_b64_tr_b16 v[182:183], v12 offset:4096\n\t" \
  "v_mfma_f32_16x16x32_bf16 v[32:35], v[152:155], v[200:203], v[32:35]\n\t" \
  "global_load_lds_dwordx4 v2, s[40:41] offset:1024 sc1\n\t" \
  "v_mfma_f32_16x16x32_bf16 v[36:39], v[152:155], v[204:207], v[36:39]\n\t" \
  "ds_read_b128 v[152:155], v10 offset:4096\n\t" \
  "v_mfma_f32_16x16x32_bf16 v[44:47], v[156:159], v[204:207], v[44:47]\n\t" \
  "ds_read_b64_tr_b16 v[184:185], v11 offset:1024\n\t" \
  "global_load_lds_dwordx4 v3, s[42:43] offset:2048\n\t" \
  "v_mfma_f32_16x16x32_bf16 v[40:43], v[156:159], v[200:203], v[40:43]\n\t" \
  "ds_read_b64_tr_b16 v[186:187], v11 offset:5120\n\t" \
  "v_mfma_f32_16x16x32_bf16 v[108:111], v[156:159], v[196:199], v[108:111]\n\t" \
  "global_load_lds_dwordx4 v[4:5], off offset:3072\n\t" \
  "v_mfma_f32_16x16x32_bf16 v[104:107], v[156:159], v[192:195], v[104:107]\n\t" \
  "ds_read_b128 v[156:159], v10 offset:5120\n\t" \
  "s_waitcnt lgkmcnt(10)\n\t" \
  "v_mfma_f32_16x16x32_bf16 v[112:115], v[160:163], v[192:195], v[112:115]\n\t" \
  "ds_read_b64_tr_b16 v[188:189], v12 offset:1024\n\t" \
  "s_add_u32 s40, s40, s78\n\t" \
  "s_addc_u32 s41, s41, s77\n\t" \
  "v_mfma_f32_16x16x32_bf16 v[116:119], v[160:163], v[196:199], v[116:119]\n\t" \
  "ds_read_b64_tr_b16 v[190:191], v12 offset:5120\n\t" \
  "s_add_u32 s42, s42, s76\n\t" \
  "s_addc_u32 s43, s43, s77\n\t" \
  "v_mfma_f32_16x16x32_bf16 v[48:51], v[160:163], v[200:203], v[48:51]\n\t" \
  "v_lshl_add_u64 v[4:5], v[4:5], 0, s[76:77]\n\t" \
  "v_mfma_f32_16x16x32_bf16 v[52:55], v[160:163], v[204:207], v[52:55]\n\t" \
  "ds_read_b128 v[160:163], v10 offset:8192\n\t" \
  "v_mfma_f32_16x16x32_bf16 v[60:63], v[164:167], v[204:207], v[60:63]\n\t" \
  "s_sub_u32 s59, 15, s66\n\t" \
  "s_cmp_lt_u32 %[w], 4\n\t" \
  "s_cselect_b32 s59, s59, s66\n\t" \
  "v_mfma_f32_16x16x32_bf16 v[56:59], v[164:167], v[200:203], v[56:59]\n\t" \
  "s_lshl_b32 s52, s59, 7\n\t" \
  "s_lshl_b32 s53, s63, 5\n\t" \
  "v_mfma_f32_16x16x32_bf16 v[124:127], v[164:167], v[196:199], v[124:127]\n\t" \
  "s_add_u32 s52, s52, s53\n\t" \
  "s_add_u32 s52, s52, 0x11000\n\t" \
  "v_mfma_f32_16x16x32_bf16 v[120:123], v[164:167], v[192:195], v[120:123]\n\t" \
  "ds_read_b128 v[164:167], v10 offset:9216\n\t" \
  "v_mfma_f32_16x16x32_bf16 v[128:131], v[168:171], v[192:195], v[128:131]\n\t" \
  "v_and_b32_e32 v13, 3, v0\n\t" \
  "v_lshlrev_b32_e32 v13, 3, v13\n\t" \
  "v_mfma_f32_16x16x32_bf16 v[132:135], v[168:171], v[196:199], v[132:135]\n\t" \
  "v_add_u32_e32 v13, s52, v13\n\t" \
  "global_load_dwordx2 v[14:15], v13, s[60:61] sc1\n\t" \
  "v_mfma_f32_16x16x32_bf16 v[64:67], v[168:171], v[200:203], v[64:67]\n\t" \
  "v_mfma_f32_16x16x32_bf16 v[68:71], v[168:171], v[204:207], v[68:71]\n\t" \
  "ds_read_b128 v[168:171], v10 offset:12288\n\t" \
  "v_mfma_f32_16x16x32_bf16 v[76:79], v[172:175], v[204:207], v[76:79]\n\t" \
  "v_mfma_f32_16x16x32_bf16 v[72:75], v[172:175], v[200:203], v[72:75]\n\t" \
  "v_mfma_f32_16x16x32_bf16 v[140:143], v[172:175], v[196:199], v[140:143]\n\t" \
  "v_mfma_f32_16x16x32_bf16 v[136:139], v[172:175], v[192:195], v[136:139]\n\t" \
  "ds_read_b128 v[172:175], v10 offset:13312\n\t" \
  "s_lshr_b32 s53, %[bid], 6\n\t" \
  "s_cmp_eq_u32 s53, 2\n\t" \
  "s_cbranch_scc1 HT2V_%=\n\t" \
  "HR2V_%=:\n\t" \
  "s_waitcnt lgkmcnt(4)\n\t" \
  "v_mfma_f32_16x16x32_bf16 v[80:83], v[144:147], v[176:179], v[80:83]\n\t" \
  "s_waitcnt vmcnt(8)\n\t" \
  "s_barrier\n\t" \
  "v_mfma_f32_16x16x32_bf16 v[84:87], v[144:147], v[180:183], v[84:87]\n\t" \
  "v_add_u32_e32 v10, 0x8000, v6\n\t" \
  "v_add_u32_e32 v11, 0x8000, v7\n\t" \
  "v_mfma_f32_16x16x32_bf16 v[16:19], v[144:147], v[184:187], v[16:19]\n\t" \
  "v_xor_b32_e32 v12, 32, v11\n\t" \
  "v_mfma_f32_16x16x32_bf16 v[20:23], v[144:147], v[188:191], v[20:23]\n\t" \
  "ds_read_b128 v[144:147], v10 offset:0\n\t" \
  "v_mfma_f32_16x16x32_bf16 v[28:31], v[148:151], v[188:191], v[28:31]\n\t" \
  "ds_read_b64_tr_b16 v[192:193], v11 offset:0\n\t" \
  "v_mfma_f32_16x16x32_bf16 v[24:27], v[148:151], v[184:187], v[24:27]\n\t" \
  "ds_read_b64_tr_b16 v[194:195], v11 offset:4096\n\t" \
  "v_mfma_f32_16x16x32_bf16 v[92:95], v[148:151], v[180:183], v[92:95]\n\t" \
  "s_add_u32 m0, s62, 0x20000\n\t" \
  "v_mfma_f32_16x16x32_bf16 v[88:91], v[148:151], v[176:179], v[88:91]\n\t" \
  "ds_read_b128 v[148:151], v10 offset:1024\n\t" \
  "v_mfma_f32_16x16x32_bf16 v[96:99], v[152:155], v[176:179], v[96:99]\n\t" \
  "ds_read_b64_tr_b16 v[196:197], v12 offset:0\n\t" \
  "global_load_lds_dwordx4 v1, s[40:41] offset:0 sc1\n\t" \
  "v_mfma_f32_16x16x32_bf16 v[100:103], v[152:155], v[180:183], v[100:103]\n\t" \
  "ds_read_b64_tr_b16 v[198:199], v12 offset:4096\n\t" \
  "v_mfma_f32_16x16x32_bf16 v[32:35], v[152:155], v[184:187], v[32:35]\n\t" \
  "global_load_lds_dwordx4 v2, s[40:41] offset:1024 sc1\n\t" \
  "v_mfma_f32_16x16x32_bf16 v[36:39], v[152:155], v[188:191], v[36:39]\n\t" \
  "ds_read_b128 v[152:155], v10 offset:4096\n\t" \
  "v_mfma_f32_16x16x32_bf16 v[44:47], v[156:159], v[188:191], v[44:47]\n\t" \
  "ds_read_b64_tr_b16 v[200:201], v11 offset:1024\n\t" \
  "global_load_lds_dwordx4 v3, s[42:43] offset:2048\n\t" \
  "v_mfma_f32_16x16x32_bf16 v[40:43], v[156:159], v[184:187], v[40:43]\n\t" \
  "ds_read_b64_tr_b16 v[202:203], v11 offset:5120\n\t" \
  "v_mfma_f32_16x16x32_bf16 v[108:111], v[156:159], v[180:183], v[108:111]\n\t" \
  "global_load_lds_dwordx4 v[4:5], off offset:3072\n\t" \
  "v_mfma_f32_16x16x32_bf16 v[104:107], v[156:159], v[176:179], v[104:107]\n\t" \
  "ds_read_b128 v[156:159], v10 offset:5120\n\t" \
  "s_waitcnt lgkmcnt(10)\n\t" \
  "v_mfma_f32_16x16x32_bf16 v[112:115], v[160:163], v[176:179], v[112:115]\n\t" \
  "ds_read_b64_tr_b16 v[204:205], v12 offset:1024\n\t" \
  "s_add_u32 s40, s40, s74\n\t" \
  "s_addc_u32 s41, s41, s73\n\t" \
  "v_mfma_f32_16x16x32_bf16 v[116:119], v[160:163], v[180:183], v[116:119]\n\t" \
  "ds_read_b64_tr_b16 v[206:207], v12 offset:5120\n\t" \
  "s_add_u32 s42, s42, s72\n\t" \
  "s_addc_u32 s43, s43, s73\n\t" \
  "v_mfma_f32_16x16x32_bf16 v[48:51], v[160:163], v[184:187], v[48:51]\n\t" \
  "v_lshl_add_u64 v[4:5], v[4:5], 0, s[72:73]\n\t" \
  "v_mfma_f32_16x16x32_bf16 v[52:55], v[160:163], v[188:191], v[52:55]\n\t" \
  "ds_read_b128 v[160:163], v10 offset:8192\n\t" \
  "v_mfma_f32_16x16x32_bf16 v[60:63], v[164:167], v[188:191], v[60:63]\n\t" \
  "v_mfma_f32_16x16x32_bf16 v[56:59], v[164:167], v[184:187], v[56:59]\n\t" \
  "v_mfma_f32_16x16x32_bf16 v[124:127], v[164:167], v[180:183], v[124:127]\n\t" \
  "v_mfma_f32_16x16x32_bf16 v[120:123], v[164:167], v[176:179], v[120:123]\n\t" \
  "ds_read_b128 v[164:167], v10 offset:9216\n\t" \
  "v_mfma_f32_16x16x32_bf16 v[128:131], v[168:171], v[176:179], v[128:131]\n\t" \
  "v_mfma_f32_16x16x32_bf16 v[132:135], v[168:171], v[180:183], v[132:135]\n\t" \
  "v_mfma_f32_16x16x32_bf16 v[64:67], v[168:171], v[184:187], v[64:67]\n\t" \
  "v_mfma_f32_16x16x32_bf16 v[68:71], v[168:171], v[188:191], v[68:71]\n\t" \
  "ds_read_b128 v[168:171], v10 offset:12288\n\t" \
  "v_mfma_f32_16x16x32_bf16 v[76:79], v[172:175], v[188:191], v[76:79]\n\t" \
  "v_mfma_f32_16x16x32_bf16 v[72:75], v[172:175], v[184:187], v[72:75]\n\t" \
  "v_mfma_f32_16x16x32_bf16 v[140:143], v[172:175], v[180:183], v[140:143]\n\t" \
  "v_mfma_f32_16x16x32_bf16 v[136:139], v[172:175], v[176:179], v[136:139]\n\t" \
  "ds_read_b128 v[172:175], v10 offset:13312\n\t" \
  "s_waitcnt lgkmcnt(4)\n\t" \
  "v_mfma_f32_16x16x32_bf16 v[80:83], v[144:147], v[192:195], v[80:83]\n\t" \
  "s_waitcnt vmcnt(8)\n\t" \
  "s_barrier\n\t" \
  "v_mfma_f32_16x16x32_bf16 v[84:87], v[144:147], v[196:199], v[84:87]\n\t" \
  "v_add_u32_e32 v10, 0x10000, v6\n\t" \
  "v_add_u32_e32 v11, 0x10000, v7\n\t" \
  "v_mfma_f32_16x16x32_bf16 v[16:19], v[144:147], v[200:203], v[16:19]\n\t" \
  "v_xor_b32_e32 v12, 32, v11\n\t" \
  "v_mfma_f32_16x16x32_bf16 v[20:23], v[144:147], v[204:207], v[20:23]\n\t" \
  "ds_read_b128 v[144:147], v10 offset:0\n\t" \
  "v_mfma_f32_16x16x32_bf16 v[28:31], v[148:151], v[204:207], v[28:31]\n\t" \
  "ds_read_b64_tr_b16 v[176:177], v11 offset:0\n\t" \
  "v_mfma_f32_16x16x32_bf16 v[24:27], v[148:151], v[200:203], v[24:27]\n\t" \
  "ds_read_b64_tr_b16 v[178:179], v11 offset:4096\n\t" \
  "v_mfma_f32_16x16x32_bf16 v[92:95], v[148:151], v[196:199], v[92:95]\n\t" \
  "s_add_u32 m0, s62, 0x0\n\t" \
  "v_mfma_f32_16x16x32_bf16 v[88:91], v[148:151], v[192:195], v[88:91]\n\t" \
  "ds_read_b128 v[148:151], v10 offset:1024\n\t" \
  "v_mfma_f32_16x16x32_bf16 v[96:99], v[152:155], v[192:195], v[96:99]\n\t" \
  "ds_read_b64_tr_b16 v[180:181], v12 offset:0\n\t" \
  "global_load_lds_dwordx4 v1, s[40:41] offset:0 sc1\n\t" \
  "v_mfma_f32_16x16x32_bf16 v[100:103], v[152:155], v[196:199], v[100:103]\n\t" \
  "ds_read_b64_tr_b16 v[182:183], v12 offset:4096\n\t" \
  "v_mfma_f32_16x16x32_bf16 v[32:35], v[152:155], v[200:203], v[32:35]\n\t" \
  "global_load_lds_dwordx4 v2, s[40:41] offset:1024 sc1\n\t" \
  "v_mfma_f32_16x16x32_bf16 v[36:39], v[152:155], v[204:207], v[36:39]\n\t" \
  "ds_read_b128 v[152:155], v10 offset:4096\n\t" \
  "v_mfma_f32_16x16x32_bf16 v[44:47], v[156:159], v[204:207], v[44:47]\n\t" \
  "ds_read_b64_tr_b16 v[184:185], v11 offset:1024\n\t" \
  "global_load_lds_dwordx4 v3, s[42:43] offset:2048\n\t" \
  "v_mfma_f32_16x16x32_bf16 v[40:43], v[156:159], v[200:203], v[40:43]\n\t" \
  "ds_read_b64_tr_b16 v[186:187], v11 offset:5120\n\t" \
  "v_mfma_f32_16x16x32_bf16 v[108:111], v[156:159], v[196:199], v[108:111]\n\t" \
  "global_load_lds_dwordx4 v[4:5], off offset:3072\n\t" \
  "v_mfma_f32_16x16x32_bf16 v[104:107], v[156:159], v[192:195], v[104:107]\n\t" \
  "ds_read_b128 v[156:159], v10 offset:5120\n\t" \
  "s_waitcnt lgkmcnt(10)\n\t" \
  "v_mfma_f32_16x16x32_bf16 v[112:115], v[160:163], v[192:195], v[112:115]\n\t" \
  "ds_read_b64_tr_b16 v[188:189], v12 offset:1024\n\t" \
  "s_add_u32 s40, s40, s46\n\t" \
  "s_addc_u32 s41, s41, s45\n\t" \
  "v_mfma_f32_16x16x32_bf16 v[116:119], v[160:163], v[196:199], v[116:119]\n\t" \
  "ds_read_b64_tr_b16 v[190:191], v12 offset:5120\n\t" \
  "s_add_u32 s42, s42, s44\n\t" \
  "s_addc_u32 s43, s43, s45\n\t" \
  "v_mfma_f32_16x16x32_bf16 v[48:51], v[160:163], v[200:203], v[48:51]\n\t" \
  "v_lshl_add_u64 v[4:5], v[4:5], 0, s[44:45]\n\t" \
  "v_mfma_f32_16x16x32_bf16 v[52:55], v[160:163], v[204:207], v[52:55]\n\t" \
  "ds_read_b128 v[160:163], v10 offset:8192\n\t" \
  "s_waitcnt vmcnt(8)\n\t" \
  "v_cmp_ne_u64_e32 vcc, s[36:37], v[14:15]\n\t" \
  "s_nop 0\n\t" \
  "s_cbranch_vccnz PSxV_%=\n\t" \
  "PRxV_%=:\n\t" \
  "s_sub_u32 s59, 15, s66\n\t" \
  "s_cmp_lt_u32 %[w], 4\n\t" \
  "s_cselect_b32 s59, s59, s66\n\t" \
  "v_mfma_f32_16x16x32_bf16 v[60:63], v[164:167], v[204:207], v[60:63]\n\t" \
  "s_lshl_b32 s52, s59, 7\n\t" \
  "s_lshl_b32 s53, s63, 5\n\t" \
  "s_add_u32 s52, s52, s53\n\t" \
  "s_lshl_b32 s52, s52, 10\n\t" \
  "v_mfma_f32_16x16x32_bf16 v[56:59], v[164:167], v[200:203], v[56:59]\n\t" \
  "s_mov_b64 s[40:41], %[ws]\n\t" \
  "s_add_u32 s40, s40, s52\n\t" \
  "s_addc_u32 s41, s41, 0\n\t" \
  "v_lshlrev_b32_e32 v1, 4, v0\n\t" \
  "v_add_u32_e32 v2, 0x3c00, v1\n\t" \
  "s_lshl_b32 s52, s65, 8\n\t" \
  "v_mfma_f32_16x16x32_bf16 v[124:127], v[164:167], v[196:199], v[124:127]\n\t" \
  "s_lshl_b32 s53, %[w], 15\n\t" \
  "s_add_u32 s52, s52, s53\n\t" \
  "s_mov_b64 s[42:43], %[wq]\n\t" \
  "s_add_u32 s42, s42, s52\n\t" \
  "s_addc_u32 s43, s43, 0\n\t" \
  "v_mfma_f32_16x16x32_bf16 v[120:123], v[164:167], v[192:195], v[120:123]\n\t" \
  "ds_read_b128 v[164:167], v10 offset:9216\n\t" \
  "s_sub_u32 s42, s42, 2048\n\t" \
  "s_subb_u32 s43, s43, 0\n\t" \
  "s_lshr_b32 s52, s65, 3\n\t" \
  "s_sub_u32 s59, s65, s52\n\t" \
  "s_and_b32 s52, s65, 7\n\t" \
  "s_cmp_eq_u32 s52, 7\n\t" \
  "s_cselect_b32 s59, 28, s59\n\t" \
  "v_mfma_f32_16x16x32_bf16 v[128:131], v[168:171], v[192:195], v[128:131]\n\t" \
  "s_min_u32 s52, s59, 27\n\t" \
  "s_lshl_b32 s52, s52, 7\n\t" \
  "s_mul_i32 s53, %[w], 14336\n\t" \
  "s_add_u32 s52, s52, s53\n\t" \
  "v_mfma_f32_16x16x32_bf16 v[132:135], v[168:171], v[196:199], v[132:135]\n\t" \
  "v_lshrrev_b32_e32 v13, 13, v3\n\t" \
  "v_mul_u32_u24_e32 v13, 3584, v13\n\t" \
  "v_bfe_u32 v14, v3, 6, 2\n\t" \
  "v_lshl_add_u32 v13, v14, 5, v13\n\t" \
  "v_and_b32_e32 v14, 16, v3\n\t" \
  "v_mfma_f32_16x16x32_bf16 v[64:67], v[168:171], v[200:203], v[64:67]\n\t" \
  "v_add3_u32 v13, v13, v14, s52\n\t" \
  "v_and_b32_e32 v14, 32, v3\n\t" \
  "v_cmp_ne_u32_e32 vcc, 0, v14\n\t" \
  "s_mov_b64 s[52:53], %[wg]\n\t" \
  "v_mfma_f32_16x16x32_bf16 v[68:71], v[168:171], v[204:207], v[68:71]\n\t" \
  "ds_read_b128 v[168:171], v10 offset:12288\n\t" \
  "s_sub_u32 s52, s52, 3072\n\t" \
  "s_subb_u32 s53, s53, 0\n\t" \
  "s_mov_b64 s[54:55], %[wu]\n\t" \
  "s_sub_u32 s54, s54, 3072\n\t" \
  "s_subb_u32 s55, s55, 0\n\t" \
  "v_mov_b32_e32 v4, s52\n\t" \
  "v_mov_b32_e32 v5, s53\n\t" \
  "v_mfma_f32_16x16x32_bf16 v[76:79], v[172:175], v[204:207], v[76:79]\n\t" \
  "v_mov_b32_e32 v14, s54\n\t" \
  "v_mov_b32_e32 v15, s55\n\t" \
  "v_cndmask_b32_e32 v4, v4, v14, vcc\n\t" \
  "v_cndmask_b32_e32 v5, v5, v15, vcc\n\t" \
  "v_mov_b32_e32 v14, v13\n\t" \
  "v_mov_b32_e32 v15, 0\n\t" \
  "v_mfma_f32_16x16x32_bf16 v[72:75], v[172:175], v[200:203], v[72:75]\n\t" \
  "v_lshl_add_u64 v[4:5], v[4:5], 0, v[14:15]\n\t" \
  "s_mov_b32 s44, 114688\n\t" \
  "s_mov_b32 s45, 0\n\t" \
  "s_cmp_lt_u32 s59, 28\n\t" \
  "s_cbranch_scc1 NZV_%=\n\t" \
  "s_lshl_b32 s52, %[bid], 3\n\t" \
  "s_add_u32 s52, s52, %[w]\n\t" \
  "s_lshl_b32 s52, s52, 7\n\t" \
  "s_add_u32 s52, s52, 0x6f00000\n\t" \
  "s_mov_b64 s[54:55], %[ws]\n\t" \
  "s_add_u32 s54, s54, s52\n\t" \
  "s_addc_u32 s55, s55, 0\n\t" \
  "s_sub_u32 s54, s54, 3072\n\t" \
  "s_subb_u32 s55, s55, 0\n\t" \
  "v_mov_b32_e32 v4, s54\n\t" \
  "v_mov_b32_e32 v5, s55\n\t" \
  "s_mov_b32 s44, 0\n\t" \
  "NZV_%=:\n\t" \
  "v_mfma_f32_16x16x32_bf16 v[140:143], v[172:175], v[196:199], v[140:143]\n\t" \
  "v_mfma_f32_16x16x32_bf16 v[136:139], v[172:175], v[192:195], v[136:139]\n\t" \
  "ds_read_b128 v[172:175], v10 offset:13312\n\t" \
  "s_lshr_b32 s53, %[bid], 6\n\t" \
  "s_cmp_eq_u32 s53, 3\n\t" \
  "s_cbranch_scc1 HT3V_%=\n\t" \
  "HR3V_%=:\n\t" \
  "s_waitcnt lgkmcnt(4)\n\t" \
  "v_mfma_f32_16x16x32_bf16 v[80:83], v[144:147], v[176:179], v[80:83]\n\t" \
  "s_waitcnt vmcnt(8)\n\t" \
  "s_barrier\n\t" \
  "v_mfma_f32_16x16x32_bf16 v[84:87], v[144:147], v[180:183], v[84:87]\n\t" \
  "v_add_u32_e32 v10, 0x18000, v6\n\t" \
  "v_add_u32_e32 v11, 0x18000, v7\n\t" \
  "v_mfma_f32_16x16x32_bf16 v[16:19], v[144:147], v[184:187], v[16:19]\n\t" \
  "v_xor_b32_e32 v12, 32, v11\n\t" \
  "v_mfma_f32_16x16x32_bf16 v[20:23], v[144:147], v[188:191], v[20:23]\n\t" \
  "ds_read_b128 v[144:147], v10 offset:0\n\t" \
  "v_mfma_f32_16x16x32_bf16 v[28:31], v[148:151], v[188:191], v[28:31]\n\t" \
  "ds_read_b64_tr_b16 v[192:193], v11 offset:0\n\t" \
  "v_mfma_f32_16x16x32_bf16 v[24:27], v[148:151], v[184:187], v[24:27]\n\t" \
  "ds_read_b64_tr_b16 v[194:195], v11 offset:4096\n\t" \
  "v_mfma_f32_16x16x32_bf16 v[92:95], v[148:151], v[180:183], v[92:95]\n\t" \
  "s_add_u32 m0, s62, 0x8000\n\t" \
  "v_mfma_f32_16x16x32_bf16 v[88:91], v[148:151], v[176:179], v[88:91]\n\t" \
  "ds_read_b128 v[148:151], v10 offset:1024\n\t" \
  "v_mfma_f32_16x16x32_bf16 v[96:99], v[152:155], v[176:179], v[96:99]\n\t" \
  "ds_read_b64_tr_b16 v[196:197], v12 offset:0\n\t" \
  "global_load_lds_dwordx4 v1, s[40:41] offset:0 sc1\n\t" \
  "v_mfma_f32_16x16x32_bf16 v[100:103], v[152:155], v[180:183], v[100:103]\n\t" \
  "ds_read_b64_tr_b16 v[198:199], v12 offset:4096\n\t" \
  "v_mfma_f32_16x16x32_bf16 v[32:35], v[152:155], v[184:187], v[32:35]\n\t" \
  "global_load_lds_dwordx4 v2, s[40:41] offset:1024 sc1\n\t" \
  "v_mfma_f32_16x16x32_bf16 v[36:39], v[152:155], v[188:191], v[36:39]\n\t" \
  "ds_read_b128 v[152:155], v10 offset:4096\n\t" \
  "v_mfma_f32_16x16x32_bf16 v[44:47], v[156:159], v[188:191], v[44:47]\n\t" \
  "ds_read_b64_tr_b16 v[200:201], v11 offset:1024\n\t" \
  "global_load_lds_dwordx4 v3, s[42:43] offset:2048\n\t" \
  "v_mfma_f32_16x16x32_bf16 v[40:43], v[156:159], v[184:187], v[40:43]\n\t" \
  "ds_read_b64_tr_b16 v[202:203], v11 offset:5120\n\t" \
  "v_mfma_f32_16x16x32_bf16 v[108:111], v[156:159], v[180:183], v[108:111]\n\t" \
  "global_load_lds_dwordx4 v[4:5], off offset:3072\n\t" \
  "v_mfma_f32_16x16x32_bf16 v[104:107], v[156:159], v[176:179], v[104:107]\n\t" \
  "ds_read_b128 v[156:159], v10 offset:5120\n\t" \
  "s_waitcnt lgkmcnt(10)\n\t" \
  "v_mfma_f32_16x16x32_bf16 v[112:115], v[160:163], v[176:179], v[112:115]\n\t" \
  "ds_read_b64_tr_b16 v[204:205], v12 offset:1024\n\t" \
  "s_add_u32 s40, s40, 1024\n\t" \
  "s_addc_u32 s41, s41, 0\n\t" \
  "v_mfma_f32_16x16x32_bf16 v[116:119], v[160:163], v[180:183], v[116:119]\n\t" \
  "ds_read_b64_tr_b16 v[206:207], v12 offset:5120\n\t" \
  "s_add_u32 s42, s42, 0x40000\n\t" \
  "s_addc_u32 s43, s43, 0\n\t" \
  "v_mfma_f32_16x16x32_bf16 v[48:51], v[160:163], v[184:187], v[48:51]\n\t" \
  "v_lshl_add_u64 v[4:5], v[4:5], 0, s[44:45]\n\t" \
  "v_mfma_f32_16x16x32_bf16 v[52:55], v[160:163], v[188:191], v[52:55]\n\t" \
  "ds_read_b128 v[160:163], v10 offset:8192\n\t" \
  "v_mfma_f32_16x16x32_bf16 v[60:63], v[164:167], v[188:191], v[60:63]\n\t" \
  "v_mfma_f32_16x16x32_bf16 v[56:59], v[164:167], v[184:187], v[56:59]\n\t" \
  "v_mfma_f32_16x16x32_bf16 v[124:127], v[164:167], v[180:183], v[124:127]\n\t" \
  "v_mfma_f32_16x16x32_bf16 v[120:123], v[164:167], v[176:179], v[120:123]\n\t" \
  "ds_read_b128 v[164:167], v10 offset:9216\n\t" \
  "v_mfma_f32_16x16x32_bf16 v[128:131], v[168:171], v[176:179], v[128:131]\n\t" \
  "v_mfma_f32_16x16x32_bf16 v[132:135], v[168:171], v[180:183], v[132:135]\n\t" \
  "v_mfma_f32_16x16x32_bf16 v[64:67], v[168:171], v[184:187], v[64:67]\n\t" \
  "v_mfma_f32_16x16x32_bf16 v[68:71], v[168:171], v[188:191], v[68:71]\n\t" \
  "ds_read_b128 v[168:171], v10 offset:12288\n\t" \
  "v_mfma_f32_16x16x32_bf16 v[76:79], v[172:175], v[188:191], v[76:79]\n\t" \
  "v_mfma_f32_16x16x32_bf16 v[72:75], v[172:175], v[184:187], v[72:75]\n\t" \
  "v_mfma_f32_16x16x32_bf16 v[140:143], v[172:175], v[180:183], v[140:143]\n\t" \
  "v_mfma_f32_16x16x32_bf16 v[136:139], v[172:175], v[176:179], v[136:139]\n\t" \
  "ds_read_b128 v[172:175], v10 offset:13312\n\t" \
  "s_waitcnt lgkmcnt(4)\n\t" \
  "v_mfma_f32_16x16x32_bf16 v[80:83], v[144:147], v[192:195], v[80:83]\n\t" \
  "s_waitcnt vmcnt(8)\n\t" \
  "s_barrier\n\t" \
  "v_mfma_f32_16x16x32_bf16 v[84:87], v[144:147], v[196:199], v[84:87]\n\t" \
  "v_add_u32_e32 v10, 0x20000, v6\n\t" \
  "v_add_u32_e32 v11, 0x20000, v7\n\t" \
  "v_mfma_f32_16x16x32_bf16 v[16:19], v[144:147], v[200:203], v[16:19]\n\t" \
  "v_xor_b32_e32 v12, 32, v11\n\t" \
  "v_mfma_f32_16x16x32_bf16 v[20:23], v[144:147], v[204:207], v[20:23]\n\t" \
  "ds_read_b128 v[144:147], v10 offset:0\n\t" \
  "v_mfma_f32_16x16x32_bf16 v[28:31], v[148:151], v[204:207], v[28:31]\n\t" \
  "ds_read_b64_tr_b16 v[176:177], v11 offset:0\n\t" \
  "v_mfma_f32_16x16x32_bf16 v[24:27], v[148:151], v[200:203], v[24:27]\n\t" \
  "ds_read_b64_tr_b16 v[178:179], v11 offset:4096\n\t" \
  "v_mfma_f32_16x16x32_bf16 v[92:95], v[148:151], v[196:199], v[92:95]\n\t" \
  "s_add_u32 m0, s62, 0x10000\n\t" \
  "v_mfma_f32_16x16x32_bf16 v[88:91], v[148:151], v[192:195], v[88:91]\n\t" \
  "ds_read_b128 v[148:151], v10 offset:1024\n\t" \
  "v_mfma_f32_16x16x32_bf16 v[96:99], v[152:155], v[192:195], v[96:99]\n\t" \
  "ds_read_b64_tr_b16 v[180:181], v12 offset:0\n\t" \
  "global_load_lds_dwordx4 v1, s[40:41] offset:0 sc1\n\t" \
  "v_mfma_f32_16x16x32_bf16 v[100:103], v[152:155], v[196:199], v[100:103]\n\t" \
  "ds_read_b64_tr_b16 v[182:183], v12 offset:4096\n\t" \
  "v_mfma_f32_16x16x32_bf16 v[32:35], v[152:155], v[200:203], v[32:35]\n\t" \
  "global_load_lds_dwordx4 v2, s[40:41] offset:1024 sc1\n\t" \
  "v_mfma_f32_16x16x32_bf16 v[36:39], v[152:155], v[204:207], v[36:39]\n\t" \
  "ds_read_b128 v[152:155], v10 offset:4096\n\t" \
  "v_mfma_f32_16x16x32_bf16 v[44:47], v[156:159], v[204:207], v[44:47]\n\t" \
  "ds_read_b64_tr_b16 v[184:185], v11 offset:1024\n\t" \
  "global_load_lds_dwordx4 v3, s[42:43] offset:2048\n\t" \
  "v_mfma_f32_16x16x32_bf16 v[40:43], v[156:159], v[200:203], v[40:43]\n\t" \
  "ds_read_b64_tr_b16 v[186:187], v11 offset:5120\n\t" \
  "v_mfma_f32_16x16x32_bf16 v[108:111], v[156:159], v[196:199], v[108:111]\n\t" \
  "global_load_lds_dwordx4 v[4:5], off offset:3072\n\t" \
  "v_mfma_f32_16x16x32_bf16 v[104:107], v[156:159], v[192:195], v[104:107]\n\t" \
  "ds_read_b128 v[156:159], v10 offset:5120\n\t" \
  "s_waitcnt lgkmcnt(10)\n\t" \
  "v_mfma_f32_16x16x32_bf16 v[112:115], v[160:163], v[192:195], v[112:115]\n\t" \
  "ds_read_b64_tr_b16 v[188:189], v12 offset:1024\n\t" \
  "s_add_u32 s40, s40, 1024\n\t" \
  "s_addc_u32 s41, s41, 0\n\t" \
  "v_mfma_f32_16x16x32_bf16 v[116:119], v[160:163], v[196:199], v[116:119]\n\t" \
  "ds_read_b64_tr_b16 v[190:191], v12 offset:5120\n\t" \
  "s_add_u32 s42, s42, 0x40000\n\t" \
  "s_addc_u32 s43, s43, 0\n\t" \
  "v_mfma_f32_16x16x32_bf16 v[48:51], v[160:163], v[200:203], v[48:51]\n\t" \
  "v_lshl_add_u64 v[4:5], v[4:5], 0, s[44:45]\n\t" \
  "v_mfma_f32_16x16x32_bf16 v[52:55], v[160:163], v[204:207], v[52:55]\n\t" \
  "ds_read_b128 v[160:163], v10 offset:8192\n\t" \
  "v_mfma_f32_16x16x32_bf16 v[60:63], v[164:167], v[204:207], v[60:63]\n\t" \
  "v_mfma_f32_16x16x32_bf16 v[56:59], v[164:167], v[200:203], v[56:59]\n\t" \
  "v_mfma_f32_16x16x32_bf16 v[124:127], v[164:167], v[196:199], v[124:127]\n\t" \
  "v_mfma_f32_16x16x32_bf16 v[120:123], v[164:167], v[192:195], v[120:123]\n\t" \
  "ds_read_b128 v[164:167], v10 offset:9216\n\t" \
  "v_mfma_f32_16x16x32_bf16 v[128:131], v[168:171], v[192:195], v[128:131]\n\t" \
  "v_mfma_f32_16x16x32_bf16 v[132:135], v[168:171], v[196:199], v[132:135]\n\t" \
  "v_mfma_f32_16x16x32_bf16 v[64:67], v[168:171], v[200:203], v[64:67]\n\t" \
  "v_mfma_f32_16x16x32_bf16 v[68:71], v[168:171], v[204:207], v[68:71]\n\t" \
  "ds_read_b128 v[168:171], v10 offset:12288\n\t" \
  "v_mfma_f32_16x16x32_bf16 v[76:79], v[172:175], v[204:207], v[76:79]\n\t" \
  "v_mfma_f32_16x16x32_bf16 v[72:75], v[172:175], v[200:203], v[72:75]\n\t" \
  "v_mfma_f32_16x16x32_bf16 v[140:143], v[172:175], v[196:199], v[140:143]\n\t" \
  "v_mfma_f32_16x16x32_bf16 v[136:139], v[172:175], v[192:195], v[136:139]\n\t" \
  "ds_read_b128 v[172:175], v10 offset:13312\n\t" \
  "s_waitcnt lgkmcnt(4)\n\t" \
  "v_mfma_f32_16x16x32_bf16 v[80:83], v[144:147], v[176:179], v[80:83]\n\t" \
  "s_waitcnt vmcnt(8)\n\t" \
  "s_barrier\n\t" \
  "v_mfma_f32_16x16x32_bf16 v[84:87], v[144:147], v[180:183], v[84:87]\n\t" \
  "v_add_u32_e32 v10, 0x0, v6\n\t" \
  "v_add_u32_e32 v11, 0x0, v7\n\t" \
  "v_mfma_f32_16x16x32_bf16 v[16:19], v[144:147], v[184:187], v[16:19]\n\t" \
  "v_xor_b32_e32 v12, 32, v11\n\t" \
  "v_mfma_f32_16x16x32_bf16 v[20:23], v[144:147], v[188:191], v[20:23]\n\t" \
  "ds_read_b128 v[144:147], v10 offset:0\n\t" \
  "v_mfma_f32_16x16x32_bf16 v[28:31], v[148:151], v[188:191], v[28:31]\n\t" \
  "ds_read_b64_tr_b16 v[192:193], v11 offset:0\n\t" \
  "v_mfma_f32_16x16x32_bf16 v[24:27], v[148:151], v[184:187], v[24:27]\n\t" \
  "ds_read_b64_tr_b16 v[194:195], v11 offset:4096\n\t" \
  "v_mfma_f32_16x16x32_bf16 v[92:95], v[148:151], v[180:183], v[92:95]\n\t" \
  "s_add_u32 m0, s62, 0x18000\n\t" \
  "v_mfma_f32_16x16x32_bf16 v[88:91], v[148:151], v[176:179], v[88:91]\n\t" \
  "ds_read_b128 v[148:151], v10 offset:1024\n\t" \
  "v_mfma_f32_16x16x32_bf16 v[96:99], v[152:155], v[176:179], v[96:99]\n\t" \
  "ds_read_b64_tr_b16 v[196:197], v12 offset:0\n\t" \
  "global_load_lds_dwordx4 v1, s[40:41] offset:0 sc1\n\t" \
  "v_mfma_f32_16x16x32_bf16 v[100:103], v[152:155], v[180:183], v[100:103]\n\t" \
  "ds_read_b64_tr_b16 v[198:199], v12 offset:4096\n\t" \
  "v_mfma_f32_16x16x32_bf16 v[32:35], v[152:155], v[184:187], v[32:35]\n\t" \
  "global_load_lds_dwordx4 v2, s[40:41] offset:1024 sc1\n\t" \
  "v_mfma_f32_16x16x32_bf16 v[36:39], v[152:155], v[188:191], v[36:39]\n\t" \
  "ds_read_b128 v[152:155], v10 offset:4096\n\t" \
  "v_mfma_f32_16x16x32_bf16 v[44:47], v[156:159], v[188:191], v[44:47]\n\t" \
  "ds_read_b64_tr_b16 v[200:201], v11 offset:1024\n\t" \
  "global_load_lds_dwordx4 v3, s[42:43] offset:2048\n\t" \
  "v_mfma_f32_16x16x32_bf16 v[40:43], v[156:159], v[184:187], v[40:43]\n\t" \
  "ds_read_b64_tr_b16 v[202:203], v11 offset:5120\n\t" \
  "v_mfma_f32_16x16x32_bf16 v[108:111], v[156:159], v[180:183], v[108:111]\n\t" \
  "global_load_lds_dwordx4 v[4:5], off offset:3072\n\t" \
  "v_mfma_f32_16x16x32_bf16 v[104:107], v[156:159], v[176:179], v[104:107]\n\t" \
  "ds_read_b128 v[156:159], v10 offset:5120\n\t" \
  "s_waitcnt lgkmcnt(10)\n\t" \
  "v_mfma_f32_16x16x32_bf16 v[112:115], v[160:163], v[176:179], v[112:115]\n\t" \
  "ds_read_b64_tr_b16 v[204:205], v12 offset:1024\n\t" \
  "s_add_u32 s40, s40, 1024\n\t" \
  "s_addc_u32 s41, s41, 0\n\t" \
  "v_mfma_f32_16x16x32_bf16 v[116:119], v[160:163], v[180:183], v[116:119]\n\t" \
  "ds_read_b64_tr_b16 v[206:207], v12 offset:5120\n\t" \
  "s_add_u32 s42, s42, 0x40000\n\t" \
  "s_addc_u32 s43, s43, 0\n\t" \
  "v_mfma_f32_16x16x32_bf16 v[48:51], v[160:163], v[184:187], v[48:51]\n\t" \
  "v_lshl_add_u64 v[4:5], v[4:5], 0, s[44:45]\n\t" \
  "v_mfma_f32_16x16x32_bf16 v[52:55], v[160:163], v[188:191], v[52:55]\n\t" \
  "ds_read_b128 v[160:163], v10 offset:8192\n\t" \
  "v_mfma_f32_16x16x32_bf16 v[60:63], v[164:167], v[188:191], v[60:63]\n\t" \
  "v_mfma_f32_16x16x32_bf16 v[56:59], v[164:167], v[184:187], v[56:59]\n\t" \
  "v_mfma_f32_16x16x32_bf16 v[124:127], v[164:167], v[180:183], v[124:127]\n\t" \
  "v_mfma_f32_16x16x32_bf16 v[120:123], v[164:167], v[176:179], v[120:123]\n\t" \
  "ds_read_b128 v[164:167], v10 offset:9216\n\t" \
  "v_mfma_f32_16x16x32_bf16 v[128:131], v[168:171], v[176:179], v[128:131]\n\t" \
  "v_mfma_f32_16x16x32_bf16 v[132:135], v[168:171], v[180:183], v[132:135]\n\t" \
  "v_mfma_f32_16x16x32_bf16 v[64:67], v[168:171], v[184:187], v[64:67]\n\t" \
  "v_mfma_f32_16x16x32_bf16 v[68:71], v[168:171], v[188:191], v[68:71]\n\t" \
  "ds_read_b128 v[168:171], v10 offset:12288\n\t" \
  "v_mfma_f32_16x16x32_bf16 v[76:79], v[172:175], v[188:191], v[76:79]\n\t" \
  "v_mfma_f32_16x16x32_bf16 v[72:75], v[172:175], v[184:187], v[72:75]\n\t" \
  "v_mfma_f32_16x16x32_bf16 v[140:143], v[172:175], v[180:183], v[140:143]\n\t" \
  "v_mfma_f32_16x16x32_bf16 v[136:139], v[172:175], v[176:179], v[136:139]\n\t" \
  "ds_read_b128 v[172:175], v10 offset:13312\n\t" \
  "s_waitcnt lgkmcnt(4)\n\t" \
  "v_mfma_f32_16x16x32_bf16 v[80:83], v[144:147], v[192:195], v[80:83]\n\t" \
  "s_waitcnt vmcnt(8)\n\t" \
  "s_barrier\n\t" \
  "v_mfma_f32_16x16x32_bf16 v[84:87], v[144:147], v[196:199], v[84:87]\n\t" \
  "v_add_u32_e32 v10, 0x8000, v6\n\t" \
  "v_add_u32_e32 v11, 0x8000, v7\n\t" \
  "v_mfma_f32_16x16x32_bf16 v[16:19], v[144:147], v[200:203], v[16:19]\n\t" \
  "v_xor_b32_e32 v12, 32, v11\n\t" \
  "v_mfma_f32_16x16x32_bf16 v[20:23], v[144:147], v[204:207], v[20:23]\n\t" \
  "ds_read_b128 v[144:147], v10 offset:0\n\t" \
  "v_mfma_f32_16x16x32_bf16 v[28:31], v[148:151], v[204:207], v[28:31]\n\t" \
  "ds_read_b64_tr_b16 v[176:177], v11 offset:0\n\t" \
  "v_mfma_f32_16x16x32_bf16 v[24:27], v[148:151], v[200:203], v[24:27]\n\t" \
  "ds_read_b64_tr_b16 v[178:179], v11 offset:4096\n\t" \
  "v_mfma_f32_16x16x32_bf16 v[92:95], v[148:151], v[196:199], v[92:95]\n\t" \
  "s_add_u32 m0, s62, 0x20000\n\t" \
  "v_mfma_f32_16x16x32_bf16 v[88:91], v[148:151], v[192:195], v[88:91]\n\t" \
  "ds_read_b128 v[148:151], v10 offset:1024\n\t" \
  "v_mfma_f32_16x16x32_bf16 v[96:99], v[152:155], v[192:195], v[96:99]\n\t" \
  "ds_read_b64_tr_b16 v[180:181], v12 offset:0\n\t" \
  "global_load_lds_dwordx4 v1, s[40:41] offset:0 sc1\n\t" \
  "v_mfma_f32_16x16x32_bf16 v[100:103], v[152:155], v[196:199], v[100:103]\n\t" \
  "ds_read_b64_tr_b16 v[182:183], v12 offset:4096\n\t" \
  "v_mfma_f32_16x16x32_bf16 v[32:35], v[152:155], v[200:203], v[32:35]\n\t" \
  "global_load_lds_dwordx4 v2, s[40:41] offset:1024 sc1\n\t" \
  "v_mfma_f32_16x16x32_bf16 v[36:39], v[152:155], v[204:207], v[36:39]\n\t" \
  "ds_read_b128 v[152:155], v10 offset:4096\n\t" \
  "v_mfma_f32_16x16x32_bf16 v[44:47], v[156:159], v[204:207], v[44:47]\n\t" \
  "ds_read_b64_tr_b16 v[184:185], v11 offset:1024\n\t" \
  "global_load_lds_dwordx4 v3, s[42:43] offset:2048\n\t" \
  "v_mfma_f32_16x16x32_bf16 v[40:43], v[156:159], v[200:203], v[40:43]\n\t" \
  "ds_read_b64_tr_b16 v[186:187], v11 offset:5120\n\t" \
  "v_mfma_f32_16x16x32_bf16 v[108:111], v[156:159], v[196:199], v[108:111]\n\t" \
  "global_load_lds_dwordx4 v[4:5], off offset:3072\n\t" \
  "v_mfma_f32_16x16x32_bf16 v[104:107], v[156:159], v[192:195], v[104:107]\n\t" \
  "ds_read_b128 v[156:159], v10 offset:5120\n\t" \
  "s_waitcnt lgkmcnt(10)\n\t" \
  "v_mfma_f32_16x16x32_bf16 v[112:115], v[160:163], v[192:195], v[112:115]\n\t" \
  "ds_read_b64_tr_b16 v[188:189], v12 offset:1024\n\t" \
  "s_add_u32 s40, s40, 1024\n\t" \
  "s_addc_u32 s41, s41, 0\n\t" \
  "v_mfma_f32_16x16x32_bf16 v[116:119], v[160:163], v[196:199], v[116:119]\n\t" \
  "ds_read_b64_tr_b16 v[190:191], v12 offset:5120\n\t" \
  "s_add_u32 s42, s42, 0x40000\n\t" \
  "s_addc_u32 s43, s43, 0\n\t" \
  "v_mfma_f32_16x16x32_bf16 v[48:51], v[160:163], v[200:203], v[48:51]\n\t" \
  "v_lshl_add_u64 v[4:5], v[4:5], 0, s[44:45]\n\t" \
  "v_mfma_f32_16x16x32_bf16 v[52:55], v[160:163], v[204:207], v[52:55]\n\t" \
  "ds_read_b128 v[160:163], v10 offset:8192\n\t" \
  "v_mfma_f32_16x16x32_bf16 v[60:63], v[164:167], v[204:207], v[60:63]\n\t" \
  "v_mfma_f32_16x16x32_bf16 v[56:59], v[164:167], v[200:203], v[56:59]\n\t" \
  "v_mfma_f32_16x16x32_bf16 v[124:127], v[164:167], v[196:199], v[124:127]\n\t" \
  "v_mfma_f32_16x16x32_bf16 v[120:123], v[164:167], v[192:195], v[120:123]\n\t" \
  "ds_read_b128 v[164:167], v10 offset:9216\n\t" \
  "v_mfma_f32_16x16x32_bf16 v[128:131], v[168:171], v[192:195], v[128:131]\n\t" \
  "v_mfma_f32_16x16x32_bf16 v[132:135], v[168:171], v[196:199], v[132:135]\n\t" \
  "v_mfma_f32_16x16x32_bf16 v[64:67], v[168:171], v[200:203], v[64:67]\n\t" \
  "v_mfma_f32_16x16x32_bf16 v[68:71], v[168:171], v[204:207], v[68:71]\n\t" \
  "ds_read_b128 v[168:171], v10 offset:12288\n\t" \
  "v_mfma_f32_16x16x32_bf16 v[76:79], v[172:175], v[204:207], v[76:79]\n\t" \
  "v_mfma_f32_16x16x32_bf16 v[72:75], v[172:175], v[200:203], v[72:75]\n\t" \
  "v_mfma_f32_16x16x32_bf16 v[140:143], v[172:175], v[196:199], v[140:143]\n\t" \
  "v_mfma_f32_16x16x32_bf16 v[136:139], v[172:175], v[192:195], v[136:139]\n\t" \
  "ds_read_b128 v[172:175], v10 offset:13312\n\t" \
  "s_waitcnt lgkmcnt(4)\n\t" \
  "v_cvt_pk_bf16_f32 v208, v80, v81\n\t" \
  "v_cvt_pk_bf16_f32 v209, v82, v83\n\t" \
  "v_cvt_pk_bf16_f32 v210, v88, v89\n\t" \
  "v_cvt_pk_bf16_f32 v211, v90, v91\n\t" \
  "v_cvt_pk_bf16_f32 v212, v16, v17\n\t" \
  "v_cvt_pk_bf16_f32 v213, v18, v19\n\t" \
  "v_cvt_pk_bf16_f32 v214, v24, v25\n\t" \
  "v_cvt_pk_bf16_f32 v215, v26, v27\n\t" \
  "v_cvt_pk_bf16_f32 v216, v84, v85\n\t" \
  "v_cvt_pk_bf16_f32 v217, v86, v87\n\t" \
  "v_cvt_pk_bf16_f32 v218, v92, v93\n\t" \
  "v_cvt_pk_bf16_f32 v219, v94, v95\n\t" \
  "v_cvt_pk_bf16_f32 v220, v20, v21\n\t" \
  "v_cvt_pk_bf16_f32 v221, v22, v23\n\t" \
  "v_cvt_pk_bf16_f32 v222, v28, v29\n\t" \
  "v_cvt_pk_bf16_f32 v223, v30, v31\n\t" \
  "v_mfma_f32_16x16x32_bf16 v[80:83], v[176:179], v[144:147], 0\n\t" \
  "s_waitcnt vmcnt(8)\n\t" \
  "s_barrier\n\t" \
  "v_mfma_f32_16x16x32_bf16 v[84:87], v[180:183], v[144:147], 0\n\t" \
  "v_add_u32_e32 v10, 0x10000, v6\n\t" \
  "v_add_u32_e32 v11, 0x10000, v7\n\t" \
  "v_mfma_f32_16x16x32_bf16 v[16:19], v[184:187], v[144:147], 0\n\t" \
  "v_xor_b32_e32 v12, 32, v11\n\t" \
  "v_mfma_f32_16x16x32_bf16 v[20:23], v[188:191], v[144:147], 0\n\t" \
  "ds_read_b128 v[144:147], v10 offset:0\n\t" \
  "v_mfma_f32_16x16x32_bf16 v[28:31], v[188:191], v[148:151], 0\n\t" \
  "ds_read_b64_tr_b16 v[192:193], v11 offset:0\n\t" \
  "v_mfma_f32_16x16x32_bf16 v[24:27], v[184:187], v[148:151], 0\n\t" \
  "ds_read_b64_tr_b16 v[194:195], v11 offset:4096\n\t" \
  "v_mfma_f32_16x16x32_bf16 v[92:95], v[180:183], v[148:151], 0\n\t" \
  "s_add_u32 m0, s62, 0x0\n\t" \
  "v_mfma_f32_16x16x32_bf16 v[88:91], v[176:179], v[148:151], 0\n\t" \
  "ds_read_b128 v[148:151], v10 offset:1024\n\t" \
  "v_add_u32_e32 v13, 0x0, v8\n\t" \
  "global_store_dwordx4 v13, v[208:211], s[50:51] sc1\n\t" \
  "v_cvt_pk_bf16_f32 v224, v96, v97\n\t" \
  "v_cvt_pk_bf16_f32 v225, v98, v99\n\t" \
  "v_cvt_pk_bf16_f32 v226, v104, v105\n\t" \
  "v_cvt_pk_bf16_f32 v227, v106, v107\n\t" \
  "v_cvt_pk_bf16_f32 v228, v32, v33\n\t" \
  "v_cvt_pk_bf16_f32 v229, v34, v35\n\t" \
  "v_cvt_pk_bf16_f32 v230, v40, v41\n\t" \
  "v_cvt_pk_bf16_f32 v231, v42, v43\n\t" \
  "v_cvt_pk_bf16_f32 v232, v100, v101\n\t" \
  "v_cvt_pk_bf16_f32 v233, v102, v103\n\t" \
  "v_cvt_pk_bf16_f32 v234, v108, v109\n\t" \
  "v_cvt_pk_bf16_f32 v235, v110, v111\n\t" \
  "v_cvt_pk_bf16_f32 v236, v36, v37\n\t" \
  "v_cvt_pk_bf16_f32 v237, v38, v39\n\t" \
  "v_cvt_pk_bf16_f32 v238, v44, v45\n\t" \
  "v_cvt_pk_bf16_f32 v239, v46, v47\n\t" \
  "v_mfma_f32_16x16x32_bf16 v[96:99], v[176:179], v[152:155], 0\n\t" \
  "ds_read_b64_tr_b16 v[196:197], v12 offset:0\n\t" \
  "global_load_lds_dwordx4 v1, s[40:41] offset:0 sc1\n\t" \
  "v_mfma_f32_16x16x32_bf16 v[100:103], v[180:183], v[152:155], 0\n\t" \
  "ds_read_b64_tr_b16 v[198:199], v12 offset:4096\n\t" \
  "v_mfma_f32_16x16x32_bf16 v[32:35], v[184:187], v[152:155], 0\n\t" \
  "global_load_lds_dwordx4 v2, s[40:41] offset:1024 sc1\n\t" \
  "v_mfma_f32_16x16x32_bf16 v[36:39], v[188:191], v[152:155], 0\n\t" \
  "ds_read_b128 v[152:155], v10 offset:4096\n\t" \
  "v_add_u32_e32 v13, 0x110000, v8\n\t" \
  "global_store_dwordx4 v13, v[212:215], s[50:51] sc1\n\t" \
  "v_mfma_f32_16x16x32_bf16 v[44:47], v[188:191], v[156:159], 0\n\t" \
  "ds_read_b64_tr_b16 v[200:201], v11 offset:1024\n\t" \
  "global_load_lds_dwordx4 v3, s[42:43] offset:2048\n\t" \
  "v_mfma_f32_16x16x32_bf16 v[40:43], v[184:187], v[156:159], 0\n\t" \
  "ds_read_b64_tr_b16 v[202:203], v11 offset:5120\n\t" \
  "v_mfma_f32_16x16x32_bf16 v[108:111], v[180:183], v[156:159], 0\n\t" \
  "global_load_lds_dwordx4 v[4:5], off offset:3072\n\t" \
  "v_mfma_f32_16x16x32_bf16 v[104:107], v[176:179], v[156:159], 0\n\t" \
  "ds_read_b128 v[156:159], v10 offset:5120\n\t" \
  "s_waitcnt lgkmcnt(10)\n\t" \
  "v_cvt_pk_bf16_f32 v240, v112, v113\n\t" \
  "v_cvt_pk_bf16_f32 v241, v114, v115\n\t" \
  "v_cvt_pk_bf16_f32 v242, v120, v121\n\t" \
  "v_cvt_pk_bf16_f32 v243, v122, v123\n\t" \
  "v_cvt_pk_bf16_f32 v244, v48, v49\n\t" \
  "v_cvt_pk_bf16_f32 v245, v50, v51\n\t" \
  "v_cvt_pk_bf16_f32 v246, v56, v57\n\t" \
  "v_cvt_pk_bf16_f32 v247, v58, v59\n\t" \
  "v_cvt_pk_bf16_f32 v248, v116, v117\n\t" \
  "v_cvt_pk_bf16_f32 v249, v118, v119\n\t" \
  "v_cvt_pk_bf16_f32 v250, v124, v125\n\t" \
  "v_cvt_pk_bf16_f32 v251, v126, v127\n\t" \
  "v_cvt_pk_bf16_f32 v252, v52, v53\n\t" \
  "v_cvt_pk_bf16_f32 v253, v54, v55\n\t" \
  "v_cvt_pk_bf16_f32 v254, v60, v61\n\t" \
  "v_cvt_pk_bf16_f32 v255, v62, v63\n\t" \
  "v_mfma_f32_16x16x32_bf16 v[112:115], v[176:179], v[160:163], 0\n\t" \
  "ds_read_b64_tr_b16 v[204:205], v12 offset:1024\n\t" \
  "s_add_u32 s40, s40, 1024\n\t" \
  "s_addc_u32 s41, s41, 0\n\t" \
  "v_mfma_f32_16x16x32_bf16 v[116:119], v[180:183], v[160:163], 0\n\t" \
  "ds_read_b64_tr_b16 v[206:207], v12 offset:5120\n\t" \
  "s_add_u32 s42, s42, 0x40000\n\t" \
  "s_addc_u32 s43, s43, 0\n\t" \
  "v_mfma_f32_16x16x32_bf16 v[48:51], v[184:187], v[160:163], 0\n\t" \
  "v_lshl_add_u64 v[4:5], v[4:5], 0, s[44:45]\n\t" \
  "v_mfma_f32_16x16x32_bf16 v[52:55], v[188:191], v[160:163], 0\n\t" \
  "ds_read_b128 v[160:163], v10 offset:8192\n\t" \
  "v_add_u32_e32 v13, 0x0, v9\n\t" \
  "global_store_dwordx4 v13, v[216:219], s[50:51] sc1\n\t" \
  "v_mfma_f32_16x16x32_bf16 v[60:63], v[188:191], v[164:167], 0\n\t" \
  "v_mfma_f32_16x16x32_bf16 v[56:59], v[184:187], v[164:167], 0\n\t" \
  "v_add_u32_e32 v13, 0x110000, v9\n\t" \
  "global_store_dwordx4 v13, v[220:223], s[50:51] sc1\n\t" \
  "v_mfma_f32_16x16x32_bf16 v[124:127], v[180:183], v[164:167], 0\n\t" \
  "v_mfma_f32_16x16x32_bf16 v[120:123], v[176:179], v[164:167], 0\n\t" \
  "ds_read_b128 v[164:167], v10 offset:9216\n\t" \
  "v_cvt_pk_bf16_f32 v208, v128, v129\n\t" \
  "v_cvt_pk_bf16_f32 v209, v130, v131\n\t" \
  "v_cvt_pk_bf16_f32 v210, v136, v137\n\t" \
  "v_cvt_pk_bf16_f32 v211, v138, v139\n\t" \
  "v_cvt_pk_bf16_f32 v212, v64, v65\n\t" \
  "v_cvt_pk_bf16_f32 v213, v66, v67\n\t" \
  "v_cvt_pk_bf16_f32 v214, v72, v73\n\t" \
  "v_cvt_pk_bf16_f32 v215, v74, v75\n\t" \
  "v_cvt_pk_bf16_f32 v216, v132, v133\n\t" \
  "v_cvt_pk_bf16_f32 v217, v134, v135\n\t" \
  "v_cvt_pk_bf16_f32 v218, v140, v141\n\t" \
  "v_cvt_pk_bf16_f32 v219, v142, v143\n\t" \
  "v_cvt_pk_bf16_f32 v220, v68, v69\n\t" \
  "v_cvt_pk_bf16_f32 v221, v70, v71\n\t" \
  "v_cvt_pk_bf16_f32 v222, v76, v77\n\t" \
  "v_cvt_pk_bf16_f32 v223, v78, v79\n\t" \
  "v_mfma_f32_16x16x32_bf16 v[128:131], v[176:179], v[168:171], 0\n\t" \
  "v_mfma_f32_16x16x32_bf16 v[132:135], v[180:183], v[168:171], 0\n\t" \
  "v_mfma_f32_16x16x32_bf16 v[64:67], v[184:187], v[168:171], 0\n\t" \
  "v_mfma_f32_16x16x32_bf16 v[68:71], v[188:191], v[168:171], 0\n\t" \
  "ds_read_b128 v[168:171], v10 offset:12288\n\t" \
  "v_mfma_f32_16x16x32_bf16 v[76:79], v[188:191], v[172:175], 0\n\t" \
  "v_mfma_f32_16x16x32_bf16 v[72:75], v[184:187], v[172:175], 0\n\t" \
  "v_mfma_f32_16x16x32_bf16 v[140:143], v[180:183], v[172:175], 0\n\t" \
  "v_mfma_f32_16x16x32_bf16 v[136:139], v[176:179], v[172:175], 0\n\t" \
  "ds_read_b128 v[172:175], v10 offset:13312\n\t" \
  "R1_%=:\n\t" \
  "s_waitcnt lgkmcnt(4)\n\t" \
  "v_mfma_f32_16x16x32_bf16 v[80:83], v[192:195], v[144:147], v[80:83]\n\t" \
  "s_waitcnt vmcnt(12)\n\t" \
  "s_barrier\n\t" \
  "v_mfma_f32_16x16x32_bf16 v[84:87], v[196:199], v[144:147], v[84:87]\n\t" \
  "v_add_u32_e32 v10, 0x18000, v6\n\t" \
  "v_add_u32_e32 v11, 0x18000, v7\n\t" \
  "v_mfma_f32_16x16x32_bf16 v[16:19], v[200:203], v[144:147], v[16:19]\n\t" \
  "v_xor_b32_e32 v12, 32, v11\n\t" \
  "v_mfma_f32_16x16x32_bf16 v[20:23], v[204:207], v[144:147], v[20:23]\n\t" \
  "ds_read_b128 v[144:147], v10 offset:0\n\t" \
  "v_mfma_f32_16x16x32_bf16 v[28:31], v[204:207], v[148:151], v[28:31]\n\t" \
  "ds_read_b64_tr_b16 v[176:177], v11 offset:0\n\t" \
  "v_mfma_f32_16x16x32_bf16 v[24:27], v[200:203], v[148:151], v[24:27]\n\t" \
  "ds_read_b64_tr_b16 v[178:179], v11 offset:4096\n\t" \
  "v_mfma_f32_16x16x32_bf16 v[92:95], v[196:199], v[148:151], v[92:95]\n\t" \
  "s_add_u32 m0, s62, 0x8000\n\t" \
  "v_mfma_f32_16x16x32_bf16 v[88:91], v[192:195], v[148:151], v[88:91]\n\t" \
  "ds_read_b128 v[148:151], v10 offset:1024\n\t" \
  "v_mfma_f32_16x16x32_bf16 v[96:99], v[192:195], v[152:155], v[96:99]\n\t" \
  "ds_read_b64_tr_b16 v[180:181], v12 offset:0\n\t" \
  "global_load_lds_dwordx4 v1, s[40:41] offset:0 sc1\n\t" \
  "v_add_u32_e32 v13, 0x2000, v8\n\t" \
  "global_store_dwordx4 v13, v[224:227], s[50:51] sc1\n\t" \
  "v_mfma_f32_16x16x32_bf16 v[100:103], v[196:199], v[152:155], v[100:103]\n\t" \
  "ds_read_b64_tr_b16 v[182:183], v12 offset:4096\n\t" \
  "v_mfma_f32_16x16x32_bf16 v[32:35], v[200:203], v[152:155], v[32:35]\n\t" \
  "global_load_lds_dwordx4 v2, s[40:41] offset:1024 sc1\n\t" \
  "v_mfma_f32_16x16x32_bf16 v[36:39], v[204:207], v[152:155], v[36:39]\n\t" \
  "ds_read_b128 v[152:155], v10 offset:4096\n\t" \
  "v_mfma_f32_16x16x32_bf16 v[44:47], v[204:207], v[156:159], v[44:47]\n\t" \
  "ds_read_b64_tr_b16 v[184:185], v11 offset:1024\n\t" \
  "global_load_lds_dwordx4 v3, s[42:43] offset:2048\n\t" \
  "v_mfma_f32_16x16x32_bf16 v[40:43], v[200:203], v[156:159], v[40:43]\n\t" \
  "ds_read_b64_tr_b16 v[186:187], v11 offset:5120\n\t" \
  "v_mfma_f32_16x16x32_bf16 v[108:111], v[196:199], v[156:159], v[108:111]\n\t" \
  "global_load_lds_dwordx4 v[4:5], off offset:3072\n\t" \
  "v_mfma_f32_16x16x32_bf16 v[104:107], v[192:195], v[156:159], v[104:107]\n\t" \
  "ds_read_b128 v[156:159], v10 offset:5120\n\t" \
  "s_waitcnt lgkmcnt(10)\n\t" \
  "v_mfma_f32_16x16x32_bf16 v[112:115], v[192:195], v[160:163], v[112:115]\n\t" \
  "ds_read_b64_tr_b16 v[188:189], v12 offset:1024\n\t" \
  "s_add_u32 s40, s40, 1024\n\t" \
  "s_addc_u32 s41, s41, 0\n\t" \
  "v_mfma_f32_16x16x32_bf16 v[116:119], v[196:199], v[160:163], v[116:119]\n\t" \
  "ds_read_b64_tr_b16 v[190:191], v12 offset:5120\n\t" \
  "s_add_u32 s42, s42, 0x40000\n\t" \
  "s_addc_u32 s43, s43, 0\n\t" \
  "v_mfma_f32_16x16x32_bf16 v[48:51], v[200:203], v[160:163], v[48:51]\n\t" \
  "v_lshl_add_u64 v[4:5], v[4:5], 0, s[44:45]\n\t" \
  "v_mfma_f32_16x16x32_bf16 v[52:55], v[204:207], v[160:163], v[52:55]\n\t" \
  "ds_read_b128 v[160:163], v10 offset:8192\n\t" \
  "v_mfma_f32_16x16x32_bf16 v[60:63], v[204:207], v[164:167], v[60:63]\n\t" \
  "v_mfma_f32_16x16x32_bf16 v[56:59], v[200:203], v[164:167], v[56:59]\n\t" \
  "v_mfma_f32_16x16x32_bf16 v[124:127], v[196:199], v[164:167], v[124:127]\n\t" \
  "v_mfma_f32_16x16x32_bf16 v[120:123], v[192:195], v[164:167], v[120:123]\n\t" \
  "ds_read_b128 v[164:167], v10 offset:9216\n\t" \
  "v_mfma_f32_16x16x32_bf16 v[128:131], v[192:195], v[168:171], v[128:131]\n\t" \
  "v_mfma_f32_16x16x32_bf16 v[132:135], v[196:199], v[168:171], v[132:135]\n\t" \
  "v_mfma_f32_16x16x32_bf16 v[64:67], v[200:203], v[168:171], v[64:67]\n\t" \
  "v_mfma_f32_16x16x32_bf16 v[68:71], v[204:207], v[168:171], v[68:71]\n\t" \
  "ds_read_b128 v[168:171], v10 offset:12288\n\t" \
  "v_mfma_f32_16x16x32_bf16 v[76:79], v[204:207], v[172:175], v[76:79]\n\t" \
  "v_mfma_f32_16x16x32_bf16 v[72:75], v[200:203], v[172:175], v[72:75]\n\t" \
  "v_mfma_f32_16x16x32_bf16 v[140:143], v[196:199], v[172:175], v[140:143]\n\t" \
  "v_mfma_f32_16x16x32_bf16 v[136:139], v[192:195], v[172:175], v[136:139]\n\t" \
  "ds_read_b128 v[172:175], v10 offset:13312\n\t" \
  "s_waitcnt lgkmcnt(4)\n\t" \
  "v_mfma_f32_16x16x32_bf16 v[80:83], v[176:179], v[144:147], v[80:83]\n\t" \
  "s_waitcnt vmcnt(13)\n\t" \
  "s_barrier\n\t" \
  "v_mfma_f32_16x16x32_bf16 v[84:87], v[180:183], v[144:147], v[84:87]\n\t" \
  "v_add_u32_e32 v10, 0x20000, v6\n\t" \
  "v_add_u32_e32 v11, 0x20000, v7\n\t" \
  "v_mfma_f32_16x16x32_bf16 v[16:19], v[184:187], v[144:147], v[16:19]\n\t" \
  "v_xor_b32_e32 v12, 32, v11\n\t" \
  "v_mfma_f32_16x16x32_bf16 v[20:23], v[188:191], v[144:147], v[20:23]\n\t" \
  "ds_read_b128 v[144:147], v10 offset:0\n\t" \
  "v_mfma_f32_16x16x32_bf16 v[28:31], v[188:191], v[148:151], v[28:31]\n\t" \
  "ds_read_b64_tr_b16 v[192:193], v11 offset:0\n\t" \
  "v_mfma_f32_16x16x32_bf16 v[24:27], v[184:187], v[148:151], v[24:27]\n\t" \
  "ds_read_b64_tr_b16 v[194:195], v11 offset:4096\n\t" \
  "v_mfma_f32_16x16x32_bf16 v[92:95], v[180:183], v[148:151], v[92:95]\n\t" \
  "s_add_u32 m0, s62, 0x10000\n\t" \
  "v_mfma_f32_16x16x32_bf16 v[88:91], v[176:179], v[148:151], v[88:91]\n\t" \
  "ds_read_b128 v[148:151], v10 offset:1024\n\t" \
  "v_mfma_f32_16x16x32_bf16 v[96:99], v[176:179], v[152:155], v[96:99]\n\t" \
  "ds_read_b64_tr_b16 v[196:197], v12 offset:0\n\t" \
  "global_load_lds_dwordx4 v1, s[40:41] offset:0 sc1\n\t" \
  "v_add_u32_e32 v13, 0x112000, v8\n\t" \
  "global_store_dwordx4 v13, v[228:231], s[50:51] sc1\n\t" \
  "v_mfma_f32_16x16x32_bf16 v[100:103], v[180:183], v[152:155], v[100:103]\n\t" \
  "ds_read_b64_tr_b16 v[198:199], v12 offset:4096\n\t" \
  "v_mfma_f32_16x16x32_bf16 v[32:35], v[184:187], v[152:155], v[32:35]\n\t" \
  "global_load_lds_dwordx4 v2, s[40:41] offset:1024 sc1\n\t" \
  "v_mfma_f32_16x16x32_bf16 v[36:39], v[188:191], v[152:155], v[36:39]\n\t" \
  "ds_read_b128 v[152:155], v10 offset:4096\n\t" \
  "v_mfma_f32_16x16x32_bf16 v[44:47], v[188:191], v[156:159], v[44:47]\n\t" \
  "ds_read_b64_tr_b16 v[200:201], v11 offset:1024\n\t" \
  "global_load_lds_dwordx4 v3, s[42:43] offset:2048\n\t" \
  "v_mfma_f32_16x16x32_bf16 v[40:43], v[184:187], v[156:159], v[40:43]\n\t" \
  "ds_read_b64_tr_b16 v[202:203], v11 offset:5120\n\t" \
  "v_mfma_f32_16x16x32_bf16 v[108:111], v[180:183], v[156:159], v[108:111]\n\t" \
  "global_load_lds_dwordx4 v[4:5], off offset:3072\n\t" \
  "v_mfma_f32_16x16x32_bf16 v[104:107], v[176:179], v[156:159], v[104:107]\n\t" \
  "ds_read_b128 v[156:159], v10 offset:5120\n\t" \
  "s_waitcnt lgkmcnt(10)\n\t" \
  "v_mfma_f32_16x16x32_bf16 v[112:115], v[176:179], v[160:163], v[112:115]\n\t" \
  "ds_read_b64_tr_b16 v[204:205], v12 offset:1024\n\t" \
  "s_add_u32 s40, s40, 1024\n\t" \
  "s_addc_u32 s41, s41, 0\n\t" \
  "v_mfma_f32_16x16x32_bf16 v[116:119], v[180:183], v[160:163], v[116:119]\n\t" \
  "ds_read_b64_tr_b16 v[206:207], v12 offset:5120\n\t" \
  "s_add_u32 s42, s42, 0x40000\n\t" \
  "s_addc_u32 s43, s43, 0\n\t" \
  "v_mfma_f32_16x16x32_bf16 v[48:51], v[184:187], v[160:163], v[48:51]\n\t" \
  "v_lshl_add_u64 v[4:5], v[4:5], 0, s[44:45]\n\t" \
  "v_mfma_f32_16x16x32_bf16 v[52:55], v[188:191], v[160:163], v[52:55]\n\t" \
  "ds_read_b128 v[160:163], v10 offset:8192\n\t" \
  "v_mfma_f32_16x16x32_bf16 v[60:63], v[188:191], v[164:167], v[60:63]\n\t" \
  "v_mfma_f32_16x16x32_bf16 v[56:59], v[184:187], v[164:167], v[56:59]\n\t" \
  "v_mfma_f32_16x16x32_bf16 v[124:127], v[180:183], v[164:167], v[124:127]\n\t" \
  "v_mfma_f32_16x16x32_bf16 v[120:123], v[176:179], v[164:167], v[120:123]\n\t" \
  "ds_read_b128 v[164:167], v10 offset:9216\n\t" \
  "v_mfma_f32_16x16x32_bf16 v[128:131], v[176:179], v[168:171], v[128:131]\n\t" \
  "v_mfma_f32_16x16x32_bf16 v[132:135], v[180:183], v[168:171], v[132:135]\n\t" \
  "v_mfma_f32_16x16x32_bf16 v[64:67], v[184:187], v[168:171], v[64:67]\n\t" \
  "v_mfma_f32_16x16x32_bf16 v[68:71], v[188:191], v[168:171], v[68:71]\n\t" \
  "ds_read_b128 v[168:171], v10 offset:12288\n\t" \
  "v_mfma_f32_16x16x32_bf16 v[76:79], v[188:191], v[172:175], v[76:79]\n\t" \
  "v_mfma_f32_16x16x32_bf16 v[72:75], v[184:187], v[172:175], v[72:75]\n\t" \
  "v_mfma_f32_16x16x32_bf16 v[140:143], v[180:183], v[172:175], v[140:143]\n\t" \
  "v_mfma_f32_16x16x32_bf16 v[136:139], v[176:179], v[172:175], v[136:139]\n\t" \
  "ds_read_b128 v[172:175], v10 offset:13312\n\t" \
  "s_waitcnt lgkmcnt(4)\n\t" \
  "v_mfma_f32_16x16x32_bf16 v[80:83], v[192:195], v[144:147], v[80:83]\n\t" \
  "s_waitcnt vmcnt(12)\n\t" \
  "s_barrier\n\t" \
  "v_mfma_f32_16x16x32_bf16 v[84:87], v[196:199], v[144:147], v[84:87]\n\t" \
  "v_add_u32_e32 v10, 0x0, v6\n\t" \
  "v_add_u32_e32 v11, 0x0, v7\n\t" \
  "v_mfma_f32_16x16x32_bf16 v[16:19], v[200:203], v[144:147], v[16:19]\n\t" \
  "v_xor_b32_e32 v12, 32, v11\n\t" \
  "v_mfma_f32_16x16x32_bf16 v[20:23], v[204:207], v[144:147], v[20:23]\n\t" \
  "ds_read_b128 v[144:147], v10 offset:0\n\t" \
  "v_mfma_f32_16x16x32_bf16 v[28:31], v[204:207], v[148:151], v[28:31]\n\t" \
  "ds_read_b64_tr_b16 v[176:177], v11 offset:0\n\t" \
  "v_mfma_f32_16x16x32_bf16 v[24:27], v[200:203], v[148:151], v[24:27]\n\t" \
  "ds_read_b64_tr_b16 v[178:179], v11 offset:4096\n\t" \
  "v_mfma_f32_16x16x32_bf16 v[92:95], v[196:199], v[148:151], v[92:95]\n\t" \
  "s_add_u32 m0, s62, 0x18000\n\t" \
  "v_mfma_f32_16x16x32_bf16 v[88:91], v[192:195], v[148:151], v[88:91]\n\t" \
  "ds_read_b128 v[148:151], v10 offset:1024\n\t" \
  "v_mfma_f32_16x16x32_bf16 v[96:99], v[192:195], v[152:155], v[96:99]\n\t" \
  "ds_read_b64_tr_b16 v[180:181], v12 offset:0\n\t" \
  "global_load_lds_dwordx4 v1, s[40:41] offset:0 sc1\n\t" \
  "v_add_u32_e32 v13, 0x2000, v9\n\t" \
  "global_store_dwordx4 v13, v[232:235], s[50:51] sc1\n\t" \
  "v_mfma_f32_16x16x32_bf16 v[100:103], v[196:199], v[152:155], v[100:103]\n\t" \
  "ds_read_b64_tr_b16 v[182:183], v12 offset:4096\n\t" \
  "v_mfma_f32_16x16x32_bf16 v[32:35], v[200:203], v[152:155], v[32:35]\n\t" \
  "global_load_lds_dwordx4 v2, s[40:41] offset:1024 sc1\n\t" \
  "v_mfma_f32_16x16x32_bf16 v[36:39], v[204:207], v[152:155], v[36:39]\n\t" \
  "ds_read_b128 v[152:155], v10 offset:4096\n\t" \
  "v_mfma_f32_16x16x32_bf16 v[44:47], v[204:207], v[156:159], v[44:47]\n\t" \
  "ds_read_b64_tr_b16 v[184:185], v11 offset:1024\n\t" \
  "global_load_lds_dwordx4 v3, s[42:43] offset:2048\n\t" \
  "v_mfma_f32_16x16x32_bf16 v[40:43], v[200:203], v[156:159], v[40:43]\n\t" \
  "ds_read_b64_tr_b16 v[186:187], v11 offset:5120\n\t" \
  "v_mfma_f32_16x16x32_bf16 v[108:111], v[196:199], v[156:159], v[108:111]\n\t" \
  "global_load_lds_dwordx4 v[4:5], off offset:3072\n\t" \
  "v_mfma_f32_16x16x32_bf16 v[104:107], v[192:195], v[156:159], v[104:107]\n\t" \
  "ds_read_b128 v[156:159], v10 offset:5120\n\t" \
  "s_waitcnt lgkmcnt(10)\n\t" \
  "v_mfma_f32_16x16x32_bf16 v[112:115], v[192:195], v[160:163], v[112:115]\n\t" \
  "ds_read_b64_tr_b16 v[188:189], v12 offset:1024\n\t" \
  "s_add_u32 s40, s40, 1024\n\t" \
  "s_addc_u32 s41, s41, 0\n\t" \
  "v_mfma_f32_16x16x32_bf16 v[116:119], v[196:199], v[160:163], v[116:119]\n\t" \
  "ds_read_b64_tr_b16 v[190:191], v12 offset:5120\n\t" \
  "s_add_u32 s42, s42, 0x40000\n\t" \
  "s_addc_u32 s43, s43, 0\n\t" \
  "v_mfma_f32_16x16x32_bf16 v[48:51], v[200:203], v[160:163], v[48:51]\n\t" \
  "v_lshl_add_u64 v[4:5], v[4:5], 0, s[44:45]\n\t" \
  "v_mfma_f32_16x16x32_bf16 v[52:55], v[204:207], v[160:163], v[52:55]\n\t" \
  "ds_read_b128 v[160:163], v10 offset:8192\n\t" \
  "v_mfma_f32_16x16x32_bf16 v[60:63], v[204:207], v[164:167], v[60:63]\n\t" \
  "v_mfma_f32_16x16x32_bf16 v[56:59], v[200:203], v[164:167], v[56:59]\n\t" \
  "v_mfma_f32_16x16x32_bf16 v[124:127], v[196:199], v[164:167], v[124:127]\n\t" \
  "v_mfma_f32_16x16x32_bf16 v[120:123], v[192:195], v[164:167], v[120:123]\n\t" \
  "ds_read_b128 v[164:167], v10 offset:9216\n\t" \
  "v_mfma_f32_16x16x32_bf16 v[128:131], v[192:195], v[168:171], v[128:131]\n\t" \
  "v_mfma_f32_16x16x32_bf16 v[132:135], v[196:199], v[168:171], v[132:135]\n\t" \
  "v_mfma_f32_16x16x32_bf16 v[64:67], v[200:203], v[168:171], v[64:67]\n\t" \
  "v_mfma_f32_16x16x32_bf16 v[68:71], v[204:207], v[168:171], v[68:71]\n\t" \
  "ds_read_b128 v[168:171], v10 offset:12288\n\t" \
  "v_mfma_f32_16x16x32_bf16 v[76:79], v[204:207], v[172:175], v[76:79]\n\t" \
  "v_mfma_f32_16x16x32_bf16 v[72:75], v[200:203], v[172:175], v[72:75]\n\t" \
  "v_mfma_f32_16x16x32_bf16 v[140:143], v[196:199], v[172:175], v[140:143]\n\t" \
  "v_mfma_f32_16x16x32_bf16 v[136:139], v[192:195], v[172:175], v[136:139]\n\t" \
  "ds_read_b128 v[172:175], v10 offset:13312\n\t" \
  "s_waitcnt lgkmcnt(4)\n\t" \
  "v_mfma_f32_16x16x32_bf16 v[80:83], v[176:179], v[144:147], v[80:83]\n\t" \
  "s_waitcnt vmcnt(10)\n\t" \
  "s_barrier\n\t" \
  "v_mfma_f32_16x16x32_bf16 v[84:87], v[180:183], v[144:147], v[84:87]\n\t" \
  "v_add_u32_e32 v10, 0x8000, v6\n\t" \
  "v_add_u32_e32 v11, 0x8000, v7\n\t" \
  "v_mfma_f32_16x16x32_bf16 v[16:19], v[184:187], v[144:147], v[16:19]\n\t" \
  "v_xor_b32_e32 v12, 32, v11\n\t" \
  "v_mfma_f32_16x16x32_bf16 v[20:23], v[188:191], v[144:147], v[20:23]\n\t" \
  "ds_read_b128 v[144:147], v10 offset:0\n\t" \
  "v_mfma_f32_16x16x32_bf16 v[28:31], v[188:191], v[148:151], v[28:31]\n\t" \
  "ds_read_b64_tr_b16 v[192:193], v11 offset:0\n\t" \
  "v_mfma_f32_16x16x32_bf16 v[24:27], v[184:187], v[148:151], v[24:27]\n\t" \
  "ds_read_b64_tr_b16 v[194:195], v11 offset:4096\n\t" \
  "v_mfma_f32_16x16x32_bf16 v[92:95], v[180:183], v[148:151], v[92:95]\n\t" \
  "s_add_u32 m0, s62, 0x20000\n\t" \
  "v_mfma_f32_16x16x32_bf16 v[88:91], v[176:179], v[148:151], v[88:91]\n\t" \
  "ds_read_b128 v[148:151], v10 offset:1024\n\t" \
  "v_mfma_f32_16x16x32_bf16 v[96:99], v[176:179], v[152:155], v[96:99]\n\t" \
  "ds_read_b64_tr_b16 v[196:197], v12 offset:0\n\t" \
  "global_load_lds_dwordx4 v1, s[40:41] offset:0 sc1\n\t" \
  "v_add_u32_e32 v13, 0x112000, v9\n\t" \
  "global_store_dwordx4 v13, v[236:239], s[50:51] sc1\n\t" \
  "v_mfma_f32_16x16x32_bf16 v[100:103], v[180:183], v[152:155], v[100:103]\n\t" \
  "ds_read_b64_tr_b16 v[198:199], v12 offset:4096\n\t" \
  "v_mfma_f32_16x16x32_bf16 v[32:35], v[184:187], v[152:155], v[32:35]\n\t" \
  "global_load_lds_dwordx4 v2, s[40:41] offset:1024 sc1\n\t" \
  "v_mfma_f32_16x16x32_bf16 v[36:39], v[188:191], v[152:155], v[36:39]\n\t" \
  "ds_read_b128 v[152:155], v10 offset:4096\n\t" \
  "v_mfma_f32_16x16x32_bf16 v[44:47], v[188:191], v[156:159], v[44:47]\n\t" \
  "ds_read_b64_tr_b16 v[200:201], v11 offset:1024\n\t" \
  "global_load_lds_dwordx4 v3, s[42:43] offset:2048\n\t" \
  "v_mfma_f32_16x16x32_bf16 v[40:43], v[184:187], v[156:159], v[40:43]\n\t" \
  "ds_read_b64_tr_b16 v[202:203], v11 offset:5120\n\t" \
  "v_mfma_f32_16x16x32_bf16 v[108:111], v[180:183], v[156:159], v[108:111]\n\t" \
  "global_load_lds_dwordx4 v[4:5], off offset:3072\n\t" \
  "v_mfma_f32_16x16x32_bf16 v[104:107], v[176:179], v[156:159], v[104:107]\n\t" \
  "ds_read_b128 v[156:159], v10 offset:5120\n\t" \
  "s_waitcnt lgkmcnt(10)\n\t" \
  "v_mfma_f32_16x16x32_bf16 v[112:115], v[176:179], v[160:163], v[112:115]\n\t" \
  "ds_read_b64_tr_b16 v[204:205], v12 offset:1024\n\t" \
  "s_add_u32 s40, s40, 1024\n\t" \
  "s_addc_u32 s41, s41, 0\n\t" \
  "v_mfma_f32_16x16x32_bf16 v[116:119], v[180:183], v[160:163], v[116:119]\n\t" \
  "ds_read_b64_tr_b16 v[206:207], v12 offset:5120\n\t" \
  "s_add_u32 s42, s42, 0x40000\n\t" \
  "s_addc_u32 s43, s43, 0\n\t" \
  "v_mfma_f32_16x16x32_bf16 v[48:51], v[184:187], v[160:163], v[48:51]\n\t" \
  "v_lshl_add_u64 v[4:5], v[4:5], 0, s[44:45]\n\t" \
  "v_mfma_f32_16x16x32_bf16 v[52:55], v[188:191], v[160:163], v[52:55]\n\t" \
  "ds_read_b128 v[160:163], v10 offset:8192\n\t" \
  "v_mfma_f32_16x16x32_bf16 v[60:63], v[188:191], v[164:167], v[60:63]\n\t" \
  "v_mfma_f32_16x16x32_bf16 v[56:59], v[184:187], v[164:167], v[56:59]\n\t" \
  "v_mfma_f32_16x16x32_bf16 v[124:127], v[180:183], v[164:167], v[124:127]\n\t" \
  "v_mfma_f32_16x16x32_bf16 v[120:123], v[176:179], v[164:167], v[120:123]\n\t" \
  "ds_read_b128 v[164:167], v10 offset:9216\n\t" \
  "v_mfma_f32_16x16x32_bf16 v[128:131], v[176:179], v[168:171], v[128:131]\n\t" \
  "v_mfma_f32_16x16x32_bf16 v[132:135], v[180:183], v[168:171], v[132:135]\n\t" \
  "v_mfma_f32_16x16x32_bf16 v[64:67], v[184:187], v[168:171], v[64:67]\n\t" \
  "v_mfma_f32_16x16x32_bf16 v[68:71], v[188:191], v[168:171], v[68:71]\n\t" \
  "ds_read_b128 v[168:171], v10 offset:12288\n\t" \
  "v_mfma_f32_16x16x32_bf16 v[76:79], v[188:191], v[172:175], v[76:79]\n\t" \
  "v_mfma_f32_16x16x32_bf16 v[72:75], v[184:187], v[172:175], v[72:75]\n\t" \
  "v_mfma_f32_16x16x32_bf16 v[140:143], v[180:183], v[172:175], v[140:143]\n\t" \
  "v_mfma_f32_16x16x32_bf16 v[136:139], v[176:179], v[172:175], v[136:139]\n\t" \
  "ds_read_b128 v[172:175], v10 offset:13312\n\t" \
  "s_waitcnt lgkmcnt(4)\n\t" \
  "v_mfma_f32_16x16x32_bf16 v[80:83], v[192:195], v[144:147], v[80:83]\n\t" \
  "s_waitcnt vmcnt(10)\n\t" \
  "s_barrier\n\t" \
  "v_mfma_f32_16x16x32_bf16 v[84:87], v[196:199], v[144:147], v[84:87]\n\t" \
  "v_add_u32_e32 v10, 0x10000, v6\n\t" \
  "v_add_u32_e32 v11, 0x10000, v7\n\t" \
  "v_mfma_f32_16x16x32_bf16 v[16:19], v[200:203], v[144:147], v[16:19]\n\t" \
  "v_xor_b32_e32 v12, 32, v11\n\t" \
  "v_mfma_f32_16x16x32_bf16 v[20:23], v[204:207], v[144:147], v[20:23]\n\t" \
  "ds_read_b128 v[144:147], v10 offset:0\n\t" \
  "v_mfma_f32_16x16x32_bf16 v[28:31], v[204:207], v[148:151], v[28:31]\n\t" \
  "ds_read_b64_tr_b16 v[176:177], v11 offset:0\n\t" \
  "v_mfma_f32_16x16x32_bf16 v[24:27], v[200:203], v[148:151], v[24:27]\n\t" \
  "ds_read_b64_tr_b16 v[178:179], v11 offset:4096\n\t" \
  "v_mfma_f32_16x16x32_bf16 v[92:95], v[196:199], v[148:151], v[92:95]\n\t" \
  "s_add_u32 m0, s62, 0x0\n\t" \
  "v_mfma_f32_16x16x32_bf16 v[88:91], v[192:195], v[148:151], v[88:91]\n\t" \
  "ds_read_b128 v[148:151], v10 offset:1024\n\t" \
  "v_mfma_f32_16x16x32_bf16 v[96:99], v[192:195], v[152:155], v[96:99]\n\t" \
  "ds_read_b64_tr_b16 v[180:181], v12 offset:0\n\t" \
  "global_load_lds_dwordx4 v1, s[40:41] offset:0 sc1\n\t" \
  "v_add_u32_e32 v13, 0x8000, v8\n\t" \
  "global_store_dwordx4 v13, v[240:243], s[50:51] sc1\n\t" \
  "v_mfma_f32_16x16x32_bf16 v[100:103], v[196:199], v[152:155], v[100:103]\n\t" \
  "ds_read_b64_tr_b16 v[182:183], v12 offset:4096\n\t" \
  "v_mfma_f32_16x16x32_bf16 v[32:35], v[200:203], v[152:155], v[32:35]\n\t" \
  "global_load_lds_dwordx4 v2, s[40:41] offset:1024 sc1\n\t" \
  "v_mfma_f32_16x16x32_bf16 v[36:39], v[204:207], v[152:155], v[36:39]\n\t" \
  "ds_read_b128 v[152:155], v10 offset:4096\n\t" \
  "v_mfma_f32_16x16x32_bf16 v[44:47], v[204:207], v[156:159], v[44:47]\n\t" \
  "ds_read_b64_tr_b16 v[184:185], v11 offset:1024\n\t" \
  "global_load_lds_dwordx4 v3, s[42:43] offset:2048\n\t" \
  "v_mfma_f32_16x16x32_bf16 v[40:43], v[200:203], v[156:159], v[40:43]\n\t" \
  "ds_read_b64_tr_b16 v[186:187], v11 offset:5120\n\t" \
  "v_mfma_f32_16x16x32_bf16 v[108:111], v[196:199], v[156:159], v[108:111]\n\t" \
  "global_load_lds_dwordx4 v[4:5], off offset:3072\n\t" \
  "v_mfma_f32_16x16x32_bf16 v[104:107], v[192:195], v[156:159], v[104:107]\n\t" \
  "ds_read_b128 v[156:159], v10 offset:5120\n\t" \
  "s_waitcnt lgkmcnt(10)\n\t" \
  "v_mfma_f32_16x16x32_bf16 v[112:115], v[192:195], v[160:163], v[112:115]\n\t" \
  "ds_read_b64_tr_b16 v[188:189], v12 offset:1024\n\t" \
  "s_add_u32 s40, s40, 1024\n\t" \
  "s_addc_u32 s41, s41, 0\n\t" \
  "v_mfma_f32_16x16x32_bf16 v[116:119], v[196:199], v[160:163], v[116:119]\n\t" \
  "ds_read_b64_tr_b16 v[190:191], v12 offset:5120\n\t" \
  "s_add_u32 s42, s42, 0x40000\n\t" \
  "s_addc_u32 s43, s43, 0\n\t" \
  "v_mfma_f32_16x16x32_bf16 v[48:51], v[200:203], v[160:163], v[48:51]\n\t" \
  "v_lshl_add_u64 v[4:5], v[4:5], 0, s[44:45]\n\t" \
  "v_mfma_f32_16x16x32_bf16 v[52:55], v[204:207], v[160:163], v[52:55]\n\t" \
  "ds_read_b128 v[160:163], v10 offset:8192\n\t" \
  "v_mfma_f32_16x16x32_bf16 v[60:63], v[204:207], v[164:167], v[60:63]\n\t" \
  "v_mfma_f32_16x16x32_bf16 v[56:59], v[200:203], v[164:167], v[56:59]\n\t" \
  "v_mfma_f32_16x16x32_bf16 v[124:127], v[196:199], v[164:167], v[124:127]\n\t" \
  "v_mfma_f32_16x16x32_bf16 v[120:123], v[192:195], v[164:167], v[120:123]\n\t" \
  "ds_read_b128 v[164:167], v10 offset:9216\n\t" \
  "v_mfma_f32_16x16x32_bf16 v[128:131], v[192:195], v[168:171], v[128:131]\n\t" \
  "v_mfma_f32_16x16x32_bf16 v[132:135], v[196:199], v[168:171], v[132:135]\n\t" \
  "v_mfma_f32_16x16x32_bf16 v[64:67], v[200:203], v[168:171], v[64:67]\n\t" \
  "v_mfma_f32_16x16x32_bf16 v[68:71], v[204:207], v[168:171], v[68:71]\n\t" \
  "ds_read_b128 v[168:171], v10 offset:12288\n\t" \
  "v_mfma_f32_16x16x32_bf16 v[76:79], v[204:207], v[172:175], v[76:79]\n\t" \
  "v_mfma_f32_16x16x32_bf16 v[72:75], v[200:203], v[172:175], v[72:75]\n\t" \
  "v_mfma_f32_16x16x32_bf16 v[140:143], v[196:199], v[172:175], v[140:143]\n\t" \
  "v_mfma_f32_16x16x32_bf16 v[136:139], v[192:195], v[172:175], v[136:139]\n\t" \
  "ds_read_b128 v[172:175], v10 offset:13312\n\t" \
  "s_waitcnt lgkmcnt(4)\n\t" \
  "v_mfma_f32_16x16x32_bf16 v[80:83], v[176:179], v[144:147], v[80:83]\n\t" \
  "s_waitcnt vmcnt(10)\n\t" \
  "s_barrier\n\t" \
  "v_mfma_f32_16x16x32_bf16 v[84:87], v[180:183], v[144:147], v[84:87]\n\t" \
  "v_add_u32_e32 v10, 0x18000, v6\n\t" \
  "v_add_u32_e32 v11, 0x18000, v7\n\t" \
  "v_mfma_f32_16x16x32_bf16 v[16:19], v[184:187], v[144:147], v[16:19]\n\t" \
  "v_xor_b32_e32 v12, 32, v11\n\t" \
  "v_mfma_f32_16x16x32_bf16 v[20:23], v[188:191], v[144:147], v[20:23]\n\t" \
  "ds_read_b128 v[144:147], v10 offset:0\n\t" \
  "v_mfma_f32_16x16x32_bf16 v[28:31], v[188:191], v[148:151], v[28:31]\n\t" \
  "ds_read_b64_tr_b16 v[192:193], v11 offset:0\n\t" \
  "v_mfma_f32_16x16x32_bf16 v[24:27], v[184:187], v[148:151], v[24:27]\n\t" \
  "ds_read_b64_tr_b16 v[194:195], v11 offset:4096\n\t" \
  "v_mfma_f32_16x16x32_bf16 v[92:95], v[180:183], v[148:151], v[92:95]\n\t" \
  "s_add_u32 m0, s62, 0x8000\n\t" \
  "v_mfma_f32_16x16x32_bf16 v[88:91], v[176:179], v[148:151], v[88:91]\n\t" \
  "ds_read_b128 v[148:151], v10 offset:1024\n\t" \
  "v_mfma_f32_16x16x32_bf16 v[96:99], v[176:179], v[152:155], v[96:99]\n\t" \
  "ds_read_b64_tr_b16 v[196:197], v12 offset:0\n\t" \
  "global_load_lds_dwordx4 v1, s[40:41] offset:0 sc1\n\t" \
  "v_add_u32_e32 v13, 0x118000, v8\n\t" \
  "global_store_dwordx4 v13, v[244:247], s[50:51] sc1\n\t" \
  "v_mfma_f32_16x16x32_bf16 v[100:103], v[180:183], v[152:155], v[100:103]\n\t" \
  "ds_read_b64_tr_b16 v[198:199], v12 offset:4096\n\t" \
  "v_mfma_f32_16x16x32_bf16 v[32:35], v[184:187], v[152:155], v[32:35]\n\t" \
  "global_load_lds_dwordx4 v2, s[40:41] offset:1024 sc1\n\t" \
  "v_mfma_f32_16x16x32_bf16 v[36:39], v[188:191], v[152:155], v[36:39]\n\t" \
  "ds_read_b128 v[152:155], v10 offset:4096\n\t" \
  "v_mfma_f32_16x16x32_bf16 v[44:47], v[188:191], v[156:159], v[44:47]\n\t" \
  "ds_read_b64_tr_b16 v[200:201], v11 offset:1024\n\t" \
  "global_load_lds_dwordx4 v3, s[42:43] offset:2048\n\t" \
  "v_mfma_f32_16x16x32_bf16 v[40:43], v[184:187], v[156:159], v[40:43]\n\t" \
  "ds_read_b64_tr_b16 v[202:203], v11 offset:5120\n\t" \
  "v_mfma_f32_16x16x32_bf16 v[108:111], v[180:183], v[156:159], v[108:111]\n\t" \
  "global_load_lds_dwordx4 v[4:5], off offset:3072\n\t" \
  "v_mfma_f32_16x16x32_bf16 v[104:107], v[176:179], v[156:159], v[104:107]\n\t" \
  "ds_read_b128 v[156:159], v10 offset:5120\n\t" \
  "s_waitcnt lgkmcnt(10)\n\t" \
  "v_mfma_f32_16x16x32_bf16 v[112:115], v[176:179], v[160:163], v[112:115]\n\t" \
  "ds_read_b64_tr_b16 v[204:205], v12 offset:1024\n\t" \
  "s_add_u32 s40, s40, 1024\n\t" \
  "s_addc_u32 s41, s41, 0\n\t" \
  "v_mfma_f32_16x16x32_bf16 v[116:119], v[180:183], v[160:163], v[116:119]\n\t" \
  "ds_read_b64_tr_b16 v[206:207], v12 offset:5120\n\t" \
  "s_add_u32 s42, s42, 0x40000\n\t" \
  "s_addc_u32 s43, s43, 0\n\t" \
  "v_mfma_f32_16x16x32_bf16 v[48:51], v[184:187], v[160:163], v[48:51]\n\t" \
  "v_lshl_add_u64 v[4:5], v[4:5], 0, s[44:45]\n\t" \
  "v_mfma_f32_16x16x32_bf16 v[52:55], v[188:191], v[160:163], v[52:55]\n\t" \
  "ds_read_b128 v[160:163], v10 offset:8192\n\t" \
  "v_mfma_f32_16x16x32_bf16 v[60:63], v[188:191], v[164:167], v[60:63]\n\t" \
  "v_mfma_f32_16x16x32_bf16 v[56:59], v[184:187], v[164:167], v[56:59]\n\t" \
  "v_mfma_f32_16x16x32_bf16 v[124:127], v[180:183], v[164:167], v[124:127]\n\t" \
  "v_mfma_f32_16x16x32_bf16 v[120:123], v[176:179], v[164:167], v[120:123]\n\t" \
  "ds_read_b128 v[164:167], v10 offset:9216\n\t" \
  "v_mfma_f32_16x16x32_bf16 v[128:131], v[176:179], v[168:171], v[128:131]\n\t" \
  "v_mfma_f32_16x16x32_bf16 v[132:135], v[180:183], v[168:171], v[132:135]\n\t" \
  "v_mfma_f32_16x16x32_bf16 v[64:67], v[184:187], v[168:171], v[64:67]\n\t" \
  "v_mfma_f32_16x16x32_bf16 v[68:71], v[188:191], v[168:171], v[68:71]\n\t" \
  "ds_read_b128 v[168:171], v10 offset:12288\n\t" \
  "v_mfma_f32_16x16x32_bf16 v[76:79], v[188:191], v[172:175], v[76:79]\n\t" \
  "v_mfma_f32_16x16x32_bf16 v[72:75], v[184:187], v[172:175], v[72:75]\n\t" \
  "v_mfma_f32_16x16x32_bf16 v[140:143], v[180:183], v[172:175], v[140:143]\n\t" \
  "v_mfma_f32_16x16x32_bf16 v[136:139], v[176:179], v[172:175], v[136:139]\n\t" \
  "ds_read_b128 v[172:175], v10 offset:13312\n\t" \
  "s_waitcnt lgkmcnt(4)\n\t" \
  "v_mfma_f32_16x16x32_bf16 v[80:83], v[192:195], v[144:147], v[80:83]\n\t" \
  "s_waitcnt vmcnt(10)\n\t" \
  "s_barrier\n\t" \
  "v_mfma_f32_16x16x32_bf16 v[84:87], v[196:199], v[144:147], v[84:87]\n\t" \
  "v_add_u32_e32 v10, 0x20000, v6\n\t" \
  "v_add_u32_e32 v11, 0x20000, v7\n\t" \
  "v_mfma_f32_16x16x32_bf16 v[16:19], v[200:203], v[144:147], v[16:19]\n\t" \
  "v_xor_b32_e32 v12, 32, v11\n\t" \
  "v_mfma_f32_16x16x32_bf16 v[20:23], v[204:207], v[144:147], v[20:23]\n\t" \
  "ds_read_b128 v[144:147], v10 offset:0\n\t" \
  "v_mfma_f32_16x16x32_bf16 v[28:31], v[204:207], v[148:151], v[28:31]\n\t" \
  "ds_read_b64_tr_b16 v[176:177], v11 offset:0\n\t" \
  "v_mfma_f32_16x16x32_bf16 v[24:27], v[200:203], v[148:151], v[24:27]\n\t" \
  "ds_read_b64_tr_b16 v[178:179], v11 offset:4096\n\t" \
  "v_mfma_f32_16x16x32_bf16 v[92:95], v[196:199], v[148:151], v[92:95]\n\t" \
  "s_add_u32 m0, s62, 0x10000\n\t" \
  "v_mfma_f32_16x16x32_bf16 v[88:91], v[192:195], v[148:151], v[88:91]\n\t" \
  "ds_read_b128 v[148:151], v10 offset:1024\n\t" \
  "v_mfma_f32_16x16x32_bf16 v[96:99], v[192:195], v[152:155], v[96:99]\n\t" \
  "ds_read_b64_tr_b16 v[180:181], v12 offset:0\n\t" \
  "global_load_lds_dwordx4 v1, s[40:41] offset:0 sc1\n\t" \
  "v_add_u32_e32 v13, 0x8000, v9\n\t" \
  "global_store_dwordx4 v13, v[248:251], s[50:51] sc1\n\t" \
  "v_mfma_f32_16x16x32_bf16 v[100:103], v[196:199], v[152:155], v[100:103]\n\t" \
  "ds_read_b64_tr_b16 v[182:183], v12 offset:4096\n\t" \
  "v_mfma_f32_16x16x32_bf16 v[32:35], v[200:203], v[152:155], v[32:35]\n\t" \
  "global_load_lds_dwordx4 v2, s[40:41] offset:1024 sc1\n\t" \
  "v_mfma_f32_16x16x32_bf16 v[36:39], v[204:207], v[152:155], v[36:39]\n\t" \
  "ds_read_b128 v[152:155], v10 offset:4096\n\t" \
  "v_mfma_f32_16x16x32_bf16 v[44:47], v[204:207], v[156:159], v[44:47]\n\t" \
  "ds_read_b64_tr_b16 v[184:185], v11 offset:1024\n\t" \
  "global_load_lds_dwordx4 v3, s[42:43] offset:2048\n\t" \
  "v_mfma_f32_16x16x32_bf16 v[40:43], v[200:203], v[156:159], v[40:43]\n\t" \
  "ds_read_b64_tr_b16 v[186:187], v11 offset:5120\n\t" \
  "v_mfma_f32_16x16x32_bf16 v[108:111], v[196:199], v[156:159], v[108:111]\n\t" \
  "global_load_lds_dwordx4 v[4:5], off offset:3072\n\t" \
  "v_mfma_f32_16x16x32_bf16 v[104:107], v[192:195], v[156:159], v[104:107]\n\t" \
  "ds_read_b128 v[156:159], v10 offset:5120\n\t" \
  "s_waitcnt lgkmcnt(10)\n\t" \
  "v_mfma_f32_16x16x32_bf16 v[112:115], v[192:195], v[160:163], v[112:115]\n\t" \
  "ds_read_b64_tr_b16 v[188:189], v12 offset:1024\n\t" \
  "s_add_u32 s40, s40, 1024\n\t" \
  "s_addc_u32 s41, s41, 0\n\t" \
  "v_mfma_f32_16x16x32_bf16 v[116:119], v[196:199], v[160:163], v[116:119]\n\t" \
  "ds_read_b64_tr_b16 v[190:191], v12 offset:5120\n\t" \
  "s_add_u32 s42, s42, 0x40000\n\t" \
  "s_addc_u32 s43, s43, 0\n\t" \
  "v_mfma_f32_16x16x32_bf16 v[48:51], v[200:203], v[160:163], v[48:51]\n\t" \
  "v_lshl_add_u64 v[4:5], v[4:5], 0, s[44:45]\n\t" \
  "v_mfma_f32_16x16x32_bf16 v[52:55], v[204:207], v[160:163], v[52:55]\n\t" \
  "ds_read_b128 v[160:163], v10 offset:8192\n\t" \
  "v_mfma_f32_16x16x32_bf16 v[60:63], v[204:207], v[164:167], v[60:63]\n\t" \
  "v_mfma_f32_16x16x32_bf16 v[56:59], v[200:203], v[164:167], v[56:59]\n\t" \
  "v_mfma_f32_16x16x32_bf16 v[124:127], v[196:199], v[164:167], v[124:127]\n\t" \
  "v_mfma_f32_16x16x32_bf16 v[120:123], v[192:195], v[164:167], v[120:123]\n\t" \
  "ds_read_b128 v[164:167], v10 offset:9216\n\t" \
  "v_mfma_f32_16x16x32_bf16 v[128:131], v[192:195], v[168:171], v[128:131]\n\t" \
  "v_mfma_f32_16x16x32_bf16 v[132:135], v[196:199], v[168:171], v[132:135]\n\t" \
  "v_mfma_f32_16x16x32_bf16 v[64:67], v[200:203], v[168:171], v[64:67]\n\t" \
  "v_mfma_f32_16x16x32_bf16 v[68:71], v[204:207], v[168:171], v[68:71]\n\t" \
  "ds_read_b128 v[168:171], v10 offset:12288\n\t" \
  "v_mfma_f32_16x16x32_bf16 v[76:79], v[204:207], v[172:175], v[76:79]\n\t" \
  "v_mfma_f32_16x16x32_bf16 v[72:75], v[200:203], v[172:175], v[72:75]\n\t" \
  "v_mfma_f32_16x16x32_bf16 v[140:143], v[196:199], v[172:175], v[140:143]\n\t" \
  "v_mfma_f32_16x16x32_bf16 v[136:139], v[192:195], v[172:175], v[136:139]\n\t" \
  "ds_read_b128 v[172:175], v10 offset:13312\n\t" \
  "s_waitcnt lgkmcnt(4)\n\t" \
  "v_mfma_f32_16x16x32_bf16 v[80:83], v[176:179], v[144:147], v[80:83]\n\t" \
  "s_waitcnt vmcnt(10)\n\t" \
  "s_barrier\n\t" \
  "v_mfma_f32_16x16x32_bf16 v[84:87], v[180:183], v[144:147], v[84:87]\n\t" \
  "v_add_u32_e32 v10, 0x0, v6\n\t" \
  "v_add_u32_e32 v11, 0x0, v7\n\t" \
  "v_mfma_f32_16x16x32_bf16 v[16:19], v[184:187], v[144:147], v[16:19]\n\t" \
  "v_xor_b32_e32 v12, 32, v11\n\t" \
  "v_mfma_f32_16x16x32_bf16 v[20:23], v[188:191], v[144:147], v[20:23]\n\t" \
  "ds_read_b128 v[144:147], v10 offset:0\n\t" \
  "v_mfma_f32_16x16x32_bf16 v[28:31], v[188:191], v[148:151], v[28:31]\n\t" \
  "ds_read_b64_tr_b16 v[192:193], v11 offset:0\n\t" \
  "v_mfma_f32_16x16x32_bf16 v[24:27], v[184:187], v[148:151], v[24:27]\n\t" \
  "ds_read_b64_tr_b16 v[194:195], v11 offset:4096\n\t" \
  "v_mfma_f32_16x16x32_bf16 v[92:95], v[180:183], v[148:151], v[92:95]\n\t" \
  "s_add_u32 m0, s62, 0x18000\n\t" \
  "v_mfma_f32_16x16x32_bf16 v[88:91], v[176:179], v[148:151], v[88:91]\n\t" \
  "ds_read_b128 v[148:151], v10 offset:1024\n\t" \
  "v_mfma_f32_16x16x32_bf16 v[96:99], v[176:179], v[152:155], v[96:99]\n\t" \
  "ds_read_b64_tr_b16 v[196:197], v12 offset:0\n\t" \
  "global_load_lds_dwordx4 v1, s[40:41] offset:0 sc1\n\t" \
  "v_add_u32_e32 v13, 0x118000, v9\n\t" \
  "global_store_dwordx4 v13, v[252:255], s[50:51] sc1\n\t" \
  "v_mfma_f32_16x16x32_bf16 v[100:103], v[180:183], v[152:155], v[100:103]\n\t" \
  "ds_read_b64_tr_b16 v[198:199], v12 offset:4096\n\t" \
  "v_mfma_f32_16x16x32_bf16 v[32:35], v[184:187], v[152:155], v[32:35]\n\t" \
  "global_load_lds_dwordx4 v2, s[40:41] offset:1024 sc1\n\t" \
  "v_mfma_f32_16x16x32_bf16 v[36:39], v[188:191], v[152:155], v[36:39]\n\t" \
  "ds_read_b128 v[152:155], v10 offset:4096\n\t" \
  "v_mfma_f32_16x16x32_bf16 v[44:47], v[188:191], v[156:159], v[44:47]\n\t" \
  "ds_read_b64_tr_b16 v[200:201], v11 offset:1024\n\t" \
  "global_load_lds_dwordx4 v3, s[42:43] offset:2048\n\t" \
  "v_mfma_f32_16x16x32_bf16 v[40:43], v[184:187], v[156:159], v[40:43]\n\t" \
  "ds_read_b64_tr_b16 v[202:203], v11 offset:5120\n\t" \
  "v_mfma_f32_16x16x32_bf16 v[108:111], v[180:183], v[156:159], v[108:111]\n\t" \
  "global_load_lds_dwordx4 v[4:5], off offset:3072\n\t" \
  "v_mfma_f32_16x16x32_bf16 v[104:107], v[176:179], v[156:159], v[104:107]\n\t" \
  "ds_read_b128 v[156:159], v10 offset:5120\n\t" \
  "s_waitcnt lgkmcnt(10)\n\t" \
  "v_mfma_f32_16x16x32_bf16 v[112:115], v[176:179], v[160:163], v[112:115]\n\t" \
  "ds_read_b64_tr_b16 v[204:205], v12 offset:1024\n\t" \
  "s_add_u32 s40, s40, 1024\n\t" \
  "s_addc_u32 s41, s41, 0\n\t" \
  "v_mfma_f32_16x16x32_bf16 v[116:119], v[180:183], v[160:163], v[116:119]\n\t" \
  "ds_read_b64_tr_b16 v[206:207], v12 offset:5120\n\t" \
  "s_add_u32 s42, s42, 0x40000\n\t" \
  "s_addc_u32 s43, s43, 0\n\t" \
  "v_mfma_f32_16x16x32_bf16 v[48:51], v[184:187], v[160:163], v[48:51]\n\t" \
  "v_lshl_add_u64 v[4:5], v[4:5], 0, s[44:45]\n\t" \
  "v_mfma_f32_16x16x32_bf16 v[52:55], v[188:191], v[160:163], v[52:55]\n\t" \
  "ds_read_b128 v[160:163], v10 offset:8192\n\t" \
  "v_mfma_f32_16x16x32_bf16 v[60:63], v[188:191], v[164:167], v[60:63]\n\t" \
  "v_mfma_f32_16x16x32_bf16 v[56:59], v[184:187], v[164:167], v[56:59]\n\t" \
  "v_mfma_f32_16x16x32_bf16 v[124:127], v[180:183], v[164:167], v[124:127]\n\t" \
  "v_mfma_f32_16x16x32_bf16 v[120:123], v[176:179], v[164:167], v[120:123]\n\t" \
  "ds_read_b128 v[164:167], v10 offset:9216\n\t" \
  "v_mfma_f32_16x16x32_bf16 v[128:131], v[176:179], v[168:171], v[128:131]\n\t" \
  "v_mfma_f32_16x16x32_bf16 v[132:135], v[180:183], v[168:171], v[132:135]\n\t" \
  "v_mfma_f32_16x16x32_bf16 v[64:67], v[184:187], v[168:171], v[64:67]\n\t" \
  "v_mfma_f32_16x16x32_bf16 v[68:71], v[188:191], v[168:171], v[68:71]\n\t" \
  "ds_read_b128 v[168:171], v10 offset:12288\n\t" \
  "v_mfma_f32_16x16x32_bf16 v[76:79], v[188:191], v[172:175], v[76:79]\n\t" \
  "v_mfma_f32_16x16x32_bf16 v[72:75], v[184:187], v[172:175], v[72:75]\n\t" \
  "v_mfma_f32_16x16x32_bf16 v[140:143], v[180:183], v[172:175], v[140:143]\n\t" \
  "v_mfma_f32_16x16x32_bf16 v[136:139], v[176:179], v[172:175], v[136:139]\n\t" \
  "ds_read_b128 v[172:175], v10 offset:13312\n\t" \
  "s_waitcnt lgkmcnt(4)\n\t" \
  "v_mfma_f32_16x16x32_bf16 v[80:83], v[192:195], v[144:147], v[80:83]\n\t" \
  "s_waitcnt vmcnt(10)\n\t" \
  "s_barrier\n\t" \
  "v_mfma_f32_16x16x32_bf16 v[84:87], v[196:199], v[144:147], v[84:87]\n\t" \
  "v_add_u32_e32 v10, 0x8000, v6\n\t" \
  "v_add_u32_e32 v11, 0x8000, v7\n\t" \
  "v_mfma_f32_16x16x32_bf16 v[16:19], v[200:203], v[144:147], v[16:19]\n\t" \
  "v_xor_b32_e32 v12, 32, v11\n\t" \
  "v_mfma_f32_16x16x32_bf16 v[20:23], v[204:207], v[144:147], v[20:23]\n\t" \
  "ds_read_b128 v[144:147], v10 offset:0\n\t" \
  "v_mfma_f32_16x16x32_bf16 v[28:31], v[204:207], v[148:151], v[28:31]\n\t" \
  "ds_read_b64_tr_b16 v[176:177], v11 offset:0\n\t" \
  "v_mfma_f32_16x16x32_bf16 v[24:27], v[200:203], v[148:151], v[24:27]\n\t" \
  "ds_read_b64_tr_b16 v[178:179], v11 offset:4096\n\t" \
  "v_mfma_f32_16x16x32_bf16 v[92:95], v[196:199], v[148:151], v[92:95]\n\t" \
  "s_add_u32 m0, s62, 0x20000\n\t" \
  "v_mfma_f32_16x16x32_bf16 v[88:91], v[192:195], v[148:151], v[88:91]\n\t" \
  "ds_read_b128 v[148:151], v10 offset:1024\n\t" \
  "v_mfma_f32_16x16x32_bf16 v[96:99], v[192:195], v[152:155], v[96:99]\n\t" \
  "ds_read_b64_tr_b16 v[180:181], v12 offset:0\n\t" \
  "global_load_lds_dwordx4 v1, s[40:41] offset:0 sc1\n\t" \
  "v_add_u32_e32 v13, 0xa000, v8\n\t" \
  "global_store_dwordx4 v13, v[208:211], s[50:51] sc1\n\t" \
  "v_mfma_f32_16x16x32_bf16 v[100:103], v[196:199], v[152:155], v[100:103]\n\t" \
  "ds_read_b64_tr_b16 v[182:183], v12 offset:4096\n\t" \
  "v_mfma_f32_16x16x32_bf16 v[32:35], v[200:203], v[152:155], v[32:35]\n\t" \
  "global_load_lds_dwordx4 v2, s[40:41] offset:1024 sc1\n\t" \
  "v_mfma_f32_16x16x32_bf16 v[36:39], v[204:207], v[152:155], v[36:39]\n\t" \
  "ds_read_b128 v[152:155], v10 offset:4096\n\t" \
  "v_mfma_f32_16x16x32_bf16 v[44:47], v[204:207], v[156:159], v[44:47]\n\t" \
  "ds_read_b64_tr_b16 v[184:185], v11 offset:1024\n\t" \
  "global_load_lds_dwordx4 v3, s[42:43] offset:2048\n\t" \
  "v_mfma_f32_16x16x32_bf16 v[40:43], v[200:203], v[156:159], v[40:43]\n\t" \
  "ds_read_b64_tr_b16 v[186:187], v11 offset:5120\n\t" \
  "v_mfma_f32_16x16x32_bf16 v[108:111], v[196:199], v[156:159], v[108:111]\n\t" \
  "global_load_lds_dwordx4 v[4:5], off offset:3072\n\t" \
  "v_mfma_f32_16x16x32_bf16 v[104:107], v[192:195], v[156:159], v[104:107]\n\t" \
  "ds_read_b128 v[156:159], v10 offset:5120\n\t" \
  "s_waitcnt lgkmcnt(10)\n\t" \
  "v_mfma_f32_16x16x32_bf16 v[112:115], v[192:195], v[160:163], v[112:115]\n\t" \
  "ds_read_b64_tr_b16 v[188:189], v12 offset:1024\n\t" \
  "s_add_u32 s40, s40, 1024\n\t" \
  "s_addc_u32 s41, s41, 0\n\t" \
  "v_mfma_f32_16x16x32_bf16 v[116:119], v[196:199], v[160:163], v[116:119]\n\t" \
  "ds_read_b64_tr_b16 v[190:191], v12 offset:5120\n\t" \
  "s_add_u32 s42, s42, 0x40000\n\t" \
  "s_addc_u32 s43, s43, 0\n\t" \
  "v_mfma_f32_16x16x32_bf16 v[48:51], v[200:203], v[160:163], v[48:51]\n\t" \
  "v_lshl_add_u64 v[4:5], v[4:5], 0, s[44:45]\n\t" \
  "v_mfma_f32_16x16x32_bf16 v[52:55], v[204:207], v[160:163], v[52:55]\n\t" \
  "ds_read_b128 v[160:163], v10 offset:8192\n\t" \
  "v_mfma_f32_16x16x32_bf16 v[60:63], v[204:207], v[164:167], v[60:63]\n\t" \
  "v_mfma_f32_16x16x32_bf16 v[56:59], v[200:203], v[164:167], v[56:59]\n\t" \
  "v_mfma_f32_16x16x32_bf16 v[124:127], v[196:199], v[164:167], v[124:127]\n\t" \
  "v_mfma_f32_16x16x32_bf16 v[120:123], v[192:195], v[164:167], v[120:123]\n\t" \
  "ds_read_b128 v[164:167], v10 offset:9216\n\t" \
  "v_mfma_f32_16x16x32_bf16 v[128:131], v[192:195], v[168:171], v[128:131]\n\t" \
  "v_mfma_f32_16x16x32_bf16 v[132:135], v[196:199], v[168:171], v[132:135]\n\t" \
  "v_mfma_f32_16x16x32_bf16 v[64:67], v[200:203], v[168:171], v[64:67]\n\t" \
  "v_mfma_f32_16x16x32_bf16 v[68:71], v[204:207], v[168:171], v[68:71]\n\t" \
  "ds_read_b128 v[168:171], v10 offset:12288\n\t" \
  "v_mfma_f32_16x16x32_bf16 v[76:79], v[204:207], v[172:175], v[76:79]\n\t" \
  "v_mfma_f32_16x16x32_bf16 v[72:75], v[200:203], v[172:175], v[72:75]\n\t" \
  "v_mfma_f32_16x16x32_bf16 v[140:143], v[196:199], v[172:175], v[140:143]\n\t" \
  "v_mfma_f32_16x16x32_bf16 v[136:139], v[192:195], v[172:175], v[136:139]\n\t" \
  "ds_read_b128 v[172:175], v10 offset:13312\n\t" \
  "s_waitcnt lgkmcnt(4)\n\t" \
  "v_mfma_f32_16x16x32_bf16 v[80:83], v[176:179], v[144:147], v[80:83]\n\t" \
  "s_waitcnt vmcnt(10)\n\t" \
  "s_barrier\n\t" \
  "v_mfma_f32_16x16x32_bf16 v[84:87], v[180:183], v[144:147], v[84:87]\n\t" \
  "v_add_u32_e32 v10, 0x10000, v6\n\t" \
  "v_add_u32_e32 v11, 0x10000, v7\n\t" \
  "v_mfma_f32_16x16x32_bf16 v[16:19], v[184:187], v[144:147], v[16:19]\n\t" \
  "v_xor_b32_e32 v12, 32, v11\n\t" \
  "v_mfma_f32_16x16x32_bf16 v[20:23], v[188:191], v[144:147], v[20:23]\n\t" \
  "ds_read_b128 v[144:147], v10 offset:0\n\t" \
  "v_mfma_f32_16x16x32_bf16 v[28:31], v[188:191], v[148:151], v[28:31]\n\t" \
  "ds_read_b64_tr_b16 v[192:193], v11 offset:0\n\t" \
  "v_mfma_f32_16x16x32_bf16 v[24:27], v[184:187], v[148:151], v[24:27]\n\t" \
  "ds_read_b64_tr_b16 v[194:195], v11 offset:4096\n\t" \
  "v_mfma_f32_16x16x32_bf16 v[92:95], v[180:183], v[148:151], v[92:95]\n\t" \
  "s_add_u32 m0, s62, 0x0\n\t" \
  "v_mfma_f32_16x16x32_bf16 v[88:91], v[176:179], v[148:151], v[88:91]\n\t" \
  "ds_read_b128 v[148:151], v10 offset:1024\n\t" \
  "v_mfma_f32_16x16x32_bf16 v[96:99], v[176:179], v[152:155], v[96:99]\n\t" \
  "ds_read_b64_tr_b16 v[196:197], v12 offset:0\n\t" \
  "global_load_lds_dwordx4 v1, s[40:41] offset:0 sc1\n\t" \
  "v_add_u32_e32 v13, 0x11a000, v8\n\t" \
  "global_store_dwordx4 v13, v[212:215], s[50:51] sc1\n\t" \
  "v_mfma_f32_16x16x32_bf16 v[100:103], v[180:183], v[152:155], v[100:103]\n\t" \
  "ds_read_b64_tr_b16 v[198:199], v12 offset:4096\n\t" \
  "v_mfma_f32_16x16x32_bf16 v[32:35], v[184:187], v[152:155], v[32:35]\n\t" \
  "global_load_lds_dwordx4 v2, s[40:41] offset:1024 sc1\n\t" \
  "v_mfma_f32_16x16x32_bf16 v[36:39], v[188:191], v[152:155], v[36:39]\n\t" \
  "ds_read_b128 v[152:155], v10 offset:4096\n\t" \
  "v_mfma_f32_16x16x32_bf16 v[44:47], v[188:191], v[156:159], v[44:47]\n\t" \
  "ds_read_b64_tr_b16 v[200:201], v11 offset:1024\n\t" \
  "global_load_lds_dwordx4 v3, s[42:43] offset:2048\n\t" \
  "v_mfma_f32_16x16x32_bf16 v[40:43], v[184:187], v[156:159], v[40:43]\n\t" \
  "ds_read_b64_tr_b16 v[202:203], v11 offset:5120\n\t" \
  "v_mfma_f32_16x16x32_bf16 v[108:111], v[180:183], v[156:159], v[108:111]\n\t" \
  "global_load_lds_dwordx4 v[4:5], off offset:3072\n\t" \
  "v_mfma_f32_16x16x32_bf16 v[104:107], v[176:179], v[156:159], v[104:107]\n\t" \
  "ds_read_b128 v[156:159], v10 offset:5120\n\t" \
  "s_waitcnt lgkmcnt(10)\n\t" \
  "v_mfma_f32_16x16x32_bf16 v[112:115], v[176:179], v[160:163], v[112:115]\n\t" \
  "ds_read_b64_tr_b16 v[204:205], v12 offset:1024\n\t" \
  "s_add_u32 s40, s40, 1024\n\t" \
  "s_addc_u32 s41, s41, 0\n\t" \
  "v_mfma_f32_16x16x32_bf16 v[116:119], v[180:183], v[160:163], v[116:119]\n\t" \
  "ds_read_b64_tr_b16 v[206:207], v12 offset:5120\n\t" \
  "s_add_u32 s42, s42, 0x40000\n\t" \
  "s_addc_u32 s43, s43, 0\n\t" \
  "v_mfma_f32_16x16x32_bf16 v[48:51], v[184:187], v[160:163], v[48:51]\n\t" \
  "v_lshl_add_u64 v[4:5], v[4:5], 0, s[44:45]\n\t" \
  "v_mfma_f32_16x16x32_bf16 v[52:55], v[188:191], v[160:163], v[52:55]\n\t" \
  "ds_read_b128 v[160:163], v10 offset:8192\n\t" \
  "v_mfma_f32_16x16x32_bf16 v[60:63], v[188:191], v[164:167], v[60:63]\n\t" \
  "v_mfma_f32_16x16x32_bf16 v[56:59], v[184:187], v[164:167], v[56:59]\n\t" \
  "v_mfma_f32_16x16x32_bf16 v[124:127], v[180:183], v[164:167], v[124:127]\n\t" \
  "v_mfma_f32_16x16x32_bf16 v[120:123], v[176:179], v[164:167], v[120:123]\n\t" \
  "ds_read_b128 v[164:167], v10 offset:9216\n\t" \
  "v_mfma_f32_16x16x32_bf16 v[128:131], v[176:179], v[168:171], v[128:131]\n\t" \
  "v_mfma_f32_16x16x32_bf16 v[132:135], v[180:183], v[168:171], v[132:135]\n\t" \
  "v_mfma_f32_16x16x32_bf16 v[64:67], v[184:187], v[168:171], v[64:67]\n\t" \
  "v_mfma_f32_16x16x32_bf16 v[68:71], v[188:191], v[168:171], v[68:71]\n\t" \
  "ds_read_b128 v[168:171], v10 offset:12288\n\t" \
  "v_mfma_f32_16x16x32_bf16 v[76:79], v[188:191], v[172:175], v[76:79]\n\t" \
  "v_mfma_f32_16x16x32_bf16 v[72:75], v[184:187], v[172:175], v[72:75]\n\t" \
  "v_mfma_f32_16x16x32_bf16 v[140:143], v[180:183], v[172:175], v[140:143]\n\t" \
  "v_mfma_f32_16x16x32_bf16 v[136:139], v[176:179], v[172:175], v[136:139]\n\t" \
  "ds_read_b128 v[172:175], v10 offset:13312\n\t" \
  "s_waitcnt lgkmcnt(4)\n\t" \
  "v_mfma_f32_16x16x32_bf16 v[80:83], v[192:195], v[144:147], v[80:83]\n\t" \
  "s_waitcnt vmcnt(10)\n\t" \
  "s_barrier\n\t" \
  "v_mfma_f32_16x16x32_bf16 v[84:87], v[196:199], v[144:147], v[84:87]\n\t" \
  "v_add_u32_e32 v10, 0x18000, v6\n\t" \
  "v_add_u32_e32 v11, 0x18000, v7\n\t" \
  "v_mfma_f32_16x16x32_bf16 v[16:19], v[200:203], v[144:147], v[16:19]\n\t" \
  "v_xor_b32_e32 v12, 32, v11\n\t" \
  "v_mfma_f32_16x16x32_bf16 v[20:23], v[204:207], v[144:147], v[20:23]\n\t" \
  "ds_read_b128 v[144:147], v10 offset:0\n\t" \
  "v_mfma_f32_16x16x32_bf16 v[28:31], v[204:207], v[148:151], v[28:31]\n\t" \
  "ds_read_b64_tr_b16 v[176:177], v11 offset:0\n\t" \
  "v_mfma_f32_16x16x32_bf16 v[24:27], v[200:203], v[148:151], v[24:27]\n\t" \
  "ds_read_b64_tr_b16 v[178:179], v11 offset:4096\n\t" \
  "v_mfma_f32_16x16x32_bf16 v[92:95], v[196:199], v[148:151], v[92:95]\n\t" \
  "s_add_u32 m0, s62, 0x8000\n\t" \
  "v_mfma_f32_16x16x32_bf16 v[88:91], v[192:195], v[148:151], v[88:91]\n\t" \
  "ds_read_b128 v[148:151], v10 offset:1024\n\t" \
  "v_mfma_f32_16x16x32_bf16 v[96:99], v[192:195], v[152:155], v[96:99]\n\t" \
  "ds_read_b64_tr_b16 v[180:181], v12 offset:0\n\t" \
  "global_load_lds_dwordx4 v1, s[40:41] offset:0 sc1\n\t" \
  "v_add_u32_e32 v13, 0xa000, v9\n\t" \
  "global_store_dwordx4 v13, v[216:219], s[50:51] sc1\n\t" \
  "s_nop 1\n\t" \
  "v_add_u32_e32 v13, 0x11a000, v9\n\t" \
  "global_store_dwordx4 v13, v[220:223], s[50:51] sc1\n\t" \
  "v_mfma_f32_16x16x32_bf16 v[100:103], v[196:199], v[152:155], v[100:103]\n\t" \
  "ds_read_b64_tr_b16 v[182:183], v12 offset:4096\n\t" \
  "v_mfma_f32_16x16x32_bf16 v[32:35], v[200:203], v[152:155], v[32:35]\n\t" \
  "global_load_lds_dwordx4 v2, s[40:41] offset:1024 sc1\n\t" \
  "v_mfma_f32_16x16x32_bf16 v[36:39], v[204:207], v[152:155], v[36:39]\n\t" \
  "ds_read_b128 v[152:155], v10 offset:4096\n\t" \
  "v_mfma_f32_16x16x32_bf16 v[44:47], v[204:207], v[156:159], v[44:47]\n\t" \
  "ds_read_b64_tr_b16 v[184:185], v11 offset:1024\n\t" \
  "global_load_lds_dwordx4 v3, s[42:43] offset:2048\n\t" \
  "v_mfma_f32_16x16x32_bf16 v[40:43], v[200:203], v[156:159], v[40:43]\n\t" \
  "ds_read_b64_tr_b16 v[186:187], v11 offset:5120\n\t" \
  "v_mfma_f32_16x16x32_bf16 v[108:111], v[196:199], v[156:159], v[108:111]\n\t" \
  "global_load_lds_dwordx4 v[4:5], off offset:3072\n\t" \
  "v_mfma_f32_16x16x32_bf16 v[104:107], v[192:195], v[156:159], v[104:107]\n\t" \
  "ds_read_b128 v[156:159], v10 offset:5120\n\t" \
  "s_waitcnt lgkmcnt(10)\n\t" \
  "v_mfma_f32_16x16x32_bf16 v[112:115], v[192:195], v[160:163], v[112:115]\n\t" \
  "ds_read_b64_tr_b16 v[188:189], v12 offset:1024\n\t" \
  "s_add_u32 s40, s40, 1024\n\t" \
  "s_addc_u32 s41, s41, 0\n\t" \
  "v_mfma_f32_16x16x32_bf16 v[116:119], v[196:199], v[160:163], v[116:119]\n\t" \
  "ds_read_b64_tr_b16 v[190:191], v12 offset:5120\n\t" \
  "s_add_u32 s42, s42, 0x40000\n\t" \
  "s_addc_u32 s43, s43, 0\n\t" \
  "v_mfma_f32_16x16x32_bf16 v[48:51], v[200:203], v[160:163], v[48:51]\n\t" \
  "v_lshl_add_u64 v[4:5], v[4:5], 0, s[44:45]\n\t" \
  "v_mfma_f32_16x16x32_bf16 v[52:55], v[204:207], v[160:163], v[52:55]\n\t" \
  "ds_read_b128 v[160:163], v10 offset:8192\n\t" \
  "v_mfma_f32_16x16x32_bf16 v[60:63], v[204:207], v[164:167], v[60:63]\n\t" \
  "v_mfma_f32_16x16x32_bf16 v[56:59], v[200:203], v[164:167], v[56:59]\n\t" \
  "v_mfma_f32_16x16x32_bf16 v[124:127], v[196:199], v[164:167], v[124:127]\n\t" \
  "v_mfma_f32_16x16x32_bf16 v[120:123], v[192:195], v[164:167], v[120:123]\n\t" \
  "ds_read_b128 v[164:167], v10 offset:9216\n\t" \
  "v_mfma_f32_16x16x32_bf16 v[128:131], v[192:195], v[168:171], v[128:131]\n\t" \
  "v_mfma_f32_16x16x32_bf16 v[132:135], v[196:199], v[168:171], v[132:135]\n\t" \
  "v_mfma_f32_16x16x32_bf16 v[64:67], v[200:203], v[168:171], v[64:67]\n\t" \
  "v_mfma_f32_16x16x32_bf16 v[68:71], v[204:207], v[168:171], v[68:71]\n\t" \
  "ds_read_b128 v[168:171], v10 offset:12288\n\t" \
  "v_mfma_f32_16x16x32_bf16 v[76:79], v[204:207], v[172:175], v[76:79]\n\t" \
  "v_mfma_f32_16x16x32_bf16 v[72:75], v[200:203], v[172:175], v[72:75]\n\t" \
  "v_mfma_f32_16x16x32_bf16 v[140:143], v[196:199], v[172:175], v[140:143]\n\t" \
  "v_mfma_f32_16x16x32_bf16 v[136:139], v[192:195], v[172:175], v[136:139]\n\t" \
  "ds_read_b128 v[172:175], v10 offset:13312\n\t" \
  "s_waitcnt lgkmcnt(4)\n\t" \
  "v_mfma_f32_16x16x32_bf16 v[80:83], v[176:179], v[144:147], v[80:83]\n\t" \
  "s_waitcnt vmcnt(11)\n\t" \
  "s_barrier\n\t" \
  "v_mfma_f32_16x16x32_bf16 v[84:87], v[180:183], v[144:147], v[84:87]\n\t" \
  "v_add_u32_e32 v10, 0x20000, v6\n\t" \
  "v_add_u32_e32 v11, 0x20000, v7\n\t" \
  "v_mfma_f32_16x16x32_bf16 v[16:19], v[184:187], v[144:147], v[16:19]\n\t" \
  "v_xor_b32_e32 v12, 32, v11\n\t" \
  "v_mfma_f32_16x16x32_bf16 v[20:23], v[188:191], v[144:147], v[20:23]\n\t" \
  "ds_read_b128 v[144:147], v10 offset:0\n\t" \
  "v_mfma_f32_16x16x32_bf16 v[28:31], v[188:191], v[148:151], v[28:31]\n\t" \
  "ds_read_b64_tr_b16 v[192:193], v11 offset:0\n\t" \
  "v_mfma_f32_16x16x32_bf16 v[24:27], v[184:187], v[148:151], v[24:27]\n\t" \
  "ds_read_b64_tr_b16 v[194:195], v11 offset:4096\n\t" \
  "v_mfma_f32_16x16x32_bf16 v[92:95], v[180:183], v[148:151], v[92:95]\n\t" \
  "v_mfma_f32_16x16x32_bf16 v[88:91], v[176:179], v[148:151], v[88:91]\n\t" \
  "ds_read_b128 v[148:151], v10 offset:1024\n\t" \
  "v_mfma_f32_16x16x32_bf16 v[96:99], v[176:179], v[152:155], v[96:99]\n\t" \
  "ds_read_b64_tr_b16 v[196:197], v12 offset:0\n\t" \
  "v_mfma_f32_16x16x32_bf16 v[100:103], v[180:183], v[152:155], v[100:103]\n\t" \
  "ds_read_b64_tr_b16 v[198:199], v12 offset:4096\n\t" \
  "v_mfma_f32_16x16x32_bf16 v[32:35], v[184:187], v[152:155], v[32:35]\n\t" \
  "v_mfma_f32_16x16x32_bf16 v[36:39], v[188:191], v[152:155], v[36:39]\n\t" \
  "ds_read_b128 v[152:155], v10 offset:4096\n\t" \
  "v_mfma_f32_16x16x32_bf16 v[44:47], v[188:191], v[156:159], v[44:47]\n\t" \
  "ds_read_b64_tr_b16 v[200:201], v11 offset:1024\n\t" \
  "v_mfma_f32_16x16x32_bf16 v[40:43], v[184:187], v[156:159], v[40:43]\n\t" \
  "ds_read_b64_tr_b16 v[202:203], v11 offset:5120\n\t" \
  "v_mfma_f32_16x16x32_bf16 v[108:111], v[180:183], v[156:159], v[108:111]\n\t" \
  "v_mfma_f32_16x16x32_bf16 v[104:107], v[176:179], v[156:159], v[104:107]\n\t" \
  "ds_read_b128 v[156:159], v10 offset:5120\n\t" \
  "s_waitcnt lgkmcnt(10)\n\t" \
  "v_mfma_f32_16x16x32_bf16 v[112:115], v[176:179], v[160:163], v[112:115]\n\t" \
  "ds_read_b64_tr_b16 v[204:205], v12 offset:1024\n\t" \
  "v_mfma_f32_16x16x32_bf16 v[116:119], v[180:183], v[160:163], v[116:119]\n\t" \
  "ds_read_b64_tr_b16 v[206:207], v12 offset:5120\n\t" \
  "v_mfma_f32_16x16x32_bf16 v[48:51], v[184:187], v[160:163], v[48:51]\n\t" \
  "v_mfma_f32_16x16x32_bf16 v[52:55], v[188:191], v[160:163], v[52:55]\n\t" \
  "ds_read_b128 v[160:163], v10 offset:8192\n\t" \
  "v_mfma_f32_16x16x32_bf16 v[60:63], v[188:191], v[164:167], v[60:63]\n\t" \
  "s_cmp_lt_u32 s66, 4\n\t" \
  "s_cbranch_scc1 CX_%=\n\t" \
  "s_getpc_b64 s[54:55]\n\t" \
  "CT1_%=:\n\t" \
  "s_lshl_b32 s52, %[w], 13\n\t" \
  "v_lshl_add_u32 v14, v0, 7, s52\n\t" \
  "v_min_u32_e32 v14, FIN_%=-CT1_%=, v14\n\t" \
  "global_load_dword v15, v14, s[54:55]\n\t" \
  "CX_%=:\n\t" \
  "v_mfma_f32_16x16x32_bf16 v[56:59], v[184:187], v[164:167], v[56:59]\n\t" \
  "v_mfma_f32_16x16x32_bf16 v[124:127], v[180:183], v[164:167], v[124:127]\n\t" \
  "v_mfma_f32_16x16x32_bf16 v[120:123], v[176:179], v[164:167], v[120:123]\n\t" \
  "ds_read_b128 v[164:167], v10 offset:9216\n\t" \
  "v_mfma_f32_16x16x32_bf16 v[128:131], v[176:179], v[168:171], v[128:131]\n\t" \
  "v_mfma_f32_16x16x32_bf16 v[132:135], v[180:183], v[168:171], v[132:135]\n\t" \
  "v_mfma_f32_16x16x32_bf16 v[64:67], v[184:187], v[168:171], v[64:67]\n\t" \
  "v_mfma_f32_16x16x32_bf16 v[68:71], v[188:191], v[168:171], v[68:71]\n\t" \
  "ds_read_b128 v[168:171], v10 offset:12288\n\t" \
  "v_mfma_f32_16x16x32_bf16 v[76:79], v[188:191], v[172:175], v[76:79]\n\t" \
  "v_mfma_f32_16x16x32_bf16 v[72:75], v[184:187], v[172:175], v[72:75]\n\t" \
  "v_mfma_f32_16x16x32_bf16 v[140:143], v[180:183], v[172:175], v[140:143]\n\t" \
  "v_mfma_f32_16x16x32_bf16 v[136:139], v[176:179], v[172:175], v[136:139]\n\t" \
  "ds_read_b128 v[172:175], v10 offset:13312\n\t" \
  "s_waitcnt lgkmcnt(4)\n\t" \
  "v_mfma_f32_16x16x32_bf16 v[80:83], v[192:195], v[144:147], v[80:83]\n\t" \
  "s_waitcnt vmcnt(0)\n\t" \
  "s_barrier\n\t" \
  "v_mfma_f32_16x16x32_bf16 v[84:87], v[196:199], v[144:147], v[84:87]\n\t" \
  "v_add_u32_e32 v10, 0x0, v6\n\t" \
  "v_add_u32_e32 v11, 0x0, v7\n\t" \
  "v_mfma_f32_16x16x32_bf16 v[16:19], v[200:203], v[144:147], v[16:19]\n\t" \
  "v_xor_b32_e32 v12, 32, v11\n\t" \
  "s_bfe_u32 s52, %[bid], 0x10006\n\t" \
  "s_lshl_b32 s53, s38, 1\n\t" \
  "s_add_u32 s52, s52, s53\n\t" \
  "s_lshl_b32 s52, s52, 1\n\t" \
  "s_add_u32 s52, s52, s64\n\t" \
  "s_lshl_b32 s52, s52, 3\n\t" \
  "s_add_u32 s52, s52, s66\n\t" \
  "s_lshl_b32 s52, s52, 3\n\t" \
  "s_add_u32 s52, s52, 0x12000\n\t" \
  "s_cmp_lg_u32 %[w], 0\n\t" \
  "s_cbranch_scc1 PBk_%=\n\t" \
  "v_mov_b32_e32 v14, s36\n\t" \
  "v_mov_b32_e32 v15, s37\n\t" \
  "v_mov_b32_e32 v13, s52\n\t" \
  "s_mov_b64 exec, 1\n\t" \
  "global_store_dwordx2 v13, v[14:15], s[60:61] sc1\n\t" \
  "s_mov_b64 exec, -1\n\t" \
  "PBk_%=:\n\t" \
  "v_mfma_f32_16x16x32_bf16 v[20:23], v[204:207], v[144:147], v[20:23]\n\t" \
  "ds_read_b128 v[144:147], v10 offset:0\n\t" \
  "v_mfma_f32_16x16x32_bf16 v[28:31], v[204:207], v[148:151], v[28:31]\n\t" \
  "ds_read_b64_tr_b16 v[176:177], v11 offset:0\n\t" \
  "v_mfma_f32_16x16x32_bf16 v[24:27], v[200:203], v[148:151], v[24:27]\n\t" \
  "ds_read_b64_tr_b16 v[178:179], v11 offset:4096\n\t" \
  "v_mfma_f32_16x16x32_bf16 v[92:95], v[196:199], v[148:151], v[92:95]\n\t" \
  "v_mfma_f32_16x16x32_bf16 v[88:91], v[192:195], v[148:151], v[88:91]\n\t" \
  "ds_read_b128 v[148:151], v10 offset:1024\n\t" \
  "v_mfma_f32_16x16x32_bf16 v[96:99], v[192:195], v[152:155], v[96:99]\n\t" \
  "ds_read_b64_tr_b16 v[180:181], v12 offset:0\n\t" \
  "v_mfma_f32_16x16x32_bf16 v[100:103], v[196:199], v[152:155], v[100:103]\n\t" \
  "ds_read_b64_tr_b16 v[182:183], v12 offset:4096\n\t" \
  "v_mfma_f32_16x16x32_bf16 v[32:35], v[200:203], v[152:155], v[32:35]\n\t" \
  "s_lshr_b32 s52, s65, 1\n\t" \
  "s_lshl_b32 s52, s52, 7\n\t" \
  "s_add_u32 s52, s52, 0x12000\n\t" \
  "v_mfma_f32_16x16x32_bf16 v[36:39], v[204:207], v[152:155], v[36:39]\n\t" \
  "ds_read_b128 v[152:155], v10 offset:4096\n\t" \
  "v_mfma_f32_16x16x32_bf16 v[44:47], v[204:207], v[156:159], v[44:47]\n\t" \
  "ds_read_b64_tr_b16 v[184:185], v11 offset:1024\n\t" \
  "v_mfma_f32_16x16x32_bf16 v[40:43], v[200:203], v[156:159], v[40:43]\n\t" \
  "ds_read_b64_tr_b16 v[186:187], v11 offset:5120\n\t" \
  "v_mfma_f32_16x16x32_bf16 v[108:111], v[196:199], v[156:159], v[108:111]\n\t" \
  "v_and_b32_e32 v3, 15, v0\n\t" \
  "v_lshlrev_b32_e32 v3, 3, v3\n\t" \
  "v_add_u32_e32 v3, s52, v3\n\t" \
  "v_and_b32_e32 v2, 0xffffffc7, v3\n\t" \
  "v_mfma_f32_16x16x32_bf16 v[104:107], v[192:195], v[156:159], v[104:107]\n\t" \
  "ds_read_b128 v[156:159], v10 offset:5120\n\t" \
  "s_waitcnt lgkmcnt(10)\n\t" \
  "v_mfma_f32_16x16x32_bf16 v[112:115], v[192:195], v[160:163], v[112:115]\n\t" \
  "ds_read_b64_tr_b16 v[188:189], v12 offset:1024\n\t" \
  "v_mfma_f32_16x16x32_bf16 v[116:119], v[196:199], v[160:163], v[116:119]\n\t" \
  "ds_read_b64_tr_b16 v[190:191], v12 offset:5120\n\t" \
  "v_mfma_f32_16x16x32_bf16 v[48:51], v[200:203], v[160:163], v[48:51]\n\t" \
  "v_lshlrev_b32_e32 v1, 4, v0\n\t" \
  "v_mfma_f32_16x16x32_bf16 v[52:55], v[204:207], v[160:163], v[52:55]\n\t" \
  "ds_read_b128 v[160:163], v10 offset:8192\n\t" \
  "v_mfma_f32_16x16x32_bf16 v[60:63], v[204:207], v[164:167], v[60:63]\n\t" \
  "v_mfma_f32_16x16x32_bf16 v[56:59], v[200:203], v[164:167], v[56:59]\n\t" \
  "v_mfma_f32_16x16x32_bf16 v[124:127], v[196:199], v[164:167], v[124:127]\n\t" \
  "s_mul_i32 s52, s65, 1114112\n\t" \
  "s_add_u32 s52, s52, 0x1400000\n\t" \
  "s_lshr_b32 s53, %[w], 2\n\t" \
  "s_lshl_b32 s53, s53, 14\n\t" \
  "s_add_u32 s53, s53, s62\n\t" \
  "s_add_u32 s53, s53, s52\n\t" \
  "v_mfma_f32_16x16x32_bf16 v[120:123], v[192:195], v[164:167], v[120:123]\n\t" \
  "ds_read_b128 v[164:167], v10 offset:9216\n\t" \
  "v_mfma_f32_16x16x32_bf16 v[128:131], v[192:195], v[168:171], v[128:131]\n\t" \
  "v_mfma_f32_16x16x32_bf16 v[132:135], v[196:199], v[168:171], v[132:135]\n\t" \
  "v_mfma_f32_16x16x32_bf16 v[64:67], v[200:203], v[168:171], v[64:67]\n\t" \
  "s_mov_b64 s[40:41], %[ws]\n\t" \
  "s_add_u32 s40, s40, s53\n\t" \
  "s_addc_u32 s41, s41, 0\n\t" \
  "v_mfma_f32_16x16x32_bf16 v[68:71], v[204:207], v[168:171], v[68:71]\n\t" \
  "ds_read_b128 v[168:171], v10 offset:12288\n\t" \
  "v_mfma_f32_16x16x32_bf16 v[76:79], v[204:207], v[172:175], v[76:79]\n\t" \
  "v_mfma_f32_16x16x32_bf16 v[72:75], v[200:203], v[172:175], v[72:75]\n\t" \
  "v_mfma_f32_16x16x32_bf16 v[140:143], v[196:199], v[172:175], v[140:143]\n\t" \
  "s_add_u32 s52, s52, s62\n\t" \
  "s_add_u32 s52, s52, 0x10000\n\t" \
  "s_mov_b64 s[82:83], %[ws]\n\t" \
  "s_add_u32 s82, s82, s52\n\t" \
  "s_addc_u32 s83, s83, 0\n\t" \
  "v_mfma_f32_16x16x32_bf16 v[136:139], v[192:195], v[172:175], v[136:139]\n\t" \
  "ds_read_b128 v[172:175], v10 offset:13312\n\t" \
  "s_waitcnt lgkmcnt(4)\n\t" \
  "v_mfma_f32_16x16x32_bf16 v[80:83], v[176:179], v[144:147], v[80:83]\n\t" \
  "s_barrier\n\t" \
  "v_mfma_f32_16x16x32_bf16 v[84:87], v[180:183], v[144:147], v[84:87]\n\t" \
  "v_add_u32_e32 v10, 0x8000, v6\n\t" \
  "v_add_u32_e32 v11, 0x8000, v7\n\t" \
  "v_mfma_f32_16x16x32_bf16 v[16:19], v[184:187], v[144:147], v[16:19]\n\t" \
  "v_xor_b32_e32 v12, 32, v11\n\t" \
  "v_mfma_f32_16x16x32_bf16 v[20:23], v[188:191], v[144:147], v[20:23]\n\t" \
  "ds_read_b128 v[144:147], v10 offset:0\n\t" \
  "v_mfma_f32_16x16x32_bf16 v[28:31], v[188:191], v[148:151], v[28:31]\n\t" \
  "ds_read_b64_tr_b16 v[192:193], v11 offset:0\n\t" \
  "v_mfma_f32_16x16x32_bf16 v[24:27], v[184:187], v[148:151], v[24:27]\n\t" \
  "ds_read_b64_tr_b16 v[194:195], v11 offset:4096\n\t" \
  "v_mfma_f32_16x16x32_bf16 v[92:95], v[180:183], v[148:151], v[92:95]\n\t" \
  "s_sub_u32 s59, 15, s66\n\t" \
  "s_cmp_lt_u32 %[w], 4\n\t" \
  "s_cselect_b32 s59, s59, s66\n\t" \
  "s_lshl_b32 s52, s59, 7\n\t" \
  "s_lshl_b32 s53, s63, 5\n\t" \
  "s_add_u32 s52, s52, s53\n\t" \
  "s_lshl_b32 s67, s59, 1\n\t" \
  "s_add_u32 s67, s67, 2\n\t" \
  "v_mfma_f32_16x16x32_bf16 v[88:91], v[176:179], v[148:151], v[88:91]\n\t" \
  "ds_read_b128 v[148:151], v10 offset:1024\n\t" \
  "v_mfma_f32_16x16x32_bf16 v[96:99], v[176:179], v[152:155], v[96:99]\n\t" \
  "ds_read_b64_tr_b16 v[196:197], v12 offset:0\n\t" \
  "v_mfma_f32_16x16x32_bf16 v[100:103], v[180:183], v[152:155], v[100:103]\n\t" \
  "ds_read_b64_tr_b16 v[198:199], v12 offset:4096\n\t" \
  "v_mfma_f32_16x16x32_bf16 v[32:35], v[184:187], v[152:155], v[32:35]\n\t" \
  "s_lshl_b32 s68, s66, 1\n\t" \
  "s_sub_u32 s68, 32, s68\n\t" \
  "s_mul_i32 s52, s59, 28\n\t" \
  "s_lshr_b32 s54, s65, 3\n\t" \
  "s_sub_u32 s64, s65, s54\n\t" \
  "s_and_b32 s54, s65, 7\n\t" \
  "s_cmp_eq_u32 s54, 7\n\t" \
  "s_cselect_b32 s64, 28, s64\n\t" \
  "s_add_u32 s52, s52, s64\n\t" \
  "s_lshl_b32 s52, s52, 14\n\t" \
  "v_mfma_f32_16x16x32_bf16 v[36:39], v[188:191], v[152:155], v[36:39]\n\t" \
  "ds_read_b128 v[152:155], v10 offset:4096\n\t" \
  "v_mfma_f32_16x16x32_bf16 v[44:47], v[188:191], v[156:159], v[44:47]\n\t" \
  "ds_read_b64_tr_b16 v[200:201], v11 offset:1024\n\t" \
  "v_mfma_f32_16x16x32_bf16 v[40:43], v[184:187], v[156:159], v[40:43]\n\t" \
  "ds_read_b64_tr_b16 v[202:203], v11 offset:5120\n\t" \
  "v_mfma_f32_16x16x32_bf16 v[108:111], v[180:183], v[156:159], v[108:111]\n\t" \
  "s_lshr_b32 s54, s63, 1\n\t" \
  "s_lshl_b32 s54, s54, 13\n\t" \
  "s_add_u32 s52, s52, s54\n\t" \
  "s_and_b32 s54, %[w], 1\n\t" \
  "s_lshl_b32 s54, s54, 9\n\t" \
  "s_add_u32 s52, s52, s54\n\t" \
  "s_add_u32 s52, s52, 0x4800000\n\t" \
  "s_mov_b64 s[50:51], %[ws]\n\t" \
  "s_add_u32 s50, s50, s52\n\t" \
  "s_addc_u32 s51, s51, 0\n\t" \
  "v_mfma_f32_16x16x32_bf16 v[104:107], v[176:179], v[156:159], v[104:107]\n\t" \
  "ds_read_b128 v[156:159], v10 offset:5120\n\t" \
  "s_waitcnt lgkmcnt(10)\n\t" \
  "v_mfma_f32_16x16x32_bf16 v[112:115], v[176:179], v[160:163], v[112:115]\n\t" \
  "ds_read_b64_tr_b16 v[204:205], v12 offset:1024\n\t" \
  "v_mfma_f32_16x16x32_bf16 v[116:119], v[180:183], v[160:163], v[116:119]\n\t" \
  "ds_read_b64_tr_b16 v[206:207], v12 offset:5120\n\t" \
  "v_mfma_f32_16x16x32_bf16 v[48:51], v[184:187], v[160:163], v[48:51]\n\t" \
  "s_lshl_b32 s52, s59, 6\n\t" \
  "s_lshl_b32 s54, s65, 1\n\t" \
  "s_add_u32 s52, s52, s54\n\t" \
  "s_lshl_b32 s52, s52, 14\n\t" \
  "s_lshl_b32 s54, s63, 11\n\t" \
  "v_mfma_f32_16x16x32_bf16 v[52:55], v[188:191], v[160:163], v[52:55]\n\t" \
  "ds_read_b128 v[160:163], v10 offset:8192\n\t" \
  "v_mfma_f32_16x16x32_bf16 v[60:63], v[188:191], v[164:167], v[60:63]\n\t" \
  "global_load_dwordx2 v[14:15], v2, s[60:61] sc1\n\t" \
  "v_mfma_f32_16x16x32_bf16 v[56:59], v[184:187], v[164:167], v[56:59]\n\t" \
  "v_mfma_f32_16x16x32_bf16 v[124:127], v[180:183], v[164:167], v[124:127]\n\t" \
  "s_add_u32 s52, s52, s54\n\t" \
  "s_add_u32 s52, s52, 0x3800000\n\t" \
  "s_mov_b64 s[56:57], %[ws]\n\t" \
  "s_add_u32 s56, s56, s52\n\t" \
  "s_addc_u32 s57, s57, 0\n\t" \
  "v_mfma_f32_16x16x32_bf16 v[120:123], v[176:179], v[164:167], v[120:123]\n\t" \
  "ds_read_b128 v[164:167], v10 offset:9216\n\t" \
  "v_mfma_f32_16x16x32_bf16 v[128:131], v[176:179], v[168:171], v[128:131]\n\t" \
  "v_mfma_f32_16x16x32_bf16 v[132:135], v[180:183], v[168:171], v[132:135]\n\t" \
  "v_mfma_f32_16x16x32_bf16 v[64:67], v[184:187], v[168:171], v[64:67]\n\t" \
  "v_mfma_f32_16x16x32_bf16 v[68:71], v[188:191], v[168:171], v[68:71]\n\t" \
  "ds_read_b128 v[168:171], v10 offset:12288\n\t" \
  "v_mfma_f32_16x16x32_bf16 v[76:79], v[188:191], v[172:175], v[76:79]\n\t" \
  "v_mfma_f32_16x16x32_bf16 v[72:75], v[184:187], v[172:175], v[72:75]\n\t" \
  "v_mfma_f32_16x16x32_bf16 v[140:143], v[180:183], v[172:175], v[140:143]\n\t" \
  "v_mfma_f32_16x16x32_bf16 v[136:139], v[176:179], v[172:175], v[136:139]\n\t" \
  "ds_read_b128 v[172:175], v10 offset:13312\n\t" \
  "s_waitcnt lgkmcnt(4)\n\t" \
  "v_mfma_f32_16x16x32_bf16 v[80:83], v[192:195], v[144:147], v[80:83]\n\t" \
  "s_waitcnt lgkmcnt(0)\n\t" \
  "s_barrier\n\t" \
  "v_mfma_f32_16x16x32_bf16 v[84:87], v[196:199], v[144:147], v[84:87]\n\t" \
  "s_waitcnt vmcnt(0)\n\t" \
  "v_cmp_ne_u64_e32 vcc, s[36:37], v[14:15]\n\t" \
  "s_nop 0\n\t" \
  "s_cbranch_vccnz PSk_%=\n\t" \
  "PRk_%=:\n\t" \
  "s_add_u32 m0, s62, 0x20000\n\t" \
  "s_cmp_lt_u32 s64, 28\n\t" \
  "s_cselect_b64 vcc, -1, 0\n\t" \
  "v_mfma_f32_16x16x32_bf16 v[16:19], v[200:203], v[144:147], v[16:19]\n\t" \
  "global_load_lds_dwordx4 v1, s[40:41] offset:0 sc1\n\t" \
  "v_and_b32_e32 v10, 15, v0\n\t" \
  "v_lshrrev_b32_e32 v11, 4, v0\n\t" \
  "v_lshlrev_b32_e32 v5, 2, v11\n\t" \
  "v_sub_u32_e32 v2, v10, v5\n\t" \
  "v_mfma_f32_16x16x32_bf16 v[20:23], v[204:207], v[144:147], v[20:23]\n\t" \
  "v_lshlrev_b32_e32 v10, 4, v10\n\t" \
  "s_lshr_b32 s52, %[w], 2\n\t" \
  "s_lshl_b32 s52, s52, 15\n\t" \
  "v_mfma_f32_16x16x32_bf16 v[28:31], v[204:207], v[148:151], v[28:31]\n\t" \
  "global_load_lds_dwordx4 v1, s[40:41] offset:1024 sc1\n\t" \
  "s_lshl_b32 s53, s63, 10\n\t" \
  "s_add_u32 s52, s52, s53\n\t" \
  "v_add_u32_e32 v6, s52, v1\n\t" \
  "v_lshrrev_b32_e32 v7, 1, v11\n\t" \
  "v_mfma_f32_16x16x32_bf16 v[24:27], v[200:203], v[148:151], v[24:27]\n\t" \
  "v_lshl_or_b32 v7, v7, 8, v10\n\t" \
  "v_and_b32_e32 v5, 1, v11\n\t" \
  "v_lshl_or_b32 v7, v5, 3, v7\n\t" \
  "v_mfma_f32_16x16x32_bf16 v[92:95], v[196:199], v[148:151], v[92:95]\n\t" \
  "global_load_lds_dwordx4 v1, s[40:41] offset:2048 sc1\n\t" \
  "v_mov_b32_e32 v8, v1\n\t" \
  "v_add_u32_e32 v5, 0x20000, v1\n\t" \
  "s_lshl_b32 s52, %[w], 13\n\t" \
  "v_add_u32_e32 v4, s52, v1\n\t" \
  "v_mfma_f32_16x16x32_bf16 v[88:91], v[192:195], v[148:151], v[88:91]\n\t" \
  "v_add_u32_e32 v9, 0x1000, v1\n\t" \
  "v_mov_b32_e32 v10, 0x3e0293ee\n\t" \
  "v_mov_b32_e32 v12, 0xbfb8aa3b\n\t" \
  "v_mfma_f32_16x16x32_bf16 v[96:99], v[192:195], v[152:155], v[96:99]\n\t" \
  "global_load_lds_dwordx4 v1, s[40:41] offset:3072 sc1\n\t" \
  "v_mul_f32_e32 v80, v80, v10\n\t" \
  "v_mul_f32_e32 v81, v81, v10\n\t" \
  "v_mul_f32_e32 v82, v82, v10\n\t" \
  "v_mul_f32_e32 v83, v83, v10\n\t" \
  "v_mfma_f32_16x16x32_bf16 v[100:103], v[196:199], v[152:155], v[100:103]\n\t" \
  "v_mul_f32_e32 v84, v84, v10\n\t" \
  "v_mul_f32_e32 v85, v85, v10\n\t" \
  "v_mul_f32_e32 v86, v86, v10\n\t" \
  "v_mfma_f32_16x16x32_bf16 v[32:35], v[200:203], v[152:155], v[32:35]\n\t" \
  "v_mul_f32_e32 v87, v87, v10\n\t" \
  "v_cvt_pk_bf16_f32 v208, v80, v81\n\t" \
  "v_cvt_pk_bf16_f32 v209, v82, v83\n\t" \
  "v_mfma_f32_16x16x32_bf16 v[36:39], v[204:207], v[152:155], v[36:39]\n\t" \
  "v_cvt_pk_bf16_f32 v210, v84, v85\n\t" \
  "v_cvt_pk_bf16_f32 v211, v86, v87\n\t" \
  "ds_write_b128 v6, v[208:211] offset:0\n\t" \
  "v_mfma_f32_16x16x32_bf16 v[44:47], v[204:207], v[156:159], v[44:47]\n\t" \
  "v_mul_f32_e32 v88, v88, v10\n\t" \
  "v_mul_f32_e32 v89, v89, v10\n\t" \
  "v_mul_f32_e32 v90, v90, v10\n\t" \
  "v_mul_f32_e32 v91, v91, v10\n\t" \
  "v_mfma_f32_16x16x32_bf16 v[40:43], v[200:203], v[156:159], v[40:43]\n\t" \
  "v_mul_f32_e32 v92, v92, v10\n\t" \
  "v_mul_f32_e32 v93, v93, v10\n\t" \
  "v_mul_f32_e32 v94, v94, v10\n\t" \
  "v_mfma_f32_16x16x32_bf16 v[108:111], v[196:199], v[156:159], v[108:111]\n\t" \
  "v_mul_f32_e32 v95, v95, v10\n\t" \
  "v_cvt_pk_bf16_f32 v212, v88, v89\n\t" \
  "v_cvt_pk_bf16_f32 v213, v90, v91\n\t" \
  "v_mfma_f32_16x16x32_bf16 v[104:107], v[192:195], v[156:159], v[104:107]\n\t" \
  "v_cvt_pk_bf16_f32 v214, v92, v93\n\t" \
  "v_cvt_pk_bf16_f32 v215, v94, v95\n\t" \
  "ds_write_b128 v6, v[212:215] offset:4096\n\t" \
  "v_mfma_f32_16x16x32_bf16 v[112:115], v[192:195], v[160:163], v[112:115]\n\t" \
  "v_mul_f32_e32 v96, v96, v10\n\t" \
  "v_mul_f32_e32 v97, v97, v10\n\t" \
  "v_mul_f32_e32 v98, v98, v10\n\t" \
  "v_mul_f32_e32 v99, v99, v10\n\t" \
  "v_mfma_f32_16x16x32_bf16 v[116:119], v[196:199], v[160:163], v[116:119]\n\t" \
  "v_mul_f32_e32 v100, v100, v10\n\t" \
  "v_mul_f32_e32 v101, v101, v10\n\t" \
  "v_mul_f32_e32 v102, v102, v10\n\t" \
  "v_mfma_f32_16x16x32_bf16 v[48:51], v[200:203], v[160:163], v[48:51]\n\t" \
  "v_mul_f32_e32 v103, v103, v10\n\t" \
  "v_cvt_pk_bf16_f32 v216, v96, v97\n\t" \
  "v_cvt_pk_bf16_f32 v217, v98, v99\n\t" \
  "v_mfma_f32_16x16x32_bf16 v[52:55], v[204:207], v[160:163], v[52:55]\n\t" \
  "v_cvt_pk_bf16_f32 v218, v100, v101\n\t" \
  "v_cvt_pk_bf16_f32 v219, v102, v103\n\t" \
  "ds_write_b128 v6, v[216:219] offset:8192\n\t" \
  "v_mfma_f32_16x16x32_bf16 v[60:63], v[204:207], v[164:167], v[60:63]\n\t" \
  "v_mul_f32_e32 v104, v104, v10\n\t" \
  "v_mul_f32_e32 v105, v105, v10\n\t" \
  "v_mul_f32_e32 v106, v106, v10\n\t" \
  "v_mul_f32_e32 v107, v107, v10\n\t" \
  "v_mfma_f32_16x16x32_bf16 v[56:59], v[200:203], v[164:167], v[56:59]\n\t" \
  "v_mul_f32_e32 v108, v108, v10\n\t" \
  "v_mul_f32_e32 v109, v109, v10\n\t" \
  "v_mul_f32_e32 v110, v110, v10\n\t" \
  "v_mfma_f32_16x16x32_bf16 v[124:127], v[196:199], v[164:167], v[124:127]\n\t" \
  "v_mul_f32_e32 v111, v111, v10\n\t" \
  "v_cvt_pk_bf16_f32 v220, v104, v105\n\t" \
  "v_cvt_pk_bf16_f32 v221, v106, v107\n\t" \
  "v_mfma_f32_16x16x32_bf16 v[120:123], v[192:195], v[164:167], v[120:123]\n\t" \
  "v_cvt_pk_bf16_f32 v222, v108, v109\n\t" \
  "v_cvt_pk_bf16_f32 v223, v110, v111\n\t" \
  "ds_write_b128 v6, v[220:223] offset:12288\n\t" \
  "v_mfma_f32_16x16x32_bf16 v[128:131], v[192:195], v[168:171], v[128:131]\n\t" \
  "v_mul_f32_e32 v112, v112, v10\n\t" \
  "v_mul_f32_e32 v113, v113, v10\n\t" \
  "v_mul_f32_e32 v114, v114, v10\n\t" \
  "v_mul_f32_e32 v115, v115, v10\n\t" \
  "v_mfma_f32_16x16x32_bf16 v[132:135], v[196:199], v[168:171], v[132:135]\n\t" \
  "v_mul_f32_e32 v116, v116, v10\n\t" \
  "v_mul_f32_e32 v117, v117, v10\n\t" \
  "v_mul_f32_e32 v118, v118, v10\n\t" \
  "v_mfma_f32_16x16x32_bf16 v[64:67], v[200:203], v[168:171], v[64:67]\n\t" \
  "v_mul_f32_e32 v119, v119, v10\n\t" \
  "v_cvt_pk_bf16_f32 v224, v112, v113\n\t" \
  "v_cvt_pk_bf16_f32 v225, v114, v115\n\t" \
  "v_mfma_f32_16x16x32_bf16 v[68:71], v[204:207], v[168:171], v[68:71]\n\t" \
  "v_cvt_pk_bf16_f32 v226, v116, v117\n\t" \
  "v_cvt_pk_bf16_f32 v227, v118, v119\n\t" \
  "ds_write_b128 v6, v[224:227] offset:16384\n\t" \
  "v_mfma_f32_16x16x32_bf16 v[76:79], v[204:207], v[172:175], v[76:79]\n\t" \
  "v_mul_f32_e32 v120, v120, v10\n\t" \
  "v_mul_f32_e32 v121, v121, v10\n\t" \
  "v_mul_f32_e32 v122, v122, v10\n\t" \
  "v_mul_f32_e32 v123, v123, v10\n\t" \
  "v_mfma_f32_16x16x32_bf16 v[72:75], v[200:203], v[172:175], v[72:75]\n\t" \
  "v_mul_f32_e32 v124, v124, v10\n\t" \
  "v_mul_f32_e32 v125, v125, v10\n\t" \
  "v_mul_f32_e32 v126, v126, v10\n\t" \
  "v_mfma_f32_16x16x32_bf16 v[140:143], v[196:199], v[172:175], v[140:143]\n\t" \
  "v_mul_f32_e32 v127, v127, v10\n\t" \
  "v_cvt_pk_bf16_f32 v228, v120, v121\n\t" \
  "v_cvt_pk_bf16_f32 v229, v122, v123\n\t" \
  "v_mfma_f32_16x16x32_bf16 v[136:139], v[192:195], v[172:175], v[136:139]\n\t" \
  "v_cvt_pk_bf16_f32 v230, v124, v125\n\t" \
  "v_cvt_pk_bf16_f32 v231, v126, v127\n\t" \
  "ds_write_b128 v6, v[228:231] offset:20480\n\t" \
  "v_mul_f32_e32 v128, v128, v10\n\t" \
  "v_mul_f32_e32 v129, v129, v10\n\t" \
  "v_mul_f32_e32 v130, v130, v10\n\t" \
  "v_mul_f32_e32 v131, v131, v10\n\t" \
  "v_mul_f32_e32 v132, v132, v10\n\t" \
  "v_mul_f32_e32 v133, v133, v10\n\t" \
  "v_mul_f32_e32 v134, v134, v10\n\t" \
  "v_mul_f32_e32 v135, v135, v10\n\t" \
  "v_cvt_pk_bf16_f32 v232, v128, v129\n\t" \
  "v_cvt_pk_bf16_f32 v233, v130, v131\n\t" \
  "v_cvt_pk_bf16_f32 v234, v132, v133\n\t" \
  "v_cvt_pk_bf16_f32 v235, v134, v135\n\t" \
  "ds_write_b128 v6, v[232:235] offset:24576\n\t" \
  "s_nop 0\n\t" \
  "v_mul_f32_e32 v136, v136, v10\n\t" \
  "v_mul_f32_e32 v137, v137, v10\n\t" \
  "v_mul_f32_e32 v138, v138, v10\n\t" \
  "v_mul_f32_e32 v139, v139, v10\n\t" \
  "v_mul_f32_e32 v140, v140, v10\n\t" \
  "v_mul_f32_e32 v141, v141, v10\n\t" \
  "v_mul_f32_e32 v142, v142, v10\n\t" \
  "v_mul_f32_e32 v143, v143, v10\n\t" \
  "v_cvt_pk_bf16_f32 v236, v136, v137\n\t" \
  "v_cvt_pk_bf16_f32 v237, v138, v139\n\t" \
  "v_cvt_pk_bf16_f32 v238, v140, v141\n\t" \
  "v_cvt_pk_bf16_f32 v239, v142, v143\n\t" \
  "ds_write_b128 v6, v[236:239] offset:28672\n\t" \
  "s_mul_i32 s52, s65, 1114112\n\t" \
  "s_add_u32 s52, s52, 0x1400000\n\t" \
  "s_lshl_b32 s42, s63, 13\n\t" \
  "s_add_u32 s52, s52, s42\n\t" \
  "s_add_u32 s52, s52, 0x20000\n\t" \
  "s_mov_b64 s[40:41], %[ws]\n\t" \
  "s_add_u32 s40, s40, s52\n\t" \
  "s_addc_u32 s41, s41, 0\n\t" \
  "s_mov_b32 s44, 0\n\t" \
  "s_mov_b32 s45, 0x41000000\n\t" \
  "s_mov_b32 s43, 1\n\t" \
  "s_sub_u32 s58, s67, 3\n\t" \
  "v_mul_f32_e32 v16, v16, v12\n\t" \
  "s_add_u32 m0, s62, 0x10000\n\t" \
  "s_nop 0\n\t" \
  "global_load_lds_dwordx4 v1, s[82:83] offset:0 sc1\n\t" \
  "v_mul_f32_e32 v17, v17, v12\n\t" \
  "v_mul_f32_e32 v18, v18, v12\n\t" \
  "v_mul_f32_e32 v19, v19, v12\n\t" \
  "v_mul_f32_e32 v24, v24, v12\n\t" \
  "v_mul_f32_e32 v25, v25, v12\n\t" \
  "v_mul_f32_e32 v26, v26, v12\n\t" \
  "v_mul_f32_e32 v27, v27, v12\n\t" \
  "v_exp_f32_e32 v16, v16\n\t" \
  "global_load_lds_dwordx4 v1, s[82:83] offset:1024 sc1\n\t" \
  "v_exp_f32_e32 v17, v17\n\t" \
  "v_exp_f32_e32 v18, v18\n\t" \
  "v_exp_f32_e32 v19, v19\n\t" \
  "v_exp_f32_e32 v24, v24\n\t" \
  "v_exp_f32_e32 v25, v25\n\t" \
  "v_exp_f32_e32 v26, v26\n\t" \
  "v_exp_f32_e32 v27, v27\n\t" \
  "v_add_f32_e32 v16, 1.0, v16\n\t" \
  "global_load_lds_dwordx4 v1, s[82:83] offset:2048 sc1\n\t" \
  "v_add_f32_e32 v17, 1.0, v17\n\t" \
  "v_add_f32_e32 v18, 1.0, v18\n\t" \
  "v_add_f32_e32 v19, 1.0, v19\n\t" \
  "v_add_f32_e32 v24, 1.0, v24\n\t" \
  "v_add_f32_e32 v25, 1.0, v25\n\t" \
  "v_add_f32_e32 v26, 1.0, v26\n\t" \
  "v_add_f32_e32 v27, 1.0, v27\n\t" \
  "v_rcp_f32_e32 v16, v16\n\t" \
  "global_load_lds_dwordx4 v1, s[82:83] offset:3072 sc1\n\t" \
  "v_rcp_f32_e32 v17, v17\n\t" \
  "v_rcp_f32_e32 v18, v18\n\t" \
  "v_rcp_f32_e32 v19, v19\n\t" \
  "v_rcp_f32_e32 v24, v24\n\t" \
  "v_rcp_f32_e32 v25, v25\n\t" \
  "v_rcp_f32_e32 v26, v26\n\t" \
  "v_rcp_f32_e32 v27, v27\n\t" \
  "v_mul_f32_e32 v16, v16, v20\n\t" \
  "v_mul_f32_e32 v17, v17, v21\n\t" \
  "v_mul_f32_e32 v18, v18, v22\n\t" \
  "v_mul_f32_e32 v19, v19, v23\n\t" \
  "v_mul_f32_e32 v24, v24, v28\n\t" \
  "v_mul_f32_e32 v25, v25, v29\n\t" \
  "v_mul_f32_e32 v26, v26, v30\n\t" \
  "v_mul_f32_e32 v27, v27, v31\n\t" \
  "v_cvt_pk_bf16_f32 v20, v16, v17\n\t" \
  "v_cvt_pk_bf16_f32 v21, v18, v19\n\t" \
  "v_cvt_pk_bf16_f32 v28, v24, v25\n\t" \
  "v_cvt_pk_bf16_f32 v29, v26, v27\n\t" \
  "s_cbranch_vccz G1_%=\n\t" \
  "global_store_dwordx2 v7, v[20:21], s[50:51] offset:0 sc1\n\t" \
  "global_store_dwordx2 v7, v[28:29], s[50:51] offset:1024 sc1\n\t" \
  "G1_%=:\n\t" \
  "s_cbranch_vccnz G5_%=\n\t" \
  "s_waitcnt vmcnt(4)\n\t" \
  "G5_%=:\n\t" \
  "s_waitcnt vmcnt(6) lgkmcnt(0)\n\t" \
  "s_barrier\n\t" \
  "ds_read_b128 v[80:83], v4 offset:0\n\t" \
  "ds_read_b128 v[96:99], v4 offset:4096\n\t" \
  "ds_read_b128 v[84:87], v4 offset:1024\n\t" \
  "ds_read_b128 v[100:103], v4 offset:5120\n\t" \
  "ds_read_b128 v[88:91], v4 offset:2048\n\t" \
  "ds_read_b128 v[104:107], v4 offset:6144\n\t" \
  "ds_read_b128 v[92:95], v4 offset:3072\n\t" \
  "ds_read_b128 v[108:111], v4 offset:7168\n\t" \
  "ds_read_b128 v[192:195], v5 offset:0\n\t" \
  "ds_read_b128 v[196:199], v5 offset:4096\n\t" \
  "ds_read_b128 v[200:203], v5 offset:1024\n\t" \
  "ds_read_b128 v[204:207], v5 offset:5120\n\t" \
  "ds_read_b128 v[208:211], v5 offset:2048\n\t" \
  "ds_read_b128 v[212:215], v5 offset:6144\n\t" \
  "ds_read_b128 v[216:219], v5 offset:3072\n\t" \
  "ds_read_b128 v[220:223], v5 offset:7168\n\t" \
  "ds_read_b128 v[224:227], v5 offset:8192\n\t" \
  "ds_read_b128 v[228:231], v5 offset:12288\n\t" \
  "ds_read_b128 v[232:235], v5 offset:9216\n\t" \
  "ds_read_b128 v[236:239], v5 offset:13312\n\t" \
  "ds_read_b128 v[240:243], v5 offset:10240\n\t" \
  "ds_read_b128 v[244:247], v5 offset:14336\n\t" \
  "ds_read_b128 v[248:251], v5 offset:11264\n\t" \
  "ds_read_b128 v[252:255], v5 offset:15360\n\t" \
  "v_mul_f32_e32 v32, v32, v12\n\t" \
  "s_add_u32 s82, s82, 0x8000\n\t" \
  "s_addc_u32 s83, s83, 0\n\t" \
  "s_add_u32 m0, s62, 0x18000\n\t" \
  "s_nop 0\n\t" \
  "global_load_lds_dwordx4 v1, s[82:83] offset:0 sc1\n\t" \
  "v_mul_f32_e32 v33, v33, v12\n\t" \
  "v_mul_f32_e32 v34, v34, v12\n\t" \
  "v_mul_f32_e32 v35, v35, v12\n\t" \
  "v_mul_f32_e32 v40, v40, v12\n\t" \
  "v_mul_f32_e32 v41, v41, v12\n\t" \
  "v_mul_f32_e32 v42, v42, v12\n\t" \
  "s_waitcnt lgkmcnt(12)\n\t" \
  "v_mfma_f32_16x16x32_bf16 v[112:115], v[192:195], v[80:83], 0\n\t" \
  "v_mul_f32_e32 v43, v43, v12\n\t" \
  "v_exp_f32_e32 v32, v32\n\t" \
  "global_load_lds_dwordx4 v1, s[82:83] offset:1024 sc1\n\t" \
  "v_exp_f32_e32 v33, v33\n\t" \
  "v_mfma_f32_16x16x32_bf16 v[120:123], v[192:195], v[96:99], 0\n\t" \
  "v_exp_f32_e32 v34, v34\n\t" \
  "v_exp_f32_e32 v35, v35\n\t" \
  "v_exp_f32_e32 v40, v40\n\t" \
  "v_exp_f32_e32 v41, v41\n\t" \
  "v_mfma_f32_16x16x32_bf16 v[124:127], v[196:199], v[96:99], 0\n\t" \
  "v_exp_f32_e32 v42, v42\n\t" \
  "v_exp_f32_e32 v43, v43\n\t" \
  "v_add_f32_e32 v32, 1.0, v32\n\t" \
  "global_load_lds_dwordx4 v1, s[82:83] offset:2048 sc1\n\t" \
  "v_mfma_f32_16x16x32_bf16 v[116:119], v[196:199], v[80:83], 0\n\t" \
  "v_add_f32_e32 v33, 1.0, v33\n\t" \
  "v_add_f32_e32 v34, 1.0, v34\n\t" \
  "v_add_f32_e32 v35, 1.0, v35\n\t" \
  "v_add_f32_e32 v40, 1.0, v40\n\t" \
  "v_mfma_f32_16x16x32_bf16 v[112:115], v[200:203], v[84:87], v[112:115]\n\t" \
  "v_add_f32_e32 v41, 1.0, v41\n\t" \
  "v_add_f32_e32 v42, 1.0, v42\n\t" \
  "v_add_f32_e32 v43, 1.0, v43\n\t" \
  "v_rcp_f32_e32 v32, v32\n\t" \
  "v_mfma_f32_16x16x32_bf16 v[120:123], v[200:203], v[100:103], v[120:123]\n\t" \
  "global_load_lds_dwordx4 v1, s[82:83] offset:3072 sc1\n\t" \
  "v_rcp_f32_e32 v33, v33\n\t" \
  "v_rcp_f32_e32 v34, v34\n\t" \
  "v_rcp_f32_e32 v35, v35\n\t" \
  "v_mfma_f32_16x16x32_bf16 v[124:127], v[204:207], v[100:103], v[124:127]\n\t" \
  "v_rcp_f32_e32 v40, v40\n\t" \
  "v_rcp_f32_e32 v41, v41\n\t" \
  "v_rcp_f32_e32 v42, v42\n\t" \
  "v_rcp_f32_e32 v43, v43\n\t" \
  "v_mfma_f32_16x16x32_bf16 v[116:119], v[204:207], v[84:87], v[116:119]\n\t" \
  "v_mul_f32_e32 v32, v32, v36\n\t" \
  "v_mul_f32_e32 v33, v33, v37\n\t" \
  "v_mul_f32_e32 v34, v34, v38\n\t" \
  "v_mul_f32_e32 v35, v35, v39\n\t" \
  "s_waitcnt lgkmcnt(8)\n\t" \
  "v_mfma_f32_16x16x32_bf16 v[112:115], v[208:211], v[88:91], v[112:115]\n\t" \
  "v_mul_f32_e32 v40, v40, v44\n\t" \
  "v_mul_f32_e32 v41, v41, v45\n\t" \
  "v_mul_f32_e32 v42, v42, v46\n\t" \
  "v_mul_f32_e32 v43, v43, v47\n\t" \
  "v_mfma_f32_16x16x32_bf16 v[120:123], v[208:211], v[104:107], v[120:123]\n\t" \
  "v_cvt_pk_bf16_f32 v36, v32, v33\n\t" \
  "v_cvt_pk_bf16_f32 v37, v34, v35\n\t" \
  "v_cvt_pk_bf16_f32 v44, v40, v41\n\t" \
  "v_cvt_pk_bf16_f32 v45, v42, v43\n\t" \
  "s_cbranch_vccz G2_%=\n\t" \
  "global_store_dwordx2 v7, v[36:37], s[50:51] offset:2048 sc1\n\t" \
  "global_store_dwordx2 v7, v[44:45], s[50:51] offset:3072 sc1\n\t" \
  "G2_%=:\n\t" \
  "s_add_u32 s50, s50, 4096\n\t" \
  "s_addc_u32 s51, s51, 0\n\t" \
  "v_mfma_f32_16x16x32_bf16 v[124:127], v[212:215], v[104:107], v[124:127]\n\t" \
  "v_mul_f32_e32 v48, v48, v12\n\t" \
  "v_mul_f32_e32 v49, v49, v12\n\t" \
  "v_mul_f32_e32 v50, v50, v12\n\t" \
  "v_mul_f32_e32 v51, v51, v12\n\t" \
  "v_mfma_f32_16x16x32_bf16 v[116:119], v[212:215], v[88:91], v[116:119]\n\t" \
  "v_mul_f32_e32 v56, v56, v12\n\t" \
  "v_mul_f32_e32 v57, v57, v12\n\t" \
  "v_mul_f32_e32 v58, v58, v12\n\t" \
  "v_mul_f32_e32 v59, v59, v12\n\t" \
  "v_mfma_f32_16x16x32_bf16 v[112:115], v[216:219], v[92:95], v[112:115]\n\t" \
  "v_exp_f32_e32 v48, v48\n\t" \
  "v_exp_f32_e32 v49, v49\n\t" \
  "v_exp_f32_e32 v50, v50\n\t" \
  "v_exp_f32_e32 v51, v51\n\t" \
  "v_mfma_f32_16x16x32_bf16 v[120:123], v[216:219], v[108:111], v[120:123]\n\t" \
  "v_exp_f32_e32 v56, v56\n\t" \
  "v_exp_f32_e32 v57, v57\n\t" \
  "v_exp_f32_e32 v58, v58\n\t" \
  "v_exp_f32_e32 v59, v59\n\t" \
  "v_mfma_f32_16x16x32_bf16 v[124:127], v[220:223], v[108:111], v[124:127]\n\t" \
  "v_add_f32_e32 v48, 1.0, v48\n\t" \
  "v_add_f32_e32 v49, 1.0, v49\n\t" \
  "v_add_f32_e32 v50, 1.0, v50\n\t" \
  "v_add_f32_e32 v51, 1.0, v51\n\t" \
  "v_mfma_f32_16x16x32_bf16 v[116:119], v[220:223], v[92:95], v[116:119]\n\t" \
  "v_add_f32_e32 v56, 1.0, v56\n\t" \
  "v_add_f32_e32 v57, 1.0, v57\n\t" \
  "v_add_f32_e32 v58, 1.0, v58\n\t" \
  "v_add_f32_e32 v59, 1.0, v59\n\t" \
  "s_waitcnt lgkmcnt(4)\n\t" \
  "v_mfma_f32_16x16x32_bf16 v[128:131], v[224:227], v[80:83], 0\n\t" \
  "v_rcp_f32_e32 v48, v48\n\t" \
  "v_rcp_f32_e32 v49, v49\n\t" \
  "v_rcp_f32_e32 v50, v50\n\t" \
  "v_rcp_f32_e32 v51, v51\n\t" \
  "v_mfma_f32_16x16x32_bf16 v[136:139], v[224:227], v[96:99], 0\n\t" \
  "v_rcp_f32_e32 v56, v56\n\t" \
  "v_rcp_f32_e32 v57, v57\n\t" \
  "v_rcp_f32_e32 v58, v58\n\t" \
  "v_rcp_f32_e32 v59, v59\n\t" \
  "v_mfma_f32_16x16x32_bf16 v[140:143], v[228:231], v[96:99], 0\n\t" \
  "v_mul_f32_e32 v48, v48, v52\n\t" \
  "v_mul_f32_e32 v49, v49, v53\n\t" \
  "v_mul_f32_e32 v50, v50, v54\n\t" \
  "v_mul_f32_e32 v51, v51, v55\n\t" \
  "v_mfma_f32_16x16x32_bf16 v[132:135], v[228:231], v[80:83], 0\n\t" \
  "v_mul_f32_e32 v56, v56, v60\n\t" \
  "v_mul_f32_e32 v57, v57, v61\n\t" \
  "v_mul_f32_e32 v58, v58, v62\n\t" \
  "v_mul_f32_e32 v59, v59, v63\n\t" \
  "v_mfma_f32_16x16x32_bf16 v[128:131], v[232:235], v[84:87], v[128:131]\n\t" \
  "v_cvt_pk_bf16_f32 v52, v48, v49\n\t" \
  "v_cvt_pk_bf16_f32 v53, v50, v51\n\t" \
  "v_cvt_pk_bf16_f32 v60, v56, v57\n\t" \
  "v_cvt_pk_bf16_f32 v61, v58, v59\n\t" \
  "s_cbranch_vccz G3_%=\n\t" \
  "global_store_dwordx2 v7, v[52:53], s[50:51] offset:0 sc1\n\t" \
  "global_store_dwordx2 v7, v[60:61], s[50:51] offset:1024 sc1\n\t" \
  "G3_%=:\n\t" \
  "v_mfma_f32_16x16x32_bf16 v[136:139], v[232:235], v[100:103], v[136:139]\n\t" \
  "v_mul_f32_e32 v64, v64, v12\n\t" \
  "v_mul_f32_e32 v65, v65, v12\n\t" \
  "v_mul_f32_e32 v66, v66, v12\n\t" \
  "v_mul_f32_e32 v67, v67, v12\n\t" \
  "v_mfma_f32_16x16x32_bf16 v[140:143], v[236:239], v[100:103], v[140:143]\n\t" \
  "v_mul_f32_e32 v72, v72, v12\n\t" \
  "v_mul_f32_e32 v73, v73, v12\n\t" \
  "v_mul_f32_e32 v74, v74, v12\n\t" \
  "v_mul_f32_e32 v75, v75, v12\n\t" \
  "v_mfma_f32_16x16x32_bf16 v[132:135], v[236:239], v[84:87], v[132:135]\n\t" \
  "v_exp_f32_e32 v64, v64\n\t" \
  "v_exp_f32_e32 v65, v65\n\t" \
  "v_exp_f32_e32 v66, v66\n\t" \
  "v_exp_f32_e32 v67, v67\n\t" \
  "s_waitcnt lgkmcnt(0)\n\t" \
  "v_mfma_f32_16x16x32_bf16 v[128:131], v[240:243], v[88:91], v[128:131]\n\t" \
  "v_exp_f32_e32 v72, v72\n\t" \
  "v_exp_f32_e32 v73, v73\n\t" \
  "v_exp_f32_e32 v74, v74\n\t" \
  "v_exp_f32_e32 v75, v75\n\t" \
  "v_mfma_f32_16x16x32_bf16 v[136:139], v[240:243], v[104:107], v[136:139]\n\t" \
  "v_add_f32_e32 v64, 1.0, v64\n\t" \
  "v_add_f32_e32 v65, 1.0, v65\n\t" \
  "v_add_f32_e32 v66, 1.0, v66\n\t" \
  "v_add_f32_e32 v67, 1.0, v67\n\t" \
  "v_mfma_f32_16x16x32_bf16 v[140:143], v[244:247], v[104:107], v[140:143]\n\t" \
  "v_add_f32_e32 v72, 1.0, v72\n\t" \
  "v_add_f32_e32 v73, 1.0, v73\n\t" \
  "v_add_f32_e32 v74, 1.0, v74\n\t" \
  "v_add_f32_e32 v75, 1.0, v75\n\t" \
  "v_mfma_f32_16x16x32_bf16 v[132:135], v[244:247], v[88:91], v[132:135]\n\t" \
  "v_rcp_f32_e32 v64, v64\n\t" \
  "v_rcp_f32_e32 v65, v65\n\t" \
  "v_rcp_f32_e32 v66, v66\n\t" \
  "v_rcp_f32_e32 v67, v67\n\t" \
  "v_mfma_f32_16x16x32_bf16 v[128:131], v[248:251], v[92:95], v[128:131]\n\t" \
  "v_rcp_f32_e32 v72, v72\n\t" \
  "v_rcp_f32_e32 v73, v73\n\t" \
  "v_rcp_f32_e32 v74, v74\n\t" \
  "v_rcp_f32_e32 v75, v75\n\t" \
  "v_mfma_f32_16x16x32_bf16 v[136:139], v[248:251], v[108:111], v[136:139]\n\t" \
  "v_mul_f32_e32 v64, v64, v68\n\t" \
  "v_mul_f32_e32 v65, v65, v69\n\t" \
  "v_mul_f32_e32 v66, v66, v70\n\t" \
  "v_mul_f32_e32 v67, v67, v71\n\t" \
  "v_mfma_f32_16x16x32_bf16 v[140:143], v[252:255], v[108:111], v[140:143]\n\t" \
  "v_mul_f32_e32 v72, v72, v76\n\t" \
  "v_mul_f32_e32 v73, v73, v77\n\t" \
  "v_mul_f32_e32 v74, v74, v78\n\t" \
  "v_mul_f32_e32 v75, v75, v79\n\t" \
  "v_mfma_f32_16x16x32_bf16 v[132:135], v[252:255], v[92:95], v[132:135]\n\t" \
  "v_cvt_pk_bf16_f32 v68, v64, v65\n\t" \
  "v_cvt_pk_bf16_f32 v69, v66, v67\n\t" \
  "v_cvt_pk_bf16_f32 v76, v72, v73\n\t" \
  "v_cvt_pk_bf16_f32 v77, v74, v75\n\t" \
  "s_cbranch_vccz G4_%=\n\t" \
  "global_store_dwordx2 v7, v[68:69], s[50:51] offset:2048 sc1\n\t" \
  "global_store_dwordx2 v7, v[76:77], s[50:51] offset:3072 sc1\n\t" \
  "G4_%=:\n\t" \
  "ds_read_b128 v[192:195], v5 offset:16384\n\t" \
  "ds_read_b128 v[196:199], v5 offset:20480\n\t" \
  "ds_read_b128 v[200:203], v5 offset:17408\n\t" \
  "ds_read_b128 v[204:207], v5 offset:21504\n\t" \
  "ds_read_b128 v[208:211], v5 offset:18432\n\t" \
  "ds_read_b128 v[212:215], v5 offset:22528\n\t" \
  "ds_read_b128 v[216:219], v5 offset:19456\n\t" \
  "ds_read_b128 v[220:223], v5 offset:23552\n\t" \
  "ds_read_b128 v[224:227], v5 offset:24576\n\t" \
  "ds_read_b128 v[228:231], v5 offset:28672\n\t" \
  "ds_read_b128 v[232:235], v5 offset:25600\n\t" \
  "ds_read_b128 v[236:239], v5 offset:29696\n\t" \
  "ds_read_b128 v[240:243], v5 offset:26624\n\t" \
  "ds_read_b128 v[244:247], v5 offset:30720\n\t" \
  "ds_read_b128 v[248:251], v5 offset:27648\n\t" \
  "ds_read_b128 v[252:255], v5 offset:31744\n\t" \
  "s_cmp_lt_u32 %[w], 4\n\t" \
  "s_cbranch_scc1 K2_%=\n\t" \
  "global_load_dwordx2 v[144:145], v3, s[60:61] sc1\n\t" \
  "K2_%=:\n\t" \
  "v_mov_b32_e32 v4, 0\n\t" \
  "v_mov_b32_e32 v5, 0\n\t" \
  "v_mov_b32_e32 v6, 0\n\t" \
  "v_mov_b32_e32 v7, 0\n\t" \
  "v_mov_b32_e32 v12, 0\n\t" \
  "v_mov_b32_e32 v13, 0\n\t" \
  "v_mov_b32_e32 v14, 0\n\t" \
  "v_mov_b32_e32 v15, 0\n\t" \
  "v_mov_b32_e32 v188, 0x3f803f80\n\t" \
  "v_mov_b32_e32 v189, 0x3f803f80\n\t" \
  "v_mov_b32_e32 v190, 0x3f803f80\n\t" \
  "v_mov_b32_e32 v191, 0x3f803f80\n\t" \
  "v_mov_b32_e32 v184, 0xff800000\n\t" \
  "s_cmp_lg_u32 s67, 2\n\t" \
  "s_cbranch_scc1 NMF_%=\n\t" \
  "v_cmp_gt_i32_e64 s[48:49], 0, v2\n\t" \
  "v_cmp_gt_i32_e64 s[50:51], 1, v2\n\t" \
  "v_cmp_gt_i32_e64 s[52:53], 2, v2\n\t" \
  "v_cmp_gt_i32_e64 s[54:55], 3, v2\n\t" \
  "s_nop 1\n\t" \
  "s_cmp_gt_u32 s63, 0\n\t" \
  "s_cbranch_scc1 MX1_%=\n\t" \
  "v_mov_b32_e32 v116, v184\n\t" \
  "v_mov_b32_e32 v117, v184\n\t" \
  "v_mov_b32_e32 v118, v184\n\t" \
  "v_mov_b32_e32 v119, v184\n\t" \
  "v_cndmask_b32_e64 v112, v112, v184, s[48:49]\n\t" \
  "v_cndmask_b32_e64 v113, v113, v184, s[50:51]\n\t" \
  "v_cndmask_b32_e64 v114, v114, v184, s[52:53]\n\t" \
  "v_cndmask_b32_e64 v115, v115, v184, s[54:55]\n\t" \
  "v_cndmask_b32_e64 v124, v124, v184, s[48:49]\n\t" \
  "v_cndmask_b32_e64 v125, v125, v184, s[50:51]\n\t" \
  "v_cndmask_b32_e64 v126, v126, v184, s[52:53]\n\t" \
  "v_cndmask_b32_e64 v127, v127, v184, s[54:55]\n\t" \
  "MX1_%=:\n\t" \
  "s_cmp_gt_u32 s63, 1\n\t" \
  "s_cbranch_scc1 MX2_%=\n\t" \
  "v_mov_b32_e32 v132, v184\n\t" \
  "v_mov_b32_e32 v133, v184\n\t" \
  "v_mov_b32_e32 v134, v184\n\t" \
  "v_mov_b32_e32 v135, v184\n\t" \
  "s_cmp_eq_u32 s63, 1\n\t" \
  "s_cbranch_scc1 MD2_%=\n\t" \
  "v_mov_b32_e32 v128, v184\n\t" \
  "v_mov_b32_e32 v129, v184\n\t" \
  "v_mov_b32_e32 v130, v184\n\t" \
  "v_mov_b32_e32 v131, v184\n\t" \
  "v_mov_b32_e32 v136, v184\n\t" \
  "v_mov_b32_e32 v137, v184\n\t" \
  "v_mov_b32_e32 v138, v184\n\t" \
  "v_mov_b32_e32 v139, v184\n\t" \
  "v_mov_b32_e32 v140, v184\n\t" \
  "v_mov_b32_e32 v141, v184\n\t" \
  "v_mov_b32_e32 v142, v184\n\t" \
  "v_mov_b32_e32 v143, v184\n\t" \
  "s_branch MX2_%=\n\t" \
  "MD2_%=:\n\t" \
  "v_cndmask_b32_e64 v128, v128, v184, s[48:49]\n\t" \
  "v_cndmask_b32_e64 v129, v129, v184, s[50:51]\n\t" \
  "v_cndmask_b32_e64 v130, v130, v184, s[52:53]\n\t" \
  "v_cndmask_b32_e64 v131, v131, v184, s[54:55]\n\t" \
  "v_cndmask_b32_e64 v140, v140, v184, s[48:49]\n\t" \
  "v_cndmask_b32_e64 v141, v141, v184, s[50:51]\n\t" \
  "v_cndmask_b32_e64 v142, v142, v184, s[52:53]\n\t" \
  "v_cndmask_b32_e64 v143, v143, v184, s[54:55]\n\t" \
  "MX2_%=:\n\t" \
  "NMF_%=:\n\t" \
  "v_mfma_f32_16x16x32_bf16 v[16:19], v[4:7], v[188:191], 0\n\t" \
  "v_max3_f32 v10, v112, v113, v114\n\t" \
  "v_mfma_f32_16x16x32_bf16 v[20:23], v[4:7], v[188:191], 0\n\t" \
  "v_max3_f32 v11, v120, v121, v122\n\t" \
  "v_mfma_f32_16x16x32_bf16 v[24:27], v[4:7], v[188:191], 0\n\t" \
  "v_max3_f32 v10, v10, v115, v116\n\t" \
  "v_mfma_f32_16x16x32_bf16 v[28:31], v[4:7], v[188:191], 0\n\t" \
  "v_max3_f32 v11, v11, v123, v124\n\t" \
  "v_mfma_f32_16x16x32_bf16 v[32:35], v[4:7], v[188:191], 0\n\t" \
  "v_max3_f32 v10, v10, v117, v118\n\t" \
  "v_mfma_f32_16x16x32_bf16 v[36:39], v[4:7], v[188:191], 0\n\t" \
  "v_max3_f32 v11, v11, v125, v126\n\t" \
  "v_mfma_f32_16x16x32_bf16 v[40:43], v[4:7], v[188:191], 0\n\t" \
  "v_max3_f32 v10, v10, v119, v128\n\t" \
  "v_mfma_f32_16x16x32_bf16 v[44:47], v[4:7], v[188:191], 0\n\t" \
  "v_max3_f32 v11, v11, v127, v136\n\t" \
  "v_mfma_f32_16x16x32_bf16 v[48:51], v[4:7], v[188:191], 0\n\t" \
  "v_max3_f32 v10, v10, v129, v130\n\t" \
  "v_mfma_f32_16x16x32_bf16 v[52:55], v[4:7], v[188:191], 0\n\t" \
  "v_max3_f32 v11, v11, v137, v138\n\t" \
  "v_mfma_f32_16x16x32_bf16 v[56:59], v[4:7], v[188:191], 0\n\t" \
  "v_max3_f32 v10, v10, v131, v132\n\t" \
  "v_mfma_f32_16x16x32_bf16 v[60:63], v[4:7], v[188:191], 0\n\t" \
  "v_max3_f32 v11, v11, v139, v140\n\t" \
  "v_mfma_f32_16x16x32_bf16 v[64:67], v[4:7], v[188:191], 0\n\t" \
  "v_max3_f32 v10, v10, v133, v134\n\t" \
  "v_mfma_f32_16x16x32_bf16 v[68:71], v[4:7], v[188:191], 0\n\t" \
  "v_max3_f32 v11, v11, v141, v142\n\t" \
  "v_mfma_f32_16x16x32_bf16 v[72:75], v[4:7], v[188:191], 0\n\t" \
  "v_max_f32_e32 v10, v10, v135\n\t" \
  "v_mfma_f32_16x16x32_bf16 v[76:79], v[4:7], v[188:191], 0\n\t" \
  "v_max_f32_e32 v11, v11, v143\n\t" \
  "v_mov_b32_e32 v186, v10\n\t" \
  "v_mov_b32_e32 v187, v11\n\t" \
  "s_nop 0\n\t" \
  "v_permlane16_swap_b32_e32 v10, v186\n\t" \
  "v_permlane16_swap_b32_e32 v11, v187\n\t" \
  "s_nop 0\n\t" \
  "v_max_f32_e32 v10, v10, v186\n\t" \
  "v_max_f32_e32 v11, v11, v187\n\t" \
  "v_mov_b32_e32 v186, v10\n\t" \
  "v_mov_b32_e32 v187, v11\n\t" \
  "s_nop 0\n\t" \
  "v_permlane32_swap_b32_e32 v10, v186\n\t" \
  "v_permlane32_swap_b32_e32 v11, v187\n\t" \
  "s_nop 0\n\t" \
  "v_max_f32_e32 v10, v10, v186\n\t" \
  "v_max_f32_e32 v11, v11, v187\n\t" \
  "v_sub_f32_e32 v112, v112, v10\n\t" \
  "v_sub_f32_e32 v113, v113, v10\n\t" \
  "v_sub_f32_e32 v114, v114, v10\n\t" \
  "v_sub_f32_e32 v115, v115, v10\n\t" \
  "v_sub_f32_e32 v116, v116, v10\n\t" \
  "v_sub_f32_e32 v117, v117, v10\n\t" \
  "v_sub_f32_e32 v118, v118, v10\n\t" \
  "v_sub_f32_e32 v119, v119, v10\n\t" \
  "v_sub_f32_e32 v120, v120, v11\n\t" \
  "v_sub_f32_e32 v121, v121, v11\n\t" \
  "v_sub_f32_e32 v122, v122, v11\n\t" \
  "v_sub_f32_e32 v123, v123, v11\n\t" \
  "v_sub_f32_e32 v124, v124, v11\n\t" \
  "v_sub_f32_e32 v125, v125, v11\n\t" \
  "v_sub_f32_e32 v126, v126, v11\n\t" \
  "v_sub_f32_e32 v127, v127, v11\n\t" \
  "v_sub_f32_e32 v128, v128, v10\n\t" \
  "v_sub_f32_e32 v129, v129, v10\n\t" \
  "v_sub_f32_e32 v130, v130, v10\n\t" \
  "v_sub_f32_e32 v131, v131, v10\n\t" \
  "v_sub_f32_e32 v132, v132, v10\n\t" \
  "v_sub_f32_e32 v133, v133, v10\n\t" \
  "v_sub_f32_e32 v134, v134, v10\n\t" \
  "v_sub_f32_e32 v135, v135, v10\n\t" \
  "v_sub_f32_e32 v136, v136, v11\n\t" \
  "v_sub_f32_e32 v137, v137, v11\n\t" \
  "v_sub_f32_e32 v138, v138, v11\n\t" \
  "v_sub_f32_e32 v139, v139, v11\n\t" \
  "v_sub_f32_e32 v140, v140, v11\n\t" \
  "v_sub_f32_e32 v141, v141, v11\n\t" \
  "v_sub_f32_e32 v142, v142, v11\n\t" \
  "v_sub_f32_e32 v143, v143, v11\n\t" \
  "v_sub_f32_e32 v176, 0, v10\n\t" \
  "v_sub_f32_e32 v177, 0, v10\n\t" \
  "v_sub_f32_e32 v178, 0, v10\n\t" \
  "v_sub_f32_e32 v179, 0, v10\n\t" \
  "v_sub_f32_e32 v180, 0, v11\n\t" \
  "v_sub_f32_e32 v181, 0, v11\n\t" \
  "v_sub_f32_e32 v182, 0, v11\n\t" \
  "v_sub_f32_e32 v183, 0, v11\n\t" \
  "v_exp_f32_e32 v112, v112\n\t" \
  "v_exp_f32_e32 v113, v113\n\t" \
  "v_exp_f32_e32 v114, v114\n\t" \
  "v_exp_f32_e32 v115, v115\n\t" \
  "v_exp_f32_e32 v116, v116\n\t" \
  "v_exp_f32_e32 v117, v117\n\t" \
  "v_exp_f32_e32 v118, v118\n\t" \
  "v_exp_f32_e32 v119, v119\n\t" \
  "v_exp_f32_e32 v120, v120\n\t" \
  "v_exp_f32_e32 v121, v121\n\t" \
  "v_exp_f32_e32 v122, v122\n\t" \
  "v_cvt_pk_bf16_f32 v112, v112, v113\n\t" \
  "v_cvt_pk_bf16_f32 v113, v114, v115\n\t" \
  "s_cbranch_vccnz G6_%=\n\t" \
  "s_waitcnt vmcnt(0)\n\t" \
  "G6_%=:\n\t" \
  "s_waitcnt vmcnt(6)\n\t" \
  "s_cmp_gt_u32 %[w], 3\n\t" \
  "s_cbranch_scc1 SH_%=\n\t" \
  "s_mov_b32 s81, 1\n\t" \
  "v_xor_b32_e32 v8, 0x10000, v8\n\t" \
  "LPL_%=:\n\t" \
  "s_waitcnt lgkmcnt(8)\n\t" \
  "v_mfma_f32_16x16x32_bf16 v[144:147], v[192:195], v[80:83], v[176:179]\n\t" \
  "s_barrier\n\t" \
  "v_exp_f32_e32 v123, v123\n\t" \
  "v_mfma_f32_16x16x32_bf16 v[152:155], v[192:195], v[96:99], v[180:183]\n\t" \
  "ds_read_b128 v[192:195], v8 offset:16384\n\t" \
  "v_cvt_pk_bf16_f32 v114, v116, v117\n\t" \
  "v_mfma_f32_16x16x32_bf16 v[156:159], v[196:199], v[96:99], v[180:183]\n\t" \
  "v_exp_f32_e32 v124, v124\n\t" \
  "v_mfma_f32_16x16x32_bf16 v[148:151], v[196:199], v[80:83], v[176:179]\n\t" \
  "ds_read_b128 v[196:199], v8 offset:17408\n\t" \
  "v_cvt_pk_bf16_f32 v115, v118, v119\n\t" \
  "v_mfma_f32_16x16x32_bf16 v[144:147], v[200:203], v[84:87], v[144:147]\n\t" \
  "v_exp_f32_e32 v125, v125\n\t" \
  "v_mfma_f32_16x16x32_bf16 v[152:155], v[200:203], v[100:103], v[152:155]\n\t" \
  "ds_read_b128 v[200:203], v8 offset:18432\n\t" \
  "v_cvt_pk_bf16_f32 v120, v120, v121\n\t" \
  "v_mfma_f32_16x16x32_bf16 v[156:159], v[204:207], v[100:103], v[156:159]\n\t" \
  "v_exp_f32_e32 v126, v126\n\t" \
  "v_mfma_f32_16x16x32_bf16 v[148:151], v[204:207], v[84:87], v[148:151]\n\t" \
  "ds_read_b128 v[204:207], v8 offset:19456\n\t" \
  "v_cvt_pk_bf16_f32 v121, v122, v123\n\t" \
  "v_mfma_f32_16x16x32_bf16 v[144:147], v[208:211], v[88:91], v[144:147]\n\t" \
  "v_exp_f32_e32 v127, v127\n\t" \
  "v_mfma_f32_16x16x32_bf16 v[152:155], v[208:211], v[104:107], v[152:155]\n\t" \
  "ds_read_b128 v[208:211], v8 offset:20480\n\t" \
  "v_cvt_pk_bf16_f32 v122, v124, v125\n\t" \
  "v_mfma_f32_16x16x32_bf16 v[156:159], v[212:215], v[104:107], v[156:159]\n\t" \
  "v_exp_f32_e32 v128, v128\n\t" \
  "v_mfma_f32_16x16x32_bf16 v[148:151], v[212:215], v[88:91], v[148:151]\n\t" \
  "ds_read_b128 v[212:215], v8 offset:21504\n\t" \
  "v_cvt_pk_bf16_f32 v123, v126, v127\n\t" \
  "v_mfma_f32_16x16x32_bf16 v[144:147], v[216:219], v[92:95], v[144:147]\n\t" \
  "v_exp_f32_e32 v129, v129\n\t" \
  "v_mfma_f32_16x16x32_bf16 v[152:155], v[216:219], v[108:111], v[152:155]\n\t" \
  "ds_read_b128 v[216:219], v8 offset:22528\n\t" \
  "v_mfma_f32_16x16x32_bf16 v[156:159], v[220:223], v[108:111], v[156:159]\n\t" \
  "v_exp_f32_e32 v130, v130\n\t" \
  "v_mfma_f32_16x16x32_bf16 v[148:151], v[220:223], v[92:95], v[148:151]\n\t" \
  "ds_read_b128 v[220:223], v8 offset:23552\n\t" \
  "s_waitcnt lgkmcnt(8)\n\t" \
  "v_mfma_f32_16x16x32_bf16 v[160:163], v[224:227], v[80:83], v[176:179]\n\t" \
  "v_exp_f32_e32 v131, v131\n\t" \
  "v_mfma_f32_16x16x32_bf16 v[168:171], v[224:227], v[96:99], v[180:183]\n\t" \
  "ds_read_b128 v[224:227], v8 offset:24576\n\t" \
  "v_cvt_pk_bf16_f32 v128, v128, v129\n\t" \
  "v_mfma_f32_16x16x32_bf16 v[172:175], v[228:231], v[96:99], v[180:183]\n\t" \
  "v_exp_f32_e32 v132, v132\n\t" \
  "v_mfma_f32_16x16x32_bf16 v[164:167], v[228:231], v[80:83], v[176:179]\n\t" \
  "ds_read_b128 v[228:231], v8 offset:25600\n\t" \
  "v_cvt_pk_bf16_f32 v129, v130, v131\n\t" \
  "v_mfma_f32_16x16x32_bf16 v[160:163], v[232:235], v[84:87], v[160:163]\n\t" \
  "v_exp_f32_e32 v133, v133\n\t" \
  "v_mfma_f32_16x16x32_bf16 v[168:171], v[232:235], v[100:103], v[168:171]\n\t" \
  "ds_read_b128 v[232:235], v8 offset:26624\n\t" \
  "v_max3_f32 v10, v144, v145, v146\n\t" \
  "v_mfma_f32_16x16x32_bf16 v[172:175], v[236:239], v[100:103], v[172:175]\n\t" \
  "v_exp_f32_e32 v134, v134\n\t" \
  "v_mfma_f32_16x16x32_bf16 v[164:167], v[236:239], v[84:87], v[164:167]\n\t" \
  "ds_read_b128 v[236:239], v8 offset:27648\n\t" \
  "v_max3_f32 v11, v147, v148, v149\n\t" \
  "v_mfma_f32_16x16x32_bf16 v[160:163], v[240:243], v[88:91], v[160:163]\n\t" \
  "v_exp_f32_e32 v135, v135\n\t" \
  "v_mfma_f32_16x16x32_bf16 v[168:171], v[240:243], v[104:107], v[168:171]\n\t" \
  "ds_read_b128 v[240:243], v8 offset:28672\n\t" \
  "v_max3_f32 v10, v10, v150, v151\n\t" \
  "v_mfma_f32_16x16x32_bf16 v[172:175], v[244:247], v[104:107], v[172:175]\n\t" \
  "v_exp_f32_e32 v136, v136\n\t" \
  "v_mfma_f32_16x16x32_bf16 v[164:167], v[244:247], v[88:91], v[164:167]\n\t" \
  "ds_read_b128 v[244:247], v8 offset:29696\n\t" \
  "v_max3_f32 v11, v11, v152, v153\n\t" \
  "v_mfma_f32_16x16x32_bf16 v[160:163], v[248:251], v[92:95], v[160:163]\n\t" \
  "v_exp_f32_e32 v137, v137\n\t" \
  "v_mfma_f32_16x16x32_bf16 v[168:171], v[248:251], v[108:111], v[168:171]\n\t" \
  "ds_read_b128 v[248:251], v8 offset:30720\n\t" \
  "v_max3_f32 v10, v10, v154, v155\n\t" \
  "v_mfma_f32_16x16x32_bf16 v[172:175], v[252:255], v[108:111], v[172:175]\n\t" \
  "v_exp_f32_e32 v138, v138\n\t" \
  "v_mfma_f32_16x16x32_bf16 v[164:167], v[252:255], v[92:95], v[164:167]\n\t" \
  "ds_read_b128 v[252:255], v8 offset:31744\n\t" \
  "v_max3_f32 v11, v11, v156, v157\n\t" \
  "s_waitcnt lgkmcnt(8)\n\t" \
  "v_mfma_f32_16x16x32_bf16 v[16:19], v[192:195], v[112:115], v[16:19]\n\t" \
  "v_exp_f32_e32 v139, v139\n\t" \
  "v_mfma_f32_16x16x32_bf16 v[24:27], v[192:195], v[120:123], v[24:27]\n\t" \
  "ds_read_b128 v[192:195], v8 offset:0\n\t" \
  "v_max3_f32 v10, v10, v158, v159\n\t" \
  "v_mfma_f32_16x16x32_bf16 v[12:15], v[188:191], v[120:123], v[12:15]\n\t" \
  "v_exp_f32_e32 v140, v140\n\t" \
  "v_mfma_f32_16x16x32_bf16 v[28:31], v[196:199], v[120:123], v[28:31]\n\t" \
  "v_exp_f32_e32 v141, v141\n\t" \
  "v_mfma_f32_16x16x32_bf16 v[20:23], v[196:199], v[112:115], v[20:23]\n\t" \
  "ds_read_b128 v[196:199], v8 offset:4096\n\t" \
  "v_cvt_pk_bf16_f32 v130, v132, v133\n\t" \
  "v_mfma_f32_16x16x32_bf16 v[4:7], v[188:191], v[112:115], v[4:7]\n\t" \
  "v_cvt_pk_bf16_f32 v131, v134, v135\n\t" \
  "v_cvt_pk_bf16_f32 v136, v136, v137\n\t" \
  "v_mfma_f32_16x16x32_bf16 v[32:35], v[200:203], v[112:115], v[32:35]\n\t" \
  "v_exp_f32_e32 v142, v142\n\t" \
  "v_mfma_f32_16x16x32_bf16 v[40:43], v[200:203], v[120:123], v[40:43]\n\t" \
  "ds_read_b128 v[200:203], v8 offset:1024\n\t" \
  "v_max3_f32 v11, v11, v160, v161\n\t" \
  "v_mfma_f32_16x16x32_bf16 v[44:47], v[204:207], v[120:123], v[44:47]\n\t" \
  "v_exp_f32_e32 v143, v143\n\t" \
  "v_mfma_f32_16x16x32_bf16 v[36:39], v[204:207], v[112:115], v[36:39]\n\t" \
  "ds_read_b128 v[204:207], v8 offset:5120\n\t" \
  "v_max3_f32 v10, v10, v162, v163\n\t" \
  "v_mfma_f32_16x16x32_bf16 v[48:51], v[208:211], v[112:115], v[48:51]\n\t" \
  "v_max3_f32 v11, v11, v164, v165\n\t" \
  "v_max3_f32 v10, v10, v166, v167\n\t" \
  "v_mfma_f32_16x16x32_bf16 v[56:59], v[208:211], v[120:123], v[56:59]\n\t" \
  "ds_read_b128 v[208:211], v8 offset:2048\n\t" \
  "v_cvt_pk_bf16_f32 v137, v138, v139\n\t" \
  "v_mfma_f32_16x16x32_bf16 v[60:63], v[212:215], v[120:123], v[60:63]\n\t" \
  "v_max3_f32 v11, v11, v168, v169\n\t" \
  "v_max3_f32 v10, v10, v170, v171\n\t" \
  "v_mfma_f32_16x16x32_bf16 v[52:55], v[212:215], v[112:115], v[52:55]\n\t" \
  "ds_read_b128 v[212:215], v8 offset:6144\n\t" \
  "v_cvt_pk_bf16_f32 v138, v140, v141\n\t" \
  "v_mfma_f32_16x16x32_bf16 v[64:67], v[216:219], v[112:115], v[64:67]\n\t" \
  "v_max3_f32 v11, v11, v172, v173\n\t" \
  "v_max3_f32 v10, v10, v174, v175\n\t" \
  "v_mfma_f32_16x16x32_bf16 v[72:75], v[216:219], v[120:123], v[72:75]\n\t" \
  "ds_read_b128 v[216:219], v8 offset:3072\n\t" \
  "v_cvt_pk_bf16_f32 v139, v142, v143\n\t" \
  "v_max_f32_e32 v10, v10, v11\n\t" \
  "v_mfma_f32_16x16x32_bf16 v[76:79], v[220:223], v[120:123], v[76:79]\n\t" \
  "v_cmp_lt_f32_e32 vcc, s45, v10\n\t" \
  "v_mfma_f32_16x16x32_bf16 v[68:71], v[220:223], v[112:115], v[68:71]\n\t" \
  "ds_read_b128 v[220:223], v8 offset:7168\n\t" \
  "s_cbranch_vccnz RALa_%=\n\t" \
  "RArLa_%=:\n\t" \
  "v_exp_f32_e32 v144, v144\n\t" \
  "s_waitcnt lgkmcnt(8)\n\t" \
  "v_mfma_f32_16x16x32_bf16 v[16:19], v[224:227], v[128:131], v[16:19]\n\t" \
  "v_exp_f32_e32 v145, v145\n\t" \
  "v_mfma_f32_16x16x32_bf16 v[24:27], v[224:227], v[136:139], v[24:27]\n\t" \
  "ds_read_b128 v[224:227], v8 offset:8192\n\t" \
  "v_mfma_f32_16x16x32_bf16 v[28:31], v[228:231], v[136:139], v[28:31]\n\t" \
  "v_exp_f32_e32 v146, v146\n\t" \
  "v_mfma_f32_16x16x32_bf16 v[20:23], v[228:231], v[128:131], v[20:23]\n\t" \
  "ds_read_b128 v[228:231], v8 offset:12288\n\t" \
  "v_mfma_f32_16x16x32_bf16 v[4:7], v[188:191], v[128:131], v[4:7]\n\t" \
  "v_exp_f32_e32 v147, v147\n\t" \
  "v_mfma_f32_16x16x32_bf16 v[32:35], v[232:235], v[128:131], v[32:35]\n\t" \
  "v_exp_f32_e32 v148, v148\n\t" \
  "v_mfma_f32_16x16x32_bf16 v[40:43], v[232:235], v[136:139], v[40:43]\n\t" \
  "ds_read_b128 v[232:235], v8 offset:9216\n\t" \
  "s_cmp_eq_u32 s43, s81\n\t" \
  "v_mfma_f32_16x16x32_bf16 v[44:47], v[236:239], v[136:139], v[44:47]\n\t" \
  "v_exp_f32_e32 v149, v149\n\t" \
  "v_mfma_f32_16x16x32_bf16 v[36:39], v[236:239], v[128:131], v[36:39]\n\t" \
  "ds_read_b128 v[236:239], v8 offset:13312\n\t" \
  "v_mfma_f32_16x16x32_bf16 v[48:51], v[240:243], v[128:131], v[48:51]\n\t" \
  "v_exp_f32_e32 v150, v150\n\t" \
  "v_mfma_f32_16x16x32_bf16 v[56:59], v[240:243], v[136:139], v[56:59]\n\t" \
  "ds_read_b128 v[240:243], v8 offset:10240\n\t" \
  "v_mfma_f32_16x16x32_bf16 v[12:15], v[188:191], v[136:139], v[12:15]\n\t" \
  "v_exp_f32_e32 v151, v151\n\t" \
  "v_mfma_f32_16x16x32_bf16 v[60:63], v[244:247], v[136:139], v[60:63]\n\t" \
  "v_exp_f32_e32 v152, v152\n\t" \
  "v_mfma_f32_16x16x32_bf16 v[52:55], v[244:247], v[128:131], v[52:55]\n\t" \
  "ds_read_b128 v[244:247], v8 offset:14336\n\t" \
  "v_cvt_pk_bf16_f32 v144, v144, v145\n\t" \
  "v_mfma_f32_16x16x32_bf16 v[64:67], v[248:251], v[128:131], v[64:67]\n\t" \
  "v_exp_f32_e32 v153, v153\n\t" \
  "v_mfma_f32_16x16x32_bf16 v[72:75], v[248:251], v[136:139], v[72:75]\n\t" \
  "ds_read_b128 v[248:251], v8 offset:11264\n\t" \
  "v_cvt_pk_bf16_f32 v145, v146, v147\n\t" \
  "v_mfma_f32_16x16x32_bf16 v[76:79], v[252:255], v[136:139], v[76:79]\n\t" \
  "v_exp_f32_e32 v154, v154\n\t" \
  "v_mfma_f32_16x16x32_bf16 v[68:71], v[252:255], v[128:131], v[68:71]\n\t" \
  "ds_read_b128 v[252:255], v8 offset:15360\n\t" \
  "s_cbranch_scc1 DPLa_%=\n\t" \
  "DBLa_%=:\n\t" \
  "s_waitcnt lgkmcnt(8)\n\t" \
  "v_mfma_f32_16x16x32_bf16 v[112:115], v[192:195], v[80:83], v[176:179]\n\t" \
  "s_barrier\n\t" \
  "v_exp_f32_e32 v155, v155\n\t" \
  "v_mfma_f32_16x16x32_bf16 v[120:123], v[192:195], v[96:99], v[180:183]\n\t" \
  "ds_read_b128 v[192:195], v8 offset:49152\n\t" \
  "v_cvt_pk_bf16_f32 v146, v148, v149\n\t" \
  "v_mfma_f32_16x16x32_bf16 v[124:127], v[196:199], v[96:99], v[180:183]\n\t" \
  "v_exp_f32_e32 v156, v156\n\t" \
  "v_mfma_f32_16x16x32_bf16 v[116:119], v[196:199], v[80:83], v[176:179]\n\t" \
  "ds_read_b128 v[196:199], v8 offset:50176\n\t" \
  "v_cvt_pk_bf16_f32 v147, v150, v151\n\t" \
  "v_mfma_f32_16x16x32_bf16 v[112:115], v[200:203], v[84:87], v[112:115]\n\t" \
  "v_exp_f32_e32 v157, v157\n\t" \
  "v_mfma_f32_16x16x32_bf16 v[120:123], v[200:203], v[100:103], v[120:123]\n\t" \
  "ds_read_b128 v[200:203], v8 offset:51200\n\t" \
  "v_cvt_pk_bf16_f32 v152, v152, v153\n\t" \
  "v_mfma_f32_16x16x32_bf16 v[124:127], v[204:207], v[100:103], v[124:127]\n\t" \
  "v_exp_f32_e32 v158, v158\n\t" \
  "v_mfma_f32_16x16x32_bf16 v[116:119], v[204:207], v[84:87], v[116:119]\n\t" \
  "ds_read_b128 v[204:207], v8 offset:52224\n\t" \
  "v_cvt_pk_bf16_f32 v153, v154, v155\n\t" \
  "v_mfma_f32_16x16x32_bf16 v[112:115], v[208:211], v[88:91], v[112:115]\n\t" \
  "v_exp_f32_e32 v159, v159\n\t" \
  "v_mfma_f32_16x16x32_bf16 v[120:123], v[208:211], v[104:107], v[120:123]\n\t" \
  "ds_read_b128 v[208:211], v8 offset:53248\n\t" \
  "v_cvt_pk_bf16_f32 v154, v156, v157\n\t" \
  "v_mfma_f32_16x16x32_bf16 v[124:127], v[212:215], v[104:107], v[124:127]\n\t" \
  "v_exp_f32_e32 v160, v160\n\t" \
  "v_mfma_f32_16x16x32_bf16 v[116:119], v[212:215], v[88:91], v[116:119]\n\t" \
  "ds_read_b128 v[212:215], v8 offset:54272\n\t" \
  "v_cvt_pk_bf16_f32 v155, v158, v159\n\t" \
  "v_mfma_f32_16x16x32_bf16 v[112:115], v[216:219], v[92:95], v[112:115]\n\t" \
  "v_exp_f32_e32 v161, v161\n\t" \
  "v_mfma_f32_16x16x32_bf16 v[120:123], v[216:219], v[108:111], v[120:123]\n\t" \
  "ds_read_b128 v[216:219], v8 offset:55296\n\t" \
  "v_mfma_f32_16x16x32_bf16 v[124:127], v[220:223], v[108:111], v[124:127]\n\t" \
  "v_exp_f32_e32 v162, v162\n\t" \
  "v_mfma_f32_16x16x32_bf16 v[116:119], v[220:223], v[92:95], v[116:119]\n\t" \
  "ds_read_b128 v[220:223], v8 offset:56320\n\t" \
  "s_waitcnt lgkmcnt(8)\n\t" \
  "v_mfma_f32_16x16x32_bf16 v[128:131], v[224:227], v[80:83], v[176:179]\n\t" \
  "v_exp_f32_e32 v163, v163\n\t" \
  "v_mfma_f32_16x16x32_bf16 v[136:139], v[224:227], v[96:99], v[180:183]\n\t" \
  "ds_read_b128 v[224:227], v8 offset:57344\n\t" \
  "v_cvt_pk_bf16_f32 v160, v160, v161\n\t" \
  "v_mfma_f32_16x16x32_bf16 v[140:143], v[228:231], v[96:99], v[180:183]\n\t" \
  "v_exp_f32_e32 v164, v164\n\t" \
  "v_mfma_f32_16x16x32_bf16 v[132:135], v[228:231], v[80:83], v[176:179]\n\t" \
  "ds_read_b128 v[228:231], v8 offset:58368\n\t" \
  "v_cvt_pk_bf16_f32 v161, v162, v163\n\t" \
  "v_mfma_f32_16x16x32_bf16 v[128:131], v[232:235], v[84:87], v[128:131]\n\t" \
  "v_exp_f32_e32 v165, v165\n\t" \
  "v_mfma_f32_16x16x32_bf16 v[136:139], v[232:235], v[100:103], v[136:139]\n\t" \
  "ds_read_b128 v[232:235], v8 offset:59392\n\t" \
  "v_max3_f32 v10, v112, v113, v114\n\t" \
  "v_mfma_f32_16x16x32_bf16 v[140:143], v[236:239], v[100:103], v[140:143]\n\t" \
  "v_exp_f32_e32 v166, v166\n\t" \
  "v_mfma_f32_16x16x32_bf16 v[132:135], v[236:239], v[84:87], v[132:135]\n\t" \
  "ds_read_b128 v[236:239], v8 offset:60416\n\t" \
  "v_max3_f32 v11, v115, v116, v117\n\t" \
  "v_mfma_f32_16x16x32_bf16 v[128:131], v[240:243], v[88:91], v[128:131]\n\t" \
  "v_exp_f32_e32 v167, v167\n\t" \
  "v_mfma_f32_16x16x32_bf16 v[136:139], v[240:243], v[104:107], v[136:139]\n\t" \
  "ds_read_b128 v[240:243], v8 offset:61440\n\t" \
  "v_max3_f32 v10, v10, v118, v119\n\t" \
  "v_mfma_f32_16x16x32_bf16 v[140:143], v[244:247], v[104:107], v[140:143]\n\t" \
  "v_exp_f32_e32 v168, v168\n\t" \
  "v_mfma_f32_16x16x32_bf16 v[132:135], v[244:247], v[88:91], v[132:135]\n\t" \
  "ds_read_b128 v[244:247], v8 offset:62464\n\t" \
  "v_max3_f32 v11, v11, v120, v121\n\t" \
  "v_mfma_f32_16x16x32_bf16 v[128:131], v[248:251], v[92:95], v[128:131]\n\t" \
  "v_exp_f32_e32 v169, v169\n\t" \
  "v_mfma_f32_16x16x32_bf16 v[136:139], v[248:251], v[108:111], v[136:139]\n\t" \
  "ds_read_b128 v[248:251], v8 offset:63488\n\t" \
  "v_max3_f32 v10, v10, v122, v123\n\t" \
  "v_mfma_f32_16x16x32_bf16 v[140:143], v[252:255], v[108:111], v[140:143]\n\t" \
  "v_exp_f32_e32 v170, v170\n\t" \
  "v_mfma_f32_16x16x32_bf16 v[132:135], v[252:255], v[92:95], v[132:135]\n\t" \
  "ds_read_b128 v[252:255], v8 offset:64512\n\t" \
  "v_max3_f32 v11, v11, v124, v125\n\t" \
  "s_waitcnt lgkmcnt(8)\n\t" \
  "v_mfma_f32_16x16x32_bf16 v[16:19], v[192:195], v[144:147], v[16:19]\n\t" \
  "v_exp_f32_e32 v171, v171\n\t" \
  "v_mfma_f32_16x16x32_bf16 v[24:27], v[192:195], v[152:155], v[24:27]\n\t" \
  "ds_read_b128 v[192:195], v8 offset:32768\n\t" \
  "v_max3_f32 v10, v10, v126, v127\n\t" \
  "v_mfma_f32_16x16x32_bf16 v[12:15], v[188:191], v[152:155], v[12:15]\n\t" \
  "v_exp_f32_e32 v172, v172\n\t" \
  "v_mfma_f32_16x16x32_bf16 v[28:31], v[196:199], v[152:155], v[28:31]\n\t" \
  "v_exp_f32_e32 v173, v173\n\t" \
  "v_mfma_f32_16x16x32_bf16 v[20:23], v[196:199], v[144:147], v[20:23]\n\t" \
  "ds_read_b128 v[196:199], v8 offset:36864\n\t" \
  "v_cvt_pk_bf16_f32 v162, v164, v165\n\t" \
  "v_mfma_f32_16x16x32_bf16 v[4:7], v[188:191], v[144:147], v[4:7]\n\t" \
  "v_cvt_pk_bf16_f32 v163, v166, v167\n\t" \
  "v_cvt_pk_bf16_f32 v168, v168, v169\n\t" \
  "v_mfma_f32_16x16x32_bf16 v[32:35], v[200:203], v[144:147], v[32:35]\n\t" \
  "v_exp_f32_e32 v174, v174\n\t" \
  "v_mfma_f32_16x16x32_bf16 v[40:43], v[200:203], v[152:155], v[40:43]\n\t" \
  "ds_read_b128 v[200:203], v8 offset:33792\n\t" \
  "v_max3_f32 v11, v11, v128, v129\n\t" \
  "v_mfma_f32_16x16x32_bf16 v[44:47], v[204:207], v[152:155], v[44:47]\n\t" \
  "v_exp_f32_e32 v175, v175\n\t" \
  "v_mfma_f32_16x16x32_bf16 v[36:39], v[204:207], v[144:147], v[36:39]\n\t" \
  "ds_read_b128 v[204:207], v8 offset:37888\n\t" \
  "v_max3_f32 v10, v10, v130, v131\n\t" \
  "v_mfma_f32_16x16x32_bf16 v[48:51], v[208:211], v[144:147], v[48:51]\n\t" \
  "v_max3_f32 v11, v11, v132, v133\n\t" \
  "v_max3_f32 v10, v10, v134, v135\n\t" \
  "v_mfma_f32_16x16x32_bf16 v[56:59], v[208:211], v[152:155], v[56:59]\n\t" \
  "ds_read_b128 v[208:211], v8 offset:34816\n\t" \
  "v_cvt_pk_bf16_f32 v169, v170, v171\n\t" \
  "v_mfma_f32_16x16x32_bf16 v[60:63], v[212:215], v[152:155], v[60:63]\n\t" \
  "v_max3_f32 v11, v11, v136, v137\n\t" \
  "v_max3_f32 v10, v10, v138, v139\n\t" \
  "v_mfma_f32_16x16x32_bf16 v[52:55], v[212:215], v[144:147], v[52:55]\n\t" \
  "ds_read_b128 v[212:215], v8 offset:38912\n\t" \
  "v_cvt_pk_bf16_f32 v170, v172, v173\n\t" \
  "v_mfma_f32_16x16x32_bf16 v[64:67], v[216:219], v[144:147], v[64:67]\n\t" \
  "v_max3_f32 v11, v11, v140, v141\n\t" \
  "v_max3_f32 v10, v10, v142, v143\n\t" \
  "v_mfma_f32_16x16x32_bf16 v[72:75], v[216:219], v[152:155], v[72:75]\n\t" \
  "ds_read_b128 v[216:219], v8 offset:35840\n\t" \
  "v_cvt_pk_bf16_f32 v171, v174, v175\n\t" \
  "v_max_f32_e32 v10, v10, v11\n\t" \
  "v_mfma_f32_16x16x32_bf16 v[76:79], v[220:223], v[152:155], v[76:79]\n\t" \
  "v_cmp_lt_f32_e32 vcc, s45, v10\n\t" \
  "v_mfma_f32_16x16x32_bf16 v[68:71], v[220:223], v[144:147], v[68:71]\n\t" \
  "ds_read_b128 v[220:223], v8 offset:39936\n\t" \
  "s_cbranch_vccnz RALb_%=\n\t" \
  "RArLb_%=:\n\t" \
  "v_exp_f32_e32 v112, v112\n\t" \
  "s_waitcnt lgkmcnt(8)\n\t" \
  "v_mfma_f32_16x16x32_bf16 v[16:19], v[224:227], v[160:163], v[16:19]\n\t" \
  "v_exp_f32_e32 v113, v113\n\t" \
  "v_mfma_f32_16x16x32_bf16 v[24:27], v[224:227], v[168:171], v[24:27]\n\t" \
  "ds_read_b128 v[224:227], v8 offset:40960\n\t" \
  "v_mfma_f32_16x16x32_bf16 v[28:31], v[228:231], v[168:171], v[28:31]\n\t" \
  "v_exp_f32_e32 v114, v114\n\t" \
  "v_mfma_f32_16x16x32_bf16 v[20:23], v[228:231], v[160:163], v[20:23]\n\t" \
  "ds_read_b128 v[228:231], v8 offset:45056\n\t" \
  "s_add_u32 s43, s43, 2\n\t" \
  "v_mfma_f32_16x16x32_bf16 v[4:7], v[188:191], v[160:163], v[4:7]\n\t" \
  "v_exp_f32_e32 v115, v115\n\t" \
  "v_mfma_f32_16x16x32_bf16 v[32:35], v[232:235], v[160:163], v[32:35]\n\t" \
  "v_exp_f32_e32 v116, v116\n\t" \
  "v_mfma_f32_16x16x32_bf16 v[40:43], v[232:235], v[168:171], v[40:43]\n\t" \
  "ds_read_b128 v[232:235], v8 offset:41984\n\t" \
  "s_cmp_eq_u32 s44, 0\n\t" \
  "v_mfma_f32_16x16x32_bf16 v[44:47], v[236:239], v[168:171], v[44:47]\n\t" \
  "v_exp_f32_e32 v117, v117\n\t" \
  "v_mfma_f32_16x16x32_bf16 v[36:39], v[236:239], v[160:163], v[36:39]\n\t" \
  "ds_read_b128 v[236:239], v8 offset:46080\n\t" \
  "v_mfma_f32_16x16x32_bf16 v[48:51], v[240:243], v[160:163], v[48:51]\n\t" \
  "v_exp_f32_e32 v118, v118\n\t" \
  "v_mfma_f32_16x16x32_bf16 v[56:59], v[240:243], v[168:171], v[56:59]\n\t" \
  "ds_read_b128 v[240:243], v8 offset:43008\n\t" \
  "v_mfma_f32_16x16x32_bf16 v[12:15], v[188:191], v[168:171], v[12:15]\n\t" \
  "v_exp_f32_e32 v119, v119\n\t" \
  "v_mfma_f32_16x16x32_bf16 v[60:63], v[244:247], v[168:171], v[60:63]\n\t" \
  "v_exp_f32_e32 v120, v120\n\t" \
  "v_mfma_f32_16x16x32_bf16 v[52:55], v[244:247], v[160:163], v[52:55]\n\t" \
  "ds_read_b128 v[244:247], v8 offset:47104\n\t" \
  "v_cvt_pk_bf16_f32 v112, v112, v113\n\t" \
  "v_mfma_f32_16x16x32_bf16 v[64:67], v[248:251], v[160:163], v[64:67]\n\t" \
  "v_exp_f32_e32 v121, v121\n\t" \
  "v_mfma_f32_16x16x32_bf16 v[72:75], v[248:251], v[168:171], v[72:75]\n\t" \
  "ds_read_b128 v[248:251], v8 offset:44032\n\t" \
  "v_cvt_pk_bf16_f32 v113, v114, v115\n\t" \
  "v_mfma_f32_16x16x32_bf16 v[76:79], v[252:255], v[168:171], v[76:79]\n\t" \
  "v_exp_f32_e32 v122, v122\n\t" \
  "v_mfma_f32_16x16x32_bf16 v[68:71], v[252:255], v[160:163], v[68:71]\n\t" \
  "ds_read_b128 v[252:255], v8 offset:48128\n\t" \
  "v_xor_b32_e32 v8, 0x10000, v8\n\t" \
  "s_cbranch_scc1 LPL_%=\n\t" \
  "s_branch RBLb_%=\n\t" \
  "RBrLb_%=:\n\t" \
  "s_mov_b32 s81, s58\n\t" \
  "s_branch LPL_%=\n\t" \
  "TEL_%=:\n\t" \
  "s_waitcnt lgkmcnt(8)\n\t" \
  "v_mfma_f32_16x16x32_bf16 v[112:115], v[192:195], v[80:83], v[176:179]\n\t" \
  "s_barrier\n\t" \
  "v_exp_f32_e32 v155, v155\n\t" \
  "v_mfma_f32_16x16x32_bf16 v[120:123], v[192:195], v[96:99], v[180:183]\n\t" \
  "ds_read_b128 v[192:195], v8 offset:49152\n\t" \
  "v_cvt_pk_bf16_f32 v146, v148, v149\n\t" \
  "v_mfma_f32_16x16x32_bf16 v[124:127], v[196:199], v[96:99], v[180:183]\n\t" \
  "v_exp_f32_e32 v156, v156\n\t" \
  "v_mfma_f32_16x16x32_bf16 v[116:119], v[196:199], v[80:83], v[176:179]\n\t" \
  "ds_read_b128 v[196:199], v8 offset:50176\n\t" \
  "v_cvt_pk_bf16_f32 v147, v150, v151\n\t" \
  "v_mfma_f32_16x16x32_bf16 v[112:115], v[200:203], v[84:87], v[112:115]\n\t" \
  "v_exp_f32_e32 v157, v157\n\t" \
  "v_mfma_f32_16x16x32_bf16 v[120:123], v[200:203], v[100:103], v[120:123]\n\t" \
  "ds_read_b128 v[200:203], v8 offset:51200\n\t" \
  "v_cvt_pk_bf16_f32 v152, v152, v153\n\t" \
  "v_mfma_f32_16x16x32_bf16 v[124:127], v[204:207], v[100:103], v[124:127]\n\t" \
  "v_exp_f32_e32 v158, v158\n\t" \
  "v_mfma_f32_16x16x32_bf16 v[116:119], v[204:207], v[84:87], v[116:119]\n\t" \
  "ds_read_b128 v[204:207], v8 offset:52224\n\t" \
  "v_cvt_pk_bf16_f32 v153, v154, v155\n\t" \
  "v_mfma_f32_16x16x32_bf16 v[112:115], v[208:211], v[88:91], v[112:115]\n\t" \
  "v_exp_f32_e32 v159, v159\n\t" \
  "v_mfma_f32_16x16x32_bf16 v[120:123], v[208:211], v[104:107], v[120:123]\n\t" \
  "ds_read_b128 v[208:211], v8 offset:53248\n\t" \
  "v_cvt_pk_bf16_f32 v154, v156, v157\n\t" \
  "v_mfma_f32_16x16x32_bf16 v[124:127], v[212:215], v[104:107], v[124:127]\n\t" \
  "v_exp_f32_e32 v160, v160\n\t" \
  "v_mfma_f32_16x16x32_bf16 v[116:119], v[212:215], v[88:91], v[116:119]\n\t" \
  "ds_read_b128 v[212:215], v8 offset:54272\n\t" \
  "v_cvt_pk_bf16_f32 v155, v158, v159\n\t" \
  "v_mfma_f32_16x16x32_bf16 v[112:115], v[216:219], v[92:95], v[112:115]\n\t" \
  "v_exp_f32_e32 v161, v161\n\t" \
  "v_mfma_f32_16x16x32_bf16 v[120:123], v[216:219], v[108:111], v[120:123]\n\t" \
  "ds_read_b128 v[216:219], v8 offset:55296\n\t" \
  "v_cmp_gt_i32_e64 s[48:49], 0, v2\n\t" \
  "v_cmp_gt_i32_e64 s[50:51], 1, v2\n\t" \
  "v_mfma_f32_16x16x32_bf16 v[124:127], v[220:223], v[108:111], v[124:127]\n\t" \
  "v_exp_f32_e32 v162, v162\n\t" \
  "v_mfma_f32_16x16x32_bf16 v[116:119], v[220:223], v[92:95], v[116:119]\n\t" \
  "ds_read_b128 v[220:223], v8 offset:56320\n\t" \
  "v_cmp_gt_i32_e64 s[52:53], 2, v2\n\t" \
  "v_cmp_gt_i32_e64 s[54:55], 3, v2\n\t" \
  "s_waitcnt lgkmcnt(8)\n\t" \
  "v_mfma_f32_16x16x32_bf16 v[128:131], v[224:227], v[80:83], v[176:179]\n\t" \
  "v_exp_f32_e32 v163, v163\n\t" \
  "v_mfma_f32_16x16x32_bf16 v[136:139], v[224:227], v[96:99], v[180:183]\n\t" \
  "ds_read_b128 v[224:227], v8 offset:57344\n\t" \
  "v_cvt_pk_bf16_f32 v160, v160, v161\n\t" \
  "v_mfma_f32_16x16x32_bf16 v[140:143], v[228:231], v[96:99], v[180:183]\n\t" \
  "v_exp_f32_e32 v164, v164\n\t" \
  "v_mfma_f32_16x16x32_bf16 v[132:135], v[228:231], v[80:83], v[176:179]\n\t" \
  "ds_read_b128 v[228:231], v8 offset:58368\n\t" \
  "v_cvt_pk_bf16_f32 v161, v162, v163\n\t" \
  "v_mfma_f32_16x16x32_bf16 v[128:131], v[232:235], v[84:87], v[128:131]\n\t" \
  "v_exp_f32_e32 v165, v165\n\t" \
  "v_mfma_f32_16x16x32_bf16 v[136:139], v[232:235], v[100:103], v[136:139]\n\t" \
  "ds_read_b128 v[232:235], v8 offset:59392\n\t" \
  "s_cmp_gt_u32 s63, 0\n\t" \
  "s_cbranch_scc1 MX3_%=\n\t" \
  "v_mov_b32_e32 v116, v184\n\t" \
  "v_mov_b32_e32 v117, v184\n\t" \
  "v_mov_b32_e32 v118, v184\n\t" \
  "v_mov_b32_e32 v119, v184\n\t" \
  "v_cndmask_b32_e64 v112, v112, v184, s[48:49]\n\t" \
  "v_cndmask_b32_e64 v113, v113, v184, s[50:51]\n\t" \
  "v_cndmask_b32_e64 v114, v114, v184, s[52:53]\n\t" \
  "v_cndmask_b32_e64 v115, v115, v184, s[54:55]\n\t" \
  "v_cndmask_b32_e64 v124, v124, v184, s[48:49]\n\t" \
  "v_cndmask_b32_e64 v125, v125, v184, s[50:51]\n\t" \
  "v_cndmask_b32_e64 v126, v126, v184, s[52:53]\n\t" \
  "v_cndmask_b32_e64 v127, v127, v184, s[54:55]\n\t" \
  "MX3_%=:\n\t" \
  "v_max3_f32 v10, v112, v113, v114\n\t" \
  "v_mfma_f32_16x16x32_bf16 v[140:143], v[236:239], v[100:103], v[140:143]\n\t" \
  "v_exp_f32_e32 v166, v166\n\t" \
  "v_mfma_f32_16x16x32_bf16 v[132:135], v[236:239], v[84:87], v[132:135]\n\t" \
  "ds_read_b128 v[236:239], v8 offset:60416\n\t" \
  "v_max3_f32 v11, v115, v116, v117\n\t" \
  "v_mfma_f32_16x16x32_bf16 v[128:131], v[240:243], v[88:91], v[128:131]\n\t" \
  "v_exp_f32_e32 v167, v167\n\t" \
  "v_mfma_f32_16x16x32_bf16 v[136:139], v[240:243], v[104:107], v[136:139]\n\t" \
  "ds_read_b128 v[240:243], v8 offset:61440\n\t" \
  "v_max3_f32 v10, v10, v118, v119\n\t" \
  "v_mfma_f32_16x16x32_bf16 v[140:143], v[244:247], v[104:107], v[140:143]\n\t" \
  "v_exp_f32_e32 v168, v168\n\t" \
  "v_mfma_f32_16x16x32_bf16 v[132:135], v[244:247], v[88:91], v[132:135]\n\t" \
  "ds_read_b128 v[244:247], v8 offset:62464\n\t" \
  "v_max3_f32 v11, v11, v120, v121\n\t" \
  "v_mfma_f32_16x16x32_bf16 v[128:131], v[248:251], v[92:95], v[128:131]\n\t" \
  "v_exp_f32_e32 v169, v169\n\t" \
  "v_mfma_f32_16x16x32_bf16 v[136:139], v[248:251], v[108:111], v[136:139]\n\t" \
  "ds_read_b128 v[248:251], v8 offset:63488\n\t" \
  "v_max3_f32 v10, v10, v122, v123\n\t" \
  "v_mfma_f32_16x16x32_bf16 v[140:143], v[252:255], v[108:111], v[140:143]\n\t" \
  "v_exp_f32_e32 v170, v170\n\t" \
  "v_mfma_f32_16x16x32_bf16 v[132:135], v[252:255], v[92:95], v[132:135]\n\t" \
  "ds_read_b128 v[252:255], v8 offset:64512\n\t" \
  "v_max3_f32 v11, v11, v124, v125\n\t" \
  "s_waitcnt lgkmcnt(8)\n\t" \
  "v_mfma_f32_16x16x32_bf16 v[16:19], v[192:195], v[144:147], v[16:19]\n\t" \
  "v_exp_f32_e32 v171, v171\n\t" \
  "v_mfma_f32_16x16x32_bf16 v[24:27], v[192:195], v[152:155], v[24:27]\n\t" \
  "ds_read_b128 v[192:195], v8 offset:32768\n\t" \
  "v_max3_f32 v10, v10, v126, v127\n\t" \
  "v_mfma_f32_16x16x32_bf16 v[12:15], v[188:191], v[152:155], v[12:15]\n\t" \
  "v_exp_f32_e32 v172, v172\n\t" \
  "v_mfma_f32_16x16x32_bf16 v[28:31], v[196:199], v[152:155], v[28:31]\n\t" \
  "v_exp_f32_e32 v173, v173\n\t" \
  "v_mfma_f32_16x16x32_bf16 v[20:23], v[196:199], v[144:147], v[20:23]\n\t" \
  "ds_read_b128 v[196:199], v8 offset:36864\n\t" \
  "v_cvt_pk_bf16_f32 v162, v164, v165\n\t" \
  "v_mfma_f32_16x16x32_bf16 v[4:7], v[188:191], v[144:147], v[4:7]\n\t" \
  "v_cvt_pk_bf16_f32 v163, v166, v167\n\t" \
  "v_cvt_pk_bf16_f32 v168, v168, v169\n\t" \
  "v_mfma_f32_16x16x32_bf16 v[32:35], v[200:203], v[144:147], v[32:35]\n\t" \
  "v_exp_f32_e32 v174, v174\n\t" \
  "v_mfma_f32_16x16x32_bf16 v[40:43], v[200:203], v[152:155], v[40:43]\n\t" \
  "ds_read_b128 v[200:203], v8 offset:33792\n\t" \
  "s_cmp_gt_u32 s63, 1\n\t" \
  "s_cbranch_scc1 MX4_%=\n\t" \
  "v_mov_b32_e32 v132, v184\n\t" \
  "v_mov_b32_e32 v133, v184\n\t" \
  "v_mov_b32_e32 v134, v184\n\t" \
  "v_mov_b32_e32 v135, v184\n\t" \
  "s_cmp_eq_u32 s63, 1\n\t" \
  "s_cbranch_scc1 MD4_%=\n\t" \
  "v_mov_b32_e32 v128, v184\n\t" \
  "v_mov_b32_e32 v129, v184\n\t" \
  "v_mov_b32_e32 v130, v184\n\t" \
  "v_mov_b32_e32 v131, v184\n\t" \
  "v_mov_b32_e32 v136, v184\n\t" \
  "v_mov_b32_e32 v137, v184\n\t" \
  "v_mov_b32_e32 v138, v184\n\t" \
  "v_mov_b32_e32 v139, v184\n\t" \
  "v_mov_b32_e32 v140, v184\n\t" \
  "v_mov_b32_e32 v141, v184\n\t" \
  "v_mov_b32_e32 v142, v184\n\t" \
  "v_mov_b32_e32 v143, v184\n\t" \
  "s_branch MX4_%=\n\t" \
  "MD4_%=:\n\t" \
  "v_cndmask_b32_e64 v128, v128, v184, s[48:49]\n\t" \
  "v_cndmask_b32_e64 v129, v129, v184, s[50:51]\n\t" \
  "v_cndmask_b32_e64 v130, v130, v184, s[52:53]\n\t" \
  "v_cndmask_b32_e64 v131, v131, v184, s[54:55]\n\t" \
  "v_cndmask_b32_e64 v140, v140, v184, s[48:49]\n\t" \
  "v_cndmask_b32_e64 v141, v141, v184, s[50:51]\n\t" \
  "v_cndmask_b32_e64 v142, v142, v184, s[52:53]\n\t" \
  "v_cndmask_b32_e64 v143, v143, v184, s[54:55]\n\t" \
  "MX4_%=:\n\t" \
  "v_max3_f32 v11, v11, v128, v129\n\t" \
  "v_mfma_f32_16x16x32_bf16 v[44:47], v[204:207], v[152:155], v[44:47]\n\t" \
  "v_exp_f32_e32 v175, v175\n\t" \
  "v_mfma_f32_16x16x32_bf16 v[36:39], v[204:207], v[144:147], v[36:39]\n\t" \
  "ds_read_b128 v[204:207], v8 offset:37888\n\t" \
  "v_max3_f32 v10, v10, v130, v131\n\t" \
  "v_mfma_f32_16x16x32_bf16 v[48:51], v[208:211], v[144:147], v[48:51]\n\t" \
  "v_max3_f32 v11, v11, v132, v133\n\t" \
  "v_max3_f32 v10, v10, v134, v135\n\t" \
  "v_mfma_f32_16x16x32_bf16 v[56:59], v[208:211], v[152:155], v[56:59]\n\t" \
  "ds_read_b128 v[208:211], v8 offset:34816\n\t" \
  "v_cvt_pk_bf16_f32 v169, v170, v171\n\t" \
  "v_mfma_f32_16x16x32_bf16 v[60:63], v[212:215], v[152:155], v[60:63]\n\t" \
  "v_max3_f32 v11, v11, v136, v137\n\t" \
  "v_max3_f32 v10, v10, v138, v139\n\t" \
  "v_mfma_f32_16x16x32_bf16 v[52:55], v[212:215], v[144:147], v[52:55]\n\t" \
  "ds_read_b128 v[212:215], v8 offset:38912\n\t" \
  "v_cvt_pk_bf16_f32 v170, v172, v173\n\t" \
  "v_mfma_f32_16x16x32_bf16 v[64:67], v[216:219], v[144:147], v[64:67]\n\t" \
  "v_max3_f32 v11, v11, v140, v141\n\t" \
  "v_max3_f32 v10, v10, v142, v143\n\t" \
  "v_mfma_f32_16x16x32_bf16 v[72:75], v[216:219], v[152:155], v[72:75]\n\t" \
  "ds_read_b128 v[216:219], v8 offset:35840\n\t" \
  "v_cvt_pk_bf16_f32 v171, v174, v175\n\t" \
  "v_max_f32_e32 v10, v10, v11\n\t" \
  "v_mfma_f32_16x16x32_bf16 v[76:79], v[220:223], v[152:155], v[76:79]\n\t" \
  "v_cmp_lt_f32_e32 vcc, s45, v10\n\t" \
  "v_mfma_f32_16x16x32_bf16 v[68:71], v[220:223], v[144:147], v[68:71]\n\t" \
  "ds_read_b128 v[220:223], v8 offset:39936\n\t" \
  "s_cbranch_vccnz RALc_%=\n\t" \
  "RArLc_%=:\n\t" \
  "v_exp_f32_e32 v112, v112\n\t" \
  "s_waitcnt lgkmcnt(8)\n\t" \
  "v_mfma_f32_16x16x32_bf16 v[16:19], v[224:227], v[160:163], v[16:19]\n\t" \
  "v_exp_f32_e32 v113, v113\n\t" \
  "v_mfma_f32_16x16x32_bf16 v[24:27], v[224:227], v[168:171], v[24:27]\n\t" \
  "ds_read_b128 v[224:227], v8 offset:40960\n\t" \
  "v_mfma_f32_16x16x32_bf16 v[28:31], v[228:231], v[168:171], v[28:31]\n\t" \
  "v_exp_f32_e32 v114, v114\n\t" \
  "v_mfma_f32_16x16x32_bf16 v[20:23], v[228:231], v[160:163], v[20:23]\n\t" \
  "ds_read_b128 v[228:231], v8 offset:45056\n\t" \
  "v_mfma_f32_16x16x32_bf16 v[4:7], v[188:191], v[160:163], v[4:7]\n\t" \
  "v_exp_f32_e32 v115, v115\n\t" \
  "v_mfma_f32_16x16x32_bf16 v[32:35], v[232:235], v[160:163], v[32:35]\n\t" \
  "v_exp_f32_e32 v116, v116\n\t" \
  "v_mfma_f32_16x16x32_bf16 v[40:43], v[232:235], v[168:171], v[40:43]\n\t" \
  "ds_read_b128 v[232:235], v8 offset:41984\n\t" \
  "v_mfma_f32_16x16x32_bf16 v[44:47], v[236:239], v[168:171], v[44:47]\n\t" \
  "v_exp_f32_e32 v117, v117\n\t" \
  "v_mfma_f32_16x16x32_bf16 v[36:39], v[236:239], v[160:163], v[36:39]\n\t" \
  "ds_read_b128 v[236:239], v8 offset:46080\n\t" \
  "v_mfma_f32_16x16x32_bf16 v[48:51], v[240:243], v[160:163], v[48:51]\n\t" \
  "v_exp_f32_e32 v118, v118\n\t" \
  "v_mfma_f32_16x16x32_bf16 v[56:59], v[240:243], v[168:171], v[56:59]\n\t" \
  "ds_read_b128 v[240:243], v8 offset:43008\n\t" \
  "v_mfma_f32_16x16x32_bf16 v[12:15], v[188:191], v[168:171], v[12:15]\n\t" \
  "v_exp_f32_e32 v119, v119\n\t" \
  "v_mfma_f32_16x16x32_bf16 v[60:63], v[244:247], v[168:171], v[60:63]\n\t" \
  "v_exp_f32_e32 v120, v120\n\t" \
  "v_mfma_f32_16x16x32_bf16 v[52:55], v[244:247], v[160:163], v[52:55]\n\t" \
  "ds_read_b128 v[244:247], v8 offset:47104\n\t" \
  "v_cvt_pk_bf16_f32 v112, v112, v113\n\t" \
  "v_mfma_f32_16x16x32_bf16 v[64:67], v[248:251], v[160:163], v[64:67]\n\t" \
  "v_exp_f32_e32 v121, v121\n\t" \
  "v_mfma_f32_16x16x32_bf16 v[72:75], v[248:251], v[168:171], v[72:75]\n\t" \
  "ds_read_b128 v[248:251], v8 offset:44032\n\t" \
  "v_cvt_pk_bf16_f32 v113, v114, v115\n\t" \
  "v_mfma_f32_16x16x32_bf16 v[76:79], v[252:255], v[168:171], v[76:79]\n\t" \
  "v_exp_f32_e32 v122, v122\n\t" \
  "v_mfma_f32_16x16x32_bf16 v[68:71], v[252:255], v[160:163], v[68:71]\n\t" \
  "ds_read_b128 v[252:255], v8 offset:48128\n\t" \
  "s_cmp_lg_u32 s44, 0\n\t" \
  "s_cbranch_scc1 RBLc_%=\n\t" \
  "RBrLc_%=:\n\t" \
  "TOL_%=:\n\t" \
  "s_cmp_lt_u32 s63, 2\n\t" \
  "s_cbranch_scc1 SKL_%=\n\t" \
  "v_xor_b32_e32 v8, 0x10000, v8\n\t" \
  "s_waitcnt lgkmcnt(8)\n\t" \
  "v_mfma_f32_16x16x32_bf16 v[144:147], v[192:195], v[80:83], v[176:179]\n\t" \
  "s_barrier\n\t" \
  "v_exp_f32_e32 v123, v123\n\t" \
  "v_mfma_f32_16x16x32_bf16 v[152:155], v[192:195], v[96:99], v[180:183]\n\t" \
  "ds_read_b128 v[192:195], v8 offset:16384\n\t" \
  "v_cvt_pk_bf16_f32 v114, v116, v117\n\t" \
  "v_mfma_f32_16x16x32_bf16 v[156:159], v[196:199], v[96:99], v[180:183]\n\t" \
  "v_exp_f32_e32 v124, v124\n\t" \
  "v_mfma_f32_16x16x32_bf16 v[148:151], v[196:199], v[80:83], v[176:179]\n\t" \
  "ds_read_b128 v[196:199], v8 offset:17408\n\t" \
  "v_cvt_pk_bf16_f32 v115, v118, v119\n\t" \
  "v_mfma_f32_16x16x32_bf16 v[144:147], v[200:203], v[84:87], v[144:147]\n\t" \
  "v_exp_f32_e32 v125, v125\n\t" \
  "v_mfma_f32_16x16x32_bf16 v[152:155], v[200:203], v[100:103], v[152:155]\n\t" \
  "ds_read_b128 v[200:203], v8 offset:18432\n\t" \
  "v_cvt_pk_bf16_f32 v120, v120, v121\n\t" \
  "v_mfma_f32_16x16x32_bf16 v[156:159], v[204:207], v[100:103], v[156:159]\n\t" \
  "v_exp_f32_e32 v126, v126\n\t" \
  "v_mfma_f32_16x16x32_bf16 v[148:151], v[204:207], v[84:87], v[148:151]\n\t" \
  "ds_read_b128 v[204:207], v8 offset:19456\n\t" \
  "v_cvt_pk_bf16_f32 v121, v122, v123\n\t" \
  "v_mfma_f32_16x16x32_bf16 v[144:147], v[208:211], v[88:91], v[144:147]\n\t" \
  "v_exp_f32_e32 v127, v127\n\t" \
  "v_mfma_f32_16x16x32_bf16 v[152:155], v[208:211], v[104:107], v[152:155]\n\t" \
  "ds_read_b128 v[208:211], v8 offset:20480\n\t" \
  "v_cvt_pk_bf16_f32 v122, v124, v125\n\t" \
  "v_mfma_f32_16x16x32_bf16 v[156:159], v[212:215], v[104:107], v[156:159]\n\t" \
  "v_exp_f32_e32 v128, v128\n\t" \
  "v_mfma_f32_16x16x32_bf16 v[148:151], v[212:215], v[88:91], v[148:151]\n\t" \
  "ds_read_b128 v[212:215], v8 offset:21504\n\t" \
  "v_cvt_pk_bf16_f32 v123, v126, v127\n\t" \
  "v_mfma_f32_16x16x32_bf16 v[144:147], v[216:219], v[92:95], v[144:147]\n\t" \
  "v_exp_f32_e32 v129, v129\n\t" \
  "v_mfma_f32_16x16x32_bf16 v[152:155], v[216:219], v[108:111], v[152:155]\n\t" \
  "ds_read_b128 v[216:219], v8 offset:22528\n\t" \
  "v_cmp_gt_i32_e64 s[48:49], 0, v2\n\t" \
  "v_cmp_gt_i32_e64 s[50:51], 1, v2\n\t" \
  "v_mfma_f32_16x16x32_bf16 v[156:159], v[220:223], v[108:111], v[156:159]\n\t" \
  "v_exp_f32_e32 v130, v130\n\t" \
  "v_mfma_f32_16x16x32_bf16 v[148:151], v[220:223], v[92:95], v[148:151]\n\t" \
  "ds_read_b128 v[220:223], v8 offset:23552\n\t" \
  "v_cmp_gt_i32_e64 s[52:53], 2, v2\n\t" \
  "v_cmp_gt_i32_e64 s[54:55], 3, v2\n\t" \
  "s_waitcnt lgkmcnt(8)\n\t" \
  "v_mfma_f32_16x16x32_bf16 v[160:163], v[224:227], v[80:83], v[176:179]\n\t" \
  "v_exp_f32_e32 v131, v131\n\t" \
  "v_mfma_f32_16x16x32_bf16 v[168:171], v[224:227], v[96:99], v[180:183]\n\t" \
  "ds_read_b128 v[224:227], v8 offset:24576\n\t" \
  "v_cvt_pk_bf16_f32 v128, v128, v129\n\t" \
  "v_mfma_f32_16x16x32_bf16 v[172:175], v[228:231], v[96:99], v[180:183]\n\t" \
  "v_exp_f32_e32 v132, v132\n\t" \
  "v_mfma_f32_16x16x32_bf16 v[164:167], v[228:231], v[80:83], v[176:179]\n\t" \
  "ds_read_b128 v[228:231], v8 offset:25600\n\t" \
  "v_cvt_pk_bf16_f32 v129, v130, v131\n\t" \
  "v_mfma_f32_16x16x32_bf16 v[160:163], v[232:235], v[84:87], v[160:163]\n\t" \
  "v_exp_f32_e32 v133, v133\n\t" \
  "v_mfma_f32_16x16x32_bf16 v[168:171], v[232:235], v[100:103], v[168:171]\n\t" \
  "ds_read_b128 v[232:235], v8 offset:26624\n\t" \
  "s_cmp_gt_u32 s63, 2\n\t" \
  "s_cbranch_scc1 MX5_%=\n\t" \
  "v_mov_b32_e32 v148, v184\n\t" \
  "v_mov_b32_e32 v149, v184\n\t" \
  "v_mov_b32_e32 v150, v184\n\t" \
  "v_mov_b32_e32 v151, v184\n\t" \
  "s_cmp_eq_u32 s63, 2\n\t" \
  "s_cbranch_scc1 MD5_%=\n\t" \
  "v_mov_b32_e32 v144, v184\n\t" \
  "v_mov_b32_e32 v145, v184\n\t" \
  "v_mov_b32_e32 v146, v184\n\t" \
  "v_mov_b32_e32 v147, v184\n\t" \
  "v_mov_b32_e32 v152, v184\n\t" \
  "v_mov_b32_e32 v153, v184\n\t" \
  "v_mov_b32_e32 v154, v184\n\t" \
  "v_mov_b32_e32 v155, v184\n\t" \
  "v_mov_b32_e32 v156, v184\n\t" \
  "v_mov_b32_e32 v157, v184\n\t" \
  "v_mov_b32_e32 v158, v184\n\t" \
  "v_mov_b32_e32 v159, v184\n\t" \
  "s_branch MX5_%=\n\t" \
  "MD5_%=:\n\t" \
  "v_cndmask_b32_e64 v144, v144, v184, s[48:49]\n\t" \
  "v_cndmask_b32_e64 v145, v145, v184, s[50:51]\n\t" \
  "v_cndmask_b32_e64 v146, v146, v184, s[52:53]\n\t" \
  "v_cndmask_b32_e64 v147, v147, v184, s[54:55]\n\t" \
  "v_cndmask_b32_e64 v156, v156, v184, s[48:49]\n\t" \
  "v_cndmask_b32_e64 v157, v157, v184, s[50:51]\n\t" \
  "v_cndmask_b32_e64 v158, v158, v184, s[52:53]\n\t" \
  "v_cndmask_b32_e64 v159, v159, v184, s[54:55]\n\t" \
  "MX5_%=:\n\t" \
  "v_max3_f32 v10, v144, v145, v146\n\t" \
  "v_mfma_f32_16x16x32_bf16 v[172:175], v[236:239], v[100:103], v[172:175]\n\t" \
  "v_exp_f32_e32 v134, v134\n\t" \
  "v_mfma_f32_16x16x32_bf16 v[164:167], v[236:239], v[84:87], v[164:167]\n\t" \
  "ds_read_b128 v[236:239], v8 offset:27648\n\t" \
  "v_max3_f32 v11, v147, v148, v149\n\t" \
  "v_mfma_f32_16x16x32_bf16 v[160:163], v[240:243], v[88:91], v[160:163]\n\t" \
  "v_exp_f32_e32 v135, v135\n\t" \
  "v_mfma_f32_16x16x32_bf16 v[168:171], v[240:243], v[104:107], v[168:171]\n\t" \
  "ds_read_b128 v[240:243], v8 offset:28672\n\t" \
  "v_max3_f32 v10, v10, v150, v151\n\t" \
  "v_mfma_f32_16x16x32_bf16 v[172:175], v[244:247], v[104:107], v[172:175]\n\t" \
  "v_exp_f32_e32 v136, v136\n\t" \
  "v_mfma_f32_16x16x32_bf16 v[164:167], v[244:247], v[88:91], v[164:167]\n\t" \
  "ds_read_b128 v[244:247], v8 offset:29696\n\t" \
  "v_max3_f32 v11, v11, v152, v153\n\t" \
  "v_mfma_f32_16x16x32_bf16 v[160:163], v[248:251], v[92:95], v[160:163]\n\t" \
  "v_exp_f32_e32 v137, v137\n\t" \
  "v_mfma_f32_16x16x32_bf16 v[168:171], v[248:251], v[108:111], v[168:171]\n\t" \
  "ds_read_b128 v[248:251], v8 offset:30720\n\t" \
  "v_max3_f32 v10, v10, v154, v155\n\t" \
  "v_mfma_f32_16x16x32_bf16 v[172:175], v[252:255], v[108:111], v[172:175]\n\t" \
  "v_exp_f32_e32 v138, v138\n\t" \
  "v_mfma_f32_16x16x32_bf16 v[164:167], v[252:255], v[92:95], v[164:167]\n\t" \
  "ds_read_b128 v[252:255], v8 offset:31744\n\t" \
  "v_max3_f32 v11, v11, v156, v157\n\t" \
  "s_waitcnt lgkmcnt(8)\n\t" \
  "v_mfma_f32_16x16x32_bf16 v[16:19], v[192:195], v[112:115], v[16:19]\n\t" \
  "v_exp_f32_e32 v139, v139\n\t" \
  "v_mfma_f32_16x16x32_bf16 v[24:27], v[192:195], v[120:123], v[24:27]\n\t" \
  "v_max3_f32 v10, v10, v158, v159\n\t" \
  "v_mfma_f32_16x16x32_bf16 v[12:15], v[188:191], v[120:123], v[12:15]\n\t" \
  "v_exp_f32_e32 v140, v140\n\t" \
  "v_mfma_f32_16x16x32_bf16 v[28:31], v[196:199], v[120:123], v[28:31]\n\t" \
  "v_exp_f32_e32 v141, v141\n\t" \
  "v_mfma_f32_16x16x32_bf16 v[20:23], v[196:199], v[112:115], v[20:23]\n\t" \
  "v_cvt_pk_bf16_f32 v130, v132, v133\n\t" \
  "v_mfma_f32_16x16x32_bf16 v[4:7], v[188:191], v[112:115], v[4:7]\n\t" \
  "v_cvt_pk_bf16_f32 v131, v134, v135\n\t" \
  "v_cvt_pk_bf16_f32 v136, v136, v137\n\t" \
  "v_mfma_f32_16x16x32_bf16 v[32:35], v[200:203], v[112:115], v[32:35]\n\t" \
  "v_exp_f32_e32 v142, v142\n\t" \
  "v_mfma_f32_16x16x32_bf16 v[40:43], v[200:203], v[120:123], v[40:43]\n\t" \
  "v_mov_b32_e32 v164, v184\n\t" \
  "v_mov_b32_e32 v165, v184\n\t" \
  "v_mov_b32_e32 v166, v184\n\t" \
  "v_mov_b32_e32 v167, v184\n\t" \
  "s_cmp_eq_u32 s63, 3\n\t" \
  "s_cbranch_scc1 MD6_%=\n\t" \
  "v_mov_b32_e32 v160, v184\n\t" \
  "v_mov_b32_e32 v161, v184\n\t" \
  "v_mov_b32_e32 v162, v184\n\t" \
  "v_mov_b32_e32 v163, v184\n\t" \
  "v_mov_b32_e32 v168, v184\n\t" \
  "v_mov_b32_e32 v169, v184\n\t" \
  "v_mov_b32_e32 v170, v184\n\t" \
  "v_mov_b32_e32 v171, v184\n\t" \
  "v_mov_b32_e32 v172, v184\n\t" \
  "v_mov_b32_e32 v173, v184\n\t" \
  "v_mov_b32_e32 v174, v184\n\t" \
  "v_mov_b32_e32 v175, v184\n\t" \
  "s_branch MX6_%=\n\t" \
  "MD6_%=:\n\t" \
  "v_cndmask_b32_e64 v160, v160, v184, s[48:49]\n\t" \
  "v_cndmask_b32_e64 v161, v161, v184, s[50:51]\n\t" \
  "v_cndmask_b32_e64 v162, v162, v184, s[52:53]\n\t" \
  "v_cndmask_b32_e64 v163, v163, v184, s[54:55]\n\t" \
  "v_cndmask_b32_e64 v172, v172, v184, s[48:49]\n\t" \
  "v_cndmask_b32_e64 v173, v173, v184, s[50:51]\n\t" \
  "v_cndmask_b32_e64 v174, v174, v184, s[52:53]\n\t" \
  "v_cndmask_b32_e64 v175, v175, v184, s[54:55]\n\t" \
  "MX6_%=:\n\t" \
  "v_max3_f32 v11, v11, v160, v161\n\t" \
  "v_mfma_f32_16x16x32_bf16 v[44:47], v[204:207], v[120:123], v[44:47]\n\t" \
  "v_exp_f32_e32 v143, v143\n\t" \
  "v_mfma_f32_16x16x32_bf16 v[36:39], v[204:207], v[112:115], v[36:39]\n\t" \
  "v_max3_f32 v10, v10, v162, v163\n\t" \
  "v_mfma_f32_16x16x32_bf16 v[48:51], v[208:211], v[112:115], v[48:51]\n\t" \
  "v_max3_f32 v11, v11, v164, v165\n\t" \
  "v_max3_f32 v10, v10, v166, v167\n\t" \
  "v_mfma_f32_16x16x32_bf16 v[56:59], v[208:211], v[120:123], v[56:59]\n\t" \
  "v_cvt_pk_bf16_f32 v137, v138, v139\n\t" \
  "v_mfma_f32_16x16x32_bf16 v[60:63], v[212:215], v[120:123], v[60:63]\n\t" \
  "v_max3_f32 v11, v11, v168, v169\n\t" \
  "v_max3_f32 v10, v10, v170, v171\n\t" \
  "v_mfma_f32_16x16x32_bf16 v[52:55], v[212:215], v[112:115], v[52:55]\n\t" \
  "v_cvt_pk_bf16_f32 v138, v140, v141\n\t" \
  "v_mfma_f32_16x16x32_bf16 v[64:67], v[216:219], v[112:115], v[64:67]\n\t" \
  "v_max3_f32 v11, v11, v172, v173\n\t" \
  "v_max3_f32 v10, v10, v174, v175\n\t" \
  "v_mfma_f32_16x16x32_bf16 v[72:75], v[216:219], v[120:123], v[72:75]\n\t" \
  "v_cvt_pk_bf16_f32 v139, v142, v143\n\t" \
  "v_max_f32_e32 v10, v10, v11\n\t" \
  "v_mfma_f32_16x16x32_bf16 v[76:79], v[220:223], v[120:123], v[76:79]\n\t" \
  "v_cmp_lt_f32_e32 vcc, s45, v10\n\t" \
  "v_mfma_f32_16x16x32_bf16 v[68:71], v[220:223], v[112:115], v[68:71]\n\t" \
  "s_cbranch_vccnz RALd_%=\n\t" \
  "RArLd_%=:\n\t" \
  "v_exp_f32_e32 v144, v144\n\t" \
  "s_waitcnt lgkmcnt(0)\n\t" \
  "v_mfma_f32_16x16x32_bf16 v[16:19], v[224:227], v[128:131], v[16:19]\n\t" \
  "v_exp_f32_e32 v145, v145\n\t" \
  "v_mfma_f32_16x16x32_bf16 v[24:27], v[224:227], v[136:139], v[24:27]\n\t" \
  "v_mfma_f32_16x16x32_bf16 v[28:31], v[228:231], v[136:139], v[28:31]\n\t" \
  "v_exp_f32_e32 v146, v146\n\t" \
  "v_mfma_f32_16x16x32_bf16 v[20:23], v[228:231], v[128:131], v[20:23]\n\t" \
  "v_mfma_f32_16x16x32_bf16 v[4:7], v[188:191], v[128:131], v[4:7]\n\t" \
  "v_exp_f32_e32 v147, v147\n\t" \
  "v_mfma_f32_16x16x32_bf16 v[32:35], v[232:235], v[128:131], v[32:35]\n\t" \
  "v_exp_f32_e32 v148, v148\n\t" \
  "v_mfma_f32_16x16x32_bf16 v[40:43], v[232:235], v[136:139], v[40:43]\n\t" \
  "v_mfma_f32_16x16x32_bf16 v[44:47], v[236:239], v[136:139], v[44:47]\n\t" \
  "v_exp_f32_e32 v149, v149\n\t" \
  "v_mfma_f32_16x16x32_bf16 v[36:39], v[236:239], v[128:131], v[36:39]\n\t" \
  "v_mfma_f32_16x16x32_bf16 v[48:51], v[240:243], v[128:131], v[48:51]\n\t" \
  "v_exp_f32_e32 v150, v150\n\t" \
  "v_mfma_f32_16x16x32_bf16 v[56:59], v[240:243], v[136:139], v[56:59]\n\t" \
  "v_mfma_f32_16x16x32_bf16 v[12:15], v[188:191], v[136:139], v[12:15]\n\t" \
  "v_exp_f32_e32 v151, v151\n\t" \
  "v_mfma_f32_16x16x32_bf16 v[60:63], v[244:247], v[136:139], v[60:63]\n\t" \
  "v_exp_f32_e32 v152, v152\n\t" \
  "v_mfma_f32_16x16x32_bf16 v[52:55], v[244:247], v[128:131], v[52:55]\n\t" \
  "v_cvt_pk_bf16_f32 v144, v144, v145\n\t" \
  "v_mfma_f32_16x16x32_bf16 v[64:67], v[248:251], v[128:131], v[64:67]\n\t" \
  "v_exp_f32_e32 v153, v153\n\t" \
  "v_mfma_f32_16x16x32_bf16 v[72:75], v[248:251], v[136:139], v[72:75]\n\t" \
  "v_cvt_pk_bf16_f32 v145, v146, v147\n\t" \
  "v_mfma_f32_16x16x32_bf16 v[76:79], v[252:255], v[136:139], v[76:79]\n\t" \
  "v_exp_f32_e32 v154, v154\n\t" \
  "v_mfma_f32_16x16x32_bf16 v[68:71], v[252:255], v[128:131], v[68:71]\n\t" \
  "s_cmp_lg_u32 s44, 0\n\t" \
  "s_cbranch_scc1 RBLd_%=\n\t" \
  "RBrLd_%=:\n\t" \
  "s_barrier\n\t" \
  "ds_read_b128 v[192:195], v8 offset:49152\n\t" \
  "v_exp_f32_e32 v155, v155\n\t" \
  "ds_read_b128 v[196:199], v8 offset:50176\n\t" \
  "v_cvt_pk_bf16_f32 v146, v148, v149\n\t" \
  "ds_read_b128 v[200:203], v8 offset:51200\n\t" \
  "v_exp_f32_e32 v156, v156\n\t" \
  "ds_read_b128 v[204:207], v8 offset:52224\n\t" \
  "v_cvt_pk_bf16_f32 v147, v150, v151\n\t" \
  "ds_read_b128 v[208:211], v8 offset:53248\n\t" \
  "v_exp_f32_e32 v157, v157\n\t" \
  "ds_read_b128 v[212:215], v8 offset:54272\n\t" \
  "v_cvt_pk_bf16_f32 v152, v152, v153\n\t" \
  "ds_read_b128 v[216:219], v8 offset:55296\n\t" \
  "v_exp_f32_e32 v158, v158\n\t" \
  "ds_read_b128 v[220:223], v8 offset:56320\n\t" \
  "v_cvt_pk_bf16_f32 v153, v154, v155\n\t" \
  "ds_read_b128 v[224:227], v8 offset:57344\n\t" \
  "v_exp_f32_e32 v159, v159\n\t" \
  "ds_read_b128 v[228:231], v8 offset:58368\n\t" \
  "v_cvt_pk_bf16_f32 v154, v156, v157\n\t" \
  "ds_read_b128 v[232:235], v8 offset:59392\n\t" \
  "v_exp_f32_e32 v160, v160\n\t" \
  "ds_read_b128 v[236:239], v8 offset:60416\n\t" \
  "v_cvt_pk_bf16_f32 v155, v158, v159\n\t" \
  "ds_read_b128 v[240:243], v8 offset:61440\n\t" \
  "v_exp_f32_e32 v161, v161\n\t" \
  "ds_read_b128 v[244:247], v8 offset:62464\n\t" \
  "v_exp_f32_e32 v162, v162\n\t" \
  "ds_read_b128 v[248:251], v8 offset:63488\n\t" \
  "v_cvt_pk_bf16_f32 v160, v160, v161\n\t" \
  "ds_read_b128 v[252:255], v8 offset:64512\n\t" \
  "v_exp_f32_e32 v163, v163\n\t" \
  "s_waitcnt lgkmcnt(8)\n\t" \
  "v_mfma_f32_16x16x32_bf16 v[16:19], v[192:195], v[144:147], v[16:19]\n\t" \
  "v_exp_f32_e32 v164, v164\n\t" \
  "v_cvt_pk_bf16_f32 v161, v162, v163\n\t" \
  "v_mfma_f32_16x16x32_bf16 v[24:27], v[192:195], v[152:155], v[24:27]\n\t" \
  "v_exp_f32_e32 v165, v165\n\t" \
  "v_mfma_f32_16x16x32_bf16 v[28:31], v[196:199], v[152:155], v[28:31]\n\t" \
  "v_exp_f32_e32 v166, v166\n\t" \
  "v_mfma_f32_16x16x32_bf16 v[20:23], v[196:199], v[144:147], v[20:23]\n\t" \
  "v_cvt_pk_bf16_f32 v162, v164, v165\n\t" \
  "v_mfma_f32_16x16x32_bf16 v[32:35], v[200:203], v[144:147], v[32:35]\n\t" \
  "v_exp_f32_e32 v167, v167\n\t" \
  "v_exp_f32_e32 v168, v168\n\t" \
  "v_mfma_f32_16x16x32_bf16 v[40:43], v[200:203], v[152:155], v[40:43]\n\t" \
  "v_cvt_pk_bf16_f32 v163, v166, v167\n\t" \
  "v_mfma_f32_16x16x32_bf16 v[44:47], v[204:207], v[152:155], v[44:47]\n\t" \
  "v_exp_f32_e32 v169, v169\n\t" \
  "v_mfma_f32_16x16x32_bf16 v[36:39], v[204:207], v[144:147], v[36:39]\n\t" \
  "v_exp_f32_e32 v170, v170\n\t" \
  "v_mfma_f32_16x16x32_bf16 v[48:51], v[208:211], v[144:147], v[48:51]\n\t" \
  "v_cvt_pk_bf16_f32 v168, v168, v169\n\t" \
  "v_exp_f32_e32 v171, v171\n\t" \
  "v_mfma_f32_16x16x32_bf16 v[56:59], v[208:211], v[152:155], v[56:59]\n\t" \
  "v_exp_f32_e32 v172, v172\n\t" \
  "v_mfma_f32_16x16x32_bf16 v[60:63], v[212:215], v[152:155], v[60:63]\n\t" \
  "v_cvt_pk_bf16_f32 v169, v170, v171\n\t" \
  "v_mfma_f32_16x16x32_bf16 v[52:55], v[212:215], v[144:147], v[52:55]\n\t" \
  "v_exp_f32_e32 v173, v173\n\t" \
  "v_mfma_f32_16x16x32_bf16 v[64:67], v[216:219], v[144:147], v[64:67]\n\t" \
  "v_exp_f32_e32 v174, v174\n\t" \
  "v_cvt_pk_bf16_f32 v170, v172, v173\n\t" \
  "v_mfma_f32_16x16x32_bf16 v[72:75], v[216:219], v[152:155], v[72:75]\n\t" \
  "v_exp_f32_e32 v175, v175\n\t" \
  "v_mfma_f32_16x16x32_bf16 v[76:79], v[220:223], v[152:155], v[76:79]\n\t" \
  "s_nop 0\n\t" \
  "v_mfma_f32_16x16x32_bf16 v[68:71], v[220:223], v[144:147], v[68:71]\n\t" \
  "v_cvt_pk_bf16_f32 v171, v174, v175\n\t" \
  "v_mfma_f32_16x16x32_bf16 v[4:7], v[188:191], v[144:147], v[4:7]\n\t" \
  "v_mfma_f32_16x16x32_bf16 v[12:15], v[188:191], v[152:155], v[12:15]\n\t" \
  "s_waitcnt lgkmcnt(0)\n\t" \
  "v_mfma_f32_16x16x32_bf16 v[4:7], v[188:191], v[160:163], v[4:7]\n\t" \
  "v_mfma_f32_16x16x32_bf16 v[12:15], v[188:191], v[168:171], v[12:15]\n\t" \
  "v_mfma_f32_16x16x32_bf16 v[16:19], v[224:227], v[160:163], v[16:19]\n\t" \
  "v_mfma_f32_16x16x32_bf16 v[24:27], v[224:227], v[168:171], v[24:27]\n\t" \
  "v_mfma_f32_16x16x32_bf16 v[28:31], v[228:231], v[168:171], v[28:31]\n\t" \
  "v_mfma_f32_16x16x32_bf16 v[20:23], v[228:231], v[160:163], v[20:23]\n\t" \
  "v_mfma_f32_16x16x32_bf16 v[32:35], v[232:235], v[160:163], v[32:35]\n\t" \
  "v_mfma_f32_16x16x32_bf16 v[40:43], v[232:235], v[168:171], v[40:43]\n\t" \
  "v_mfma_f32_16x16x32_bf16 v[44:47], v[236:239], v[168:171], v[44:47]\n\t" \
  "v_mfma_f32_16x16x32_bf16 v[36:39], v[236:239], v[160:163], v[36:39]\n\t" \
  "v_mfma_f32_16x16x32_bf16 v[48:51], v[240:243], v[160:163], v[48:51]\n\t" \
  "v_mfma_f32_16x16x32_bf16 v[56:59], v[240:243], v[168:171], v[56:59]\n\t" \
  "v_mfma_f32_16x16x32_bf16 v[60:63], v[244:247], v[168:171], v[60:63]\n\t" \
  "v_mfma_f32_16x16x32_bf16 v[52:55], v[244:247], v[160:163], v[52:55]\n\t" \
  "v_rcp_f32_e32 v4, v4\n\t" \
  "v_rcp_f32_e32 v5, v5\n\t" \
  "v_rcp_f32_e32 v12, v12\n\t" \
  "v_rcp_f32_e32 v13, v13\n\t" \
  "v_mfma_f32_16x16x32_bf16 v[64:67], v[248:251], v[160:163], v[64:67]\n\t" \
  "v_mfma_f32_16x16x32_bf16 v[72:75], v[248:251], v[168:171], v[72:75]\n\t" \
  "v_mfma_f32_16x16x32_bf16 v[76:79], v[252:255], v[168:171], v[76:79]\n\t" \
  "v_mfma_f32_16x16x32_bf16 v[68:71], v[252:255], v[160:163], v[68:71]\n\t" \
  "EPL_%=:\n\t" \
  "v_pk_mul_f32 v[16:17], v[16:17], v[4:5]\n\t" \
  "v_pk_mul_f32 v[18:19], v[18:19], v[4:5]\n\t" \
  "v_pk_mul_f32 v[20:21], v[20:21], v[4:5]\n\t" \
  "v_pk_mul_f32 v[22:23], v[22:23], v[4:5]\n\t" \
  "v_cvt_pk_bf16_f32 v192, v16, v17\n\t" \
  "v_cvt_pk_bf16_f32 v193, v18, v19\n\t" \
  "v_cvt_pk_bf16_f32 v194, v20, v21\n\t" \
  "v_cvt_pk_bf16_f32 v195, v22, v23\n\t" \
  "global_store_dwordx4 v1, v[192:195], s[56:57] offset:0 sc1\n\t" \
  "v_pk_mul_f32 v[24:25], v[24:25], v[12:13]\n\t" \
  "v_pk_mul_f32 v[26:27], v[26:27], v[12:13]\n\t" \
  "v_pk_mul_f32 v[28:29], v[28:29], v[12:13]\n\t" \
  "v_pk_mul_f32 v[30:31], v[30:31], v[12:13]\n\t" \
  "v_cvt_pk_bf16_f32 v196, v24, v25\n\t" \
  "v_cvt_pk_bf16_f32 v197, v26, v27\n\t" \
  "v_cvt_pk_bf16_f32 v198, v28, v29\n\t" \
  "v_cvt_pk_bf16_f32 v199, v30, v31\n\t" \
  "global_store_dwordx4 v1, v[196:199], s[56:57] offset:1024 sc1\n\t" \
  "s_add_u32 s56, s56, 0x2000\n\t" \
  "s_addc_u32 s57, s57, 0\n\t" \
  "v_pk_mul_f32 v[32:33], v[32:33], v[4:5]\n\t" \
  "v_pk_mul_f32 v[34:35], v[34:35], v[4:5]\n\t" \
  "v_pk_mul_f32 v[36:37], v[36:37], v[4:5]\n\t" \
  "v_pk_mul_f32 v[38:39], v[38:39], v[4:5]\n\t" \
  "v_cvt_pk_bf16_f32 v200, v32, v33\n\t" \
  "v_cvt_pk_bf16_f32 v201, v34, v35\n\t" \
  "v_cvt_pk_bf16_f32 v202, v36, v37\n\t" \
  "v_cvt_pk_bf16_f32 v203, v38, v39\n\t" \
  "global_store_dwordx4 v1, v[200:203], s[56:57] offset:0 sc1\n\t" \
  "v_pk_mul_f32 v[40:41], v[40:41], v[12:13]\n\t" \
  "v_pk_mul_f32 v[42:43], v[42:43], v[12:13]\n\t" \
  "v_pk_mul_f32 v[44:45], v[44:45], v[12:13]\n\t" \
  "v_pk_mul_f32 v[46:47], v[46:47], v[12:13]\n\t" \
  "v_cvt_pk_bf16_f32 v204, v40, v41\n\t" \
  "v_cvt_pk_bf16_f32 v205, v42, v43\n\t" \
  "v_cvt_pk_bf16_f32 v206, v44, v45\n\t" \
  "v_cvt_pk_bf16_f32 v207, v46, v47\n\t" \
  "global_store_dwordx4 v1, v[204:207], s[56:57] offset:1024 sc1\n\t" \
  "s_add_u32 s56, s56, 0x2000\n\t" \
  "s_addc_u32 s57, s57, 0\n\t" \
  "v_pk_mul_f32 v[48:49], v[48:49], v[4:5]\n\t" \
  "v_pk_mul_f32 v[50:51], v[50:51], v[4:5]\n\t" \
  "v_pk_mul_f32 v[52:53], v[52:53], v[4:5]\n\t" \
  "v_pk_mul_f32 v[54:55], v[54:55], v[4:5]\n\t" \
  "v_cvt_pk_bf16_f32 v208, v48, v49\n\t" \
  "v_cvt_pk_bf16_f32 v209, v50, v51\n\t" \
  "v_cvt_pk_bf16_f32 v210, v52, v53\n\t" \
  "v_cvt_pk_bf16_f32 v211, v54, v55\n\t" \
  "global_store_dwordx4 v1, v[208:211], s[56:57] offset:0 sc1\n\t" \
  "v_pk_mul_f32 v[56:57], v[56:57], v[12:13]\n\t" \
  "v_pk_mul_f32 v[58:59], v[58:59], v[12:13]\n\t" \
  "v_pk_mul_f32 v[60:61], v[60:61], v[12:13]\n\t" \
  "v_pk_mul_f32 v[62:63], v[62:63], v[12:13]\n\t" \
  "v_cvt_pk_bf16_f32 v212, v56, v57\n\t" \
  "v_cvt_pk_bf16_f32 v213, v58, v59\n\t" \
  "v_cvt_pk_bf16_f32 v214, v60, v61\n\t" \
  "v_cvt_pk_bf16_f32 v215, v62, v63\n\t" \
  "global_store_dwordx4 v1, v[212:215], s[56:57] offset:1024 sc1\n\t" \
  "s_add_u32 s56, s56, 0x2000\n\t" \
  "s_addc_u32 s57, s57, 0\n\t" \
  "v_pk_mul_f32 v[64:65], v[64:65], v[4:5]\n\t" \
  "v_pk_mul_f32 v[66:67], v[66:67], v[4:5]\n\t" \
  "v_pk_mul_f32 v[68:69], v[68:69], v[4:5]\n\t" \
  "v_pk_mul_f32 v[70:71], v[70:71], v[4:5]\n\t" \
  "v_cvt_pk_bf16_f32 v216, v64, v65\n\t" \
  "v_cvt_pk_bf16_f32 v217, v66, v67\n\t" \
  "v_cvt_pk_bf16_f32 v218, v68, v69\n\t" \
  "v_cvt_pk_bf16_f32 v219, v70, v71\n\t" \
  "global_store_dwordx4 v1, v[216:219], s[56:57] offset:0 sc1\n\t" \
  "v_pk_mul_f32 v[72:73], v[72:73], v[12:13]\n\t" \
  "v_pk_mul_f32 v[74:75], v[74:75], v[12:13]\n\t" \
  "v_pk_mul_f32 v[76:77], v[76:77], v[12:13]\n\t" \
  "v_pk_mul_f32 v[78:79], v[78:79], v[12:13]\n\t" \
  "v_cvt_pk_bf16_f32 v220, v72, v73\n\t" \
  "v_cvt_pk_bf16_f32 v221, v74, v75\n\t" \
  "v_cvt_pk_bf16_f32 v222, v76, v77\n\t" \
  "v_cvt_pk_bf16_f32 v223, v78, v79\n\t" \
  "global_store_dwordx4 v1, v[220:223], s[56:57] offset:1024 sc1\n\t" \
  "s_add_u32 s56, s56, 0x2000\n\t" \
  "s_addc_u32 s57, s57, 0\n\t" \
  "s_waitcnt vmcnt(8)\n\t" \
  "s_and_b32 s56, s38, 1\n\t" \
  "s_lshr_b32 s58, s38, 1\n\t" \
  "s_mov_b32 s70, 7\n\t" \
  "s_mul_i32 s71, s58, 7\n\t" \
  "s_lshr_b32 s52, s39, 2\n\t" \
  "s_lshl_b32 s52, s52, 1\n\t" \
  "s_add_u32 s56, s56, s52\n\t" \
  "s_and_b32 s57, s39, 3\n\t" \
  "s_sub_u32 s49, s68, 2\n\t" \
  "s_and_b32 s49, s49, 2\n\t" \
  "s_lshl_b32 s49, s49, 15\n\t" \
  "s_mov_b32 s48, 0x20000\n\t" \
  "s_mov_b32 s50, 0x20000\n\t" \
  "s_lshr_b32 s52, s63, 1\n\t" \
  "s_and_b32 s53, s63, 1\n\t" \
  "s_lshl_b32 s54, s52, 12\n\t" \
  "v_add_u32_e32 v4, s54, v1\n\t" \
  "v_lshrrev_b32_e32 v6, 2, v0\n\t" \
  "v_and_b32_e32 v7, 7, v6\n\t" \
  "s_lshl_b32 s54, s53, 2\n\t" \
  "v_xor_b32_e32 v7, s54, v7\n\t" \
  "v_lshlrev_b32_e32 v7, 5, v7\n\t" \
  "v_lshl_or_b32 v7, v6, 8, v7\n\t" \
  "v_and_b32_e32 v6, 3, v0\n\t" \
  "v_lshlrev_b32_e32 v6, 3, v6\n\t" \
  "v_add_u32_e32 v6, 16384, v6\n\t" \
  "v_add_u32_e32 v5, v6, v7\n\t" \
  "s_lshl_b32 s82, s56, 2\n\t" \
  "s_add_u32 s82, s82, s57\n\t" \
  "s_lshl_b32 s52, s82, 18\n\t" \
  "s_add_u32 s52, s52, 0x5000000\n\t" \
  "s_lshl_b32 s53, s63, 15\n\t" \
  "s_lshl_b32 s54, s58, 13\n\t" \
  "s_add_u32 s53, s53, s54\n\t" \
  "s_add_u32 s53, s53, s52\n\t" \
  "s_mov_b64 s[72:73], %[ws]\n\t" \
  "s_add_u32 s72, s72, s53\n\t" \
  "s_addc_u32 s73, s73, 0\n\t" \
  "s_lshl_b32 s53, s58, 15\n\t" \
  "s_add_u32 s52, s52, s53\n\t" \
  "s_lshr_b32 s53, s63, 1\n\t" \
  "s_lshl_b32 s53, s53, 12\n\t" \
  "s_and_b32 s54, s63, 1\n\t" \
  "s_lshl_b32 s54, s54, 10\n\t" \
  "s_add_u32 s83, s53, s54\n\t" \
  "s_add_u32 s52, s52, s83\n\t" \
  "s_mov_b64 s[78:79], %[ws]\n\t" \
  "s_add_u32 s78, s78, s52\n\t" \
  "s_addc_u32 s79, s79, 0\n\t" \
  "v_add_u32_e32 v2, 4096, v1\n\t" \
  "s_cmp_lg_u32 s63, s58\n\t" \
  "s_cselect_b64 vcc, -1, 0\n\t" \
  "v_and_b32_e32 v15, 15, v0\n\t" \
  "v_lshlrev_b32_e32 v15, 10, v15\n\t" \
  "v_lshrrev_b32_e32 v14, 4, v0\n\t" \
  "v_lshl_or_b32 v15, v14, 3, v15\n\t" \
  "v_add_u32_e32 v14, 16384, v15\n\t" \
  "s_lshl_b32 s52, s56, 17\n\t" \
  "s_lshr_b32 s53, s58, 1\n\t" \
  "s_lshl_b32 s53, s53, 16\n\t" \
  "s_add_u32 s52, s52, s53\n\t" \
  "s_lshr_b32 s53, s63, 1\n\t" \
  "s_lshl_b32 s53, s53, 15\n\t" \
  "s_add_u32 s52, s52, s53\n\t" \
  "s_lshl_b32 s53, s57, 8\n\t" \
  "s_add_u32 s52, s52, s53\n\t" \
  "s_and_b32 s53, s58, 1\n\t" \
  "s_lshl_b32 s53, s53, 7\n\t" \
  "s_add_u32 s52, s52, s53\n\t" \
  "s_and_b32 s53, s63, 1\n\t" \
  "s_lshl_b32 s53, s53, 6\n\t" \
  "s_add_u32 s52, s52, s53\n\t" \
  "s_mov_b64 s[74:75], %[x]\n\t" \
  "s_add_u32 s74, s74, s52\n\t" \
  "s_addc_u32 s75, s75, 0\n\t" \
  "s_mov_b64 s[76:77], %[out]\n\t" \
  "s_add_u32 s76, s76, s52\n\t" \
  "s_addc_u32 s77, s77, 0\n\t" \
  "s_sub_u32 s59, 15, s66\n\t" \
  "s_cmp_lt_u32 %[w], 4\n\t" \
  "s_cselect_b32 s59, s59, s66\n\t" \
  "s_lshl_b32 s52, s59, 7\n\t" \
  "s_lshl_b32 s53, s63, 5\n\t" \
  "s_add_u32 s52, s52, s53\n\t" \
  "s_lshl_b32 s52, s59, 5\n\t" \
  "s_add_u32 s52, s52, s65\n\t" \
  "s_lshl_b32 s52, s52, 5\n\t" \
  "s_lshr_b32 s53, s53, 2\n\t" \
  "s_add_u32 s52, s52, s53\n\t" \
  "s_add_u32 s52, s52, 0x18000\n\t" \
  "s_waitcnt vmcnt(0)\n\t" \
  "v_mov_b32_e32 v6, s36\n\t" \
  "v_mov_b32_e32 v7, s37\n\t" \
  "v_mov_b32_e32 v3, s52\n\t" \
  "s_mov_b64 exec, 1\n\t" \
  "global_store_dwordx2 v3, v[6:7], s[60:61] sc1\n\t" \
  "s_mov_b64 exec, -1\n\t" \
  "global_load_dwordx2 v[224:225], v15, s[74:75] offset:0\n\t" \
  "global_load_dwordx2 v[226:227], v15, s[74:75] offset:32\n\t" \
  "global_load_dwordx2 v[228:229], v14, s[74:75] offset:0\n\t" \
  "global_load_dwordx2 v[230:231], v14, s[74:75] offset:32\n\t" \
  "v_add_u32_e32 v8, s50, v4\n\t" \
  "v_add_u32_e32 v9, s50, v5\n\t" \
  "v_xor_b32_e32 v10, 32, v9\n\t" \
  "v_xor_b32_e32 v11, 64, v9\n\t" \
  "v_xor_b32_e32 v12, 96, v9\n\t" \
  "s_waitcnt lgkmcnt(0)\n\t" \
  "s_barrier\n\t" \
  "ds_read_b64_tr_b16 v[112:113], v9 offset:0\n\t" \
  "ds_read_b64_tr_b16 v[114:115], v9 offset:4096\n\t" \
  "ds_read_b64_tr_b16 v[116:117], v10 offset:0\n\t" \
  "ds_read_b64_tr_b16 v[118:119], v10 offset:4096\n\t" \
  "ds_read_b64_tr_b16 v[120:121], v11 offset:0\n\t" \
  "ds_read_b64_tr_b16 v[122:123], v11 offset:4096\n\t" \
  "ds_read_b64_tr_b16 v[124:125], v12 offset:0\n\t" \
  "ds_read_b64_tr_b16 v[126:127], v12 offset:4096\n\t" \
  "ds_read_b128 v[80:83], v8 offset:0\n\t" \
  "ds_read_b128 v[84:87], v8 offset:1024\n\t" \
  "ds_read_b128 v[88:91], v8 offset:2048\n\t" \
  "ds_read_b128 v[92:95], v8 offset:3072\n\t" \
  "s_add_u32 s48, s48, 0x8000\n\t" \
  "s_cmp_eq_u32 s48, 0x28000\n\t" \
  "s_cselect_b32 s48, 0, s48\n\t" \
  "s_xor_b32 s50, s48, s49\n\t" \
  "s_cmp_eq_u32 s48, 0x20000\n\t" \
  "s_cselect_b32 s50, s48, s50\n\t" \
  "s_waitcnt lgkmcnt(0)\n\t" \
  "v_mfma_f32_16x16x32_bf16 v[160:163], v[112:115], v[80:83], 0\n\t" \
  "ds_read_b64_tr_b16 v[128:129], v9 offset:8192\n\t" \
  "ds_read_b64_tr_b16 v[130:131], v9 offset:12288\n\t" \
  "v_mfma_f32_16x16x32_bf16 v[164:167], v[116:119], v[80:83], 0\n\t" \
  "ds_read_b64_tr_b16 v[132:133], v10 offset:8192\n\t" \
  "v_mfma_f32_16x16x32_bf16 v[168:171], v[120:123], v[80:83], 0\n\t" \
  "ds_read_b64_tr_b16 v[134:135], v10 offset:12288\n\t" \
  "ds_read_b64_tr_b16 v[136:137], v11 offset:8192\n\t" \
  "v_mfma_f32_16x16x32_bf16 v[172:175], v[124:127], v[80:83], 0\n\t" \
  "ds_read_b64_tr_b16 v[138:139], v11 offset:12288\n\t" \
  "v_mfma_f32_16x16x32_bf16 v[188:191], v[124:127], v[84:87], 0\n\t" \
  "ds_read_b64_tr_b16 v[140:141], v12 offset:8192\n\t" \
  "ds_read_b64_tr_b16 v[142:143], v12 offset:12288\n\t" \
  "v_mfma_f32_16x16x32_bf16 v[184:187], v[120:123], v[84:87], 0\n\t" \
  "ds_read_b128 v[96:99], v8 offset:8192\n\t" \
  "v_mfma_f32_16x16x32_bf16 v[180:183], v[116:119], v[84:87], 0\n\t" \
  "ds_read_b128 v[100:103], v8 offset:9216\n\t" \
  "ds_read_b128 v[104:107], v8 offset:10240\n\t" \
  "v_mfma_f32_16x16x32_bf16 v[176:179], v[112:115], v[84:87], 0\n\t" \
  "ds_read_b128 v[108:111], v8 offset:11264\n\t" \
  "v_mfma_f32_16x16x32_bf16 v[192:195], v[112:115], v[88:91], 0\n\t" \
  "v_mfma_f32_16x16x32_bf16 v[196:199], v[116:119], v[88:91], 0\n\t" \
  "v_mfma_f32_16x16x32_bf16 v[200:203], v[120:123], v[88:91], 0\n\t" \
  "v_mfma_f32_16x16x32_bf16 v[204:207], v[124:127], v[88:91], 0\n\t" \
  "v_mfma_f32_16x16x32_bf16 v[220:223], v[124:127], v[92:95], 0\n\t" \
  "v_mfma_f32_16x16x32_bf16 v[216:219], v[120:123], v[92:95], 0\n\t" \
  "v_mfma_f32_16x16x32_bf16 v[212:215], v[116:119], v[92:95], 0\n\t" \
  "v_mfma_f32_16x16x32_bf16 v[208:211], v[112:115], v[92:95], 0\n\t" \
  "s_sub_u32 s51, s70, 1\n\t" \
  "s_cmp_eq_u32 s51, 0\n\t" \
  "s_cbranch_scc1 XCD1_%=\n\t" \
  "CD1_%=:\n\t" \
  "v_add_u32_e32 v8, s50, v4\n\t" \
  "v_add_u32_e32 v9, s50, v5\n\t" \
  "v_xor_b32_e32 v10, 32, v9\n\t" \
  "v_xor_b32_e32 v11, 64, v9\n\t" \
  "v_xor_b32_e32 v12, 96, v9\n\t" \
  "s_waitcnt lgkmcnt(0)\n\t" \
  "s_barrier\n\t" \
  "v_mfma_f32_16x16x32_bf16 v[160:163], v[128:131], v[96:99], v[160:163]\n\t" \
  "ds_read_b64_tr_b16 v[112:113], v9 offset:0\n\t" \
  "ds_read_b64_tr_b16 v[114:115], v9 offset:4096\n\t" \
  "v_mfma_f32_16x16x32_bf16 v[164:167], v[132:135], v[96:99], v[164:167]\n\t" \
  "ds_read_b64_tr_b16 v[116:117], v10 offset:0\n\t" \
  "v_mfma_f32_16x16x32_bf16 v[168:171], v[136:139], v[96:99], v[168:171]\n\t" \
  "ds_read_b64_tr_b16 v[118:119], v10 offset:4096\n\t" \
  "ds_read_b64_tr_b16 v[120:121], v11 offset:0\n\t" \
  "v_mfma_f32_16x16x32_bf16 v[172:175], v[140:143], v[96:99], v[172:175]\n\t" \
  "ds_read_b64_tr_b16 v[122:123], v11 offset:4096\n\t" \
  "v_mfma_f32_16x16x32_bf16 v[188:191], v[140:143], v[100:103], v[188:191]\n\t" \
  "ds_read_b64_tr_b16 v[124:125], v12 offset:0\n\t" \
  "ds_read_b64_tr_b16 v[126:127], v12 offset:4096\n\t" \
  "v_mfma_f32_16x16x32_bf16 v[184:187], v[136:139], v[100:103], v[184:187]\n\t" \
  "ds_read_b128 v[80:83], v8 offset:0\n\t" \
  "v_mfma_f32_16x16x32_bf16 v[180:183], v[132:135], v[100:103], v[180:183]\n\t" \
  "ds_read_b128 v[84:87], v8 offset:1024\n\t" \
  "ds_read_b128 v[88:91], v8 offset:2048\n\t" \
  "v_mfma_f32_16x16x32_bf16 v[176:179], v[128:131], v[100:103], v[176:179]\n\t" \
  "ds_read_b128 v[92:95], v8 offset:3072\n\t" \
  "v_mfma_f32_16x16x32_bf16 v[192:195], v[128:131], v[104:107], v[192:195]\n\t" \
  "s_add_u32 s48, s48, 0x8000\n\t" \
  "v_mfma_f32_16x16x32_bf16 v[196:199], v[132:135], v[104:107], v[196:199]\n\t" \
  "v_mfma_f32_16x16x32_bf16 v[200:203], v[136:139], v[104:107], v[200:203]\n\t" \
  "s_cmp_eq_u32 s48, 0x28000\n\t" \
  "s_cselect_b32 s48, 0, s48\n\t" \
  "v_mfma_f32_16x16x32_bf16 v[204:207], v[140:143], v[104:107], v[204:207]\n\t" \
  "v_mfma_f32_16x16x32_bf16 v[220:223], v[140:143], v[108:111], v[220:223]\n\t" \
  "s_xor_b32 s50, s48, s49\n\t" \
  "v_mfma_f32_16x16x32_bf16 v[216:219], v[136:139], v[108:111], v[216:219]\n\t" \
  "v_mfma_f32_16x16x32_bf16 v[212:215], v[132:135], v[108:111], v[212:215]\n\t" \
  "s_cmp_eq_u32 s48, 0x20000\n\t" \
  "s_cselect_b32 s50, s48, s50\n\t" \
  "v_mfma_f32_16x16x32_bf16 v[208:211], v[128:131], v[108:111], v[208:211]\n\t" \
  "s_waitcnt lgkmcnt(0)\n\t" \
  "v_mfma_f32_16x16x32_bf16 v[160:163], v[112:115], v[80:83], v[160:163]\n\t" \
  "ds_read_b64_tr_b16 v[128:129], v9 offset:8192\n\t" \
  "ds_read_b64_tr_b16 v[130:131], v9 offset:12288\n\t" \
  "v_mfma_f32_16x16x32_bf16 v[164:167], v[116:119], v[80:83], v[164:167]\n\t" \
  "ds_read_b64_tr_b16 v[132:133], v10 offset:8192\n\t" \
  "v_mfma_f32_16x16x32_bf16 v[168:171], v[120:123], v[80:83], v[168:171]\n\t" \
  "ds_read_b64_tr_b16 v[134:135], v10 offset:12288\n\t" \
  "ds_read_b64_tr_b16 v[136:137], v11 offset:8192\n\t" \
  "v_mfma_f32_16x16x32_bf16 v[172:175], v[124:127], v[80:83], v[172:175]\n\t" \
  "ds_read_b64_tr_b16 v[138:139], v11 offset:12288\n\t" \
  "v_mfma_f32_16x16x32_bf16 v[188:191], v[124:127], v[84:87], v[188:191]\n\t" \
  "ds_read_b64_tr_b16 v[140:141], v12 offset:8192\n\t" \
  "ds_read_b64_tr_b16 v[142:143], v12 offset:12288\n\t" \
  "v_mfma_f32_16x16x32_bf16 v[184:187], v[120:123], v[84:87], v[184:187]\n\t" \
  "ds_read_b128 v[96:99], v8 offset:8192\n\t" \
  "v_mfma_f32_16x16x32_bf16 v[180:183], v[116:119], v[84:87], v[180:183]\n\t" \
  "ds_read_b128 v[100:103], v8 offset:9216\n\t" \
  "ds_read_b128 v[104:107], v8 offset:10240\n\t" \
  "v_mfma_f32_16x16x32_bf16 v[176:179], v[112:115], v[84:87], v[176:179]\n\t" \
  "ds_read_b128 v[108:111], v8 offset:11264\n\t" \
  "v_mfma_f32_16x16x32_bf16 v[192:195], v[112:115], v[88:91], v[192:195]\n\t" \
  "v_mfma_f32_16x16x32_bf16 v[196:199], v[116:119], v[88:91], v[196:199]\n\t" \
  "v_mfma_f32_16x16x32_bf16 v[200:203], v[120:123], v[88:91], v[200:203]\n\t" \
  "v_mfma_f32_16x16x32_bf16 v[204:207], v[124:127], v[88:91], v[204:207]\n\t" \
  "v_mfma_f32_16x16x32_bf16 v[220:223], v[124:127], v[92:95], v[220:223]\n\t" \
  "v_mfma_f32_16x16x32_bf16 v[216:219], v[120:123], v[92:95], v[216:219]\n\t" \
  "v_mfma_f32_16x16x32_bf16 v[212:215], v[116:119], v[92:95], v[212:215]\n\t" \
  "v_mfma_f32_16x16x32_bf16 v[208:211], v[112:115], v[92:95], v[208:211]\n\t" \
  "s_sub_u32 s51, s51, 1\n\t" \
  "s_cmp_lg_u32 s51, 0\n\t" \
  "s_cbranch_scc1 CD1_%=\n\t" \
  "XCD1_%=:\n\t" \
  "v_add_u32_e32 v8, s50, v4\n\t" \
  "v_add_u32_e32 v9, s50, v5\n\t" \
  "v_xor_b32_e32 v10, 32, v9\n\t" \
  "v_xor_b32_e32 v11, 64, v9\n\t" \
  "v_xor_b32_e32 v12, 96, v9\n\t" \
  "s_waitcnt lgkmcnt(0)\n\t" \
  "s_barrier\n\t" \
  "v_mfma_f32_16x16x32_bf16 v[160:163], v[128:131], v[96:99], v[160:163]\n\t" \
  "ds_read_b64_tr_b16 v[112:113], v9 offset:0\n\t" \
  "ds_read_b64_tr_b16 v[114:115], v9 offset:4096\n\t" \
  "v_mfma_f32_16x16x32_bf16 v[164:167], v[132:135], v[96:99], v[164:167]\n\t" \
  "ds_read_b64_tr_b16 v[116:117], v10 offset:0\n\t" \
  "v_mfma_f32_16x16x32_bf16 v[168:171], v[136:139], v[96:99], v[168:171]\n\t" \
  "ds_read_b64_tr_b16 v[118:119], v10 offset:4096\n\t" \
  "ds_read_b64_tr_b16 v[120:121], v11 offset:0\n\t" \
  "v_mfma_f32_16x16x32_bf16 v[172:175], v[140:143], v[96:99], v[172:175]\n\t" \
  "ds_read_b64_tr_b16 v[122:123], v11 offset:4096\n\t" \
  "v_mfma_f32_16x16x32_bf16 v[188:191], v[140:143], v[100:103], v[188:191]\n\t" \
  "ds_read_b64_tr_b16 v[124:125], v12 offset:0\n\t" \
  "ds_read_b64_tr_b16 v[126:127], v12 offset:4096\n\t" \
  "v_mfma_f32_16x16x32_bf16 v[184:187], v[136:139], v[100:103], v[184:187]\n\t" \
  "ds_read_b128 v[80:83], v8 offset:0\n\t" \
  "v_mfma_f32_16x16x32_bf16 v[180:183], v[132:135], v[100:103], v[180:183]\n\t" \
  "ds_read_b128 v[84:87], v8 offset:1024\n\t" \
  "ds_read_b128 v[88:91], v8 offset:2048\n\t" \
  "v_mfma_f32_16x16x32_bf16 v[176:179], v[128:131], v[100:103], v[176:179]\n\t" \
  "ds_read_b128 v[92:95], v8 offset:3072\n\t" \
  "v_mfma_f32_16x16x32_bf16 v[192:195], v[128:131], v[104:107], v[192:195]\n\t" \
  "s_add_u32 s48, s48, 0x8000\n\t" \
  "v_mfma_f32_16x16x32_bf16 v[196:199], v[132:135], v[104:107], v[196:199]\n\t" \
  "v_mfma_f32_16x16x32_bf16 v[200:203], v[136:139], v[104:107], v[200:203]\n\t" \
  "s_cmp_eq_u32 s48, 0x28000\n\t" \
  "s_cselect_b32 s48, 0, s48\n\t" \
  "v_mfma_f32_16x16x32_bf16 v[204:207], v[140:143], v[104:107], v[204:207]\n\t" \
  "v_mfma_f32_16x16x32_bf16 v[220:223], v[140:143], v[108:111], v[220:223]\n\t" \
  "s_xor_b32 s50, s48, s49\n\t" \
  "v_mfma_f32_16x16x32_bf16 v[216:219], v[136:139], v[108:111], v[216:219]\n\t" \
  "v_mfma_f32_16x16x32_bf16 v[212:215], v[132:135], v[108:111], v[212:215]\n\t" \
  "s_cmp_eq_u32 s48, 0x20000\n\t" \
  "s_cselect_b32 s50, s48, s50\n\t" \
  "v_mfma_f32_16x16x32_bf16 v[208:211], v[128:131], v[108:111], v[208:211]\n\t" \
  "s_waitcnt lgkmcnt(0)\n\t" \
  "v_mfma_f32_16x16x32_bf16 v[16:19], v[112:115], v[80:83], 0\n\t" \
  "ds_read_b64_tr_b16 v[128:129], v9 offset:8192\n\t" \
  "ds_read_b64_tr_b16 v[130:131], v9 offset:12288\n\t" \
  "v_mfma_f32_16x16x32_bf16 v[20:23], v[116:119], v[80:83], 0\n\t" \
  "ds_read_b64_tr_b16 v[132:133], v10 offset:8192\n\t" \
  "v_mfma_f32_16x16x32_bf16 v[24:27], v[120:123], v[80:83], 0\n\t" \
  "ds_read_b64_tr_b16 v[134:135], v10 offset:12288\n\t" \
  "ds_read_b64_tr_b16 v[136:137], v11 offset:8192\n\t" \
  "v_mfma_f32_16x16x32_bf16 v[28:31], v[124:127], v[80:83], 0\n\t" \
  "ds_read_b64_tr_b16 v[138:139], v11 offset:12288\n\t" \
  "v_mfma_f32_16x16x32_bf16 v[44:47], v[124:127], v[84:87], 0\n\t" \
  "ds_read_b64_tr_b16 v[140:141], v12 offset:8192\n\t" \
  "ds_read_b64_tr_b16 v[142:143], v12 offset:12288\n\t" \
  "v_mfma_f32_16x16x32_bf16 v[40:43], v[120:123], v[84:87], 0\n\t" \
  "ds_read_b128 v[96:99], v8 offset:8192\n\t" \
  "v_mfma_f32_16x16x32_bf16 v[36:39], v[116:119], v[84:87], 0\n\t" \
  "ds_read_b128 v[100:103], v8 offset:9216\n\t" \
  "ds_read_b128 v[104:107], v8 offset:10240\n\t" \
  "v_mfma_f32_16x16x32_bf16 v[32:35], v[112:115], v[84:87], 0\n\t" \
  "ds_read_b128 v[108:111], v8 offset:11264\n\t" \
  "v_mfma_f32_16x16x32_bf16 v[48:51], v[112:115], v[88:91], 0\n\t" \
  "v_cvt_pk_bf16_f32 v144, v160, v161\n\t" \
  "v_mfma_f32_16x16x32_bf16 v[52:55], v[116:119], v[88:91], 0\n\t" \
  "v_cvt_pk_bf16_f32 v145, v162, v163\n\t" \
  "v_mfma_f32_16x16x32_bf16 v[56:59], v[120:123], v[88:91], 0\n\t" \
  "v_cvt_pk_bf16_f32 v146, v164, v165\n\t" \
  "v_mfma_f32_16x16x32_bf16 v[60:63], v[124:127], v[88:91], 0\n\t" \
  "v_cvt_pk_bf16_f32 v147, v166, v167\n\t" \
  "s_cbranch_vccz G7_%=\n\t" \
  "global_store_dwordx4 v1, v[144:147], s[72:73] offset:0 sc1\n\t" \
  "G7_%=:\n\t" \
  "v_mfma_f32_16x16x32_bf16 v[76:79], v[124:127], v[92:95], 0\n\t" \
  "v_cvt_pk_bf16_f32 v148, v168, v169\n\t" \
  "v_mfma_f32_16x16x32_bf16 v[72:75], v[120:123], v[92:95], 0\n\t" \
  "v_cvt_pk_bf16_f32 v149, v170, v171\n\t" \
  "v_mfma_f32_16x16x32_bf16 v[68:71], v[116:119], v[92:95], 0\n\t" \
  "v_cvt_pk_bf16_f32 v150, v172, v173\n\t" \
  "v_mfma_f32_16x16x32_bf16 v[64:67], v[112:115], v[92:95], 0\n\t" \
  "v_cvt_pk_bf16_f32 v151, v174, v175\n\t" \
  "s_cbranch_vccz G8_%=\n\t" \
  "global_store_dwordx4 v1, v[148:151], s[72:73] offset:1024 sc1\n\t" \
  "G8_%=:\n\t" \
  "v_add_u32_e32 v8, s50, v4\n\t" \
  "v_add_u32_e32 v9, s50, v5\n\t" \
  "v_xor_b32_e32 v10, 32, v9\n\t" \
  "v_xor_b32_e32 v11, 64, v9\n\t" \
  "v_xor_b32_e32 v12, 96, v9\n\t" \
  "s_waitcnt lgkmcnt(0)\n\t" \
  "s_barrier\n\t" \
  "v_mfma_f32_16x16x32_bf16 v[16:19], v[128:131], v[96:99], v[16:19]\n\t" \
  "ds_read_b64_tr_b16 v[112:113], v9 offset:0\n\t" \
  "ds_read_b64_tr_b16 v[114:115], v9 offset:4096\n\t" \
  "v_mfma_f32_16x16x32_bf16 v[20:23], v[132:135], v[96:99], v[20:23]\n\t" \
  "ds_read_b64_tr_b16 v[116:117], v10 offset:0\n\t" \
  "v_mfma_f32_16x16x32_bf16 v[24:27], v[136:139], v[96:99], v[24:27]\n\t" \
  "ds_read_b64_tr_b16 v[118:119], v10 offset:4096\n\t" \
  "ds_read_b64_tr_b16 v[120:121], v11 offset:0\n\t" \
  "v_mfma_f32_16x16x32_bf16 v[28:31], v[140:143], v[96:99], v[28:31]\n\t" \
  "ds_read_b64_tr_b16 v[122:123], v11 offset:4096\n\t" \
  "v_mfma_f32_16x16x32_bf16 v[44:47], v[140:143], v[100:103], v[44:47]\n\t" \
  "ds_read_b64_tr_b16 v[124:125], v12 offset:0\n\t" \
  "ds_read_b64_tr_b16 v[126:127], v12 offset:4096\n\t" \
  "v_mfma_f32_16x16x32_bf16 v[40:43], v[136:139], v[100:103], v[40:43]\n\t" \
  "ds_read_b128 v[80:83], v8 offset:0\n\t" \
  "v_mfma_f32_16x16x32_bf16 v[36:39], v[132:135], v[100:103], v[36:39]\n\t" \
  "ds_read_b128 v[84:87], v8 offset:1024\n\t" \
  "ds_read_b128 v[88:91], v8 offset:2048\n\t" \
  "v_mfma_f32_16x16x32_bf16 v[32:35], v[128:131], v[100:103], v[32:35]\n\t" \
  "ds_read_b128 v[92:95], v8 offset:3072\n\t" \
  "v_mfma_f32_16x16x32_bf16 v[48:51], v[128:131], v[104:107], v[48:51]\n\t" \
  "s_add_u32 s48, s48, 0x8000\n\t" \
  "s_cmp_eq_u32 s48, 0x28000\n\t" \
  "s_cselect_b32 s48, 0, s48\n\t" \
  "v_mfma_f32_16x16x32_bf16 v[52:55], v[132:135], v[104:107], v[52:55]\n\t" \
  "s_xor_b32 s50, s48, s49\n\t" \
  "v_mfma_f32_16x16x32_bf16 v[56:59], v[136:139], v[104:107], v[56:59]\n\t" \
  "s_cmp_eq_u32 s48, 0x20000\n\t" \
  "s_cselect_b32 s50, s48, s50\n\t" \
  "v_cvt_pk_bf16_f32 v152, v176, v177\n\t" \
  "v_mfma_f32_16x16x32_bf16 v[60:63], v[140:143], v[104:107], v[60:63]\n\t" \
  "v_cvt_pk_bf16_f32 v153, v178, v179\n\t" \
  "v_mfma_f32_16x16x32_bf16 v[76:79], v[140:143], v[108:111], v[76:79]\n\t" \
  "v_cvt_pk_bf16_f32 v154, v180, v181\n\t" \
  "v_cvt_pk_bf16_f32 v155, v182, v183\n\t" \
  "s_cbranch_vccz G9_%=\n\t" \
  "global_store_dwordx4 v1, v[152:155], s[72:73] offset:2048 sc1\n\t" \
  "G9_%=:\n\t" \
  "v_mfma_f32_16x16x32_bf16 v[72:75], v[136:139], v[108:111], v[72:75]\n\t" \
  "v_cvt_pk_bf16_f32 v156, v184, v185\n\t" \
  "v_mfma_f32_16x16x32_bf16 v[68:71], v[132:135], v[108:111], v[68:71]\n\t" \
  "v_cvt_pk_bf16_f32 v157, v186, v187\n\t" \
  "v_cvt_pk_bf16_f32 v158, v188, v189\n\t" \
  "v_mfma_f32_16x16x32_bf16 v[64:67], v[128:131], v[108:111], v[64:67]\n\t" \
  "v_cvt_pk_bf16_f32 v159, v190, v191\n\t" \
  "s_cbranch_vccz G10_%=\n\t" \
  "global_store_dwordx4 v1, v[156:159], s[72:73] offset:3072 sc1\n\t" \
  "G10_%=:\n\t" \
  "s_waitcnt lgkmcnt(0)\n\t" \
  "v_mfma_f32_16x16x32_bf16 v[16:19], v[112:115], v[80:83], v[16:19]\n\t" \
  "ds_read_b64_tr_b16 v[128:129], v9 offset:8192\n\t" \
  "ds_read_b64_tr_b16 v[130:131], v9 offset:12288\n\t" \
  "v_mfma_f32_16x16x32_bf16 v[20:23], v[116:119], v[80:83], v[20:23]\n\t" \
  "ds_read_b64_tr_b16 v[132:133], v10 offset:8192\n\t" \
  "v_mfma_f32_16x16x32_bf16 v[24:27], v[120:123], v[80:83], v[24:27]\n\t" \
  "ds_read_b64_tr_b16 v[134:135], v10 offset:12288\n\t" \
  "ds_read_b64_tr_b16 v[136:137], v11 offset:8192\n\t" \
  "v_mfma_f32_16x16x32_bf16 v[28:31], v[124:127], v[80:83], v[28:31]\n\t" \
  "ds_read_b64_tr_b16 v[138:139], v11 offset:12288\n\t" \
  "v_mfma_f32_16x16x32_bf16 v[44:47], v[124:127], v[84:87], v[44:47]\n\t" \
  "ds_read_b64_tr_b16 v[140:141], v12 offset:8192\n\t" \
  "ds_read_b64_tr_b16 v[142:143], v12 offset:12288\n\t" \
  "v_mfma_f32_16x16x32_bf16 v[40:43], v[120:123], v[84:87], v[40:43]\n\t" \
  "ds_read_b128 v[96:99], v8 offset:8192\n\t" \
  "v_mfma_f32_16x16x32_bf16 v[36:39], v[116:119], v[84:87], v[36:39]\n\t" \
  "ds_read_b128 v[100:103], v8 offset:9216\n\t" \
  "ds_read_b128 v[104:107], v8 offset:10240\n\t" \
  "v_mfma_f32_16x16x32_bf16 v[32:35], v[112:115], v[84:87], v[32:35]\n\t" \
  "ds_read_b128 v[108:111], v8 offset:11264\n\t" \
  "v_mfma_f32_16x16x32_bf16 v[48:51], v[112:115], v[88:91], v[48:51]\n\t" \
  "v_cvt_pk_bf16_f32 v144, v192, v193\n\t" \
  "v_mfma_f32_16x16x32_bf16 v[52:55], v[116:119], v[88:91], v[52:55]\n\t" \
  "v_cvt_pk_bf16_f32 v145, v194, v195\n\t" \
  "v_mfma_f32_16x16x32_bf16 v[56:59], v[120:123], v[88:91], v[56:59]\n\t" \
  "v_cvt_pk_bf16_f32 v146, v196, v197\n\t" \
  "v_mfma_f32_16x16x32_bf16 v[60:63], v[124:127], v[88:91], v[60:63]\n\t" \
  "v_cvt_pk_bf16_f32 v147, v198, v199\n\t" \
  "s_cbranch_vccz G11_%=\n\t" \
  "global_store_dwordx4 v2, v[144:147], s[72:73] offset:0 sc1\n\t" \
  "G11_%=:\n\t" \
  "v_mfma_f32_16x16x32_bf16 v[76:79], v[124:127], v[92:95], v[76:79]\n\t" \
  "v_cvt_pk_bf16_f32 v148, v200, v201\n\t" \
  "v_mfma_f32_16x16x32_bf16 v[72:75], v[120:123], v[92:95], v[72:75]\n\t" \
  "v_cvt_pk_bf16_f32 v149, v202, v203\n\t" \
  "v_mfma_f32_16x16x32_bf16 v[68:71], v[116:119], v[92:95], v[68:71]\n\t" \
  "v_cvt_pk_bf16_f32 v150, v204, v205\n\t" \
  "v_mfma_f32_16x16x32_bf16 v[64:67], v[112:115], v[92:95], v[64:67]\n\t" \
  "v_cvt_pk_bf16_f32 v151, v206, v207\n\t" \
  "s_cbranch_vccz G12_%=\n\t" \
  "global_store_dwordx4 v2, v[148:151], s[72:73] offset:1024 sc1\n\t" \
  "G12_%=:\n\t" \
  "v_add_u32_e32 v8, s50, v4\n\t" \
  "v_add_u32_e32 v9, s50, v5\n\t" \
  "v_xor_b32_e32 v10, 32, v9\n\t" \
  "v_xor_b32_e32 v11, 64, v9\n\t" \
  "v_xor_b32_e32 v12, 96, v9\n\t" \
  "s_waitcnt lgkmcnt(0)\n\t" \
  "s_barrier\n\t" \
  "v_mfma_f32_16x16x32_bf16 v[16:19], v[128:131], v[96:99], v[16:19]\n\t" \
  "ds_read_b64_tr_b16 v[112:113], v9 offset:0\n\t" \
  "ds_read_b64_tr_b16 v[114:115], v9 offset:4096\n\t" \
  "v_mfma_f32_16x16x32_bf16 v[20:23], v[132:135], v[96:99], v[20:23]\n\t" \
  "ds_read_b64_tr_b16 v[116:117], v10 offset:0\n\t" \
  "v_mfma_f32_16x16x32_bf16 v[24:27], v[136:139], v[96:99], v[24:27]\n\t" \
  "ds_read_b64_tr_b16 v[118:119], v10 offset:4096\n\t" \
  "ds_read_b64_tr_b16 v[120:121], v11 offset:0\n\t" \
  "v_mfma_f32_16x16x32_bf16 v[28:31], v[140:143], v[96:99], v[28:31]\n\t" \
  "ds_read_b64_tr_b16 v[122:123], v11 offset:4096\n\t" \
  "v_mfma_f32_16x16x32_bf16 v[44:47], v[140:143], v[100:103], v[44:47]\n\t" \
  "ds_read_b64_tr_b16 v[124:125], v12 offset:0\n\t" \
  "ds_read_b64_tr_b16 v[126:127], v12 offset:4096\n\t" \
  "v_mfma_f32_16x16x32_bf16 v[40:43], v[136:139], v[100:103], v[40:43]\n\t" \
  "ds_read_b128 v[80:83], v8 offset:0\n\t" \
  "v_mfma_f32_16x16x32_bf16 v[36:39], v[132:135], v[100:103], v[36:39]\n\t" \
  "ds_read_b128 v[84:87], v8 offset:1024\n\t" \
  "ds_read_b128 v[88:91], v8 offset:2048\n\t" \
  "v_mfma_f32_16x16x32_bf16 v[32:35], v[128:131], v[100:103], v[32:35]\n\t" \
  "ds_read_b128 v[92:95], v8 offset:3072\n\t" \
  "v_mfma_f32_16x16x32_bf16 v[48:51], v[128:131], v[104:107], v[48:51]\n\t" \
  "s_add_u32 s48, s48, 0x8000\n\t" \
  "s_cmp_eq_u32 s48, 0x28000\n\t" \
  "s_cselect_b32 s48, 0, s48\n\t" \
  "v_mfma_f32_16x16x32_bf16 v[52:55], v[132:135], v[104:107], v[52:55]\n\t" \
  "s_xor_b32 s50, s48, s49\n\t" \
  "v_mfma_f32_16x16x32_bf16 v[56:59], v[136:139], v[104:107], v[56:59]\n\t" \
  "s_cmp_eq_u32 s48, 0x20000\n\t" \
  "s_cselect_b32 s50, s48, s50\n\t" \
  "v_cvt_pk_bf16_f32 v152, v208, v209\n\t" \
  "v_mfma_f32_16x16x32_bf16 v[60:63], v[140:143], v[104:107], v[60:63]\n\t" \
  "v_cvt_pk_bf16_f32 v153, v210, v211\n\t" \
  "v_mfma_f32_16x16x32_bf16 v[76:79], v[140:143], v[108:111], v[76:79]\n\t" \
  "v_cvt_pk_bf16_f32 v154, v212, v213\n\t" \
  "v_cvt_pk_bf16_f32 v155, v214, v215\n\t" \
  "s_cbranch_vccz G13_%=\n\t" \
  "global_store_dwordx4 v2, v[152:155], s[72:73] offset:2048 sc1\n\t" \
  "G13_%=:\n\t" \
  "v_mfma_f32_16x16x32_bf16 v[72:75], v[136:139], v[108:111], v[72:75]\n\t" \
  "v_cvt_pk_bf16_f32 v156, v216, v217\n\t" \
  "v_mfma_f32_16x16x32_bf16 v[68:71], v[132:135], v[108:111], v[68:71]\n\t" \
  "v_cvt_pk_bf16_f32 v157, v218, v219\n\t" \
  "v_cvt_pk_bf16_f32 v158, v220, v221\n\t" \
  "v_mfma_f32_16x16x32_bf16 v[64:67], v[128:131], v[108:111], v[64:67]\n\t" \
  "v_cvt_pk_bf16_f32 v159, v222, v223\n\t" \
  "s_cbranch_vccz G14_%=\n\t" \
  "global_store_dwordx4 v2, v[156:159], s[72:73] offset:3072 sc1\n\t" \
  "G14_%=:\n\t" \
  "s_waitcnt lgkmcnt(0)\n\t" \
  "v_mfma_f32_16x16x32_bf16 v[16:19], v[112:115], v[80:83], v[16:19]\n\t" \
  "ds_read_b64_tr_b16 v[128:129], v9 offset:8192\n\t" \
  "ds_read_b64_tr_b16 v[130:131], v9 offset:12288\n\t" \
  "v_mfma_f32_16x16x32_bf16 v[20:23], v[116:119], v[80:83], v[20:23]\n\t" \
  "ds_read_b64_tr_b16 v[132:133], v10 offset:8192\n\t" \
  "v_mfma_f32_16x16x32_bf16 v[24:27], v[120:123], v[80:83], v[24:27]\n\t" \
  "ds_read_b64_tr_b16 v[134:135], v10 offset:12288\n\t" \
  "ds_read_b64_tr_b16 v[136:137], v11 offset:8192\n\t" \
  "v_mfma_f32_16x16x32_bf16 v[28:31], v[124:127], v[80:83], v[28:31]\n\t" \
  "ds_read_b64_tr_b16 v[138:139], v11 offset:12288\n\t" \
  "v_mfma_f32_16x16x32_bf16 v[44:47], v[124:127], v[84:87], v[44:47]\n\t" \
  "ds_read_b64_tr_b16 v[140:141], v12 offset:8192\n\t" \
  "ds_read_b64_tr_b16 v[142:143], v12 offset:12288\n\t" \
  "v_mfma_f32_16x16x32_bf16 v[40:43], v[120:123], v[84:87], v[40:43]\n\t" \
  "ds_read_b128 v[96:99], v8 offset:8192\n\t" \
  "v_mfma_f32_16x16x32_bf16 v[36:39], v[116:119], v[84:87], v[36:39]\n\t" \
  "ds_read_b128 v[100:103], v8 offset:9216\n\t" \
  "ds_read_b128 v[104:107], v8 offset:10240\n\t" \
  "v_mfma_f32_16x16x32_bf16 v[32:35], v[112:115], v[84:87], v[32:35]\n\t" \
  "ds_read_b128 v[108:111], v8 offset:11264\n\t" \
  "v_mfma_f32_16x16x32_bf16 v[48:51], v[112:115], v[88:91], v[48:51]\n\t" \
  "s_lshl_b32 s52, s82, 8\n\t" \
  "s_add_u32 s52, s52, 0x1c000\n\t" \
  "v_mfma_f32_16x16x32_bf16 v[52:55], v[116:119], v[88:91], v[52:55]\n\t" \
  "s_lshl_b32 s53, s63, 5\n\t" \
  "v_mfma_f32_16x16x32_bf16 v[56:59], v[120:123], v[88:91], v[56:59]\n\t" \
  "s_lshl_b32 s54, s58, 3\n\t" \
  "s_add_u32 s53, s53, s54\n\t" \
  "v_mfma_f32_16x16x32_bf16 v[60:63], v[124:127], v[88:91], v[60:63]\n\t" \
  "s_add_u32 s53, s53, s52\n\t" \
  "v_mfma_f32_16x16x32_bf16 v[76:79], v[124:127], v[92:95], v[76:79]\n\t" \
  "s_lshl_b32 s54, s58, 5\n\t" \
  "s_add_u32 s52, s52, s54\n\t" \
  "v_mfma_f32_16x16x32_bf16 v[72:75], v[120:123], v[92:95], v[72:75]\n\t" \
  "v_and_b32_e32 v13, 3, v0\n\t" \
  "v_mfma_f32_16x16x32_bf16 v[68:71], v[116:119], v[92:95], v[68:71]\n\t" \
  "v_lshlrev_b32_e32 v13, 3, v13\n\t" \
  "v_add_u32_e32 v13, s52, v13\n\t" \
  "v_mfma_f32_16x16x32_bf16 v[64:67], v[112:115], v[92:95], v[64:67]\n\t" \
  "s_add_u32 s72, s72, 0x20000\n\t" \
  "s_addc_u32 s73, s73, 0\n\t" \
  "s_mov_b32 s51, 3\n\t" \
  "CS1_%=:\n\t" \
  "v_add_u32_e32 v8, s50, v4\n\t" \
  "v_add_u32_e32 v9, s50, v5\n\t" \
  "v_xor_b32_e32 v10, 32, v9\n\t" \
  "v_xor_b32_e32 v11, 64, v9\n\t" \
  "v_xor_b32_e32 v12, 96, v9\n\t" \
  "s_waitcnt lgkmcnt(0)\n\t" \
  "s_barrier\n\t" \
  "v_mfma_f32_16x16x32_bf16 v[16:19], v[128:131], v[96:99], v[16:19]\n\t" \
  "ds_read_b64_tr_b16 v[112:113], v9 offset:0\n\t" \
  "ds_read_b64_tr_b16 v[114:115], v9 offset:4096\n\t" \
  "v_mfma_f32_16x16x32_bf16 v[20:23], v[132:135], v[96:99], v[20:23]\n\t" \
  "ds_read_b64_tr_b16 v[116:117], v10 offset:0\n\t" \
  "v_mfma_f32_16x16x32_bf16 v[24:27], v[136:139], v[96:99], v[24:27]\n\t" \
  "ds_read_b64_tr_b16 v[118:119], v10 offset:4096\n\t" \
  "ds_read_b64_tr_b16 v[120:121], v11 offset:0\n\t" \
  "v_mfma_f32_16x16x32_bf16 v[28:31], v[140:143], v[96:99], v[28:31]\n\t" \
  "ds_read_b64_tr_b16 v[122:123], v11 offset:4096\n\t" \
  "v_mfma_f32_16x16x32_bf16 v[44:47], v[140:143], v[100:103], v[44:47]\n\t" \
  "ds_read_b64_tr_b16 v[124:125], v12 offset:0\n\t" \
  "ds_read_b64_tr_b16 v[126:127], v12 offset:4096\n\t" \
  "v_mfma_f32_16x16x32_bf16 v[40:43], v[136:139], v[100:103], v[40:43]\n\t" \
  "ds_read_b128 v[80:83], v8 offset:0\n\t" \
  "v_mfma_f32_16x16x32_bf16 v[36:39], v[132:135], v[100:103], v[36:39]\n\t" \
  "ds_read_b128 v[84:87], v8 offset:1024\n\t" \
  "ds_read_b128 v[88:91], v8 offset:2048\n\t" \
  "v_mfma_f32_16x16x32_bf16 v[32:35], v[128:131], v[100:103], v[32:35]\n\t" \
  "ds_read_b128 v[92:95], v8 offset:3072\n\t" \
  "v_mfma_f32_16x16x32_bf16 v[48:51], v[128:131], v[104:107], v[48:51]\n\t" \
  "s_add_u32 s48, s48, 0x8000\n\t" \
  "v_mfma_f32_16x16x32_bf16 v[52:55], v[132:135], v[104:107], v[52:55]\n\t" \
  "v_mfma_f32_16x16x32_bf16 v[56:59], v[136:139], v[104:107], v[56:59]\n\t" \
  "s_cmp_eq_u32 s48, 0x28000\n\t" \
  "s_cselect_b32 s48, 0, s48\n\t" \
  "v_mfma_f32_16x16x32_bf16 v[60:63], v[140:143], v[104:107], v[60:63]\n\t" \
  "v_mfma_f32_16x16x32_bf16 v[76:79], v[140:143], v[108:111], v[76:79]\n\t" \
  "s_xor_b32 s50, s48, s49\n\t" \
  "v_mfma_f32_16x16x32_bf16 v[72:75], v[136:139], v[108:111], v[72:75]\n\t" \
  "v_mfma_f32_16x16x32_bf16 v[68:71], v[132:135], v[108:111], v[68:71]\n\t" \
  "s_cmp_eq_u32 s48, 0x20000\n\t" \
  "s_cselect_b32 s50, s48, s50\n\t" \
  "v_mfma_f32_16x16x32_bf16 v[64:67], v[128:131], v[108:111], v[64:67]\n\t" \
  "s_waitcnt lgkmcnt(0)\n\t" \
  "v_mfma_f32_16x16x32_bf16 v[16:19], v[112:115], v[80:83], v[16:19]\n\t" \
  "ds_read_b64_tr_b16 v[128:129], v9 offset:8192\n\t" \
  "ds_read_b64_tr_b16 v[130:131], v9 offset:12288\n\t" \
  "v_mfma_f32_16x16x32_bf16 v[20:23], v[116:119], v[80:83], v[20:23]\n\t" \
  "ds_read_b64_tr_b16 v[132:133], v10 offset:8192\n\t" \
  "v_mfma_f32_16x16x32_bf16 v[24:27], v[120:123], v[80:83], v[24:27]\n\t" \
  "ds_read_b64_tr_b16 v[134:135], v10 offset:12288\n\t" \
  "ds_read_b64_tr_b16 v[136:137], v11 offset:8192\n\t" \
  "v_mfma_f32_16x16x32_bf16 v[28:31], v[124:127], v[80:83], v[28:31]\n\t" \
  "ds_read_b64_tr_b16 v[138:139], v11 offset:12288\n\t" \
  "v_mfma_f32_16x16x32_bf16 v[44:47], v[124:127], v[84:87], v[44:47]\n\t" \
  "ds_read_b64_tr_b16 v[140:141], v12 offset:8192\n\t" \
  "ds_read_b64_tr_b16 v[142:143], v12 offset:12288\n\t" \
  "v_mfma_f32_16x16x32_bf16 v[40:43], v[120:123], v[84:87], v[40:43]\n\t" \
  "ds_read_b128 v[96:99], v8 offset:8192\n\t" \
  "v_mfma_f32_16x16x32_bf16 v[36:39], v[116:119], v[84:87], v[36:39]\n\t" \
  "ds_read_b128 v[100:103], v8 offset:9216\n\t" \
  "ds_read_b128 v[104:107], v8 offset:10240\n\t" \
  "v_mfma_f32_16x16x32_bf16 v[32:35], v[112:115], v[84:87], v[32:35]\n\t" \
  "ds_read_b128 v[108:111], v8 offset:11264\n\t" \
  "v_mfma_f32_16x16x32_bf16 v[48:51], v[112:115], v[88:91], v[48:51]\n\t" \
  "v_mfma_f32_16x16x32_bf16 v[52:55], v[116:119], v[88:91], v[52:55]\n\t" \
  "v_mfma_f32_16x16x32_bf16 v[56:59], v[120:123], v[88:91], v[56:59]\n\t" \
  "v_mfma_f32_16x16x32_bf16 v[60:63], v[124:127], v[88:91], v[60:63]\n\t" \
  "v_mfma_f32_16x16x32_bf16 v[76:79], v[124:127], v[92:95], v[76:79]\n\t" \
  "v_mfma_f32_16x16x32_bf16 v[72:75], v[120:123], v[92:95], v[72:75]\n\t" \
  "v_mfma_f32_16x16x32_bf16 v[68:71], v[116:119], v[92:95], v[68:71]\n\t" \
  "v_mfma_f32_16x16x32_bf16 v[64:67], v[112:115], v[92:95], v[64:67]\n\t" \
  "s_sub_u32 s51, s51, 1\n\t" \
  "s_cmp_lg_u32 s51, 0\n\t" \
  "s_cbranch_scc1 CS1_%=\n\t" \
  "v_add_u32_e32 v8, s50, v4\n\t" \
  "v_add_u32_e32 v9, s50, v5\n\t" \
  "v_xor_b32_e32 v10, 32, v9\n\t" \
  "v_xor_b32_e32 v11, 64, v9\n\t" \
  "v_xor_b32_e32 v12, 96, v9\n\t" \
  "s_waitcnt lgkmcnt(0)\n\t" \
  "s_barrier\n\t" \
  "v_mfma_f32_16x16x32_bf16 v[16:19], v[128:131], v[96:99], v[16:19]\n\t" \
  "ds_read_b64_tr_b16 v[112:113], v9 offset:0\n\t" \
  "ds_read_b64_tr_b16 v[114:115], v9 offset:4096\n\t" \
  "v_mfma_f32_16x16x32_bf16 v[20:23], v[132:135], v[96:99], v[20:23]\n\t" \
  "ds_read_b64_tr_b16 v[116:117], v10 offset:0\n\t" \
  "v_mfma_f32_16x16x32_bf16 v[24:27], v[136:139], v[96:99], v[24:27]\n\t" \
  "ds_read_b64_tr_b16 v[118:119], v10 offset:4096\n\t" \
  "ds_read_b64_tr_b16 v[120:121], v11 offset:0\n\t" \
  "v_mfma_f32_16x16x32_bf16 v[28:31], v[140:143], v[96:99], v[28:31]\n\t" \
  "ds_read_b64_tr_b16 v[122:123], v11 offset:4096\n\t" \
  "v_mfma_f32_16x16x32_bf16 v[44:47], v[140:143], v[100:103], v[44:47]\n\t" \
  "ds_read_b64_tr_b16 v[124:125], v12 offset:0\n\t" \
  "ds_read_b64_tr_b16 v[126:127], v12 offset:4096\n\t" \
  "v_mfma_f32_16x16x32_bf16 v[40:43], v[136:139], v[100:103], v[40:43]\n\t" \
  "ds_read_b128 v[80:83], v8 offset:0\n\t" \
  "v_mfma_f32_16x16x32_bf16 v[36:39], v[132:135], v[100:103], v[36:39]\n\t" \
  "ds_read_b128 v[84:87], v8 offset:1024\n\t" \
  "ds_read_b128 v[88:91], v8 offset:2048\n\t" \
  "v_mfma_f32_16x16x32_bf16 v[32:35], v[128:131], v[100:103], v[32:35]\n\t" \
  "ds_read_b128 v[92:95], v8 offset:3072\n\t" \
  "v_mfma_f32_16x16x32_bf16 v[48:51], v[128:131], v[104:107], v[48:51]\n\t" \
  "s_add_u32 s48, s48, 0x8000\n\t" \
  "v_mfma_f32_16x16x32_bf16 v[52:55], v[132:135], v[104:107], v[52:55]\n\t" \
  "v_mfma_f32_16x16x32_bf16 v[56:59], v[136:139], v[104:107], v[56:59]\n\t" \
  "s_cmp_eq_u32 s48, 0x28000\n\t" \
  "s_cselect_b32 s48, 0, s48\n\t" \
  "v_mfma_f32_16x16x32_bf16 v[60:63], v[140:143], v[104:107], v[60:63]\n\t" \
  "v_mfma_f32_16x16x32_bf16 v[76:79], v[140:143], v[108:111], v[76:79]\n\t" \
  "s_xor_b32 s50, s48, s49\n\t" \
  "v_mfma_f32_16x16x32_bf16 v[72:75], v[136:139], v[108:111], v[72:75]\n\t" \
  "v_mfma_f32_16x16x32_bf16 v[68:71], v[132:135], v[108:111], v[68:71]\n\t" \
  "s_cmp_eq_u32 s48, 0x20000\n\t" \
  "s_cselect_b32 s50, s48, s50\n\t" \
  "v_mfma_f32_16x16x32_bf16 v[64:67], v[128:131], v[108:111], v[64:67]\n\t" \
  "s_waitcnt lgkmcnt(0)\n\t" \
  "v_mfma_f32_16x16x32_bf16 v[16:19], v[112:115], v[80:83], v[16:19]\n\t" \
  "ds_read_b64_tr_b16 v[128:129], v9 offset:8192\n\t" \
  "ds_read_b64_tr_b16 v[130:131], v9 offset:12288\n\t" \
  "v_mfma_f32_16x16x32_bf16 v[20:23], v[116:119], v[80:83], v[20:23]\n\t" \
  "ds_read_b64_tr_b16 v[132:133], v10 offset:8192\n\t" \
  "v_mfma_f32_16x16x32_bf16 v[24:27], v[120:123], v[80:83], v[24:27]\n\t" \
  "ds_read_b64_tr_b16 v[134:135], v10 offset:12288\n\t" \
  "ds_read_b64_tr_b16 v[136:137], v11 offset:8192\n\t" \
  "v_mfma_f32_16x16x32_bf16 v[28:31], v[124:127], v[80:83], v[28:31]\n\t" \
  "ds_read_b64_tr_b16 v[138:139], v11 offset:12288\n\t" \
  "v_mfma_f32_16x16x32_bf16 v[44:47], v[124:127], v[84:87], v[44:47]\n\t" \
  "ds_read_b64_tr_b16 v[140:141], v12 offset:8192\n\t" \
  "ds_read_b64_tr_b16 v[142:143], v12 offset:12288\n\t" \
  "v_mfma_f32_16x16x32_bf16 v[40:43], v[120:123], v[84:87], v[40:43]\n\t" \
  "ds_read_b128 v[96:99], v8 offset:8192\n\t" \
  "v_mfma_f32_16x16x32_bf16 v[36:39], v[116:119], v[84:87], v[36:39]\n\t" \
  "ds_read_b128 v[100:103], v8 offset:9216\n\t" \
  "ds_read_b128 v[104:107], v8 offset:10240\n\t" \
  "v_mfma_f32_16x16x32_bf16 v[32:35], v[112:115], v[84:87], v[32:35]\n\t" \
  "ds_read_b128 v[108:111], v8 offset:11264\n\t" \
  "v_mfma_f32_16x16x32_bf16 v[48:51], v[112:115], v[88:91], v[48:51]\n\t" \
  "s_waitcnt vmcnt(0)\n\t" \
  "v_mfma_f32_16x16x32_bf16 v[52:55], v[116:119], v[88:91], v[52:55]\n\t" \
  "v_mov_b32_e32 v6, s36\n\t" \
  "v_mfma_f32_16x16x32_bf16 v[56:59], v[120:123], v[88:91], v[56:59]\n\t" \
  "v_mfma_f32_16x16x32_bf16 v[60:63], v[124:127], v[88:91], v[60:63]\n\t" \
  "v_mov_b32_e32 v7, s37\n\t" \
  "v_mfma_f32_16x16x32_bf16 v[76:79], v[124:127], v[92:95], v[76:79]\n\t" \
  "v_mov_b32_e32 v3, s53\n\t" \
  "v_mfma_f32_16x16x32_bf16 v[72:75], v[120:123], v[92:95], v[72:75]\n\t" \
  "v_mfma_f32_16x16x32_bf16 v[68:71], v[116:119], v[92:95], v[68:71]\n\t" \
  "s_mov_b64 exec, 1\n\t" \
  "global_store_dwordx2 v3, v[6:7], s[60:61] sc1\n\t" \
  "s_mov_b64 exec, -1\n\t" \
  "v_mfma_f32_16x16x32_bf16 v[64:67], v[112:115], v[92:95], v[64:67]\n\t" \
  "v_add_u32_e32 v8, s50, v4\n\t" \
  "v_add_u32_e32 v9, s50, v5\n\t" \
  "v_xor_b32_e32 v10, 32, v9\n\t" \
  "v_xor_b32_e32 v11, 64, v9\n\t" \
  "v_xor_b32_e32 v12, 96, v9\n\t" \
  "s_waitcnt lgkmcnt(0)\n\t" \
  "s_barrier\n\t" \
  "v_mfma_f32_16x16x32_bf16 v[16:19], v[128:131], v[96:99], v[16:19]\n\t" \
  "ds_read_b64_tr_b16 v[112:113], v9 offset:0\n\t" \
  "ds_read_b64_tr_b16 v[114:115], v9 offset:4096\n\t" \
  "v_mfma_f32_16x16x32_bf16 v[20:23], v[132:135], v[96:99], v[20:23]\n\t" \
  "ds_read_b64_tr_b16 v[116:117], v10 offset:0\n\t" \
  "v_mfma_f32_16x16x32_bf16 v[24:27], v[136:139], v[96:99], v[24:27]\n\t" \
  "ds_read_b64_tr_b16 v[118:119], v10 offset:4096\n\t" \
  "ds_read_b64_tr_b16 v[120:121], v11 offset:0\n\t" \
  "v_mfma_f32_16x16x32_bf16 v[28:31], v[140:143], v[96:99], v[28:31]\n\t" \
  "ds_read_b64_tr_b16 v[122:123], v11 offset:4096\n\t" \
  "v_mfma_f32_16x16x32_bf16 v[44:47], v[140:143], v[100:103], v[44:47]\n\t" \
  "ds_read_b64_tr_b16 v[124:125], v12 offset:0\n\t" \
  "ds_read_b64_tr_b16 v[126:127], v12 offset:4096\n\t" \
  "v_mfma_f32_16x16x32_bf16 v[40:43], v[136:139], v[100:103], v[40:43]\n\t" \
  "ds_read_b128 v[80:83], v8 offset:0\n\t" \
  "v_mfma_f32_16x16x32_bf16 v[36:39], v[132:135], v[100:103], v[36:39]\n\t" \
  "ds_read_b128 v[84:87], v8 offset:1024\n\t" \
  "ds_read_b128 v[88:91], v8 offset:2048\n\t" \
  "v_mfma_f32_16x16x32_bf16 v[32:35], v[128:131], v[100:103], v[32:35]\n\t" \
  "ds_read_b128 v[92:95], v8 offset:3072\n\t" \
  "v_mfma_f32_16x16x32_bf16 v[48:51], v[128:131], v[104:107], v[48:51]\n\t" \
  "s_add_u32 s48, s48, 0x8000\n\t" \
  "v_mfma_f32_16x16x32_bf16 v[52:55], v[132:135], v[104:107], v[52:55]\n\t" \
  "v_mfma_f32_16x16x32_bf16 v[56:59], v[136:139], v[104:107], v[56:59]\n\t" \
  "s_cmp_eq_u32 s48, 0x28000\n\t" \
  "s_cselect_b32 s48, 0, s48\n\t" \
  "v_mfma_f32_16x16x32_bf16 v[60:63], v[140:143], v[104:107], v[60:63]\n\t" \
  "v_mfma_f32_16x16x32_bf16 v[76:79], v[140:143], v[108:111], v[76:79]\n\t" \
  "s_xor_b32 s50, s48, s49\n\t" \
  "v_mfma_f32_16x16x32_bf16 v[72:75], v[136:139], v[108:111], v[72:75]\n\t" \
  "v_mfma_f32_16x16x32_bf16 v[68:71], v[132:135], v[108:111], v[68:71]\n\t" \
  "s_cmp_eq_u32 s48, 0x20000\n\t" \
  "s_cselect_b32 s50, s48, s50\n\t" \
  "v_mfma_f32_16x16x32_bf16 v[64:67], v[128:131], v[108:111], v[64:67]\n\t" \
  "s_waitcnt lgkmcnt(0)\n\t" \
  "v_mfma_f32_16x16x32_bf16 v[16:19], v[112:115], v[80:83], v[16:19]\n\t" \
  "ds_read_b64_tr_b16 v[128:129], v9 offset:8192\n\t" \
  "ds_read_b64_tr_b16 v[130:131], v9 offset:12288\n\t" \
  "v_mfma_f32_16x16x32_bf16 v[20:23], v[116:119], v[80:83], v[20:23]\n\t" \
  "ds_read_b64_tr_b16 v[132:133], v10 offset:8192\n\t" \
  "v_mfma_f32_16x16x32_bf16 v[24:27], v[120:123], v[80:83], v[24:27]\n\t" \
  "ds_read_b64_tr_b16 v[134:135], v10 offset:12288\n\t" \
  "ds_read_b64_tr_b16 v[136:137], v11 offset:8192\n\t" \
  "v_mfma_f32_16x16x32_bf16 v[28:31], v[124:127], v[80:83], v[28:31]\n\t" \
  "ds_read_b64_tr_b16 v[138:139], v11 offset:12288\n\t" \
  "v_mfma_f32_16x16x32_bf16 v[44:47], v[124:127], v[84:87], v[44:47]\n\t" \
  "ds_read_b64_tr_b16 v[140:141], v12 offset:8192\n\t" \
  "ds_read_b64_tr_b16 v[142:143], v12 offset:12288\n\t" \
  "v_mfma_f32_16x16x32_bf16 v[40:43], v[120:123], v[84:87], v[40:43]\n\t" \
  "ds_read_b128 v[96:99], v8 offset:8192\n\t" \
  "v_mfma_f32_16x16x32_bf16 v[36:39], v[116:119], v[84:87], v[36:39]\n\t" \
  "ds_read_b128 v[100:103], v8 offset:9216\n\t" \
  "ds_read_b128 v[104:107], v8 offset:10240\n\t" \
  "v_mfma_f32_16x16x32_bf16 v[32:35], v[112:115], v[84:87], v[32:35]\n\t" \
  "ds_read_b128 v[108:111], v8 offset:11264\n\t" \
  "v_mfma_f32_16x16x32_bf16 v[48:51], v[112:115], v[88:91], v[48:51]\n\t" \
  "global_load_dwordx2 v[6:7], v13, s[60:61] sc1\n\t" \
  "v_mfma_f32_16x16x32_bf16 v[52:55], v[116:119], v[88:91], v[52:55]\n\t" \
  "v_mfma_f32_16x16x32_bf16 v[56:59], v[120:123], v[88:91], v[56:59]\n\t" \
  "v_mfma_f32_16x16x32_bf16 v[60:63], v[124:127], v[88:91], v[60:63]\n\t" \
  "v_mfma_f32_16x16x32_bf16 v[76:79], v[124:127], v[92:95], v[76:79]\n\t" \
  "v_mfma_f32_16x16x32_bf16 v[72:75], v[120:123], v[92:95], v[72:75]\n\t" \
  "v_mfma_f32_16x16x32_bf16 v[68:71], v[116:119], v[92:95], v[68:71]\n\t" \
  "v_mfma_f32_16x16x32_bf16 v[64:67], v[112:115], v[92:95], v[64:67]\n\t" \
  "s_mov_b32 s51, 3\n\t" \
  "CS2_%=:\n\t" \
  "v_add_u32_e32 v8, s50, v4\n\t" \
  "v_add_u32_e32 v9, s50, v5\n\t" \
  "v_xor_b32_e32 v10, 32, v9\n\t" \
  "v_xor_b32_e32 v11, 64, v9\n\t" \
  "v_xor_b32_e32 v12, 96, v9\n\t" \
  "s_waitcnt lgkmcnt(0)\n\t" \
  "s_barrier\n\t" \
  "v_mfma_f32_16x16x32_bf16 v[16:19], v[128:131], v[96:99], v[16:19]\n\t" \
  "ds_read_b64_tr_b16 v[112:113], v9 offset:0\n\t" \
  "ds_read_b64_tr_b16 v[114:115], v9 offset:4096\n\t" \
  "v_mfma_f32_16x16x32_bf16 v[20:23], v[132:135], v[96:99], v[20:23]\n\t" \
  "ds_read_b64_tr_b16 v[116:117], v10 offset:0\n\t" \
  "v_mfma_f32_16x16x32_bf16 v[24:27], v[136:139], v[96:99], v[24:27]\n\t" \
  "ds_read_b64_tr_b16 v[118:119], v10 offset:4096\n\t" \
  "ds_read_b64_tr_b16 v[120:121], v11 offset:0\n\t" \
  "v_mfma_f32_16x16x32_bf16 v[28:31], v[140:143], v[96:99], v[28:31]\n\t" \
  "ds_read_b64_tr_b16 v[122:123], v11 offset:4096\n\t" \
  "v_mfma_f32_16x16x32_bf16 v[44:47], v[140:143], v[100:103], v[44:47]\n\t" \
  "ds_read_b64_tr_b16 v[124:125], v12 offset:0\n\t" \
  "ds_read_b64_tr_b16 v[126:127], v12 offset:4096\n\t" \
  "v_mfma_f32_16x16x32_bf16 v[40:43], v[136:139], v[100:103], v[40:43]\n\t" \
  "ds_read_b128 v[80:83], v8 offset:0\n\t" \
  "v_mfma_f32_16x16x32_bf16 v[36:39], v[132:135], v[100:103], v[36:39]\n\t" \
  "ds_read_b128 v[84:87], v8 offset:1024\n\t" \
  "ds_read_b128 v[88:91], v8 offset:2048\n\t" \
  "v_mfma_f32_16x16x32_bf16 v[32:35], v[128:131], v[100:103], v[32:35]\n\t" \
  "ds_read_b128 v[92:95], v8 offset:3072\n\t" \
  "v_mfma_f32_16x16x32_bf16 v[48:51], v[128:131], v[104:107], v[48:51]\n\t" \
  "s_add_u32 s48, s48, 0x8000\n\t" \
  "v_mfma_f32_16x16x32_bf16 v[52:55], v[132:135], v[104:107], v[52:55]\n\t" \
  "v_mfma_f32_16x16x32_bf16 v[56:59], v[136:139], v[104:107], v[56:59]\n\t" \
  "s_cmp_eq_u32 s48, 0x28000\n\t" \
  "s_cselect_b32 s48, 0, s48\n\t" \
  "v_mfma_f32_16x16x32_bf16 v[60:63], v[140:143], v[104:107], v[60:63]\n\t" \
  "v_mfma_f32_16x16x32_bf16 v[76:79], v[140:143], v[108:111], v[76:79]\n\t" \
  "s_xor_b32 s50, s48, s49\n\t" \
  "v_mfma_f32_16x16x32_bf16 v[72:75], v[136:139], v[108:111], v[72:75]\n\t" \
  "v_mfma_f32_16x16x32_bf16 v[68:71], v[132:135], v[108:111], v[68:71]\n\t" \
  "s_cmp_eq_u32 s48, 0x20000\n\t" \
  "s_cselect_b32 s50, s48, s50\n\t" \
  "v_mfma_f32_16x16x32_bf16 v[64:67], v[128:131], v[108:111], v[64:67]\n\t" \
  "s_waitcnt lgkmcnt(0)\n\t" \
  "v_mfma_f32_16x16x32_bf16 v[16:19], v[112:115], v[80:83], v[16:19]\n\t" \
  "ds_read_b64_tr_b16 v[128:129], v9 offset:8192\n\t" \
  "ds_read_b64_tr_b16 v[130:131], v9 offset:12288\n\t" \
  "v_mfma_f32_16x16x32_bf16 v[20:23], v[116:119], v[80:83], v[20:23]\n\t" \
  "ds_read_b64_tr_b16 v[132:133], v10 offset:8192\n\t" \
  "v_mfma_f32_16x16x32_bf16 v[24:27], v[120:123], v[80:83], v[24:27]\n\t" \
  "ds_read_b64_tr_b16 v[134:135], v10 offset:12288\n\t" \
  "ds_read_b64_tr_b16 v[136:137], v11 offset:8192\n\t" \
  "v_mfma_f32_16x16x32_bf16 v[28:31], v[124:127], v[80:83], v[28:31]\n\t" \
  "ds_read_b64_tr_b16 v[138:139], v11 offset:12288\n\t" \
  "v_mfma_f32_16x16x32_bf16 v[44:47], v[124:127], v[84:87], v[44:47]\n\t" \
  "ds_read_b64_tr_b16 v[140:141], v12 offset:8192\n\t" \
  "ds_read_b64_tr_b16 v[142:143], v12 offset:12288\n\t" \
  "v_mfma_f32_16x16x32_bf16 v[40:43], v[120:123], v[84:87], v[40:43]\n\t" \
  "ds_read_b128 v[96:99], v8 offset:8192\n\t" \
  "v_mfma_f32_16x16x32_bf16 v[36:39], v[116:119], v[84:87], v[36:39]\n\t" \
  "ds_read_b128 v[100:103], v8 offset:9216\n\t" \
  "ds_read_b128 v[104:107], v8 offset:10240\n\t" \
  "v_mfma_f32_16x16x32_bf16 v[32:35], v[112:115], v[84:87], v[32:35]\n\t" \
  "ds_read_b128 v[108:111], v8 offset:11264\n\t" \
  "v_mfma_f32_16x16x32_bf16 v[48:51], v[112:115], v[88:91], v[48:51]\n\t" \
  "v_mfma_f32_16x16x32_bf16 v[52:55], v[116:119], v[88:91], v[52:55]\n\t" \
  "v_mfma_f32_16x16x32_bf16 v[56:59], v[120:123], v[88:91], v[56:59]\n\t" \
  "v_mfma_f32_16x16x32_bf16 v[60:63], v[124:127], v[88:91], v[60:63]\n\t" \
  "v_mfma_f32_16x16x32_bf16 v[76:79], v[124:127], v[92:95], v[76:79]\n\t" \
  "v_mfma_f32_16x16x32_bf16 v[72:75], v[120:123], v[92:95], v[72:75]\n\t" \
  "v_mfma_f32_16x16x32_bf16 v[68:71], v[116:119], v[92:95], v[68:71]\n\t" \
  "v_mfma_f32_16x16x32_bf16 v[64:67], v[112:115], v[92:95], v[64:67]\n\t" \
  "s_sub_u32 s51, s51, 1\n\t" \
  "s_cmp_lg_u32 s51, 0\n\t" \
  "s_cbranch_scc1 CS2_%=\n\t" \
  "s_waitcnt vmcnt(0)\n\t" \
  "v_cmp_ne_u64_e32 vcc, s[36:37], v[6:7]\n\t" \
  "s_nop 0\n\t" \
  "s_cbranch_vccnz PSxd_%=\n\t" \
  "PRxd_%=:\n\t" \
  "s_cmp_lg_u32 s63, s58\n\t" \
  "s_cselect_b64 vcc, -1, 0\n\t" \
  "v_add_u32_e32 v6, s50, v1\n\t" \
  "v_add_u32_e32 v8, s50, v4\n\t" \
  "v_add_u32_e32 v9, s50, v5\n\t" \
  "v_xor_b32_e32 v10, 32, v9\n\t" \
  "v_xor_b32_e32 v11, 64, v9\n\t" \
  "v_xor_b32_e32 v12, 96, v9\n\t" \
  "s_waitcnt lgkmcnt(0)\n\t" \
  "s_barrier\n\t" \
  "v_mfma_f32_16x16x32_bf16 v[16:19], v[128:131], v[96:99], v[16:19]\n\t" \
  "ds_read_b64_tr_b16 v[112:113], v9 offset:0\n\t" \
  "ds_read_b64_tr_b16 v[114:115], v9 offset:4096\n\t" \
  "v_mfma_f32_16x16x32_bf16 v[20:23], v[132:135], v[96:99], v[20:23]\n\t" \
  "ds_read_b64_tr_b16 v[116:117], v10 offset:0\n\t" \
  "v_mfma_f32_16x16x32_bf16 v[24:27], v[136:139], v[96:99], v[24:27]\n\t" \
  "ds_read_b64_tr_b16 v[118:119], v10 offset:4096\n\t" \
  "ds_read_b64_tr_b16 v[120:121], v11 offset:0\n\t" \
  "v_mfma_f32_16x16x32_bf16 v[28:31], v[140:143], v[96:99], v[28:31]\n\t" \
  "ds_read_b64_tr_b16 v[122:123], v11 offset:4096\n\t" \
  "v_mfma_f32_16x16x32_bf16 v[44:47], v[140:143], v[100:103], v[44:47]\n\t" \
  "ds_read_b64_tr_b16 v[124:125], v12 offset:0\n\t" \
  "ds_read_b64_tr_b16 v[126:127], v12 offset:4096\n\t" \
  "v_mfma_f32_16x16x32_bf16 v[40:43], v[136:139], v[100:103], v[40:43]\n\t" \
  "ds_read_b128 v[80:83], v8 offset:0\n\t" \
  "v_mfma_f32_16x16x32_bf16 v[36:39], v[132:135], v[100:103], v[36:39]\n\t" \
  "ds_read_b128 v[84:87], v8 offset:1024\n\t" \
  "ds_read_b128 v[88:91], v8 offset:2048\n\t" \
  "v_mfma_f32_16x16x32_bf16 v[32:35], v[128:131], v[100:103], v[32:35]\n\t" \
  "ds_read_b128 v[92:95], v8 offset:3072\n\t" \
  "v_mfma_f32_16x16x32_bf16 v[48:51], v[128:131], v[104:107], v[48:51]\n\t" \
  "s_add_u32 s48, s48, 0x8000\n\t" \
  "s_cmp_eq_u32 s48, 0x28000\n\t" \
  "s_cselect_b32 s48, 0, s48\n\t" \
  "s_xor_b32 s50, s48, s49\n\t" \
  "v_mfma_f32_16x16x32_bf16 v[52:55], v[132:135], v[104:107], v[52:55]\n\t" \
  "s_cmp_eq_u32 s48, 0x20000\n\t" \
  "s_cselect_b32 s50, s48, s50\n\t" \
  "s_add_u32 s52, s58, 1\n\t" \
  "s_and_b32 s52, s52, 3\n\t" \
  "v_mfma_f32_16x16x32_bf16 v[56:59], v[136:139], v[104:107], v[56:59]\n\t" \
  "s_lshl_b32 s52, s52, 13\n\t" \
  "v_add_u32_e32 v3, s52, v1\n\t" \
  "global_load_dwordx4 v[144:147], v3, s[78:79] offset:0 sc1\n\t" \
  "v_mfma_f32_16x16x32_bf16 v[60:63], v[140:143], v[104:107], v[60:63]\n\t" \
  "global_load_dwordx4 v[148:151], v3, s[78:79] offset:2048 sc1\n\t" \
  "s_add_u32 s52, s58, 2\n\t" \
  "v_mfma_f32_16x16x32_bf16 v[76:79], v[140:143], v[108:111], v[76:79]\n\t" \
  "s_and_b32 s52, s52, 3\n\t" \
  "s_lshl_b32 s52, s52, 13\n\t" \
  "v_add_u32_e32 v3, s52, v1\n\t" \
  "v_mfma_f32_16x16x32_bf16 v[72:75], v[136:139], v[108:111], v[72:75]\n\t" \
  "global_load_dwordx4 v[152:155], v3, s[78:79] offset:0 sc1\n\t" \
  "global_load_dwordx4 v[156:159], v3, s[78:79] offset:2048 sc1\n\t" \
  "s_add_u32 s52, s58, 3\n\t" \
  "v_mfma_f32_16x16x32_bf16 v[68:71], v[132:135], v[108:111], v[68:71]\n\t" \
  "s_and_b32 s52, s52, 3\n\t" \
  "s_lshl_b32 s52, s52, 13\n\t" \
  "v_add_u32_e32 v3, s52, v1\n\t" \
  "v_mfma_f32_16x16x32_bf16 v[64:67], v[128:131], v[108:111], v[64:67]\n\t" \
  "global_load_dwordx4 v[248:251], v3, s[78:79] offset:0 sc1\n\t" \
  "global_load_dwordx4 v[252:255], v3, s[78:79] offset:2048 sc1\n\t" \
  "s_waitcnt lgkmcnt(0)\n\t" \
  "v_mfma_f32_16x16x32_bf16 v[16:19], v[112:115], v[80:83], v[16:19]\n\t" \
  "ds_read_b64_tr_b16 v[128:129], v9 offset:8192\n\t" \
  "ds_read_b64_tr_b16 v[130:131], v9 offset:12288\n\t" \
  "v_mfma_f32_16x16x32_bf16 v[20:23], v[116:119], v[80:83], v[20:23]\n\t" \
  "ds_read_b64_tr_b16 v[132:133], v10 offset:8192\n\t" \
  "v_mfma_f32_16x16x32_bf16 v[24:27], v[120:123], v[80:83], v[24:27]\n\t" \
  "ds_read_b64_tr_b16 v[134:135], v10 offset:12288\n\t" \
  "ds_read_b64_tr_b16 v[136:137], v11 offset:8192\n\t" \
  "v_mfma_f32_16x16x32_bf16 v[28:31], v[124:127], v[80:83], v[28:31]\n\t" \
  "ds_read_b64_tr_b16 v[138:139], v11 offset:12288\n\t" \
  "v_mfma_f32_16x16x32_bf16 v[44:47], v[124:127], v[84:87], v[44:47]\n\t" \
  "ds_read_b64_tr_b16 v[140:141], v12 offset:8192\n\t" \
  "ds_read_b64_tr_b16 v[142:143], v12 offset:12288\n\t" \
  "v_mfma_f32_16x16x32_bf16 v[40:43], v[120:123], v[84:87], v[40:43]\n\t" \
  "ds_read_b128 v[96:99], v8 offset:8192\n\t" \
  "v_mfma_f32_16x16x32_bf16 v[36:39], v[116:119], v[84:87], v[36:39]\n\t" \
  "ds_read_b128 v[100:103], v8 offset:9216\n\t" \
  "ds_read_b128 v[104:107], v8 offset:10240\n\t" \
  "v_mfma_f32_16x16x32_bf16 v[32:35], v[112:115], v[84:87], v[32:35]\n\t" \
  "ds_read_b128 v[108:111], v8 offset:11264\n\t" \
  "v_mfma_f32_16x16x32_bf16 v[48:51], v[112:115], v[88:91], v[48:51]\n\t" \
  "s_add_u32 s78, s78, 0x20000\n\t" \
  "s_addc_u32 s79, s79, 0\n\t" \
  "v_mfma_f32_16x16x32_bf16 v[52:55], v[116:119], v[88:91], v[52:55]\n\t" \
  "v_mfma_f32_16x16x32_bf16 v[56:59], v[120:123], v[88:91], v[56:59]\n\t" \
  "v_mfma_f32_16x16x32_bf16 v[60:63], v[124:127], v[88:91], v[60:63]\n\t" \
  "v_mfma_f32_16x16x32_bf16 v[76:79], v[124:127], v[92:95], v[76:79]\n\t" \
  "v_mfma_f32_16x16x32_bf16 v[72:75], v[120:123], v[92:95], v[72:75]\n\t" \
  "v_mfma_f32_16x16x32_bf16 v[68:71], v[116:119], v[92:95], v[68:71]\n\t" \
  "v_mfma_f32_16x16x32_bf16 v[64:67], v[112:115], v[92:95], v[64:67]\n\t" \
  "v_add_u32_e32 v7, s50, v1\n\t" \
  "v_add_u32_e32 v8, s50, v4\n\t" \
  "v_add_u32_e32 v9, s50, v5\n\t" \
  "v_xor_b32_e32 v10, 32, v9\n\t" \
  "v_xor_b32_e32 v11, 64, v9\n\t" \
  "v_xor_b32_e32 v12, 96, v9\n\t" \
  "s_waitcnt lgkmcnt(0)\n\t" \
  "s_barrier\n\t" \
  "v_mfma_f32_16x16x32_bf16 v[16:19], v[128:131], v[96:99], v[16:19]\n\t" \
  "ds_read_b64_tr_b16 v[112:113], v9 offset:0\n\t" \
  "ds_read_b64_tr_b16 v[114:115], v9 offset:4096\n\t" \
  "v_mfma_f32_16x16x32_bf16 v[20:23], v[132:135], v[96:99], v[20:23]\n\t" \
  "ds_read_b64_tr_b16 v[116:117], v10 offset:0\n\t" \
  "v_mfma_f32_16x16x32_bf16 v[24:27], v[136:139], v[96:99], v[24:27]\n\t" \
  "ds_read_b64_tr_b16 v[118:119], v10 offset:4096\n\t" \
  "ds_read_b64_tr_b16 v[120:121], v11 offset:0\n\t" \
  "v_mfma_f32_16x16x32_bf16 v[28:31], v[140:143], v[96:99], v[28:31]\n\t" \
  "ds_read_b64_tr_b16 v[122:123], v11 offset:4096\n\t" \
  "v_mfma_f32_16x16x32_bf16 v[44:47], v[140:143], v[100:103], v[44:47]\n\t" \
  "ds_read_b64_tr_b16 v[124:125], v12 offset:0\n\t" \
  "ds_read_b64_tr_b16 v[126:127], v12 offset:4096\n\t" \
  "v_mfma_f32_16x16x32_bf16 v[40:43], v[136:139], v[100:103], v[40:43]\n\t" \
  "ds_read_b128 v[80:83], v8 offset:0\n\t" \
  "v_mfma_f32_16x16x32_bf16 v[36:39], v[132:135], v[100:103], v[36:39]\n\t" \
  "ds_read_b128 v[84:87], v8 offset:1024\n\t" \
  "ds_read_b128 v[88:91], v8 offset:2048\n\t" \
  "v_mfma_f32_16x16x32_bf16 v[32:35], v[128:131], v[100:103], v[32:35]\n\t" \
  "ds_read_b128 v[92:95], v8 offset:3072\n\t" \
  "v_mfma_f32_16x16x32_bf16 v[48:51], v[128:131], v[104:107], v[48:51]\n\t" \
  "s_add_u32 s48, s48, 0x8000\n\t" \
  "v_mfma_f32_16x16x32_bf16 v[52:55], v[132:135], v[104:107], v[52:55]\n\t" \
  "v_mfma_f32_16x16x32_bf16 v[56:59], v[136:139], v[104:107], v[56:59]\n\t" \
  "s_cmp_eq_u32 s48, 0x28000\n\t" \
  "s_cselect_b32 s48, 0, s48\n\t" \
  "v_mfma_f32_16x16x32_bf16 v[60:63], v[140:143], v[104:107], v[60:63]\n\t" \
  "v_mfma_f32_16x16x32_bf16 v[76:79], v[140:143], v[108:111], v[76:79]\n\t" \
  "s_xor_b32 s50, s48, s49\n\t" \
  "v_mfma_f32_16x16x32_bf16 v[72:75], v[136:139], v[108:111], v[72:75]\n\t" \
  "v_mfma_f32_16x16x32_bf16 v[68:71], v[132:135], v[108:111], v[68:71]\n\t" \
  "s_cmp_eq_u32 s48, 0x20000\n\t" \
  "s_cselect_b32 s50, s48, s50\n\t" \
  "v_mfma_f32_16x16x32_bf16 v[64:67], v[128:131], v[108:111], v[64:67]\n\t" \
  "s_waitcnt lgkmcnt(0)\n\t" \
  "v_mfma_f32_16x16x32_bf16 v[16:19], v[112:115], v[80:83], v[16:19]\n\t" \
  "ds_read_b64_tr_b16 v[128:129], v9 offset:8192\n\t" \
  "ds_read_b64_tr_b16 v[130:131], v9 offset:12288\n\t" \
  "v_mfma_f32_16x16x32_bf16 v[20:23], v[116:119], v[80:83], v[20:23]\n\t" \
  "ds_read_b64_tr_b16 v[132:133], v10 offset:8192\n\t" \
  "v_mfma_f32_16x16x32_bf16 v[24:27], v[120:123], v[80:83], v[24:27]\n\t" \
  "ds_read_b64_tr_b16 v[134:135], v10 offset:12288\n\t" \
  "ds_read_b64_tr_b16 v[136:137], v11 offset:8192\n\t" \
  "v_mfma_f32_16x16x32_bf16 v[28:31], v[124:127], v[80:83], v[28:31]\n\t" \
  "ds_read_b64_tr_b16 v[138:139], v11 offset:12288\n\t" \
  "v_mfma_f32_16x16x32_bf16 v[44:47], v[124:127], v[84:87], v[44:47]\n\t" \
  "ds_read_b64_tr_b16 v[140:141], v12 offset:8192\n\t" \
  "ds_read_b64_tr_b16 v[142:143], v12 offset:12288\n\t" \
  "v_mfma_f32_16x16x32_bf16 v[40:43], v[120:123], v[84:87], v[40:43]\n\t" \
  "ds_read_b128 v[96:99], v8 offset:8192\n\t" \
  "v_mfma_f32_16x16x32_bf16 v[36:39], v[116:119], v[84:87], v[36:39]\n\t" \
  "ds_read_b128 v[100:103], v8 offset:9216\n\t" \
  "ds_read_b128 v[104:107], v8 offset:10240\n\t" \
  "v_mfma_f32_16x16x32_bf16 v[32:35], v[112:115], v[84:87], v[32:35]\n\t" \
  "ds_read_b128 v[108:111], v8 offset:11264\n\t" \
  "v_mfma_f32_16x16x32_bf16 v[48:51], v[112:115], v[88:91], v[48:51]\n\t" \
  "s_cbranch_vccnz G15_%=\n\t" \
  "ds_write_b128 v6, v[160:163] offset:0\n\t" \
  "ds_write_b128 v6, v[164:167] offset:1024\n\t" \
  "G15_%=:\n\t" \
  "s_cbranch_vccnz G16_%=\n\t" \
  "ds_write_b128 v6, v[168:171] offset:2048\n\t" \
  "ds_write_b128 v6, v[172:175] offset:3072\n\t" \
  "G16_%=:\n\t" \
  "s_cbranch_vccnz G17_%=\n\t" \
  "ds_write_b128 v6, v[176:179] offset:4096\n\t" \
  "ds_write_b128 v6, v[180:183] offset:5120\n\t" \
  "G17_%=:\n\t" \
  "s_cbranch_vccnz G18_%=\n\t" \
  "ds_write_b128 v6, v[184:187] offset:6144\n\t" \
  "ds_write_b128 v6, v[188:191] offset:7168\n\t" \
  "G18_%=:\n\t" \
  "v_mfma_f32_16x16x32_bf16 v[52:55], v[116:119], v[88:91], v[52:55]\n\t" \
  "v_mfma_f32_16x16x32_bf16 v[56:59], v[120:123], v[88:91], v[56:59]\n\t" \
  "v_mfma_f32_16x16x32_bf16 v[60:63], v[124:127], v[88:91], v[60:63]\n\t" \
  "v_mfma_f32_16x16x32_bf16 v[76:79], v[124:127], v[92:95], v[76:79]\n\t" \
  "v_mfma_f32_16x16x32_bf16 v[72:75], v[120:123], v[92:95], v[72:75]\n\t" \
  "v_mfma_f32_16x16x32_bf16 v[68:71], v[116:119], v[92:95], v[68:71]\n\t" \
  "v_mfma_f32_16x16x32_bf16 v[64:67], v[112:115], v[92:95], v[64:67]\n\t" \
  "v_add_u32_e32 v8, s50, v4\n\t" \
  "v_add_u32_e32 v9, s50, v5\n\t" \
  "v_xor_b32_e32 v10, 32, v9\n\t" \
  "v_xor_b32_e32 v11, 64, v9\n\t" \
  "v_xor_b32_e32 v12, 96, v9\n\t" \
  "s_waitcnt lgkmcnt(0)\n\t" \
  "s_barrier\n\t" \
  "v_mfma_f32_16x16x32_bf16 v[16:19], v[128:131], v[96:99], v[16:19]\n\t" \
  "ds_read_b64_tr_b16 v[112:113], v9 offset:0\n\t" \
  "ds_read_b64_tr_b16 v[114:115], v9 offset:4096\n\t" \
  "v_mfma_f32_16x16x32_bf16 v[20:23], v[132:135], v[96:99], v[20:23]\n\t" \
  "ds_read_b64_tr_b16 v[116:117], v10 offset:0\n\t" \
  "v_mfma_f32_16x16x32_bf16 v[24:27], v[136:139], v[96:99], v[24:27]\n\t" \
  "ds_read_b64_tr_b16 v[118:119], v10 offset:4096\n\t" \
  "ds_read_b64_tr_b16 v[120:121], v11 offset:0\n\t" \
  "v_mfma_f32_16x16x32_bf16 v[28:31], v[140:143], v[96:99], v[28:31]\n\t" \
  "ds_read_b64_tr_b16 v[122:123], v11 offset:4096\n\t" \
  "v_mfma_f32_16x16x32_bf16 v[44:47], v[140:143], v[100:103], v[44:47]\n\t" \
  "ds_read_b64_tr_b16 v[124:125], v12 offset:0\n\t" \
  "ds_read_b64_tr_b16 v[126:127], v12 offset:4096\n\t" \
  "v_mfma_f32_16x16x32_bf16 v[40:43], v[136:139], v[100:103], v[40:43]\n\t" \
  "ds_read_b128 v[80:83], v8 offset:0\n\t" \
  "v_mfma_f32_16x16x32_bf16 v[36:39], v[132:135], v[100:103], v[36:39]\n\t" \
  "ds_read_b128 v[84:87], v8 offset:1024\n\t" \
  "ds_read_b128 v[88:91], v8 offset:2048\n\t" \
  "v_mfma_f32_16x16x32_bf16 v[32:35], v[128:131], v[100:103], v[32:35]\n\t" \
  "ds_read_b128 v[92:95], v8 offset:3072\n\t" \
  "v_mfma_f32_16x16x32_bf16 v[48:51], v[128:131], v[104:107], v[48:51]\n\t" \
  "s_add_u32 s48, s48, 0x8000\n\t" \
  "v_mfma_f32_16x16x32_bf16 v[52:55], v[132:135], v[104:107], v[52:55]\n\t" \
  "v_mfma_f32_16x16x32_bf16 v[56:59], v[136:139], v[104:107], v[56:59]\n\t" \
  "s_cmp_eq_u32 s48, 0x28000\n\t" \
  "s_cselect_b32 s48, 0, s48\n\t" \
  "v_mfma_f32_16x16x32_bf16 v[60:63], v[140:143], v[104:107], v[60:63]\n\t" \
  "v_mfma_f32_16x16x32_bf16 v[76:79], v[140:143], v[108:111], v[76:79]\n\t" \
  "s_xor_b32 s50, s48, s49\n\t" \
  "v_mfma_f32_16x16x32_bf16 v[72:75], v[136:139], v[108:111], v[72:75]\n\t" \
  "v_mfma_f32_16x16x32_bf16 v[68:71], v[132:135], v[108:111], v[68:71]\n\t" \
  "s_cmp_eq_u32 s48, 0x20000\n\t" \
  "s_cselect_b32 s50, s48, s50\n\t" \
  "v_mfma_f32_16x16x32_bf16 v[64:67], v[128:131], v[108:111], v[64:67]\n\t" \
  "s_waitcnt lgkmcnt(0)\n\t" \
  "v_mfma_f32_16x16x32_bf16 v[16:19], v[112:115], v[80:83], v[16:19]\n\t" \
  "ds_read_b64_tr_b16 v[128:129], v9 offset:8192\n\t" \
  "ds_read_b64_tr_b16 v[130:131], v9 offset:12288\n\t" \
  "v_mfma_f32_16x16x32_bf16 v[20:23], v[116:119], v[80:83], v[20:23]\n\t" \
  "ds_read_b64_tr_b16 v[132:133], v10 offset:8192\n\t" \
  "v_mfma_f32_16x16x32_bf16 v[24:27], v[120:123], v[80:83], v[24:27]\n\t" \
  "ds_read_b64_tr_b16 v[134:135], v10 offset:12288\n\t" \
  "ds_read_b64_tr_b16 v[136:137], v11 offset:8192\n\t" \
  "v_mfma_f32_16x16x32_bf16 v[28:31], v[124:127], v[80:83], v[28:31]\n\t" \
  "ds_read_b64_tr_b16 v[138:139], v11 offset:12288\n\t" \
  "v_mfma_f32_16x16x32_bf16 v[44:47], v[124:127], v[84:87], v[44:47]\n\t" \
  "ds_read_b64_tr_b16 v[140:141], v12 offset:8192\n\t" \
  "ds_read_b64_tr_b16 v[142:143], v12 offset:12288\n\t" \
  "v_mfma_f32_16x16x32_bf16 v[40:43], v[120:123], v[84:87], v[40:43]\n\t" \
  "ds_read_b128 v[96:99], v8 offset:8192\n\t" \
  "v_mfma_f32_16x16x32_bf16 v[36:39], v[116:119], v[84:87], v[36:39]\n\t" \
  "ds_read_b128 v[100:103], v8 offset:9216\n\t" \
  "ds_read_b128 v[104:107], v8 offset:10240\n\t" \
  "v_mfma_f32_16x16x32_bf16 v[32:35], v[112:115], v[84:87], v[32:35]\n\t" \
  "ds_read_b128 v[108:111], v8 offset:11264\n\t" \
  "v_mfma_f32_16x16x32_bf16 v[48:51], v[112:115], v[88:91], v[48:51]\n\t" \
  "s_cbranch_vccnz G19_%=\n\t" \
  "ds_write_b128 v6, v[192:195] offset:8192\n\t" \
  "ds_write_b128 v6, v[196:199] offset:9216\n\t" \
  "G19_%=:\n\t" \
  "s_cbranch_vccnz G20_%=\n\t" \
  "ds_write_b128 v6, v[200:203] offset:10240\n\t" \
  "ds_write_b128 v6, v[204:207] offset:11264\n\t" \
  "G20_%=:\n\t" \
  "s_cbranch_vccnz G21_%=\n\t" \
  "ds_write_b128 v6, v[208:211] offset:12288\n\t" \
  "ds_write_b128 v6, v[212:215] offset:13312\n\t" \
  "G21_%=:\n\t" \
  "s_cbranch_vccnz G22_%=\n\t" \
  "ds_write_b128 v6, v[216:219] offset:14336\n\t" \
  "ds_write_b128 v6, v[220:223] offset:15360\n\t" \
  "G22_%=:\n\t" \
  "v_mfma_f32_16x16x32_bf16 v[52:55], v[116:119], v[88:91], v[52:55]\n\t" \
  "v_mfma_f32_16x16x32_bf16 v[56:59], v[120:123], v[88:91], v[56:59]\n\t" \
  "v_mfma_f32_16x16x32_bf16 v[60:63], v[124:127], v[88:91], v[60:63]\n\t" \
  "v_mfma_f32_16x16x32_bf16 v[76:79], v[124:127], v[92:95], v[76:79]\n\t" \
  "v_mfma_f32_16x16x32_bf16 v[72:75], v[120:123], v[92:95], v[72:75]\n\t" \
  "v_mfma_f32_16x16x32_bf16 v[68:71], v[116:119], v[92:95], v[68:71]\n\t" \
  "v_mfma_f32_16x16x32_bf16 v[64:67], v[112:115], v[92:95], v[64:67]\n\t" \
  "s_mov_b32 s51, 2\n\t" \
  "CS3_%=:\n\t" \
  "v_add_u32_e32 v8, s50, v4\n\t" \
  "v_add_u32_e32 v9, s50, v5\n\t" \
  "v_xor_b32_e32 v10, 32, v9\n\t" \
  "v_xor_b32_e32 v11, 64, v9\n\t" \
  "v_xor_b32_e32 v12, 96, v9\n\t" \
  "s_waitcnt lgkmcnt(0)\n\t" \
  "s_barrier\n\t" \
  "v_mfma_f32_16x16x32_bf16 v[16:19], v[128:131], v[96:99], v[16:19]\n\t" \
  "ds_read_b64_tr_b16 v[112:113], v9 offset:0\n\t" \
  "ds_read_b64_tr_b16 v[114:115], v9 offset:4096\n\t" \
  "v_mfma_f32_16x16x32_bf16 v[20:23], v[132:135], v[96:99], v[20:23]\n\t" \
  "ds_read_b64_tr_b16 v[116:117], v10 offset:0\n\t" \
  "v_mfma_f32_16x16x32_bf16 v[24:27], v[136:139], v[96:99], v[24:27]\n\t" \
  "ds_read_b64_tr_b16 v[118:119], v10 offset:4096\n\t" \
  "ds_read_b64_tr_b16 v[120:121], v11 offset:0\n\t" \
  "v_mfma_f32_16x16x32_bf16 v[28:31], v[140:143], v[96:99], v[28:31]\n\t" \
  "ds_read_b64_tr_b16 v[122:123], v11 offset:4096\n\t" \
  "v_mfma_f32_16x16x32_bf16 v[44:47], v[140:143], v[100:103], v[44:47]\n\t" \
  "ds_read_b64_tr_b16 v[124:125], v12 offset:0\n\t" \
  "ds_read_b64_tr_b16 v[126:127], v12 offset:4096\n\t" \
  "v_mfma_f32_16x16x32_bf16 v[40:43], v[136:139], v[100:103], v[40:43]\n\t" \
  "ds_read_b128 v[80:83], v8 offset:0\n\t" \
  "v_mfma_f32_16x16x32_bf16 v[36:39], v[132:135], v[100:103], v[36:39]\n\t" \
  "ds_read_b128 v[84:87], v8 offset:1024\n\t" \
  "ds_read_b128 v[88:91], v8 offset:2048\n\t" \
  "v_mfma_f32_16x16x32_bf16 v[32:35], v[128:131], v[100:103], v[32:35]\n\t" \
  "ds_read_b128 v[92:95], v8 offset:3072\n\t" \
  "v_mfma_f32_16x16x32_bf16 v[48:51], v[128:131], v[104:107], v[48:51]\n\t" \
  "s_add_u32 s48, s48, 0x8000\n\t" \
  "v_mfma_f32_16x16x32_bf16 v[52:55], v[132:135], v[104:107], v[52:55]\n\t" \
  "v_mfma_f32_16x16x32_bf16 v[56:59], v[136:139], v[104:107], v[56:59]\n\t" \
  "s_cmp_eq_u32 s48, 0x28000\n\t" \
  "s_cselect_b32 s48, 0, s48\n\t" \
  "v_mfma_f32_16x16x32_bf16 v[60:63], v[140:143], v[104:107], v[60:63]\n\t" \
  "v_mfma_f32_16x16x32_bf16 v[76:79], v[140:143], v[108:111], v[76:79]\n\t" \
  "s_xor_b32 s50, s48, s49\n\t" \
  "v_mfma_f32_16x16x32_bf16 v[72:75], v[136:139], v[108:111], v[72:75]\n\t" \
  "v_mfma_f32_16x16x32_bf16 v[68:71], v[132:135], v[108:111], v[68:71]\n\t" \
  "s_cmp_eq_u32 s48, 0x20000\n\t" \
  "s_cselect_b32 s50, s48, s50\n\t" \
  "v_mfma_f32_16x16x32_bf16 v[64:67], v[128:131], v[108:111], v[64:67]\n\t" \
  "s_waitcnt lgkmcnt(0)\n\t" \
  "v_mfma_f32_16x16x32_bf16 v[16:19], v[112:115], v[80:83], v[16:19]\n\t" \
  "ds_read_b64_tr_b16 v[128:129], v9 offset:8192\n\t" \
  "ds_read_b64_tr_b16 v[130:131], v9 offset:12288\n\t" \
  "v_mfma_f32_16x16x32_bf16 v[20:23], v[116:119], v[80:83], v[20:23]\n\t" \
  "ds_read_b64_tr_b16 v[132:133], v10 offset:8192\n\t" \
  "v_mfma_f32_16x16x32_bf16 v[24:27], v[120:123], v[80:83], v[24:27]\n\t" \
  "ds_read_b64_tr_b16 v[134:135], v10 offset:12288\n\t" \
  "ds_read_b64_tr_b16 v[136:137], v11 offset:8192\n\t" \
  "v_mfma_f32_16x16x32_bf16 v[28:31], v[124:127], v[80:83], v[28:31]\n\t" \
  "ds_read_b64_tr_b16 v[138:139], v11 offset:12288\n\t" \
  "v_mfma_f32_16x16x32_bf16 v[44:47], v[124:127], v[84:87], v[44:47]\n\t" \
  "ds_read_b64_tr_b16 v[140:141], v12 offset:8192\n\t" \
  "ds_read_b64_tr_b16 v[142:143], v12 offset:12288\n\t" \
  "v_mfma_f32_16x16x32_bf16 v[40:43], v[120:123], v[84:87], v[40:43]\n\t" \
  "ds_read_b128 v[96:99], v8 offset:8192\n\t" \
  "v_mfma_f32_16x16x32_bf16 v[36:39], v[116:119], v[84:87], v[36:39]\n\t" \
  "ds_read_b128 v[100:103], v8 offset:9216\n\t" \
  "ds_read_b128 v[104:107], v8 offset:10240\n\t" \
  "v_mfma_f32_16x16x32_bf16 v[32:35], v[112:115], v[84:87], v[32:35]\n\t" \
  "ds_read_b128 v[108:111], v8 offset:11264\n\t" \
  "v_mfma_f32_16x16x32_bf16 v[48:51], v[112:115], v[88:91], v[48:51]\n\t" \
  "v_mfma_f32_16x16x32_bf16 v[52:55], v[116:119], v[88:91], v[52:55]\n\t" \
  "v_mfma_f32_16x16x32_bf16 v[56:59], v[120:123], v[88:91], v[56:59]\n\t" \
  "v_mfma_f32_16x16x32_bf16 v[60:63], v[124:127], v[88:91], v[60:63]\n\t" \
  "v_mfma_f32_16x16x32_bf16 v[76:79], v[124:127], v[92:95], v[76:79]\n\t" \
  "v_mfma_f32_16x16x32_bf16 v[72:75], v[120:123], v[92:95], v[72:75]\n\t" \
  "v_mfma_f32_16x16x32_bf16 v[68:71], v[116:119], v[92:95], v[68:71]\n\t" \
  "v_mfma_f32_16x16x32_bf16 v[64:67], v[112:115], v[92:95], v[64:67]\n\t" \
  "s_sub_u32 s51, s51, 1\n\t" \
  "s_cmp_lg_u32 s51, 0\n\t" \
  "s_cbranch_scc1 CS3_%=\n\t" \
  "s_waitcnt vmcnt(0) lgkmcnt(0)\n\t" \
  "v_mfma_f32_16x16x32_bf16 v[16:19], v[128:131], v[96:99], v[16:19]\n\t" \
  "v_mfma_f32_16x16x32_bf16 v[20:23], v[132:135], v[96:99], v[20:23]\n\t" \
  "v_mfma_f32_16x16x32_bf16 v[24:27], v[136:139], v[96:99], v[24:27]\n\t" \
  "v_mfma_f32_16x16x32_bf16 v[28:31], v[140:143], v[96:99], v[28:31]\n\t" \
  "v_mfma_f32_16x16x32_bf16 v[44:47], v[140:143], v[100:103], v[44:47]\n\t" \
  "s_lshl_b32 s52, s83, 1\n\t" \
  "v_add_u32_e32 v6, s52, v6\n\t" \
  "v_mfma_f32_16x16x32_bf16 v[40:43], v[136:139], v[100:103], v[40:43]\n\t" \
  "ds_read_b128 v[232:235], v6 offset:0\n\t" \
  "v_mfma_f32_16x16x32_bf16 v[36:39], v[132:135], v[100:103], v[36:39]\n\t" \
  "ds_read_b128 v[236:239], v6 offset:1024\n\t" \
  "ds_read_b128 v[240:243], v6 offset:4096\n\t" \
  "v_mfma_f32_16x16x32_bf16 v[32:35], v[128:131], v[100:103], v[32:35]\n\t" \
  "ds_read_b128 v[244:247], v6 offset:5120\n\t" \
  "v_mfma_f32_16x16x32_bf16 v[48:51], v[128:131], v[104:107], v[48:51]\n\t" \
  "v_cvt_pk_bf16_f32 v80, v16, v17\n\t" \
  "v_cvt_pk_bf16_f32 v81, v18, v19\n\t" \
  "v_mfma_f32_16x16x32_bf16 v[52:55], v[132:135], v[104:107], v[52:55]\n\t" \
  "v_cvt_pk_bf16_f32 v82, v20, v21\n\t" \
  "v_cvt_pk_bf16_f32 v83, v22, v23\n\t" \
  "s_cbranch_vccz G23_%=\n\t" \
  "global_store_dwordx4 v1, v[80:83], s[72:73] offset:0 sc1\n\t" \
  "G23_%=:\n\t" \
  "v_mfma_f32_16x16x32_bf16 v[56:59], v[136:139], v[104:107], v[56:59]\n\t" \
  "v_cvt_pk_bf16_f32 v84, v24, v25\n\t" \
  "v_cvt_pk_bf16_f32 v85, v26, v27\n\t" \
  "v_mfma_f32_16x16x32_bf16 v[60:63], v[140:143], v[104:107], v[60:63]\n\t" \
  "v_cvt_pk_bf16_f32 v86, v28, v29\n\t" \
  "v_cvt_pk_bf16_f32 v87, v30, v31\n\t" \
  "s_cbranch_vccz G24_%=\n\t" \
  "global_store_dwordx4 v1, v[84:87], s[72:73] offset:1024 sc1\n\t" \
  "G24_%=:\n\t" \
  "v_mfma_f32_16x16x32_bf16 v[76:79], v[140:143], v[108:111], v[76:79]\n\t" \
  "v_cvt_pk_bf16_f32 v88, v32, v33\n\t" \
  "v_cvt_pk_bf16_f32 v89, v34, v35\n\t" \
  "v_mfma_f32_16x16x32_bf16 v[72:75], v[136:139], v[108:111], v[72:75]\n\t" \
  "v_cvt_pk_bf16_f32 v90, v36, v37\n\t" \
  "v_cvt_pk_bf16_f32 v91, v38, v39\n\t" \
  "s_cbranch_vccz G25_%=\n\t" \
  "global_store_dwordx4 v1, v[88:91], s[72:73] offset:2048 sc1\n\t" \
  "G25_%=:\n\t" \
  "v_mfma_f32_16x16x32_bf16 v[68:71], v[132:135], v[108:111], v[68:71]\n\t" \
  "v_cvt_pk_bf16_f32 v92, v40, v41\n\t" \
  "v_cvt_pk_bf16_f32 v93, v42, v43\n\t" \
  "v_mfma_f32_16x16x32_bf16 v[64:67], v[128:131], v[108:111], v[64:67]\n\t" \
  "v_cvt_pk_bf16_f32 v94, v44, v45\n\t" \
  "v_cvt_pk_bf16_f32 v95, v46, v47\n\t" \
  "s_cbranch_vccz G26_%=\n\t" \
  "global_store_dwordx4 v1, v[92:95], s[72:73] offset:3072 sc1\n\t" \
  "G26_%=:\n\t" \
  "v_cvt_pk_bf16_f32 v112, v48, v49\n\t" \
  "v_cvt_pk_bf16_f32 v113, v50, v51\n\t" \
  "v_cvt_pk_bf16_f32 v114, v52, v53\n\t" \
  "v_cvt_pk_bf16_f32 v115, v54, v55\n\t" \
  "s_cbranch_vccz G27_%=\n\t" \
  "global_store_dwordx4 v2, v[112:115], s[72:73] offset:0 sc1\n\t" \
  "G27_%=:\n\t" \
  "v_cvt_pk_bf16_f32 v116, v56, v57\n\t" \
  "v_cvt_pk_bf16_f32 v117, v58, v59\n\t" \
  "v_cvt_pk_bf16_f32 v118, v60, v61\n\t" \
  "v_cvt_pk_bf16_f32 v119, v62, v63\n\t" \
  "s_cbranch_vccz G28_%=\n\t" \
  "global_store_dwordx4 v2, v[116:119], s[72:73] offset:1024 sc1\n\t" \
  "G28_%=:\n\t" \
  "v_cvt_pk_bf16_f32 v120, v64, v65\n\t" \
  "v_cvt_pk_bf16_f32 v121, v66, v67\n\t" \
  "v_cvt_pk_bf16_f32 v122, v68, v69\n\t" \
  "v_cvt_pk_bf16_f32 v123, v70, v71\n\t" \
  "s_cbranch_vccz G29_%=\n\t" \
  "global_store_dwordx4 v2, v[120:123], s[72:73] offset:2048 sc1\n\t" \
  "G29_%=:\n\t" \
  "v_cvt_pk_bf16_f32 v124, v72, v73\n\t" \
  "v_cvt_pk_bf16_f32 v125, v74, v75\n\t" \
  "v_cvt_pk_bf16_f32 v126, v76, v77\n\t" \
  "v_cvt_pk_bf16_f32 v127, v78, v79\n\t" \
  "s_cbranch_vccz G30_%=\n\t" \
  "global_store_dwordx4 v2, v[124:127], s[72:73] offset:3072 sc1\n\t" \
  "G30_%=:\n\t" \
  "s_cbranch_vccnz G31_%=\n\t" \
  "ds_write_b128 v7, v[16:19] offset:0\n\t" \
  "ds_write_b128 v7, v[20:23] offset:1024\n\t" \
  "ds_write_b128 v7, v[24:27] offset:2048\n\t" \
  "ds_write_b128 v7, v[28:31] offset:3072\n\t" \
  "ds_write_b128 v7, v[32:35] offset:4096\n\t" \
  "ds_write_b128 v7, v[36:39] offset:5120\n\t" \
  "ds_write_b128 v7, v[40:43] offset:6144\n\t" \
  "ds_write_b128 v7, v[44:47] offset:7168\n\t" \
  "ds_write_b128 v7, v[48:51] offset:8192\n\t" \
  "ds_write_b128 v7, v[52:55] offset:9216\n\t" \
  "ds_write_b128 v7, v[56:59] offset:10240\n\t" \
  "ds_write_b128 v7, v[60:63] offset:11264\n\t" \
  "ds_write_b128 v7, v[64:67] offset:12288\n\t" \
  "ds_write_b128 v7, v[68:71] offset:13312\n\t" \
  "ds_write_b128 v7, v[72:75] offset:14336\n\t" \
  "ds_write_b128 v7, v[76:79] offset:15360\n\t" \
  "G31_%=:\n\t" \
  "s_waitcnt lgkmcnt(0)\n\t" \
  "v_lshlrev_b32_e32 v160, 16, v144\n\t" \
  "v_lshlrev_b32_e32 v161, 16, v152\n\t" \
  "v_lshlrev_b32_e32 v162, 16, v248\n\t" \
  "v_and_b32_e32 v164, 0xffff0000, v144\n\t" \
  "v_and_b32_e32 v165, 0xffff0000, v152\n\t" \
  "v_and_b32_e32 v166, 0xffff0000, v248\n\t" \
  "v_add_f32_e32 v160, v160, v161\n\t" \
  "v_add_f32_e32 v232, v232, v162\n\t" \
  "v_add_f32_e32 v164, v164, v165\n\t" \
  "v_add_f32_e32 v233, v233, v166\n\t" \
  "v_add_f32_e32 v232, v232, v160\n\t" \
  "v_add_f32_e32 v233, v233, v164\n\t" \
  "v_lshlrev_b32_e32 v160, 16, v145\n\t" \
  "v_lshlrev_b32_e32 v161, 16, v153\n\t" \
  "v_lshlrev_b32_e32 v162, 16, v249\n\t" \
  "v_and_b32_e32 v164, 0xffff0000, v145\n\t" \
  "v_and_b32_e32 v165, 0xffff0000, v153\n\t" \
  "v_and_b32_e32 v166, 0xffff0000, v249\n\t" \
  "v_add_f32_e32 v160, v160, v161\n\t" \
  "v_add_f32_e32 v234, v234, v162\n\t" \
  "v_add_f32_e32 v164, v164, v165\n\t" \
  "v_add_f32_e32 v235, v235, v166\n\t" \
  "v_add_f32_e32 v234, v234, v160\n\t" \
  "v_add_f32_e32 v235, v235, v164\n\t" \
  "v_lshlrev_b32_e32 v160, 16, v146\n\t" \
  "v_lshlrev_b32_e32 v161, 16, v154\n\t" \
  "v_lshlrev_b32_e32 v162, 16, v250\n\t" \
  "v_and_b32_e32 v164, 0xffff0000, v146\n\t" \
  "v_and_b32_e32 v165, 0xffff0000, v154\n\t" \
  "v_and_b32_e32 v166, 0xffff0000, v250\n\t" \
  "v_add_f32_e32 v160, v160, v161\n\t" \
  "v_add_f32_e32 v236, v236, v162\n\t" \
  "v_add_f32_e32 v164, v164, v165\n\t" \
  "v_add_f32_e32 v237, v237, v166\n\t" \
  "v_add_f32_e32 v236, v236, v160\n\t" \
  "v_add_f32_e32 v237, v237, v164\n\t" \
  "v_lshlrev_b32_e32 v160, 16, v147\n\t" \
  "v_lshlrev_b32_e32 v161, 16, v155\n\t" \
  "v_lshlrev_b32_e32 v162, 16, v251\n\t" \
  "v_and_b32_e32 v164, 0xffff0000, v147\n\t" \
  "v_and_b32_e32 v165, 0xffff0000, v155\n\t" \
  "v_and_b32_e32 v166, 0xffff0000, v251\n\t" \
  "v_add_f32_e32 v160, v160, v161\n\t" \
  "v_add_f32_e32 v238, v238, v162\n\t" \
  "v_add_f32_e32 v164, v164, v165\n\t" \
  "v_add_f32_e32 v239, v239, v166\n\t" \
  "v_add_f32_e32 v238, v238, v160\n\t" \
  "v_add_f32_e32 v239, v239, v164\n\t" \
  "v_lshlrev_b32_e32 v160, 16, v148\n\t" \
  "v_lshlrev_b32_e32 v161, 16, v156\n\t" \
  "v_lshlrev_b32_e32 v162, 16, v252\n\t" \
  "v_and_b32_e32 v164, 0xffff0000, v148\n\t" \
  "v_and_b32_e32 v165, 0xffff0000, v156\n\t" \
  "v_and_b32_e32 v166, 0xffff0000, v252\n\t" \
  "v_add_f32_e32 v160, v160, v161\n\t" \
  "v_add_f32_e32 v240, v240, v162\n\t" \
  "v_add_f32_e32 v164, v164, v165\n\t" \
  "v_add_f32_e32 v241, v241, v166\n\t" \
  "v_add_f32_e32 v240, v240, v160\n\t" \
  "v_add_f32_e32 v241, v241, v164\n\t" \
  "v_lshlrev_b32_e32 v160, 16, v149\n\t" \
  "v_lshlrev_b32_e32 v161, 16, v157\n\t" \
  "v_lshlrev_b32_e32 v162, 16, v253\n\t" \
  "v_and_b32_e32 v164, 0xffff0000, v149\n\t" \
  "v_and_b32_e32 v165, 0xffff0000, v157\n\t" \
  "v_and_b32_e32 v166, 0xffff0000, v253\n\t" \
  "v_add_f32_e32 v160, v160, v161\n\t" \
  "v_add_f32_e32 v242, v242, v162\n\t" \
  "v_add_f32_e32 v164, v164, v165\n\t" \
  "v_add_f32_e32 v243, v243, v166\n\t" \
  "v_add_f32_e32 v242, v242, v160\n\t" \
  "v_add_f32_e32 v243, v243, v164\n\t" \
  "v_lshlrev_b32_e32 v160, 16, v150\n\t" \
  "v_lshlrev_b32_e32 v161, 16, v158\n\t" \
  "v_lshlrev_b32_e32 v162, 16, v254\n\t" \
  "v_and_b32_e32 v164, 0xffff0000, v150\n\t" \
  "v_and_b32_e32 v165, 0xffff0000, v158\n\t" \
  "v_and_b32_e32 v166, 0xffff0000, v254\n\t" \
  "v_add_f32_e32 v160, v160, v161\n\t" \
  "v_add_f32_e32 v244, v244, v162\n\t" \
  "v_add_f32_e32 v164, v164, v165\n\t" \
  "v_add_f32_e32 v245, v245, v166\n\t" \
  "v_add_f32_e32 v244, v244, v160\n\t" \
  "v_add_f32_e32 v245, v245, v164\n\t" \
  "v_lshlrev_b32_e32 v160, 16, v151\n\t" \
  "v_lshlrev_b32_e32 v161, 16, v159\n\t" \
  "v_lshlrev_b32_e32 v162, 16, v255\n\t" \
  "v_and_b32_e32 v164, 0xffff0000, v151\n\t" \
  "v_and_b32_e32 v165, 0xffff0000, v159\n\t" \
  "v_and_b32_e32 v166, 0xffff0000, v255\n\t" \
  "v_add_f32_e32 v160, v160, v161\n\t" \
  "v_add_f32_e32 v246, v246, v162\n\t" \
  "v_add_f32_e32 v164, v164, v165\n\t" \
  "v_add_f32_e32 v247, v247, v166\n\t" \
  "v_add_f32_e32 v246, v246, v160\n\t" \
  "v_add_f32_e32 v247, v247, v164\n\t" \
  "s_add_u32 s52, s58, 1\n\t" \
  "s_and_b32 s52, s52, 3\n\t" \
  "s_lshl_b32 s52, s52, 13\n\t" \
  "v_add_u32_e32 v3, s52, v1\n\t" \
  "global_load_dwordx4 v[16:19], v3, s[78:79] offset:0 sc1\n\t" \
  "global_load_dwordx4 v[20:23], v3, s[78:79] offset:2048 sc1\n\t" \
  "s_add_u32 s52, s58, 2\n\t" \
  "s_and_b32 s52, s52, 3\n\t" \
  "s_lshl_b32 s52, s52, 13\n\t" \
  "v_add_u32_e32 v3, s52, v1\n\t" \
  "global_load_dwordx4 v[24:27], v3, s[78:79] offset:0 sc1\n\t" \
  "global_load_dwordx4 v[28:31], v3, s[78:79] offset:2048 sc1\n\t" \
  "s_add_u32 s52, s58, 3\n\t" \
  "s_and_b32 s52, s52, 3\n\t" \
  "s_lshl_b32 s52, s52, 13\n\t" \
  "v_add_u32_e32 v3, s52, v1\n\t" \
  "global_load_dwordx4 v[32:35], v3, s[78:79] offset:0 sc1\n\t" \
  "global_load_dwordx4 v[36:39], v3, s[78:79] offset:2048 sc1\n\t" \
  "v_mul_f32_e32 v176, 0xbfb8aa3b, v232\n\t" \
  "v_mul_f32_e32 v177, 0xbfb8aa3b, v233\n\t" \
  "v_mul_f32_e32 v178, 0xbfb8aa3b, v234\n\t" \
  "v_mul_f32_e32 v179, 0xbfb8aa3b, v235\n\t" \
  "v_mul_f32_e32 v180, 0xbfb8aa3b, v236\n\t" \
  "v_mul_f32_e32 v181, 0xbfb8aa3b, v237\n\t" \
  "v_mul_f32_e32 v182, 0xbfb8aa3b, v238\n\t" \
  "v_mul_f32_e32 v183, 0xbfb8aa3b, v239\n\t" \
  "v_mul_f32_e32 v184, 0xbfb8aa3b, v240\n\t" \
  "v_mul_f32_e32 v185, 0xbfb8aa3b, v241\n\t" \
  "v_mul_f32_e32 v186, 0xbfb8aa3b, v242\n\t" \
  "v_mul_f32_e32 v187, 0xbfb8aa3b, v243\n\t" \
  "v_mul_f32_e32 v188, 0xbfb8aa3b, v244\n\t" \
  "v_mul_f32_e32 v189, 0xbfb8aa3b, v245\n\t" \
  "v_mul_f32_e32 v190, 0xbfb8aa3b, v246\n\t" \
  "v_mul_f32_e32 v191, 0xbfb8aa3b, v247\n\t" \
  "v_exp_f32_e32 v176, v176\n\t" \
  "v_exp_f32_e32 v177, v177\n\t" \
  "v_exp_f32_e32 v178, v178\n\t" \
  "v_exp_f32_e32 v179, v179\n\t" \
  "v_exp_f32_e32 v180, v180\n\t" \
  "v_exp_f32_e32 v181, v181\n\t" \
  "v_exp_f32_e32 v182, v182\n\t" \
  "v_exp_f32_e32 v183, v183\n\t" \
  "v_exp_f32_e32 v184, v184\n\t" \
  "v_exp_f32_e32 v185, v185\n\t" \
  "v_exp_f32_e32 v186, v186\n\t" \
  "v_exp_f32_e32 v187, v187\n\t" \
  "v_exp_f32_e32 v188, v188\n\t" \
  "v_exp_f32_e32 v189, v189\n\t" \
  "v_exp_f32_e32 v190, v190\n\t" \
  "v_exp_f32_e32 v191, v191\n\t" \
  "v_add_f32_e32 v176, 1.0, v176\n\t" \
  "v_add_f32_e32 v177, 1.0, v177\n\t" \
  "v_add_f32_e32 v178, 1.0, v178\n\t" \
  "v_add_f32_e32 v179, 1.0, v179\n\t" \
  "v_add_f32_e32 v180, 1.0, v180\n\t" \
  "v_add_f32_e32 v181, 1.0, v181\n\t" \
  "v_add_f32_e32 v182, 1.0, v182\n\t" \
  "v_add_f32_e32 v183, 1.0, v183\n\t" \
  "v_add_f32_e32 v184, 1.0, v184\n\t" \
  "v_add_f32_e32 v185, 1.0, v185\n\t" \
  "v_add_f32_e32 v186, 1.0, v186\n\t" \
  "v_add_f32_e32 v187, 1.0, v187\n\t" \
  "v_add_f32_e32 v188, 1.0, v188\n\t" \
  "v_add_f32_e32 v189, 1.0, v189\n\t" \
  "v_add_f32_e32 v190, 1.0, v190\n\t" \
  "v_add_f32_e32 v191, 1.0, v191\n\t" \
  "v_rcp_f32_e32 v176, v176\n\t" \
  "v_rcp_f32_e32 v177, v177\n\t" \
  "v_rcp_f32_e32 v178, v178\n\t" \
  "v_rcp_f32_e32 v179, v179\n\t" \
  "v_rcp_f32_e32 v180, v180\n\t" \
  "v_rcp_f32_e32 v181, v181\n\t" \
  "v_rcp_f32_e32 v182, v182\n\t" \
  "v_rcp_f32_e32 v183, v183\n\t" \
  "v_rcp_f32_e32 v184, v184\n\t" \
  "v_rcp_f32_e32 v185, v185\n\t" \
  "v_rcp_f32_e32 v186, v186\n\t" \
  "v_rcp_f32_e32 v187, v187\n\t" \
  "v_rcp_f32_e32 v188, v188\n\t" \
  "v_rcp_f32_e32 v189, v189\n\t" \
  "v_rcp_f32_e32 v190, v190\n\t" \
  "v_rcp_f32_e32 v191, v191\n\t" \
  "v_mul_f32_e32 v176, v176, v232\n\t" \
  "v_mul_f32_e32 v177, v177, v233\n\t" \
  "v_mul_f32_e32 v178, v178, v234\n\t" \
  "v_mul_f32_e32 v179, v179, v235\n\t" \
  "v_mul_f32_e32 v180, v180, v236\n\t" \
  "v_mul_f32_e32 v181, v181, v237\n\t" \
  "v_mul_f32_e32 v182, v182, v238\n\t" \
  "v_mul_f32_e32 v183, v183, v239\n\t" \
  "v_mul_f32_e32 v184, v184, v240\n\t" \
  "v_mul_f32_e32 v185, v185, v241\n\t" \
  "v_mul_f32_e32 v186, v186, v242\n\t" \
  "v_mul_f32_e32 v187, v187, v243\n\t" \
  "v_mul_f32_e32 v188, v188, v244\n\t" \
  "v_mul_f32_e32 v189, v189, v245\n\t" \
  "v_mul_f32_e32 v190, v190, v246\n\t" \
  "v_mul_f32_e32 v191, v191, v247\n\t" \
  "s_barrier\n\t" \
  "s_lshl_b32 s52, s83, 1\n\t" \
  "v_add_u32_e32 v7, s52, v7\n\t" \
  "ds_read_b128 v[128:131], v7 offset:0\n\t" \
  "ds_read_b128 v[132:135], v7 offset:1024\n\t" \
  "ds_read_b128 v[136:139], v7 offset:4096\n\t" \
  "ds_read_b128 v[140:143], v7 offset:5120\n\t" \
  "v_lshlrev_b32_e32 v192, 16, v224\n\t" \
  "v_and_b32_e32 v193, 0xffff0000, v224\n\t" \
  "v_lshlrev_b32_e32 v194, 16, v225\n\t" \
  "v_and_b32_e32 v195, 0xffff0000, v225\n\t" \
  "v_lshlrev_b32_e32 v196, 16, v226\n\t" \
  "v_and_b32_e32 v197, 0xffff0000, v226\n\t" \
  "v_lshlrev_b32_e32 v198, 16, v227\n\t" \
  "v_and_b32_e32 v199, 0xffff0000, v227\n\t" \
  "v_lshlrev_b32_e32 v200, 16, v228\n\t" \
  "v_and_b32_e32 v201, 0xffff0000, v228\n\t" \
  "v_lshlrev_b32_e32 v202, 16, v229\n\t" \
  "v_and_b32_e32 v203, 0xffff0000, v229\n\t" \
  "v_lshlrev_b32_e32 v204, 16, v230\n\t" \
  "v_and_b32_e32 v205, 0xffff0000, v230\n\t" \
  "v_lshlrev_b32_e32 v206, 16, v231\n\t" \
  "v_and_b32_e32 v207, 0xffff0000, v231\n\t" \
  "v_and_b32_e32 v2, 16, v0\n\t" \
  "v_lshl_add_u32 v2, v2, 1, v2\n\t" \
  "v_lshrrev_b32_e32 v2, 1, v2\n\t" \
  "v_add_u32_e32 v14, v2, v14\n\t" \
  "v_add_u32_e32 v15, v2, v15\n\t" \
  "s_waitcnt lgkmcnt(0)\n\t" \
  "s_waitcnt vmcnt(0)\n\t" \
  "v_max3_u32 v10, v16, v17, v18\n\t" \
  "v_max3_u32 v11, v19, v20, v21\n\t" \
  "v_max3_u32 v10, v10, v22, v23\n\t" \
  "v_max3_u32 v11, v11, v24, v25\n\t" \
  "v_max3_u32 v10, v10, v26, v27\n\t" \
  "v_max3_u32 v11, v11, v28, v29\n\t" \
  "v_max3_u32 v10, v10, v30, v31\n\t" \
  "v_max3_u32 v11, v11, v32, v33\n\t" \
  "v_max3_u32 v10, v10, v34, v35\n\t" \
  "v_max3_u32 v11, v11, v36, v37\n\t" \
  "v_max3_u32 v10, v10, v38, v39\n\t" \
  "v_max_u32_e32 v10, v10, v11\n\t" \
  "v_cmp_eq_u32_e32 vcc, -1, v10\n\t" \
  "s_nop 0\n\t" \
  "s_cbranch_vccnz PSz_%=\n\t" \
  "PRz_%=:\n\t" \
  "v_lshlrev_b32_e32 v160, 16, v16\n\t" \
  "v_and_b32_e32 v161, 0xffff0000, v16\n\t" \
  "v_lshlrev_b32_e32 v162, 16, v17\n\t" \
  "v_and_b32_e32 v163, 0xffff0000, v17\n\t" \
  "v_lshlrev_b32_e32 v164, 16, v18\n\t" \
  "v_and_b32_e32 v165, 0xffff0000, v18\n\t" \
  "v_lshlrev_b32_e32 v166, 16, v19\n\t" \
  "v_and_b32_e32 v167, 0xffff0000, v19\n\t" \
  "v_lshlrev_b32_e32 v168, 16, v20\n\t" \
  "v_and_b32_e32 v169, 0xffff0000, v20\n\t" \
  "v_lshlrev_b32_e32 v170, 16, v21\n\t" \
  "v_and_b32_e32 v171, 0xffff0000, v21\n\t" \
  "v_lshlrev_b32_e32 v172, 16, v22\n\t" \
  "v_and_b32_e32 v173, 0xffff0000, v22\n\t" \
  "v_lshlrev_b32_e32 v174, 16, v23\n\t" \
  "v_and_b32_e32 v175, 0xffff0000, v23\n\t" \
  "v_pk_add_f32 v[128:129], v[128:129], v[160:161]\n\t" \
  "v_pk_add_f32 v[130:131], v[130:131], v[162:163]\n\t" \
  "v_pk_add_f32 v[132:133], v[132:133], v[164:165]\n\t" \
  "v_pk_add_f32 v[134:135], v[134:135], v[166:167]\n\t" \
  "v_pk_add_f32 v[136:137], v[136:137], v[168:169]\n\t" \
  "v_pk_add_f32 v[138:139], v[138:139], v[170:171]\n\t" \
  "v_pk_add_f32 v[140:141], v[140:141], v[172:173]\n\t" \
  "v_pk_add_f32 v[142:143], v[142:143], v[174:175]\n\t" \
  "v_lshlrev_b32_e32 v160, 16, v24\n\t" \
  "v_and_b32_e32 v161, 0xffff0000, v24\n\t" \
  "v_lshlrev_b32_e32 v162, 16, v25\n\t" \
  "v_and_b32_e32 v163, 0xffff0000, v25\n\t" \
  "v_lshlrev_b32_e32 v164, 16, v26\n\t" \
  "v_and_b32_e32 v165, 0xffff0000, v26\n\t" \
  "v_lshlrev_b32_e32 v166, 16, v27\n\t" \
  "v_and_b32_e32 v167, 0xffff0000, v27\n\t" \
  "v_lshlrev_b32_e32 v168, 16, v28\n\t" \
  "v_and_b32_e32 v169, 0xffff0000, v28\n\t" \
  "v_lshlrev_b32_e32 v170, 16, v29\n\t" \
  "v_and_b32_e32 v171, 0xffff0000, v29\n\t" \
  "v_lshlrev_b32_e32 v172, 16, v30\n\t" \
  "v_and_b32_e32 v173, 0xffff0000, v30\n\t" \
  "v_lshlrev_b32_e32 v174, 16, v31\n\t" \
  "v_and_b32_e32 v175, 0xffff0000, v31\n\t" \
  "v_pk_add_f32 v[128:129], v[128:129], v[160:161]\n\t" \
  "v_pk_add_f32 v[130:131], v[130:131], v[162:163]\n\t" \
  "v_pk_add_f32 v[132:133], v[132:133], v[164:165]\n\t" \
  "v_pk_add_f32 v[134:135], v[134:135], v[166:167]\n\t" \
  "v_pk_add_f32 v[136:137], v[136:137], v[168:169]\n\t" \
  "v_pk_add_f32 v[138:139], v[138:139], v[170:171]\n\t" \
  "v_pk_add_f32 v[140:141], v[140:141], v[172:173]\n\t" \
  "v_pk_add_f32 v[142:143], v[142:143], v[174:175]\n\t" \
  "v_lshlrev_b32_e32 v160, 16, v32\n\t" \
  "v_and_b32_e32 v161, 0xffff0000, v32\n\t" \
  "v_lshlrev_b32_e32 v162, 16, v33\n\t" \
  "v_and_b32_e32 v163, 0xffff0000, v33\n\t" \
  "v_lshlrev_b32_e32 v164, 16, v34\n\t" \
  "v_and_b32_e32 v165, 0xffff0000, v34\n\t" \
  "v_lshlrev_b32_e32 v166, 16, v35\n\t" \
  "v_and_b32_e32 v167, 0xffff0000, v35\n\t" \
  "v_lshlrev_b32_e32 v168, 16, v36\n\t" \
  "v_and_b32_e32 v169, 0xffff0000, v36\n\t" \
  "v_lshlrev_b32_e32 v170, 16, v37\n\t" \
  "v_and_b32_e32 v171, 0xffff0000, v37\n\t" \
  "v_lshlrev_b32_e32 v172, 16, v38\n\t" \
  "v_and_b32_e32 v173, 0xffff0000, v38\n\t" \
  "v_lshlrev_b32_e32 v174, 16, v39\n\t" \
  "v_and_b32_e32 v175, 0xffff0000, v39\n\t" \
  "v_pk_add_f32 v[128:129], v[128:129], v[160:161]\n\t" \
  "v_pk_add_f32 v[130:131], v[130:131], v[162:163]\n\t" \
  "v_pk_add_f32 v[132:133], v[132:133], v[164:165]\n\t" \
  "v_pk_add_f32 v[134:135], v[134:135], v[166:167]\n\t" \
  "v_pk_add_f32 v[136:137], v[136:137], v[168:169]\n\t" \
  "v_pk_add_f32 v[138:139], v[138:139], v[170:171]\n\t" \
  "v_pk_add_f32 v[140:141], v[140:141], v[172:173]\n\t" \
  "v_pk_add_f32 v[142:143], v[142:143], v[174:175]\n\t" \
  "v_pk_fma_f32 v[176:177], v[176:177], v[128:129], v[192:193]\n\t" \
  "v_pk_fma_f32 v[178:179], v[178:179], v[130:131], v[194:195]\n\t" \
  "v_pk_fma_f32 v[180:181], v[180:181], v[132:133], v[196:197]\n\t" \
  "v_pk_fma_f32 v[182:183], v[182:183], v[134:135], v[198:199]\n\t" \
  "v_pk_fma_f32 v[184:185], v[184:185], v[136:137], v[200:201]\n\t" \
  "v_pk_fma_f32 v[186:187], v[186:187], v[138:139], v[202:203]\n\t" \
  "v_pk_fma_f32 v[188:189], v[188:189], v[140:141], v[204:205]\n\t" \
  "v_pk_fma_f32 v[190:191], v[190:191], v[142:143], v[206:207]\n\t" \
  "v_cvt_pk_bf16_f32 v160, v176, v177\n\t" \
  "v_cvt_pk_bf16_f32 v161, v178, v179\n\t" \
  "v_cvt_pk_bf16_f32 v162, v180, v181\n\t" \
  "v_cvt_pk_bf16_f32 v163, v182, v183\n\t" \
  "v_cvt_pk_bf16_f32 v164, v184, v185\n\t" \
  "v_cvt_pk_bf16_f32 v165, v186, v187\n\t" \
  "v_cvt_pk_bf16_f32 v166, v188, v189\n\t" \
  "v_cvt_pk_bf16_f32 v167, v190, v191\n\t" \
  "s_nop 0\n\t" \
  "v_permlane16_swap_b32_e32 v160, v162\n\t" \
  "v_permlane16_swap_b32_e32 v161, v163\n\t" \
  "v_permlane16_swap_b32_e32 v164, v166\n\t" \
  "v_permlane16_swap_b32_e32 v165, v167\n\t" \
  "global_store_dwordx4 v15, v[160:163], s[76:77] sc1\n\t" \
  "global_store_dwordx4 v14, v[164:167], s[76:77] sc1\n\t" \
  "s_branch END_%=\n\t" \
  "SH_%=:\n\t" \
  "s_setprio 1\n\t" \
  "s_waitcnt vmcnt(0)\n\t" \
  "v_cmp_ne_u64_e32 vcc, s[36:37], v[144:145]\n\t" \
  "s_nop 0\n\t" \
  "s_cbranch_vccnz PSk2_%=\n\t" \
  "PRk2_%=:\n\t" \
  "s_cmp_eq_u32 s67, 2\n\t" \
  "s_cbranch_scc1 N2_%=\n\t" \
  "LPS_%=:\n\t" \
  "v_xor_b32_e32 v8, 0x10000, v8\n\t" \
  "s_waitcnt vmcnt(8) lgkmcnt(8)\n\t" \
  "v_mfma_f32_16x16x32_bf16 v[144:147], v[192:195], v[80:83], v[176:179]\n\t" \
  "s_barrier\n\t" \
  "v_exp_f32_e32 v123, v123\n\t" \
  "v_mfma_f32_16x16x32_bf16 v[152:155], v[192:195], v[96:99], v[180:183]\n\t" \
  "ds_read_b128 v[192:195], v8 offset:16384\n\t" \
  "v_cvt_pk_bf16_f32 v114, v116, v117\n\t" \
  "v_mfma_f32_16x16x32_bf16 v[156:159], v[196:199], v[96:99], v[180:183]\n\t" \
  "v_exp_f32_e32 v124, v124\n\t" \
  "v_mfma_f32_16x16x32_bf16 v[148:151], v[196:199], v[80:83], v[176:179]\n\t" \
  "ds_read_b128 v[196:199], v8 offset:17408\n\t" \
  "v_cvt_pk_bf16_f32 v115, v118, v119\n\t" \
  "v_mfma_f32_16x16x32_bf16 v[144:147], v[200:203], v[84:87], v[144:147]\n\t" \
  "v_exp_f32_e32 v125, v125\n\t" \
  "v_mfma_f32_16x16x32_bf16 v[152:155], v[200:203], v[100:103], v[152:155]\n\t" \
  "ds_read_b128 v[200:203], v8 offset:18432\n\t" \
  "v_cvt_pk_bf16_f32 v120, v120, v121\n\t" \
  "v_mfma_f32_16x16x32_bf16 v[156:159], v[204:207], v[100:103], v[156:159]\n\t" \
  "v_exp_f32_e32 v126, v126\n\t" \
  "v_mfma_f32_16x16x32_bf16 v[148:151], v[204:207], v[84:87], v[148:151]\n\t" \
  "ds_read_b128 v[204:207], v8 offset:19456\n\t" \
  "v_cvt_pk_bf16_f32 v121, v122, v123\n\t" \
  "v_mfma_f32_16x16x32_bf16 v[144:147], v[208:211], v[88:91], v[144:147]\n\t" \
  "v_exp_f32_e32 v127, v127\n\t" \
  "v_mfma_f32_16x16x32_bf16 v[152:155], v[208:211], v[104:107], v[152:155]\n\t" \
  "ds_read_b128 v[208:211], v8 offset:20480\n\t" \
  "v_cvt_pk_bf16_f32 v122, v124, v125\n\t" \
  "v_mfma_f32_16x16x32_bf16 v[156:159], v[212:215], v[104:107], v[156:159]\n\t" \
  "v_exp_f32_e32 v128, v128\n\t" \
  "v_mfma_f32_16x16x32_bf16 v[148:151], v[212:215], v[88:91], v[148:151]\n\t" \
  "ds_read_b128 v[212:215], v8 offset:21504\n\t" \
  "v_cvt_pk_bf16_f32 v123, v126, v127\n\t" \
  "s_mov_b32 m0, s42\n\t" \
  "v_mfma_f32_16x16x32_bf16 v[144:147], v[216:219], v[92:95], v[144:147]\n\t" \
  "v_exp_f32_e32 v129, v129\n\t" \
  "v_mfma_f32_16x16x32_bf16 v[152:155], v[216:219], v[108:111], v[152:155]\n\t" \
  "ds_read_b128 v[216:219], v8 offset:22528\n\t" \
  "global_load_lds_dwordx4 v1, s[40:41] offset:0 sc1\n\t" \
  "v_mfma_f32_16x16x32_bf16 v[156:159], v[220:223], v[108:111], v[156:159]\n\t" \
  "v_exp_f32_e32 v130, v130\n\t" \
  "v_mfma_f32_16x16x32_bf16 v[148:151], v[220:223], v[92:95], v[148:151]\n\t" \
  "ds_read_b128 v[220:223], v8 offset:23552\n\t" \
  "global_load_lds_dwordx4 v1, s[40:41] offset:1024 sc1\n\t" \
  "s_waitcnt lgkmcnt(8)\n\t" \
  "v_mfma_f32_16x16x32_bf16 v[160:163], v[224:227], v[80:83], v[176:179]\n\t" \
  "v_exp_f32_e32 v131, v131\n\t" \
  "v_mfma_f32_16x16x32_bf16 v[168:171], v[224:227], v[96:99], v[180:183]\n\t" \
  "ds_read_b128 v[224:227], v8 offset:24576\n\t" \
  "v_cvt_pk_bf16_f32 v128, v128, v129\n\t" \
  "v_mfma_f32_16x16x32_bf16 v[172:175], v[228:231], v[96:99], v[180:183]\n\t" \
  "v_exp_f32_e32 v132, v132\n\t" \
  "v_mfma_f32_16x16x32_bf16 v[164:167], v[228:231], v[80:83], v[176:179]\n\t" \
  "ds_read_b128 v[228:231], v8 offset:25600\n\t" \
  "v_cvt_pk_bf16_f32 v129, v130, v131\n\t" \
  "v_mfma_f32_16x16x32_bf16 v[160:163], v[232:235], v[84:87], v[160:163]\n\t" \
  "v_exp_f32_e32 v133, v133\n\t" \
  "v_mfma_f32_16x16x32_bf16 v[168:171], v[232:235], v[100:103], v[168:171]\n\t" \
  "ds_read_b128 v[232:235], v8 offset:26624\n\t" \
  "v_max3_f32 v10, v144, v145, v146\n\t" \
  "v_mfma_f32_16x16x32_bf16 v[172:175], v[236:239], v[100:103], v[172:175]\n\t" \
  "v_exp_f32_e32 v134, v134\n\t" \
  "v_mfma_f32_16x16x32_bf16 v[164:167], v[236:239], v[84:87], v[164:167]\n\t" \
  "ds_read_b128 v[236:239], v8 offset:27648\n\t" \
  "v_max3_f32 v11, v147, v148, v149\n\t" \
  "v_mfma_f32_16x16x32_bf16 v[160:163], v[240:243], v[88:91], v[160:163]\n\t" \
  "v_exp_f32_e32 v135, v135\n\t" \
  "v_mfma_f32_16x16x32_bf16 v[168:171], v[240:243], v[104:107], v[168:171]\n\t" \
  "ds_read_b128 v[240:243], v8 offset:28672\n\t" \
  "v_max3_f32 v10, v10, v150, v151\n\t" \
  "v_mfma_f32_16x16x32_bf16 v[172:175], v[244:247], v[104:107], v[172:175]\n\t" \
  "v_exp_f32_e32 v136, v136\n\t" \
  "v_mfma_f32_16x16x32_bf16 v[164:167], v[244:247], v[88:91], v[164:167]\n\t" \
  "ds_read_b128 v[244:247], v8 offset:29696\n\t" \
  "v_max3_f32 v11, v11, v152, v153\n\t" \
  "v_mfma_f32_16x16x32_bf16 v[160:163], v[248:251], v[92:95], v[160:163]\n\t" \
  "v_exp_f32_e32 v137, v137\n\t" \
  "v_mfma_f32_16x16x32_bf16 v[168:171], v[248:251], v[108:111], v[168:171]\n\t" \
  "ds_read_b128 v[248:251], v8 offset:30720\n\t" \
  "v_max3_f32 v10, v10, v154, v155\n\t" \
  "v_mfma_f32_16x16x32_bf16 v[172:175], v[252:255], v[108:111], v[172:175]\n\t" \
  "v_exp_f32_e32 v138, v138\n\t" \
  "v_mfma_f32_16x16x32_bf16 v[164:167], v[252:255], v[92:95], v[164:167]\n\t" \
  "ds_read_b128 v[252:255], v8 offset:31744\n\t" \
  "v_max3_f32 v11, v11, v156, v157\n\t" \
  "s_waitcnt lgkmcnt(8)\n\t" \
  "v_mfma_f32_16x16x32_bf16 v[16:19], v[192:195], v[112:115], v[16:19]\n\t" \
  "v_exp_f32_e32 v139, v139\n\t" \
  "v_mfma_f32_16x16x32_bf16 v[24:27], v[192:195], v[120:123], v[24:27]\n\t" \
  "ds_read_b128 v[192:195], v8 offset:0\n\t" \
  "v_max3_f32 v10, v10, v158, v159\n\t" \
  "v_mfma_f32_16x16x32_bf16 v[12:15], v[188:191], v[120:123], v[12:15]\n\t" \
  "v_exp_f32_e32 v140, v140\n\t" \
  "v_mfma_f32_16x16x32_bf16 v[28:31], v[196:199], v[120:123], v[28:31]\n\t" \
  "v_exp_f32_e32 v141, v141\n\t" \
  "v_mfma_f32_16x16x32_bf16 v[20:23], v[196:199], v[112:115], v[20:23]\n\t" \
  "ds_read_b128 v[196:199], v8 offset:4096\n\t" \
  "v_cvt_pk_bf16_f32 v130, v132, v133\n\t" \
  "v_mfma_f32_16x16x32_bf16 v[4:7], v[188:191], v[112:115], v[4:7]\n\t" \
  "v_cvt_pk_bf16_f32 v131, v134, v135\n\t" \
  "v_cvt_pk_bf16_f32 v136, v136, v137\n\t" \
  "v_mfma_f32_16x16x32_bf16 v[32:35], v[200:203], v[112:115], v[32:35]\n\t" \
  "v_exp_f32_e32 v142, v142\n\t" \
  "v_mfma_f32_16x16x32_bf16 v[40:43], v[200:203], v[120:123], v[40:43]\n\t" \
  "ds_read_b128 v[200:203], v8 offset:1024\n\t" \
  "v_max3_f32 v11, v11, v160, v161\n\t" \
  "v_mfma_f32_16x16x32_bf16 v[44:47], v[204:207], v[120:123], v[44:47]\n\t" \
  "v_exp_f32_e32 v143, v143\n\t" \
  "v_mfma_f32_16x16x32_bf16 v[36:39], v[204:207], v[112:115], v[36:39]\n\t" \
  "ds_read_b128 v[204:207], v8 offset:5120\n\t" \
  "v_max3_f32 v10, v10, v162, v163\n\t" \
  "v_mfma_f32_16x16x32_bf16 v[48:51], v[208:211], v[112:115], v[48:51]\n\t" \
  "v_max3_f32 v11, v11, v164, v165\n\t" \
  "v_max3_f32 v10, v10, v166, v167\n\t" \
  "v_mfma_f32_16x16x32_bf16 v[56:59], v[208:211], v[120:123], v[56:59]\n\t" \
  "ds_read_b128 v[208:211], v8 offset:2048\n\t" \
  "v_cvt_pk_bf16_f32 v137, v138, v139\n\t" \
  "v_mfma_f32_16x16x32_bf16 v[60:63], v[212:215], v[120:123], v[60:63]\n\t" \
  "v_max3_f32 v11, v11, v168, v169\n\t" \
  "v_max3_f32 v10, v10, v170, v171\n\t" \
  "v_mfma_f32_16x16x32_bf16 v[52:55], v[212:215], v[112:115], v[52:55]\n\t" \
  "ds_read_b128 v[212:215], v8 offset:6144\n\t" \
  "v_cvt_pk_bf16_f32 v138, v140, v141\n\t" \
  "v_mfma_f32_16x16x32_bf16 v[64:67], v[216:219], v[112:115], v[64:67]\n\t" \
  "v_max3_f32 v11, v11, v172, v173\n\t" \
  "v_max3_f32 v10, v10, v174, v175\n\t" \
  "v_mfma_f32_16x16x32_bf16 v[72:75], v[216:219], v[120:123], v[72:75]\n\t" \
  "ds_read_b128 v[216:219], v8 offset:3072\n\t" \
  "v_cvt_pk_bf16_f32 v139, v142, v143\n\t" \
  "v_max_f32_e32 v10, v10, v11\n\t" \
  "v_mfma_f32_16x16x32_bf16 v[76:79], v[220:223], v[120:123], v[76:79]\n\t" \
  "v_cmp_lt_f32_e32 vcc, s45, v10\n\t" \
  "global_load_lds_dwordx4 v1, s[40:41] offset:2048 sc1\n\t" \
  "v_mfma_f32_16x16x32_bf16 v[68:71], v[220:223], v[112:115], v[68:71]\n\t" \
  "ds_read_b128 v[220:223], v8 offset:7168\n\t" \
  "s_cbranch_vccnz RASa_%=\n\t" \
  "RArSa_%=:\n\t" \
  "v_exp_f32_e32 v144, v144\n\t" \
  "s_waitcnt lgkmcnt(8)\n\t" \
  "v_mfma_f32_16x16x32_bf16 v[16:19], v[224:227], v[128:131], v[16:19]\n\t" \
  "v_exp_f32_e32 v145, v145\n\t" \
  "v_mfma_f32_16x16x32_bf16 v[24:27], v[224:227], v[136:139], v[24:27]\n\t" \
  "ds_read_b128 v[224:227], v8 offset:8192\n\t" \
  "global_load_lds_dwordx4 v1, s[40:41] offset:3072 sc1\n\t" \
  "s_add_u32 m0, s42, 0x1000\n\t" \
  "v_mfma_f32_16x16x32_bf16 v[28:31], v[228:231], v[136:139], v[28:31]\n\t" \
  "v_exp_f32_e32 v146, v146\n\t" \
  "v_mfma_f32_16x16x32_bf16 v[20:23], v[228:231], v[128:131], v[20:23]\n\t" \
  "ds_read_b128 v[228:231], v8 offset:12288\n\t" \
  "global_load_lds_dwordx4 v9, s[40:41] offset:0 sc1\n\t" \
  "v_mfma_f32_16x16x32_bf16 v[4:7], v[188:191], v[128:131], v[4:7]\n\t" \
  "v_exp_f32_e32 v147, v147\n\t" \
  "v_mfma_f32_16x16x32_bf16 v[32:35], v[232:235], v[128:131], v[32:35]\n\t" \
  "v_exp_f32_e32 v148, v148\n\t" \
  "v_mfma_f32_16x16x32_bf16 v[40:43], v[232:235], v[136:139], v[40:43]\n\t" \
  "ds_read_b128 v[232:235], v8 offset:9216\n\t" \
  "global_load_lds_dwordx4 v9, s[40:41] offset:1024 sc1\n\t" \
  "v_mfma_f32_16x16x32_bf16 v[44:47], v[236:239], v[136:139], v[44:47]\n\t" \
  "v_exp_f32_e32 v149, v149\n\t" \
  "v_mfma_f32_16x16x32_bf16 v[36:39], v[236:239], v[128:131], v[36:39]\n\t" \
  "ds_read_b128 v[236:239], v8 offset:13312\n\t" \
  "global_load_lds_dwordx4 v9, s[40:41] offset:2048 sc1\n\t" \
  "v_mfma_f32_16x16x32_bf16 v[48:51], v[240:243], v[128:131], v[48:51]\n\t" \
  "v_exp_f32_e32 v150, v150\n\t" \
  "v_mfma_f32_16x16x32_bf16 v[56:59], v[240:243], v[136:139], v[56:59]\n\t" \
  "ds_read_b128 v[240:243], v8 offset:10240\n\t" \
  "global_load_lds_dwordx4 v9, s[40:41] offset:3072 sc1\n\t" \
  "v_mfma_f32_16x16x32_bf16 v[12:15], v[188:191], v[136:139], v[12:15]\n\t" \
  "v_exp_f32_e32 v151, v151\n\t" \
  "v_mfma_f32_16x16x32_bf16 v[60:63], v[244:247], v[136:139], v[60:63]\n\t" \
  "v_exp_f32_e32 v152, v152\n\t" \
  "v_mfma_f32_16x16x32_bf16 v[52:55], v[244:247], v[128:131], v[52:55]\n\t" \
  "ds_read_b128 v[244:247], v8 offset:14336\n\t" \
  "v_cvt_pk_bf16_f32 v144, v144, v145\n\t" \
  "s_add_u32 s40, s40, 0x8000\n\t" \
  "s_addc_u32 s41, s41, 0\n\t" \
  "v_mfma_f32_16x16x32_bf16 v[64:67], v[248:251], v[128:131], v[64:67]\n\t" \
  "v_exp_f32_e32 v153, v153\n\t" \
  "v_mfma_f32_16x16x32_bf16 v[72:75], v[248:251], v[136:139], v[72:75]\n\t" \
  "ds_read_b128 v[248:251], v8 offset:11264\n\t" \
  "v_cvt_pk_bf16_f32 v145, v146, v147\n\t" \
  "s_add_u32 s42, s42, 0x8000\n\t" \
  "s_and_b32 s42, s42, 0x1ffff\n\t" \
  "v_mfma_f32_16x16x32_bf16 v[76:79], v[252:255], v[136:139], v[76:79]\n\t" \
  "v_exp_f32_e32 v154, v154\n\t" \
  "v_mfma_f32_16x16x32_bf16 v[68:71], v[252:255], v[128:131], v[68:71]\n\t" \
  "ds_read_b128 v[252:255], v8 offset:15360\n\t" \
  "s_cmp_lg_u32 s44, 0\n\t" \
  "s_cbranch_scc1 RBSa_%=\n\t" \
  "RBrSa_%=:\n\t" \
  "s_cmp_eq_u32 s43, 1\n\t" \
  "s_cbranch_scc1 HHS_%=\n\t" \
  "HBS_%=:\n\t" \
  "s_cmp_eq_u32 s43, s58\n\t" \
  "s_cbranch_scc1 TES_%=\n\t" \
  "s_waitcnt vmcnt(8) lgkmcnt(8)\n\t" \
  "v_mfma_f32_16x16x32_bf16 v[112:115], v[192:195], v[80:83], v[176:179]\n\t" \
  "s_barrier\n\t" \
  "v_exp_f32_e32 v155, v155\n\t" \
  "v_mfma_f32_16x16x32_bf16 v[120:123], v[192:195], v[96:99], v[180:183]\n\t" \
  "ds_read_b128 v[192:195], v8 offset:49152\n\t" \
  "v_cvt_pk_bf16_f32 v146, v148, v149\n\t" \
  "v_mfma_f32_16x16x32_bf16 v[124:127], v[196:199], v[96:99], v[180:183]\n\t" \
  "v_exp_f32_e32 v156, v156\n\t" \
  "v_mfma_f32_16x16x32_bf16 v[116:119], v[196:199], v[80:83], v[176:179]\n\t" \
  "ds_read_b128 v[196:199], v8 offset:50176\n\t" \
  "v_cvt_pk_bf16_f32 v147, v150, v151\n\t" \
  "v_mfma_f32_16x16x32_bf16 v[112:115], v[200:203], v[84:87], v[112:115]\n\t" \
  "v_exp_f32_e32 v157, v157\n\t" \
  "v_mfma_f32_16x16x32_bf16 v[120:123], v[200:203], v[100:103], v[120:123]\n\t" \
  "ds_read_b128 v[200:203], v8 offset:51200\n\t" \
  "v_cvt_pk_bf16_f32 v152, v152, v153\n\t" \
  "v_mfma_f32_16x16x32_bf16 v[124:127], v[204:207], v[100:103], v[124:127]\n\t" \
  "v_exp_f32_e32 v158, v158\n\t" \
  "v_mfma_f32_16x16x32_bf16 v[116:119], v[204:207], v[84:87], v[116:119]\n\t" \
  "ds_read_b128 v[204:207], v8 offset:52224\n\t" \
  "v_cvt_pk_bf16_f32 v153, v154, v155\n\t" \
  "v_mfma_f32_16x16x32_bf16 v[112:115], v[208:211], v[88:91], v[112:115]\n\t" \
  "v_exp_f32_e32 v159, v159\n\t" \
  "v_mfma_f32_16x16x32_bf16 v[120:123], v[208:211], v[104:107], v[120:123]\n\t" \
  "ds_read_b128 v[208:211], v8 offset:53248\n\t" \
  "v_cvt_pk_bf16_f32 v154, v156, v157\n\t" \
  "v_mfma_f32_16x16x32_bf16 v[124:127], v[212:215], v[104:107], v[124:127]\n\t" \
  "v_exp_f32_e32 v160, v160\n\t" \
  "v_mfma_f32_16x16x32_bf16 v[116:119], v[212:215], v[88:91], v[116:119]\n\t" \
  "ds_read_b128 v[212:215], v8 offset:54272\n\t" \
  "v_cvt_pk_bf16_f32 v155, v158, v159\n\t" \
  "s_mov_b32 m0, s42\n\t" \
  "v_mfma_f32_16x16x32_bf16 v[112:115], v[216:219], v[92:95], v[112:115]\n\t" \
  "v_exp_f32_e32 v161, v161\n\t" \
  "v_mfma_f32_16x16x32_bf16 v[120:123], v[216:219], v[108:111], v[120:123]\n\t" \
  "ds_read_b128 v[216:219], v8 offset:55296\n\t" \
  "global_load_lds_dwordx4 v1, s[40:41] offset:0 sc1\n\t" \
  "v_mfma_f32_16x16x32_bf16 v[124:127], v[220:223], v[108:111], v[124:127]\n\t" \
  "v_exp_f32_e32 v162, v162\n\t" \
  "v_mfma_f32_16x16x32_bf16 v[116:119], v[220:223], v[92:95], v[116:119]\n\t" \
  "ds_read_b128 v[220:223], v8 offset:56320\n\t" \
  "global_load_lds_dwordx4 v1, s[40:41] offset:1024 sc1\n\t" \
  "s_waitcnt lgkmcnt(8)\n\t" \
  "v_mfma_f32_16x16x32_bf16 v[128:131], v[224:227], v[80:83], v[176:179]\n\t" \
  "v_exp_f32_e32 v163, v163\n\t" \
  "v_mfma_f32_16x16x32_bf16 v[136:139], v[224:227], v[96:99], v[180:183]\n\t" \
  "ds_read_b128 v[224:227], v8 offset:57344\n\t" \
  "v_cvt_pk_bf16_f32 v160, v160, v161\n\t" \
  "v_mfma_f32_16x16x32_bf16 v[140:143], v[228:231], v[96:99], v[180:183]\n\t" \
  "v_exp_f32_e32 v164, v164\n\t" \
  "v_mfma_f32_16x16x32_bf16 v[132:135], v[228:231], v[80:83], v[176:179]\n\t" \
  "ds_read_b128 v[228:231], v8 offset:58368\n\t" \
  "v_cvt_pk_bf16_f32 v161, v162, v163\n\t" \
  "v_mfma_f32_16x16x32_bf16 v[128:131], v[232:235], v[84:87], v[128:131]\n\t" \
  "v_exp_f32_e32 v165, v165\n\t" \
  "v_mfma_f32_16x16x32_bf16 v[136:139], v[232:235], v[100:103], v[136:139]\n\t" \
  "ds_read_b128 v[232:235], v8 offset:59392\n\t" \
  "v_max3_f32 v10, v112, v113, v114\n\t" \
  "v_mfma_f32_16x16x32_bf16 v[140:143], v[236:239], v[100:103], v[140:143]\n\t" \
  "v_exp_f32_e32 v166, v166\n\t" \
  "v_mfma_f32_16x16x32_bf16 v[132:135], v[236:239], v[84:87], v[132:135]\n\t" \
  "ds_read_b128 v[236:239], v8 offset:60416\n\t" \
  "v_max3_f32 v11, v115, v116, v117\n\t" \
  "v_mfma_f32_16x16x32_bf16 v[128:131], v[240:243], v[88:91], v[128:131]\n\t" \
  "v_exp_f32_e32 v167, v167\n\t" \
  "v_mfma_f32_16x16x32_bf16 v[136:139], v[240:243], v[104:107], v[136:139]\n\t" \
  "ds_read_b128 v[240:243], v8 offset:61440\n\t" \
  "v_max3_f32 v10, v10, v118, v119\n\t" \
  "v_mfma_f32_16x16x32_bf16 v[140:143], v[244:247], v[104:107], v[140:143]\n\t" \
  "v_exp_f32_e32 v168, v168\n\t" \
  "v_mfma_f32_16x16x32_bf16 v[132:135], v[244:247], v[88:91], v[132:135]\n\t" \
  "ds_read_b128 v[244:247], v8 offset:62464\n\t" \
  "v_max3_f32 v11, v11, v120, v121\n\t" \
  "v_mfma_f32_16x16x32_bf16 v[128:131], v[248:251], v[92:95], v[128:131]\n\t" \
  "v_exp_f32_e32 v169, v169\n\t" \
  "v_mfma_f32_16x16x32_bf16 v[136:139], v[248:251], v[108:111], v[136:139]\n\t" \
  "ds_read_b128 v[248:251], v8 offset:63488\n\t" \
  "v_max3_f32 v10, v10, v122, v123\n\t" \
  "v_mfma_f32_16x16x32_bf16 v[140:143], v[252:255], v[108:111], v[140:143]\n\t" \
  "v_exp_f32_e32 v170, v170\n\t" \
  "v_mfma_f32_16x16x32_bf16 v[132:135], v[252:255], v[92:95], v[132:135]\n\t" \
  "ds_read_b128 v[252:255], v8 offset:64512\n\t" \
  "v_max3_f32 v11, v11, v124, v125\n\t" \
  "s_waitcnt lgkmcnt(8)\n\t" \
  "v_mfma_f32_16x16x32_bf16 v[16:19], v[192:195], v[144:147], v[16:19]\n\t" \
  "v_exp_f32_e32 v171, v171\n\t" \
  "v_mfma_f32_16x16x32_bf16 v[24:27], v[192:195], v[152:155], v[24:27]\n\t" \
  "ds_read_b128 v[192:195], v8 offset:32768\n\t" \
  "v_max3_f32 v10, v10, v126, v127\n\t" \
  "v_mfma_f32_16x16x32_bf16 v[12:15], v[188:191], v[152:155], v[12:15]\n\t" \
  "v_exp_f32_e32 v172, v172\n\t" \
  "v_mfma_f32_16x16x32_bf16 v[28:31], v[196:199], v[152:155], v[28:31]\n\t" \
  "v_exp_f32_e32 v173, v173\n\t" \
  "v_mfma_f32_16x16x32_bf16 v[20:23], v[196:199], v[144:147], v[20:23]\n\t" \
  "ds_read_b128 v[196:199], v8 offset:36864\n\t" \
  "v_cvt_pk_bf16_f32 v162, v164, v165\n\t" \
  "v_mfma_f32_16x16x32_bf16 v[4:7], v[188:191], v[144:147], v[4:7]\n\t" \
  "v_cvt_pk_bf16_f32 v163, v166, v167\n\t" \
  "v_cvt_pk_bf16_f32 v168, v168, v169\n\t" \
  "v_mfma_f32_16x16x32_bf16 v[32:35], v[200:203], v[144:147], v[32:35]\n\t" \
  "v_exp_f32_e32 v174, v174\n\t" \
  "v_mfma_f32_16x16x32_bf16 v[40:43], v[200:203], v[152:155], v[40:43]\n\t" \
  "ds_read_b128 v[200:203], v8 offset:33792\n\t" \
  "v_max3_f32 v11, v11, v128, v129\n\t" \
  "v_mfma_f32_16x16x32_bf16 v[44:47], v[204:207], v[152:155], v[44:47]\n\t" \
  "v_exp_f32_e32 v175, v175\n\t" \
  "v_mfma_f32_16x16x32_bf16 v[36:39], v[204:207], v[144:147], v[36:39]\n\t" \
  "ds_read_b128 v[204:207], v8 offset:37888\n\t" \
  "v_max3_f32 v10, v10, v130, v131\n\t" \
  "v_mfma_f32_16x16x32_bf16 v[48:51], v[208:211], v[144:147], v[48:51]\n\t" \
  "v_max3_f32 v11, v11, v132, v133\n\t" \
  "v_max3_f32 v10, v10, v134, v135\n\t" \
  "v_mfma_f32_16x16x32_bf16 v[56:59], v[208:211], v[152:155], v[56:59]\n\t" \
  "ds_read_b128 v[208:211], v8 offset:34816\n\t" \
  "v_cvt_pk_bf16_f32 v169, v170, v171\n\t" \
  "v_mfma_f32_16x16x32_bf16 v[60:63], v[212:215], v[152:155], v[60:63]\n\t" \
  "v_max3_f32 v11, v11, v136, v137\n\t" \
  "v_max3_f32 v10, v10, v138, v139\n\t" \
  "v_mfma_f32_16x16x32_bf16 v[52:55], v[212:215], v[144:147], v[52:55]\n\t" \
  "ds_read_b128 v[212:215], v8 offset:38912\n\t" \
  "v_cvt_pk_bf16_f32 v170, v172, v173\n\t" \
  "v_mfma_f32_16x16x32_bf16 v[64:67], v[216:219], v[144:147], v[64:67]\n\t" \
  "v_max3_f32 v11, v11, v140, v141\n\t" \
  "v_max3_f32 v10, v10, v142, v143\n\t" \
  "v_mfma_f32_16x16x32_bf16 v[72:75], v[216:219], v[152:155], v[72:75]\n\t" \
  "ds_read_b128 v[216:219], v8 offset:35840\n\t" \
  "v_cvt_pk_bf16_f32 v171, v174, v175\n\t" \
  "v_max_f32_e32 v10, v10, v11\n\t" \
  "v_mfma_f32_16x16x32_bf16 v[76:79], v[220:223], v[152:155], v[76:79]\n\t" \
  "v_cmp_lt_f32_e32 vcc, s45, v10\n\t" \
  "global_load_lds_dwordx4 v1, s[40:41] offset:2048 sc1\n\t" \
  "v_mfma_f32_16x16x32_bf16 v[68:71], v[220:223], v[144:147], v[68:71]\n\t" \
  "ds_read_b128 v[220:223], v8 offset:39936\n\t" \
  "s_cbranch_vccnz RASb_%=\n\t" \
  "RArSb_%=:\n\t" \
  "v_exp_f32_e32 v112, v112\n\t" \
  "s_waitcnt lgkmcnt(8)\n\t" \
  "v_mfma_f32_16x16x32_bf16 v[16:19], v[224:227], v[160:163], v[16:19]\n\t" \
  "v_exp_f32_e32 v113, v113\n\t" \
  "v_mfma_f32_16x16x32_bf16 v[24:27], v[224:227], v[168:171], v[24:27]\n\t" \
  "ds_read_b128 v[224:227], v8 offset:40960\n\t" \
  "global_load_lds_dwordx4 v1, s[40:41] offset:3072 sc1\n\t" \
  "s_add_u32 m0, s42, 0x1000\n\t" \
  "v_mfma_f32_16x16x32_bf16 v[28:31], v[228:231], v[168:171], v[28:31]\n\t" \
  "v_exp_f32_e32 v114, v114\n\t" \
  "v_mfma_f32_16x16x32_bf16 v[20:23], v[228:231], v[160:163], v[20:23]\n\t" \
  "ds_read_b128 v[228:231], v8 offset:45056\n\t" \
  "global_load_lds_dwordx4 v9, s[40:41] offset:0 sc1\n\t" \
  "v_mfma_f32_16x16x32_bf16 v[4:7], v[188:191], v[160:163], v[4:7]\n\t" \
  "v_exp_f32_e32 v115, v115\n\t" \
  "v_mfma_f32_16x16x32_bf16 v[32:35], v[232:235], v[160:163], v[32:35]\n\t" \
  "v_exp_f32_e32 v116, v116\n\t" \
  "v_mfma_f32_16x16x32_bf16 v[40:43], v[232:235], v[168:171], v[40:43]\n\t" \
  "ds_read_b128 v[232:235], v8 offset:41984\n\t" \
  "global_load_lds_dwordx4 v9, s[40:41] offset:1024 sc1\n\t" \
  "v_mfma_f32_16x16x32_bf16 v[44:47], v[236:239], v[168:171], v[44:47]\n\t" \
  "v_exp_f32_e32 v117, v117\n\t" \
  "v_mfma_f32_16x16x32_bf16 v[36:39], v[236:239], v[160:163], v[36:39]\n\t" \
  "ds_read_b128 v[236:239], v8 offset:46080\n\t" \
  "global_load_lds_dwordx4 v9, s[40:41] offset:2048 sc1\n\t" \
  "v_mfma_f32_16x16x32_bf16 v[48:51], v[240:243], v[160:163], v[48:51]\n\t" \
  "v_exp_f32_e32 v118, v118\n\t" \
  "v_mfma_f32_16x16x32_bf16 v[56:59], v[240:243], v[168:171], v[56:59]\n\t" \
  "ds_read_b128 v[240:243], v8 offset:43008\n\t" \
  "global_load_lds_dwordx4 v9, s[40:41] offset:3072 sc1\n\t" \
  "v_mfma_f32_16x16x32_bf16 v[12:15], v[188:191], v[168:171], v[12:15]\n\t" \
  "v_exp_f32_e32 v119, v119\n\t" \
  "v_mfma_f32_16x16x32_bf16 v[60:63], v[244:247], v[168:171], v[60:63]\n\t" \
  "v_exp_f32_e32 v120, v120\n\t" \
  "v_mfma_f32_16x16x32_bf16 v[52:55], v[244:247], v[160:163], v[52:55]\n\t" \
  "ds_read_b128 v[244:247], v8 offset:47104\n\t" \
  "v_cvt_pk_bf16_f32 v112, v112, v113\n\t" \
  "s_add_u32 s40, s40, 0x8000\n\t" \
  "s_addc_u32 s41, s41, 0\n\t" \
  "v_mfma_f32_16x16x32_bf16 v[64:67], v[248:251], v[160:163], v[64:67]\n\t" \
  "v_exp_f32_e32 v121, v121\n\t" \
  "v_mfma_f32_16x16x32_bf16 v[72:75], v[248:251], v[168:171], v[72:75]\n\t" \
  "ds_read_b128 v[248:251], v8 offset:44032\n\t" \
  "v_cvt_pk_bf16_f32 v113, v114, v115\n\t" \
  "s_add_u32 s42, s42, 0x8000\n\t" \
  "s_and_b32 s42, s42, 0x1ffff\n\t" \
  "v_mfma_f32_16x16x32_bf16 v[76:79], v[252:255], v[168:171], v[76:79]\n\t" \
  "v_exp_f32_e32 v122, v122\n\t" \
  "v_mfma_f32_16x16x32_bf16 v[68:71], v[252:255], v[160:163], v[68:71]\n\t" \
  "ds_read_b128 v[252:255], v8 offset:48128\n\t" \
  "s_cmp_lg_u32 s44, 0\n\t" \
  "s_cbranch_scc1 RBSb_%=\n\t" \
  "RBrSb_%=:\n\t" \
  "s_add_u32 s43, s43, 2\n\t" \
  "s_branch LPS_%=\n\t" \
  "TES_%=:\n\t" \
  "s_waitcnt vmcnt(8) lgkmcnt(8)\n\t" \
  "v_mfma_f32_16x16x32_bf16 v[112:115], v[192:195], v[80:83], v[176:179]\n\t" \
  "s_barrier\n\t" \
  "v_exp_f32_e32 v155, v155\n\t" \
  "v_mfma_f32_16x16x32_bf16 v[120:123], v[192:195], v[96:99], v[180:183]\n\t" \
  "ds_read_b128 v[192:195], v8 offset:49152\n\t" \
  "v_cvt_pk_bf16_f32 v146, v148, v149\n\t" \
  "v_mfma_f32_16x16x32_bf16 v[124:127], v[196:199], v[96:99], v[180:183]\n\t" \
  "v_exp_f32_e32 v156, v156\n\t" \
  "v_mfma_f32_16x16x32_bf16 v[116:119], v[196:199], v[80:83], v[176:179]\n\t" \
  "ds_read_b128 v[196:199], v8 offset:50176\n\t" \
  "v_cvt_pk_bf16_f32 v147, v150, v151\n\t" \
  "v_mfma_f32_16x16x32_bf16 v[112:115], v[200:203], v[84:87], v[112:115]\n\t" \
  "v_exp_f32_e32 v157, v157\n\t" \
  "v_mfma_f32_16x16x32_bf16 v[120:123], v[200:203], v[100:103], v[120:123]\n\t" \
  "ds_read_b128 v[200:203], v8 offset:51200\n\t" \
  "v_cvt_pk_bf16_f32 v152, v152, v153\n\t" \
  "v_mfma_f32_16x16x32_bf16 v[124:127], v[204:207], v[100:103], v[124:127]\n\t" \
  "v_exp_f32_e32 v158, v158\n\t" \
  "v_mfma_f32_16x16x32_bf16 v[116:119], v[204:207], v[84:87], v[116:119]\n\t" \
  "ds_read_b128 v[204:207], v8 offset:52224\n\t" \
  "v_cvt_pk_bf16_f32 v153, v154, v155\n\t" \
  "v_mfma_f32_16x16x32_bf16 v[112:115], v[208:211], v[88:91], v[112:115]\n\t" \
  "v_exp_f32_e32 v159, v159\n\t" \
  "v_mfma_f32_16x16x32_bf16 v[120:123], v[208:211], v[104:107], v[120:123]\n\t" \
  "ds_read_b128 v[208:211], v8 offset:53248\n\t" \
  "v_cvt_pk_bf16_f32 v154, v156, v157\n\t" \
  "v_mfma_f32_16x16x32_bf16 v[124:127], v[212:215], v[104:107], v[124:127]\n\t" \
  "v_exp_f32_e32 v160, v160\n\t" \
  "v_mfma_f32_16x16x32_bf16 v[116:119], v[212:215], v[88:91], v[116:119]\n\t" \
  "ds_read_b128 v[212:215], v8 offset:54272\n\t" \
  "v_cvt_pk_bf16_f32 v155, v158, v159\n\t" \
  "s_mov_b32 m0, s42\n\t" \
  "v_mfma_f32_16x16x32_bf16 v[112:115], v[216:219], v[92:95], v[112:115]\n\t" \
  "v_exp_f32_e32 v161, v161\n\t" \
  "v_mfma_f32_16x16x32_bf16 v[120:123], v[216:219], v[108:111], v[120:123]\n\t" \
  "ds_read_b128 v[216:219], v8 offset:55296\n\t" \
  "v_cmp_gt_i32_e64 s[48:49], 0, v2\n\t" \
  "v_cmp_gt_i32_e64 s[50:51], 1, v2\n\t" \
  "global_load_lds_dwordx4 v1, s[40:41] offset:0 sc1\n\t" \
  "v_mfma_f32_16x16x32_bf16 v[124:127], v[220:223], v[108:111], v[124:127]\n\t" \
  "v_exp_f32_e32 v162, v162\n\t" \
  "v_mfma_f32_16x16x32_bf16 v[116:119], v[220:223], v[92:95], v[116:119]\n\t" \
  "ds_read_b128 v[220:223], v8 offset:56320\n\t" \
  "v_cmp_gt_i32_e64 s[52:53], 2, v2\n\t" \
  "v_cmp_gt_i32_e64 s[54:55], 3, v2\n\t" \
  "global_load_lds_dwordx4 v1, s[40:41] offset:1024 sc1\n\t" \
  "s_waitcnt lgkmcnt(8)\n\t" \
  "v_mfma_f32_16x16x32_bf16 v[128:131], v[224:227], v[80:83], v[176:179]\n\t" \
  "v_exp_f32_e32 v163, v163\n\t" \
  "v_mfma_f32_16x16x32_bf16 v[136:139], v[224:227], v[96:99], v[180:183]\n\t" \
  "ds_read_b128 v[224:227], v8 offset:57344\n\t" \
  "v_cvt_pk_bf16_f32 v160, v160, v161\n\t" \
  "v_mfma_f32_16x16x32_bf16 v[140:143], v[228:231], v[96:99], v[180:183]\n\t" \
  "v_exp_f32_e32 v164, v164\n\t" \
  "v_mfma_f32_16x16x32_bf16 v[132:135], v[228:231], v[80:83], v[176:179]\n\t" \
  "ds_read_b128 v[228:231], v8 offset:58368\n\t" \
  "v_cvt_pk_bf16_f32 v161, v162, v163\n\t" \
  "v_mfma_f32_16x16x32_bf16 v[128:131], v[232:235], v[84:87], v[128:131]\n\t" \
  "v_exp_f32_e32 v165, v165\n\t" \
  "v_mfma_f32_16x16x32_bf16 v[136:139], v[232:235], v[100:103], v[136:139]\n\t" \
  "ds_read_b128 v[232:235], v8 offset:59392\n\t" \
  "s_cmp_gt_u32 s63, 0\n\t" \
  "s_cbranch_scc1 MX7_%=\n\t" \
  "v_mov_b32_e32 v116, v184\n\t" \
  "v_mov_b32_e32 v117, v184\n\t" \
  "v_mov_b32_e32 v118, v184\n\t" \
  "v_mov_b32_e32 v119, v184\n\t" \
  "v_cndmask_b32_e64 v112, v112, v184, s[48:49]\n\t" \
  "v_cndmask_b32_e64 v113, v113, v184, s[50:51]\n\t" \
  "v_cndmask_b32_e64 v114, v114, v184, s[52:53]\n\t" \
  "v_cndmask_b32_e64 v115, v115, v184, s[54:55]\n\t" \
  "v_cndmask_b32_e64 v124, v124, v184, s[48:49]\n\t" \
  "v_cndmask_b32_e64 v125, v125, v184, s[50:51]\n\t" \
  "v_cndmask_b32_e64 v126, v126, v184, s[52:53]\n\t" \
  "v_cndmask_b32_e64 v127, v127, v184, s[54:55]\n\t" \
  "MX7_%=:\n\t" \
  "v_max3_f32 v10, v112, v113, v114\n\t" \
  "v_mfma_f32_16x16x32_bf16 v[140:143], v[236:239], v[100:103], v[140:143]\n\t" \
  "v_exp_f32_e32 v166, v166\n\t" \
  "v_mfma_f32_16x16x32_bf16 v[132:135], v[236:239], v[84:87], v[132:135]\n\t" \
  "ds_read_b128 v[236:239], v8 offset:60416\n\t" \
  "v_max3_f32 v11, v115, v116, v117\n\t" \
  "v_mfma_f32_16x16x32_bf16 v[128:131], v[240:243], v[88:91], v[128:131]\n\t" \
  "v_exp_f32_e32 v167, v167\n\t" \
  "v_mfma_f32_16x16x32_bf16 v[136:139], v[240:243], v[104:107], v[136:139]\n\t" \
  "ds_read_b128 v[240:243], v8 offset:61440\n\t" \
  "v_max3_f32 v10, v10, v118, v119\n\t" \
  "v_mfma_f32_16x16x32_bf16 v[140:143], v[244:247], v[104:107], v[140:143]\n\t" \
  "v_exp_f32_e32 v168, v168\n\t" \
  "v_mfma_f32_16x16x32_bf16 v[132:135], v[244:247], v[88:91], v[132:135]\n\t" \
  "ds_read_b128 v[244:247], v8 offset:62464\n\t" \
  "v_max3_f32 v11, v11, v120, v121\n\t" \
  "v_mfma_f32_16x16x32_bf16 v[128:131], v[248:251], v[92:95], v[128:131]\n\t" \
  "v_exp_f32_e32 v169, v169\n\t" \
  "v_mfma_f32_16x16x32_bf16 v[136:139], v[248:251], v[108:111], v[136:139]\n\t" \
  "ds_read_b128 v[248:251], v8 offset:63488\n\t" \
  "v_max3_f32 v10, v10, v122, v123\n\t" \
  "v_mfma_f32_16x16x32_bf16 v[140:143], v[252:255], v[108:111], v[140:143]\n\t" \
  "v_exp_f32_e32 v170, v170\n\t" \
  "v_mfma_f32_16x16x32_bf16 v[132:135], v[252:255], v[92:95], v[132:135]\n\t" \
  "ds_read_b128 v[252:255], v8 offset:64512\n\t" \
  "v_max3_f32 v11, v11, v124, v125\n\t" \
  "s_waitcnt lgkmcnt(8)\n\t" \
  "v_mfma_f32_16x16x32_bf16 v[16:19], v[192:195], v[144:147], v[16:19]\n\t" \
  "v_exp_f32_e32 v171, v171\n\t" \
  "v_mfma_f32_16x16x32_bf16 v[24:27], v[192:195], v[152:155], v[24:27]\n\t" \
  "ds_read_b128 v[192:195], v8 offset:32768\n\t" \
  "v_max3_f32 v10, v10, v126, v127\n\t" \
  "v_mfma_f32_16x16x32_bf16 v[12:15], v[188:191], v[152:155], v[12:15]\n\t" \
  "v_exp_f32_e32 v172, v172\n\t" \
  "v_mfma_f32_16x16x32_bf16 v[28:31], v[196:199], v[152:155], v[28:31]\n\t" \
  "v_exp_f32_e32 v173, v173\n\t" \
  "v_mfma_f32_16x16x32_bf16 v[20:23], v[196:199], v[144:147], v[20:23]\n\t" \
  "ds_read_b128 v[196:199], v8 offset:36864\n\t" \
  "v_cvt_pk_bf16_f32 v162, v164, v165\n\t" \
  "v_mfma_f32_16x16x32_bf16 v[4:7], v[188:191], v[144:147], v[4:7]\n\t" \
  "v_cvt_pk_bf16_f32 v163, v166, v167\n\t" \
  "v_cvt_pk_bf16_f32 v168, v168, v169\n\t" \
  "v_mfma_f32_16x16x32_bf16 v[32:35], v[200:203], v[144:147], v[32:35]\n\t" \
  "v_exp_f32_e32 v174, v174\n\t" \
  "v_mfma_f32_16x16x32_bf16 v[40:43], v[200:203], v[152:155], v[40:43]\n\t" \
  "ds_read_b128 v[200:203], v8 offset:33792\n\t" \
  "s_cmp_gt_u32 s63, 1\n\t" \
  "s_cbranch_scc1 MX8_%=\n\t" \
  "v_mov_b32_e32 v132, v184\n\t" \
  "v_mov_b32_e32 v133, v184\n\t" \
  "v_mov_b32_e32 v134, v184\n\t" \
  "v_mov_b32_e32 v135, v184\n\t" \
  "s_cmp_eq_u32 s63, 1\n\t" \
  "s_cbranch_scc1 MD8_%=\n\t" \
  "v_mov_b32_e32 v128, v184\n\t" \
  "v_mov_b32_e32 v129, v184\n\t" \
  "v_mov_b32_e32 v130, v184\n\t" \
  "v_mov_b32_e32 v131, v184\n\t" \
  "v_mov_b32_e32 v136, v184\n\t" \
  "v_mov_b32_e32 v137, v184\n\t" \
  "v_mov_b32_e32 v138, v184\n\t" \
  "v_mov_b32_e32 v139, v184\n\t" \
  "v_mov_b32_e32 v140, v184\n\t" \
  "v_mov_b32_e32 v141, v184\n\t" \
  "v_mov_b32_e32 v142, v184\n\t" \
  "v_mov_b32_e32 v143, v184\n\t" \
  "s_branch MX8_%=\n\t" \
  "MD8_%=:\n\t" \
  "v_cndmask_b32_e64 v128, v128, v184, s[48:49]\n\t" \
  "v_cndmask_b32_e64 v129, v129, v184, s[50:51]\n\t" \
  "v_cndmask_b32_e64 v130, v130, v184, s[52:53]\n\t" \
  "v_cndmask_b32_e64 v131, v131, v184, s[54:55]\n\t" \
  "v_cndmask_b32_e64 v140, v140, v184, s[48:49]\n\t" \
  "v_cndmask_b32_e64 v141, v141, v184, s[50:51]\n\t" \
  "v_cndmask_b32_e64 v142, v142, v184, s[52:53]\n\t" \
  "v_cndmask_b32_e64 v143, v143, v184, s[54:55]\n\t" \
  "MX8_%=:\n\t" \
  "v_max3_f32 v11, v11, v128, v129\n\t" \
  "v_mfma_f32_16x16x32_bf16 v[44:47], v[204:207], v[152:155], v[44:47]\n\t" \
  "v_exp_f32_e32 v175, v175\n\t" \
  "v_mfma_f32_16x16x32_bf16 v[36:39], v[204:207], v[144:147], v[36:39]\n\t" \
  "ds_read_b128 v[204:207], v8 offset:37888\n\t" \
  "v_max3_f32 v10, v10, v130, v131\n\t" \
  "v_mfma_f32_16x16x32_bf16 v[48:51], v[208:211], v[144:147], v[48:51]\n\t" \
  "v_max3_f32 v11, v11, v132, v133\n\t" \
  "v_max3_f32 v10, v10, v134, v135\n\t" \
  "v_mfma_f32_16x16x32_bf16 v[56:59], v[208:211], v[152:155], v[56:59]\n\t" \
  "ds_read_b128 v[208:211], v8 offset:34816\n\t" \
  "v_cvt_pk_bf16_f32 v169, v170, v171\n\t" \
  "v_mfma_f32_16x16x32_bf16 v[60:63], v[212:215], v[152:155], v[60:63]\n\t" \
  "v_max3_f32 v11, v11, v136, v137\n\t" \
  "v_max3_f32 v10, v10, v138, v139\n\t" \
  "v_mfma_f32_16x16x32_bf16 v[52:55], v[212:215], v[144:147], v[52:55]\n\t" \
  "ds_read_b128 v[212:215], v8 offset:38912\n\t" \
  "v_cvt_pk_bf16_f32 v170, v172, v173\n\t" \
  "v_mfma_f32_16x16x32_bf16 v[64:67], v[216:219], v[144:147], v[64:67]\n\t" \
  "v_max3_f32 v11, v11, v140, v141\n\t" \
  "v_max3_f32 v10, v10, v142, v143\n\t" \
  "v_mfma_f32_16x16x32_bf16 v[72:75], v[216:219], v[152:155], v[72:75]\n\t" \
  "ds_read_b128 v[216:219], v8 offset:35840\n\t" \
  "v_cvt_pk_bf16_f32 v171, v174, v175\n\t" \
  "v_max_f32_e32 v10, v10, v11\n\t" \
  "v_mfma_f32_16x16x32_bf16 v[76:79], v[220:223], v[152:155], v[76:79]\n\t" \
  "v_cmp_lt_f32_e32 vcc, s45, v10\n\t" \
  "global_load_lds_dwordx4 v1, s[40:41] offset:2048 sc1\n\t" \
  "v_mfma_f32_16x16x32_bf16 v[68:71], v[220:223], v[144:147], v[68:71]\n\t" \
  "ds_read_b128 v[220:223], v8 offset:39936\n\t" \
  "s_cbranch_vccnz RASc_%=\n\t" \
  "RArSc_%=:\n\t" \
  "v_exp_f32_e32 v112, v112\n\t" \
  "s_waitcnt lgkmcnt(8)\n\t" \
  "v_mfma_f32_16x16x32_bf16 v[16:19], v[224:227], v[160:163], v[16:19]\n\t" \
  "v_exp_f32_e32 v113, v113\n\t" \
  "v_mfma_f32_16x16x32_bf16 v[24:27], v[224:227], v[168:171], v[24:27]\n\t" \
  "ds_read_b128 v[224:227], v8 offset:40960\n\t" \
  "global_load_lds_dwordx4 v1, s[40:41] offset:3072 sc1\n\t" \
  "s_add_u32 m0, s42, 0x1000\n\t" \
  "v_mfma_f32_16x16x32_bf16 v[28:31], v[228:231], v[168:171], v[28:31]\n\t" \
  "v_exp_f32_e32 v114, v114\n\t" \
  "v_mfma_f32_16x16x32_bf16 v[20:23], v[228:231], v[160:163], v[20:23]\n\t" \
  "ds_read_b128 v[228:231], v8 offset:45056\n\t" \
  "global_load_lds_dwordx4 v9, s[40:41] offset:0 sc1\n\t" \
  "v_mfma_f32_16x16x32_bf16 v[4:7], v[188:191], v[160:163], v[4:7]\n\t" \
  "v_exp_f32_e32 v115, v115\n\t" \
  "v_mfma_f32_16x16x32_bf16 v[32:35], v[232:235], v[160:163], v[32:35]\n\t" \
  "v_exp_f32_e32 v116, v116\n\t" \
  "v_mfma_f32_16x16x32_bf16 v[40:43], v[232:235], v[168:171], v[40:43]\n\t" \
  "ds_read_b128 v[232:235], v8 offset:41984\n\t" \
  "global_load_lds_dwordx4 v9, s[40:41] offset:1024 sc1\n\t" \
  "v_mfma_f32_16x16x32_bf16 v[44:47], v[236:239], v[168:171], v[44:47]\n\t" \
  "v_exp_f32_e32 v117, v117\n\t" \
  "v_mfma_f32_16x16x32_bf16 v[36:39], v[236:239], v[160:163], v[36:39]\n\t" \
  "ds_read_b128 v[236:239], v8 offset:46080\n\t" \
  "global_load_lds_dwordx4 v9, s[40:41] offset:2048 sc1\n\t" \
  "v_mfma_f32_16x16x32_bf16 v[48:51], v[240:243], v[160:163], v[48:51]\n\t" \
  "v_exp_f32_e32 v118, v118\n\t" \
  "v_mfma_f32_16x16x32_bf16 v[56:59], v[240:243], v[168:171], v[56:59]\n\t" \
  "ds_read_b128 v[240:243], v8 offset:43008\n\t" \
  "global_load_lds_dwordx4 v9, s[40:41] offset:3072 sc1\n\t" \
  "v_mfma_f32_16x16x32_bf16 v[12:15], v[188:191], v[168:171], v[12:15]\n\t" \
  "v_exp_f32_e32 v119, v119\n\t" \
  "v_mfma_f32_16x16x32_bf16 v[60:63], v[244:247], v[168:171], v[60:63]\n\t" \
  "v_exp_f32_e32 v120, v120\n\t" \
  "v_mfma_f32_16x16x32_bf16 v[52:55], v[244:247], v[160:163], v[52:55]\n\t" \
  "ds_read_b128 v[244:247], v8 offset:47104\n\t" \
  "v_cvt_pk_bf16_f32 v112, v112, v113\n\t" \
  "s_add_u32 s40, s40, 0x8000\n\t" \
  "s_addc_u32 s41, s41, 0\n\t" \
  "v_mfma_f32_16x16x32_bf16 v[64:67], v[248:251], v[160:163], v[64:67]\n\t" \
  "v_exp_f32_e32 v121, v121\n\t" \
  "v_mfma_f32_16x16x32_bf16 v[72:75], v[248:251], v[168:171], v[72:75]\n\t" \
  "ds_read_b128 v[248:251], v8 offset:44032\n\t" \
  "v_cvt_pk_bf16_f32 v113, v114, v115\n\t" \
  "s_add_u32 s42, s42, 0x8000\n\t" \
  "s_and_b32 s42, s42, 0x1ffff\n\t" \
  "v_mfma_f32_16x16x32_bf16 v[76:79], v[252:255], v[168:171], v[76:79]\n\t" \
  "v_exp_f32_e32 v122, v122\n\t" \
  "v_mfma_f32_16x16x32_bf16 v[68:71], v[252:255], v[160:163], v[68:71]\n\t" \
  "ds_read_b128 v[252:255], v8 offset:48128\n\t" \
  "s_cmp_lg_u32 s44, 0\n\t" \
  "s_cbranch_scc1 RBSc_%=\n\t" \
  "RBrSc_%=:\n\t" \
  "TOS_%=:\n\t" \
  "s_cmp_lt_u32 s63, 2\n\t" \
  "s_cbranch_scc1 SKS_%=\n\t" \
  "v_xor_b32_e32 v8, 0x10000, v8\n\t" \
  "s_waitcnt vmcnt(8) lgkmcnt(8)\n\t" \
  "v_mfma_f32_16x16x32_bf16 v[144:147], v[192:195], v[80:83], v[176:179]\n\t" \
  "s_barrier\n\t" \
  "v_exp_f32_e32 v123, v123\n\t" \
  "v_mfma_f32_16x16x32_bf16 v[152:155], v[192:195], v[96:99], v[180:183]\n\t" \
  "ds_read_b128 v[192:195], v8 offset:16384\n\t" \
  "v_cvt_pk_bf16_f32 v114, v116, v117\n\t" \
  "v_mfma_f32_16x16x32_bf16 v[156:159], v[196:199], v[96:99], v[180:183]\n\t" \
  "v_exp_f32_e32 v124, v124\n\t" \
  "v_mfma_f32_16x16x32_bf16 v[148:151], v[196:199], v[80:83], v[176:179]\n\t" \
  "ds_read_b128 v[196:199], v8 offset:17408\n\t" \
  "v_cvt_pk_bf16_f32 v115, v118, v119\n\t" \
  "v_mfma_f32_16x16x32_bf16 v[144:147], v[200:203], v[84:87], v[144:147]\n\t" \
  "v_exp_f32_e32 v125, v125\n\t" \
  "v_mfma_f32_16x16x32_bf16 v[152:155], v[200:203], v[100:103], v[152:155]\n\t" \
  "ds_read_b128 v[200:203], v8 offset:18432\n\t" \
  "v_cvt_pk_bf16_f32 v120, v120, v121\n\t" \
  "v_mfma_f32_16x16x32_bf16 v[156:159], v[204:207], v[100:103], v[156:159]\n\t" \
  "v_exp_f32_e32 v126, v126\n\t" \
  "v_mfma_f32_16x16x32_bf16 v[148:151], v[204:207], v[84:87], v[148:151]\n\t" \
  "ds_read_b128 v[204:207], v8 offset:19456\n\t" \
  "v_cvt_pk_bf16_f32 v121, v122, v123\n\t" \
  "v_mfma_f32_16x16x32_bf16 v[144:147], v[208:211], v[88:91], v[144:147]\n\t" \
  "v_exp_f32_e32 v127, v127\n\t" \
  "v_mfma_f32_16x16x32_bf16 v[152:155], v[208:211], v[104:107], v[152:155]\n\t" \
  "ds_read_b128 v[208:211], v8 offset:20480\n\t" \
  "v_cvt_pk_bf16_f32 v122, v124, v125\n\t" \
  "v_mfma_f32_16x16x32_bf16 v[156:159], v[212:215], v[104:107], v[156:159]\n\t" \
  "v_exp_f32_e32 v128, v128\n\t" \
  "v_mfma_f32_16x16x32_bf16 v[148:151], v[212:215], v[88:91], v[148:151]\n\t" \
  "ds_read_b128 v[212:215], v8 offset:21504\n\t" \
  "v_cvt_pk_bf16_f32 v123, v126, v127\n\t" \
  "s_mov_b32 m0, s42\n\t" \
  "v_mfma_f32_16x16x32_bf16 v[144:147], v[216:219], v[92:95], v[144:147]\n\t" \
  "v_exp_f32_e32 v129, v129\n\t" \
  "v_mfma_f32_16x16x32_bf16 v[152:155], v[216:219], v[108:111], v[152:155]\n\t" \
  "ds_read_b128 v[216:219], v8 offset:22528\n\t" \
  "v_cmp_gt_i32_e64 s[48:49], 0, v2\n\t" \
  "v_cmp_gt_i32_e64 s[50:51], 1, v2\n\t" \
  "global_load_lds_dwordx4 v1, s[40:41] offset:0 sc1\n\t" \
  "v_mfma_f32_16x16x32_bf16 v[156:159], v[220:223], v[108:111], v[156:159]\n\t" \
  "v_exp_f32_e32 v130, v130\n\t" \
  "v_mfma_f32_16x16x32_bf16 v[148:151], v[220:223], v[92:95], v[148:151]\n\t" \
  "ds_read_b128 v[220:223], v8 offset:23552\n\t" \
  "v_cmp_gt_i32_e64 s[52:53], 2, v2\n\t" \
  "v_cmp_gt_i32_e64 s[54:55], 3, v2\n\t" \
  "global_load_lds_dwordx4 v1, s[40:41] offset:1024 sc1\n\t" \
  "s_waitcnt lgkmcnt(8)\n\t" \
  "v_mfma_f32_16x16x32_bf16 v[160:163], v[224:227], v[80:83], v[176:179]\n\t" \
  "v_exp_f32_e32 v131, v131\n\t" \
  "v_mfma_f32_16x16x32_bf16 v[168:171], v[224:227], v[96:99], v[180:183]\n\t" \
  "ds_read_b128 v[224:227], v8 offset:24576\n\t" \
  "v_cvt_pk_bf16_f32 v128, v128, v129\n\t" \
  "v_mfma_f32_16x16x32_bf16 v[172:175], v[228:231], v[96:99], v[180:183]\n\t" \
  "v_exp_f32_e32 v132, v132\n\t" \
  "v_mfma_f32_16x16x32_bf16 v[164:167], v[228:231], v[80:83], v[176:179]\n\t" \
  "ds_read_b128 v[228:231], v8 offset:25600\n\t" \
  "v_cvt_pk_bf16_f32 v129, v130, v131\n\t" \
  "v_mfma_f32_16x16x32_bf16 v[160:163], v[232:235], v[84:87], v[160:163]\n\t" \
  "v_exp_f32_e32 v133, v133\n\t" \
  "v_mfma_f32_16x16x32_bf16 v[168:171], v[232:235], v[100:103], v[168:171]\n\t" \
  "ds_read_b128 v[232:235], v8 offset:26624\n\t" \
  "s_cmp_gt_u32 s63, 2\n\t" \
  "s_cbranch_scc1 MX9_%=\n\t" \
  "v_mov_b32_e32 v148, v184\n\t" \
  "v_mov_b32_e32 v149, v184\n\t" \
  "v_mov_b32_e32 v150, v184\n\t" \
  "v_mov_b32_e32 v151, v184\n\t" \
  "s_cmp_eq_u32 s63, 2\n\t" \
  "s_cbranch_scc1 MD9_%=\n\t" \
  "v_mov_b32_e32 v144, v184\n\t" \
  "v_mov_b32_e32 v145, v184\n\t" \
  "v_mov_b32_e32 v146, v184\n\t" \
  "v_mov_b32_e32 v147, v184\n\t" \
  "v_mov_b32_e32 v152, v184\n\t" \
  "v_mov_b32_e32 v153, v184\n\t" \
  "v_mov_b32_e32 v154, v184\n\t" \
  "v_mov_b32_e32 v155, v184\n\t" \
  "v_mov_b32_e32 v156, v184\n\t" \
  "v_mov_b32_e32 v157, v184\n\t" \
  "v_mov_b32_e32 v158, v184\n\t" \
  "v_mov_b32_e32 v159, v184\n\t" \
  "s_branch MX9_%=\n\t" \
  "MD9_%=:\n\t" \
  "v_cndmask_b32_e64 v144, v144, v184, s[48:49]\n\t" \
  "v_cndmask_b32_e64 v145, v145, v184, s[50:51]\n\t" \
  "v_cndmask_b32_e64 v146, v146, v184, s[52:53]\n\t" \
  "v_cndmask_b32_e64 v147, v147, v184, s[54:55]\n\t" \
  "v_cndmask_b32_e64 v156, v156, v184, s[48:49]\n\t" \
  "v_cndmask_b32_e64 v157, v157, v184, s[50:51]\n\t" \
  "v_cndmask_b32_e64 v158, v158, v184, s[52:53]\n\t" \
  "v_cndmask_b32_e64 v159, v159, v184, s[54:55]\n\t" \
  "MX9_%=:\n\t" \
  "v_max3_f32 v10, v144, v145, v146\n\t" \
  "v_mfma_f32_16x16x32_bf16 v[172:175], v[236:239], v[100:103], v[172:175]\n\t" \
  "v_exp_f32_e32 v134, v134\n\t" \
  "v_mfma_f32_16x16x32_bf16 v[164:167], v[236:239], v[84:87], v[164:167]\n\t" \
  "ds_read_b128 v[236:239], v8 offset:27648\n\t" \
  "v_max3_f32 v11, v147, v148, v149\n\t" \
  "v_mfma_f32_16x16x32_bf16 v[160:163], v[240:243], v[88:91], v[160:163]\n\t" \
  "v_exp_f32_e32 v135, v135\n\t" \
  "v_mfma_f32_16x16x32_bf16 v[168:171], v[240:243], v[104:107], v[168:171]\n\t" \
  "ds_read_b128 v[240:243], v8 offset:28672\n\t" \
  "v_max3_f32 v10, v10, v150, v151\n\t" \
  "v_mfma_f32_16x16x32_bf16 v[172:175], v[244:247], v[104:107], v[172:175]\n\t" \
  "v_exp_f32_e32 v136, v136\n\t" \
  "v_mfma_f32_16x16x32_bf16 v[164:167], v[244:247], v[88:91], v[164:167]\n\t" \
  "ds_read_b128 v[244:247], v8 offset:29696\n\t" \
  "v_max3_f32 v11, v11, v152, v153\n\t" \
  "v_mfma_f32_16x16x32_bf16 v[160:163], v[248:251], v[92:95], v[160:163]\n\t" \
  "v_exp_f32_e32 v137, v137\n\t" \
  "v_mfma_f32_16x16x32_bf16 v[168:171], v[248:251], v[108:111], v[168:171]\n\t" \
  "ds_read_b128 v[248:251], v8 offset:30720\n\t" \
  "v_max3_f32 v10, v10, v154, v155\n\t" \
  "v_mfma_f32_16x16x32_bf16 v[172:175], v[252:255], v[108:111], v[172:175]\n\t" \
  "v_exp_f32_e32 v138, v138\n\t" \
  "v_mfma_f32_16x16x32_bf16 v[164:167], v[252:255], v[92:95], v[164:167]\n\t" \
  "ds_read_b128 v[252:255], v8 offset:31744\n\t" \
  "v_max3_f32 v11, v11, v156, v157\n\t" \
  "s_waitcnt lgkmcnt(8)\n\t" \
  "v_mfma_f32_16x16x32_bf16 v[16:19], v[192:195], v[112:115], v[16:19]\n\t" \
  "v_exp_f32_e32 v139, v139\n\t" \
  "v_mfma_f32_16x16x32_bf16 v[24:27], v[192:195], v[120:123], v[24:27]\n\t" \
  "v_max3_f32 v10, v10, v158, v159\n\t" \
  "v_mfma_f32_16x16x32_bf16 v[12:15], v[188:191], v[120:123], v[12:15]\n\t" \
  "v_exp_f32_e32 v140, v140\n\t" \
  "v_mfma_f32_16x16x32_bf16 v[28:31], v[196:199], v[120:123], v[28:31]\n\t" \
  "v_exp_f32_e32 v141, v141\n\t" \
  "v_mfma_f32_16x16x32_bf16 v[20:23], v[196:199], v[112:115], v[20:23]\n\t" \
  "v_cvt_pk_bf16_f32 v130, v132, v133\n\t" \
  "v_mfma_f32_16x16x32_bf16 v[4:7], v[188:191], v[112:115], v[4:7]\n\t" \
  "v_cvt_pk_bf16_f32 v131, v134, v135\n\t" \
  "v_cvt_pk_bf16_f32 v136, v136, v137\n\t" \
  "v_mfma_f32_16x16x32_bf16 v[32:35], v[200:203], v[112:115], v[32:35]\n\t" \
  "v_exp_f32_e32 v142, v142\n\t" \
  "v_mfma_f32_16x16x32_bf16 v[40:43], v[200:203], v[120:123], v[40:43]\n\t" \
  "v_mov_b32_e32 v164, v184\n\t" \
  "v_mov_b32_e32 v165, v184\n\t" \
  "v_mov_b32_e32 v166, v184\n\t" \
  "v_mov_b32_e32 v167, v184\n\t" \
  "s_cmp_eq_u32 s63, 3\n\t" \
  "s_cbranch_scc1 MD10_%=\n\t" \
  "v_mov_b32_e32 v160, v184\n\t" \
  "v_mov_b32_e32 v161, v184\n\t" \
  "v_mov_b32_e32 v162, v184\n\t" \
  "v_mov_b32_e32 v163, v184\n\t" \
  "v_mov_b32_e32 v168, v184\n\t" \
  "v_mov_b32_e32 v169, v184\n\t" \
  "v_mov_b32_e32 v170, v184\n\t" \
  "v_mov_b32_e32 v171, v184\n\t" \
  "v_mov_b32_e32 v172, v184\n\t" \
  "v_mov_b32_e32 v173, v184\n\t" \
  "v_mov_b32_e32 v174, v184\n\t" \
  "v_mov_b32_e32 v175, v184\n\t" \
  "s_branch MX10_%=\n\t" \
  "MD10_%=:\n\t" \
  "v_cndmask_b32_e64 v160, v160, v184, s[48:49]\n\t" \
  "v_cndmask_b32_e64 v161, v161, v184, s[50:51]\n\t" \
  "v_cndmask_b32_e64 v162, v162, v184, s[52:53]\n\t" \
  "v_cndmask_b32_e64 v163, v163, v184, s[54:55]\n\t" \
  "v_cndmask_b32_e64 v172, v172, v184, s[48:49]\n\t" \
  "v_cndmask_b32_e64 v173, v173, v184, s[50:51]\n\t" \
  "v_cndmask_b32_e64 v174, v174, v184, s[52:53]\n\t" \
  "v_cndmask_b32_e64 v175, v175, v184, s[54:55]\n\t" \
  "MX10_%=:\n\t" \
  "v_max3_f32 v11, v11, v160, v161\n\t" \
  "v_mfma_f32_16x16x32_bf16 v[44:47], v[204:207], v[120:123], v[44:47]\n\t" \
  "v_exp_f32_e32 v143, v143\n\t" \
  "v_mfma_f32_16x16x32_bf16 v[36:39], v[204:207], v[112:115], v[36:39]\n\t" \
  "v_max3_f32 v10, v10, v162, v163\n\t" \
  "v_mfma_f32_16x16x32_bf16 v[48:51], v[208:211], v[112:115], v[48:51]\n\t" \
  "v_max3_f32 v11, v11, v164, v165\n\t" \
  "v_max3_f32 v10, v10, v166, v167\n\t" \
  "v_mfma_f32_16x16x32_bf16 v[56:59], v[208:211], v[120:123], v[56:59]\n\t" \
  "v_cvt_pk_bf16_f32 v137, v138, v139\n\t" \
  "v_mfma_f32_16x16x32_bf16 v[60:63], v[212:215], v[120:123], v[60:63]\n\t" \
  "v_max3_f32 v11, v11, v168, v169\n\t" \
  "v_max3_f32 v10, v10, v170, v171\n\t" \
  "v_mfma_f32_16x16x32_bf16 v[52:55], v[212:215], v[112:115], v[52:55]\n\t" \
  "v_cvt_pk_bf16_f32 v138, v140, v141\n\t" \
  "v_mfma_f32_16x16x32_bf16 v[64:67], v[216:219], v[112:115], v[64:67]\n\t" \
  "v_max3_f32 v11, v11, v172, v173\n\t" \
  "v_max3_f32 v10, v10, v174, v175\n\t" \
  "v_mfma_f32_16x16x32_bf16 v[72:75], v[216:219], v[120:123], v[72:75]\n\t" \
  "v_cvt_pk_bf16_f32 v139, v142, v143\n\t" \
  "v_max_f32_e32 v10, v10, v11\n\t" \
  "v_mfma_f32_16x16x32_bf16 v[76:79], v[220:223], v[120:123], v[76:79]\n\t" \
  "v_cmp_lt_f32_e32 vcc, s45, v10\n\t" \
  "global_load_lds_dwordx4 v1, s[40:41] offset:2048 sc1\n\t" \
  "v_mfma_f32_16x16x32_bf16 v[68:71], v[220:223], v[112:115], v[68:71]\n\t" \
  "s_cbranch_vccnz RASd_%=\n\t" \
  "RArSd_%=:\n\t" \
  "v_exp_f32_e32 v144, v144\n\t" \
  "s_waitcnt lgkmcnt(0)\n\t" \
  "v_mfma_f32_16x16x32_bf16 v[16:19], v[224:227], v[128:131], v[16:19]\n\t" \
  "v_exp_f32_e32 v145, v145\n\t" \
  "v_mfma_f32_16x16x32_bf16 v[24:27], v[224:227], v[136:139], v[24:27]\n\t" \
  "global_load_lds_dwordx4 v1, s[40:41] offset:3072 sc1\n\t" \
  "s_add_u32 m0, s42, 0x1000\n\t" \
  "v_mfma_f32_16x16x32_bf16 v[28:31], v[228:231], v[136:139], v[28:31]\n\t" \
  "v_exp_f32_e32 v146, v146\n\t" \
  "v_mfma_f32_16x16x32_bf16 v[20:23], v[228:231], v[128:131], v[20:23]\n\t" \
  "global_load_lds_dwordx4 v9, s[40:41] offset:0 sc1\n\t" \
  "v_mfma_f32_16x16x32_bf16 v[4:7], v[188:191], v[128:131], v[4:7]\n\t" \
  "v_exp_f32_e32 v147, v147\n\t" \
  "v_mfma_f32_16x16x32_bf16 v[32:35], v[232:235], v[128:131], v[32:35]\n\t" \
  "v_exp_f32_e32 v148, v148\n\t" \
  "v_mfma_f32_16x16x32_bf16 v[40:43], v[232:235], v[136:139], v[40:43]\n\t" \
  "global_load_lds_dwordx4 v9, s[40:41] offset:1024 sc1\n\t" \
  "v_mfma_f32_16x16x32_bf16 v[44:47], v[236:239], v[136:139], v[44:47]\n\t" \
  "v_exp_f32_e32 v149, v149\n\t" \
  "v_mfma_f32_16x16x32_bf16 v[36:39], v[236:239], v[128:131], v[36:39]\n\t" \
  "global_load_lds_dwordx4 v9, s[40:41] offset:2048 sc1\n\t" \
  "v_mfma_f32_16x16x32_bf16 v[48:51], v[240:243], v[128:131], v[48:51]\n\t" \
  "v_exp_f32_e32 v150, v150\n\t" \
  "v_mfma_f32_16x16x32_bf16 v[56:59], v[240:243], v[136:139], v[56:59]\n\t" \
  "global_load_lds_dwordx4 v9, s[40:41] offset:3072 sc1\n\t" \
  "v_mfma_f32_16x16x32_bf16 v[12:15], v[188:191], v[136:139], v[12:15]\n\t" \
  "v_exp_f32_e32 v151, v151\n\t" \
  "v_mfma_f32_16x16x32_bf16 v[60:63], v[244:247], v[136:139], v[60:63]\n\t" \
  "v_exp_f32_e32 v152, v152\n\t" \
  "v_mfma_f32_16x16x32_bf16 v[52:55], v[244:247], v[128:131], v[52:55]\n\t" \
  "v_cvt_pk_bf16_f32 v144, v144, v145\n\t" \
  "s_add_u32 s40, s40, 0x8000\n\t" \
  "s_addc_u32 s41, s41, 0\n\t" \
  "v_mfma_f32_16x16x32_bf16 v[64:67], v[248:251], v[128:131], v[64:67]\n\t" \
  "v_exp_f32_e32 v153, v153\n\t" \
  "v_mfma_f32_16x16x32_bf16 v[72:75], v[248:251], v[136:139], v[72:75]\n\t" \
  "v_cvt_pk_bf16_f32 v145, v146, v147\n\t" \
  "s_add_u32 s42, s42, 0x8000\n\t" \
  "s_and_b32 s42, s42, 0x1ffff\n\t" \
  "v_mfma_f32_16x16x32_bf16 v[76:79], v[252:255], v[136:139], v[76:79]\n\t" \
  "v_exp_f32_e32 v154, v154\n\t" \
  "v_mfma_f32_16x16x32_bf16 v[68:71], v[252:255], v[128:131], v[68:71]\n\t" \
  "s_cmp_lg_u32 s44, 0\n\t" \
  "s_cbranch_scc1 RBSd_%=\n\t" \
  "RBrSd_%=:\n\t" \
  "s_setprio 2\n\t" \
  "s_waitcnt vmcnt(8)\n\t" \
  "s_barrier\n\t" \
  "ds_read_b128 v[192:195], v8 offset:49152\n\t" \
  "v_exp_f32_e32 v155, v155\n\t" \
  "ds_read_b128 v[196:199], v8 offset:50176\n\t" \
  "v_cvt_pk_bf16_f32 v146, v148, v149\n\t" \
  "ds_read_b128 v[200:203], v8 offset:51200\n\t" \
  "v_exp_f32_e32 v156, v156\n\t" \
  "ds_read_b128 v[204:207], v8 offset:52224\n\t" \
  "v_cvt_pk_bf16_f32 v147, v150, v151\n\t" \
  "ds_read_b128 v[208:211], v8 offset:53248\n\t" \
  "v_exp_f32_e32 v157, v157\n\t" \
  "ds_read_b128 v[212:215], v8 offset:54272\n\t" \
  "v_cvt_pk_bf16_f32 v152, v152, v153\n\t" \
  "ds_read_b128 v[216:219], v8 offset:55296\n\t" \
  "v_exp_f32_e32 v158, v158\n\t" \
  "ds_read_b128 v[220:223], v8 offset:56320\n\t" \
  "v_cvt_pk_bf16_f32 v153, v154, v155\n\t" \
  "ds_read_b128 v[224:227], v8 offset:57344\n\t" \
  "v_exp_f32_e32 v159, v159\n\t" \
  "ds_read_b128 v[228:231], v8 offset:58368\n\t" \
  "v_cvt_pk_bf16_f32 v154, v156, v157\n\t" \
  "ds_read_b128 v[232:235], v8 offset:59392\n\t" \
  "v_exp_f32_e32 v160, v160\n\t" \
  "ds_read_b128 v[236:239], v8 offset:60416\n\t" \
  "v_cvt_pk_bf16_f32 v155, v158, v159\n\t" \
  "ds_read_b128 v[240:243], v8 offset:61440\n\t" \
  "v_exp_f32_e32 v161, v161\n\t" \
  "ds_read_b128 v[244:247], v8 offset:62464\n\t" \
  "v_exp_f32_e32 v162, v162\n\t" \
  "ds_read_b128 v[248:251], v8 offset:63488\n\t" \
  "v_cvt_pk_bf16_f32 v160, v160, v161\n\t" \
  "ds_read_b128 v[252:255], v8 offset:64512\n\t" \
  "v_exp_f32_e32 v163, v163\n\t" \
  "s_mov_b32 m0, s42\n\t" \
  "s_nop 0\n\t" \
  "global_load_lds_dwordx4 v1, s[40:41] offset:0 sc1\n\t" \
  "global_load_lds_dwordx4 v1, s[40:41] offset:1024 sc1\n\t" \
  "global_load_lds_dwordx4 v1, s[40:41] offset:2048 sc1\n\t" \
  "global_load_lds_dwordx4 v1, s[40:41] offset:3072 sc1\n\t" \
  "s_add_u32 m0, s42, 0x1000\n\t" \
  "s_nop 0\n\t" \
  "global_load_lds_dwordx4 v9, s[40:41] offset:0 sc1\n\t" \
  "global_load_lds_dwordx4 v9, s[40:41] offset:1024 sc1\n\t" \
  "global_load_lds_dwordx4 v9, s[40:41] offset:2048 sc1\n\t" \
  "global_load_lds_dwordx4 v9, s[40:41] offset:3072 sc1\n\t" \
  "s_add_u32 s40, s40, 0x8000\n\t" \
  "s_addc_u32 s41, s41, 0\n\t" \
  "s_add_u32 s42, s42, 0x8000\n\t" \
  "s_and_b32 s42, s42, 0x1ffff\n\t" \
  "s_waitcnt lgkmcnt(8)\n\t" \
  "v_mfma_f32_16x16x32_bf16 v[16:19], v[192:195], v[144:147], v[16:19]\n\t" \
  "v_exp_f32_e32 v164, v164\n\t" \
  "v_cvt_pk_bf16_f32 v161, v162, v163\n\t" \
  "v_mfma_f32_16x16x32_bf16 v[24:27], v[192:195], v[152:155], v[24:27]\n\t" \
  "v_exp_f32_e32 v165, v165\n\t" \
  "v_mfma_f32_16x16x32_bf16 v[28:31], v[196:199], v[152:155], v[28:31]\n\t" \
  "v_exp_f32_e32 v166, v166\n\t" \
  "v_mfma_f32_16x16x32_bf16 v[20:23], v[196:199], v[144:147], v[20:23]\n\t" \
  "v_cvt_pk_bf16_f32 v162, v164, v165\n\t" \
  "v_mfma_f32_16x16x32_bf16 v[32:35], v[200:203], v[144:147], v[32:35]\n\t" \
  "v_exp_f32_e32 v167, v167\n\t" \
  "v_exp_f32_e32 v168, v168\n\t" \
  "v_mfma_f32_16x16x32_bf16 v[40:43], v[200:203], v[152:155], v[40:43]\n\t" \
  "v_cvt_pk_bf16_f32 v163, v166, v167\n\t" \
  "v_mfma_f32_16x16x32_bf16 v[44:47], v[204:207], v[152:155], v[44:47]\n\t" \
  "v_exp_f32_e32 v169, v169\n\t" \
  "v_mfma_f32_16x16x32_bf16 v[36:39], v[204:207], v[144:147], v[36:39]\n\t" \
  "v_exp_f32_e32 v170, v170\n\t" \
  "v_mfma_f32_16x16x32_bf16 v[48:51], v[208:211], v[144:147], v[48:51]\n\t" \
  "v_cvt_pk_bf16_f32 v168, v168, v169\n\t" \
  "v_exp_f32_e32 v171, v171\n\t" \
  "v_mfma_f32_16x16x32_bf16 v[56:59], v[208:211], v[152:155], v[56:59]\n\t" \
  "v_exp_f32_e32 v172, v172\n\t" \
  "v_mfma_f32_16x16x32_bf16 v[60:63], v[212:215], v[152:155], v[60:63]\n\t" \
  "v_cvt_pk_bf16_f32 v169, v170, v171\n\t" \
  "v_mfma_f32_16x16x32_bf16 v[52:55], v[212:215], v[144:147], v[52:55]\n\t" \
  "v_exp_f32_e32 v173, v173\n\t" \
  "v_mfma_f32_16x16x32_bf16 v[64:67], v[216:219], v[144:147], v[64:67]\n\t" \
  "v_exp_f32_e32 v174, v174\n\t" \
  "v_cvt_pk_bf16_f32 v170, v172, v173\n\t" \
  "v_mfma_f32_16x16x32_bf16 v[72:75], v[216:219], v[152:155], v[72:75]\n\t" \
  "v_exp_f32_e32 v175, v175\n\t" \
  "v_mfma_f32_16x16x32_bf16 v[76:79], v[220:223], v[152:155], v[76:79]\n\t" \
  "s_nop 0\n\t" \
  "v_mfma_f32_16x16x32_bf16 v[68:71], v[220:223], v[144:147], v[68:71]\n\t" \
  "v_cvt_pk_bf16_f32 v171, v174, v175\n\t" \
  "v_mfma_f32_16x16x32_bf16 v[4:7], v[188:191], v[144:147], v[4:7]\n\t" \
  "v_mfma_f32_16x16x32_bf16 v[12:15], v[188:191], v[152:155], v[12:15]\n\t" \
  "s_waitcnt lgkmcnt(0)\n\t" \
  "v_mfma_f32_16x16x32_bf16 v[4:7], v[188:191], v[160:163], v[4:7]\n\t" \
  "v_mfma_f32_16x16x32_bf16 v[12:15], v[188:191], v[168:171], v[12:15]\n\t" \
  "v_mfma_f32_16x16x32_bf16 v[16:19], v[224:227], v[160:163], v[16:19]\n\t" \
  "v_mfma_f32_16x16x32_bf16 v[24:27], v[224:227], v[168:171], v[24:27]\n\t" \
  "v_mfma_f32_16x16x32_bf16 v[28:31], v[228:231], v[168:171], v[28:31]\n\t" \
  "v_mfma_f32_16x16x32_bf16 v[20:23], v[228:231], v[160:163], v[20:23]\n\t" \
  "v_mfma_f32_16x16x32_bf16 v[32:35], v[232:235], v[160:163], v[32:35]\n\t" \
  "v_mfma_f32_16x16x32_bf16 v[40:43], v[232:235], v[168:171], v[40:43]\n\t" \
  "v_mfma_f32_16x16x32_bf16 v[44:47], v[236:239], v[168:171], v[44:47]\n\t" \
  "v_mfma_f32_16x16x32_bf16 v[36:39], v[236:239], v[160:163], v[36:39]\n\t" \
  "v_mfma_f32_16x16x32_bf16 v[48:51], v[240:243], v[160:163], v[48:51]\n\t" \
  "v_mfma_f32_16x16x32_bf16 v[56:59], v[240:243], v[168:171], v[56:59]\n\t" \
  "v_mfma_f32_16x16x32_bf16 v[60:63], v[244:247], v[168:171], v[60:63]\n\t" \
  "v_mfma_f32_16x16x32_bf16 v[52:55], v[244:247], v[160:163], v[52:55]\n\t" \
  "v_rcp_f32_e32 v4, v4\n\t" \
  "v_rcp_f32_e32 v5, v5\n\t" \
  "v_rcp_f32_e32 v12, v12\n\t" \
  "v_rcp_f32_e32 v13, v13\n\t" \
  "v_mfma_f32_16x16x32_bf16 v[64:67], v[248:251], v[160:163], v[64:67]\n\t" \
  "v_mfma_f32_16x16x32_bf16 v[72:75], v[248:251], v[168:171], v[72:75]\n\t" \
  "v_mfma_f32_16x16x32_bf16 v[76:79], v[252:255], v[168:171], v[76:79]\n\t" \
  "v_mfma_f32_16x16x32_bf16 v[68:71], v[252:255], v[160:163], v[68:71]\n\t" \
  "EPS_%=:\n\t" \
  "s_add_u32 s43, s67, 2\n\t" \
  "s_cmp_lt_u32 s63, 2\n\t" \
  "s_cselect_b32 s81, 2, 0\n\t" \
  "s_sub_u32 s81, s68, s81\n\t" \
  "s_waitcnt vmcnt(8)\n\t" \
  "s_barrier\n\t" \
  "s_cmp_ge_u32 s43, s68\n\t" \
  "s_cbranch_scc1 LE_%=\n\t" \
  "s_mov_b32 m0, s42\n\t" \
  "s_nop 0\n\t" \
  "global_load_lds_dwordx4 v1, s[40:41] offset:0 sc1\n\t" \
  "global_load_lds_dwordx4 v1, s[40:41] offset:1024 sc1\n\t" \
  "global_load_lds_dwordx4 v1, s[40:41] offset:2048 sc1\n\t" \
  "global_load_lds_dwordx4 v1, s[40:41] offset:3072 sc1\n\t" \
  "s_add_u32 m0, s42, 0x1000\n\t" \
  "s_nop 0\n\t" \
  "global_load_lds_dwordx4 v9, s[40:41] offset:0 sc1\n\t" \
  "global_load_lds_dwordx4 v9, s[40:41] offset:1024 sc1\n\t" \
  "global_load_lds_dwordx4 v9, s[40:41] offset:2048 sc1\n\t" \
  "global_load_lds_dwordx4 v9, s[40:41] offset:3072 sc1\n\t" \
  "s_add_u32 s40, s40, 0x8000\n\t" \
  "s_addc_u32 s41, s41, 0\n\t" \
  "s_add_u32 s42, s42, 0x8000\n\t" \
  "s_and_b32 s42, s42, 0x1ffff\n\t" \
  "LE_%=:\n\t" \
  "v_mul_f32_e32 v16, v16, v4\n\t" \
  "v_mul_f32_e32 v17, v17, v4\n\t" \
  "v_mul_f32_e32 v18, v18, v4\n\t" \
  "v_mul_f32_e32 v19, v19, v4\n\t" \
  "v_mul_f32_e32 v20, v20, v4\n\t" \
  "v_mul_f32_e32 v21, v21, v4\n\t" \
  "v_mul_f32_e32 v22, v22, v4\n\t" \
  "v_mul_f32_e32 v23, v23, v4\n\t" \
  "v_cvt_pk_bf16_f32 v192, v16, v17\n\t" \
  "v_cvt_pk_bf16_f32 v193, v18, v19\n\t" \
  "v_cvt_pk_bf16_f32 v194, v20, v21\n\t" \
  "v_cvt_pk_bf16_f32 v195, v22, v23\n\t" \
  "global_store_dwordx4 v1, v[192:195], s[56:57] offset:0 sc1\n\t" \
  "v_mul_f32_e32 v24, v24, v12\n\t" \
  "v_mul_f32_e32 v25, v25, v12\n\t" \
  "v_mul_f32_e32 v26, v26, v12\n\t" \
  "v_mul_f32_e32 v27, v27, v12\n\t" \
  "v_mul_f32_e32 v28, v28, v12\n\t" \
  "v_mul_f32_e32 v29, v29, v12\n\t" \
  "v_mul_f32_e32 v30, v30, v12\n\t" \
  "v_mul_f32_e32 v31, v31, v12\n\t" \
  "v_cvt_pk_bf16_f32 v196, v24, v25\n\t" \
  "v_cvt_pk_bf16_f32 v197, v26, v27\n\t" \
  "v_cvt_pk_bf16_f32 v198, v28, v29\n\t" \
  "v_cvt_pk_bf16_f32 v199, v30, v31\n\t" \
  "global_store_dwordx4 v1, v[196:199], s[56:57] offset:1024 sc1\n\t" \
  "s_add_u32 s56, s56, 0x2000\n\t" \
  "s_addc_u32 s57, s57, 0\n\t" \
  "v_mul_f32_e32 v32, v32, v4\n\t" \
  "v_mul_f32_e32 v33, v33, v4\n\t" \
  "v_mul_f32_e32 v34, v34, v4\n\t" \
  "v_mul_f32_e32 v35, v35, v4\n\t" \
  "v_mul_f32_e32 v36, v36, v4\n\t" \
  "v_mul_f32_e32 v37, v37, v4\n\t" \
  "v_mul_f32_e32 v38, v38, v4\n\t" \
  "v_mul_f32_e32 v39, v39, v4\n\t" \
  "v_cvt_pk_bf16_f32 v200, v32, v33\n\t" \
  "v_cvt_pk_bf16_f32 v201, v34, v35\n\t" \
  "v_cvt_pk_bf16_f32 v202, v36, v37\n\t" \
  "v_cvt_pk_bf16_f32 v203, v38, v39\n\t" \
  "global_store_dwordx4 v1, v[200:203], s[56:57] offset:0 sc1\n\t" \
  "v_mul_f32_e32 v40, v40, v12\n\t" \
  "v_mul_f32_e32 v41, v41, v12\n\t" \
  "v_mul_f32_e32 v42, v42, v12\n\t" \
  "v_mul_f32_e32 v43, v43, v12\n\t" \
  "v_mul_f32_e32 v44, v44, v12\n\t" \
  "v_mul_f32_e32 v45, v45, v12\n\t" \
  "v_mul_f32_e32 v46, v46, v12\n\t" \
  "v_mul_f32_e32 v47, v47, v12\n\t" \
  "v_cvt_pk_bf16_f32 v204, v40, v41\n\t" \
  "v_cvt_pk_bf16_f32 v205, v42, v43\n\t" \
  "v_cvt_pk_bf16_f32 v206, v44, v45\n\t" \
  "v_cvt_pk_bf16_f32 v207, v46, v47\n\t" \
  "global_store_dwordx4 v1, v[204:207], s[56:57] offset:1024 sc1\n\t" \
  "s_add_u32 s56, s56, 0x2000\n\t" \
  "s_addc_u32 s57, s57, 0\n\t" \
  "v_mul_f32_e32 v48, v48, v4\n\t" \
  "v_mul_f32_e32 v49, v49, v4\n\t" \
  "v_mul_f32_e32 v50, v50, v4\n\t" \
  "v_mul_f32_e32 v51, v51, v4\n\t" \
  "v_mul_f32_e32 v52, v52, v4\n\t" \
  "v_mul_f32_e32 v53, v53, v4\n\t" \
  "v_mul_f32_e32 v54, v54, v4\n\t" \
  "v_mul_f32_e32 v55, v55, v4\n\t" \
  "v_cvt_pk_bf16_f32 v208, v48, v49\n\t" \
  "v_cvt_pk_bf16_f32 v209, v50, v51\n\t" \
  "v_cvt_pk_bf16_f32 v210, v52, v53\n\t" \
  "v_cvt_pk_bf16_f32 v211, v54, v55\n\t" \
  "global_store_dwordx4 v1, v[208:211], s[56:57] offset:0 sc1\n\t" \
  "v_mul_f32_e32 v56, v56, v12\n\t" \
  "v_mul_f32_e32 v57, v57, v12\n\t" \
  "v_mul_f32_e32 v58, v58, v12\n\t" \
  "v_mul_f32_e32 v59, v59, v12\n\t" \
  "v_mul_f32_e32 v60, v60, v12\n\t" \
  "v_mul_f32_e32 v61, v61, v12\n\t" \
  "v_mul_f32_e32 v62, v62, v12\n\t" \
  "v_mul_f32_e32 v63, v63, v12\n\t" \
  "v_cvt_pk_bf16_f32 v212, v56, v57\n\t" \
  "v_cvt_pk_bf16_f32 v213, v58, v59\n\t" \
  "v_cvt_pk_bf16_f32 v214, v60, v61\n\t" \
  "v_cvt_pk_bf16_f32 v215, v62, v63\n\t" \
  "global_store_dwordx4 v1, v[212:215], s[56:57] offset:1024 sc1\n\t" \
  "s_add_u32 s56, s56, 0x2000\n\t" \
  "s_addc_u32 s57, s57, 0\n\t" \
  "v_mul_f32_e32 v64, v64, v4\n\t" \
  "v_mul_f32_e32 v65, v65, v4\n\t" \
  "v_mul_f32_e32 v66, v66, v4\n\t" \
  "v_mul_f32_e32 v67, v67, v4\n\t" \
  "v_mul_f32_e32 v68, v68, v4\n\t" \
  "v_mul_f32_e32 v69, v69, v4\n\t" \
  "v_mul_f32_e32 v70, v70, v4\n\t" \
  "v_mul_f32_e32 v71, v71, v4\n\t" \
  "v_cvt_pk_bf16_f32 v216, v64, v65\n\t" \
  "v_cvt_pk_bf16_f32 v217, v66, v67\n\t" \
  "v_cvt_pk_bf16_f32 v218, v68, v69\n\t" \
  "v_cvt_pk_bf16_f32 v219, v70, v71\n\t" \
  "global_store_dwordx4 v1, v[216:219], s[56:57] offset:0 sc1\n\t" \
  "v_mul_f32_e32 v72, v72, v12\n\t" \
  "v_mul_f32_e32 v73, v73, v12\n\t" \
  "v_mul_f32_e32 v74, v74, v12\n\t" \
  "v_mul_f32_e32 v75, v75, v12\n\t" \
  "v_mul_f32_e32 v76, v76, v12\n\t" \
  "v_mul_f32_e32 v77, v77, v12\n\t" \
  "v_mul_f32_e32 v78, v78, v12\n\t" \
  "v_mul_f32_e32 v79, v79, v12\n\t" \
  "v_cvt_pk_bf16_f32 v220, v72, v73\n\t" \
  "v_cvt_pk_bf16_f32 v221, v74, v75\n\t" \
  "v_cvt_pk_bf16_f32 v222, v76, v77\n\t" \
  "v_cvt_pk_bf16_f32 v223, v78, v79\n\t" \
  "global_store_dwordx4 v1, v[220:223], s[56:57] offset:1024 sc1\n\t" \
  "s_add_u32 s56, s56, 0x2000\n\t" \
  "s_addc_u32 s57, s57, 0\n\t" \
  "s_and_b32 s56, s38, 1\n\t" \
  "s_lshr_b32 s58, s38, 1\n\t" \
  "s_mov_b32 s70, 7\n\t" \
  "s_mul_i32 s71, s58, 7\n\t" \
  "s_lshr_b32 s52, s39, 2\n\t" \
  "s_lshl_b32 s52, s52, 1\n\t" \
  "s_add_u32 s56, s56, s52\n\t" \
  "s_and_b32 s57, s39, 3\n\t" \
  "s_sub_u32 s49, s68, 2\n\t" \
  "s_and_b32 s49, s49, 2\n\t" \
  "s_lshl_b32 s49, s49, 15\n\t" \
  "s_mov_b32 s48, 0x20000\n\t" \
  "s_mov_b32 s50, 0x20000\n\t" \
  "s_lshl_b32 s52, s56, 20\n\t" \
  "s_lshl_b32 s53, s58, 18\n\t" \
  "s_add_u32 s52, s52, s53\n\t" \
  "s_lshl_b32 s53, s63, 12\n\t" \
  "s_add_u32 s52, s52, s53\n\t" \
  "s_add_u32 s52, s52, 0x3800000\n\t" \
  "s_mov_b64 s[72:73], %[ws]\n\t" \
  "s_add_u32 s72, s72, s52\n\t" \
  "s_addc_u32 s73, s73, 0\n\t" \
  "s_lshl_b32 s52, s58, 20\n\t" \
  "s_lshl_b32 s53, s63, 14\n\t" \
  "s_add_u32 s52, s52, s53\n\t" \
  "s_lshl_b32 s54, s57, 8\n\t" \
  "s_add_u32 s52, s52, s54\n\t" \
  "s_mov_b64 s[74:75], %[wo]\n\t" \
  "s_add_u32 s74, s74, s52\n\t" \
  "s_addc_u32 s75, s75, 0\n\t" \
  "s_mul_i32 s52, s56, 28\n\t" \
  "s_mov_b32 s53, s71\n\t" \
  "s_add_u32 s52, s52, s53\n\t" \
  "s_lshl_b32 s52, s52, 14\n\t" \
  "s_lshl_b32 s55, s63, 12\n\t" \
  "s_add_u32 s52, s52, s55\n\t" \
  "s_add_u32 s52, s52, 0x4800000\n\t" \
  "s_mov_b64 s[76:77], %[ws]\n\t" \
  "s_add_u32 s76, s76, s52\n\t" \
  "s_addc_u32 s77, s77, 0\n\t" \
  "s_lshl_b32 s52, s53, 16\n\t" \
  "s_add_u32 s52, s52, s54\n\t" \
  "s_lshr_b32 s53, s63, 1\n\t" \
  "s_lshl_b32 s53, s53, 15\n\t" \
  "s_add_u32 s52, s52, s53\n\t" \
  "s_and_b32 s53, s63, 1\n\t" \
  "s_lshl_b32 s53, s53, 12\n\t" \
  "s_add_u32 s52, s52, s53\n\t" \
  "s_mov_b64 s[78:79], %[wd]\n\t" \
  "s_add_u32 s78, s78, s52\n\t" \
  "s_addc_u32 s79, s79, 0\n\t" \
  "s_lshl_b32 s62, s63, 12\n\t" \
  "v_lshlrev_b32_e32 v34, 2, v0\n\t" \
  "v_lshrrev_b32_e32 v10, 4, v0\n\t" \
  "v_bfe_u32 v11, v0, 1, 3\n\t" \
  "v_xor_b32_e32 v11, v10, v11\n\t" \
  "v_lshlrev_b32_e32 v11, 5, v11\n\t" \
  "v_lshl_or_b32 v11, v10, 10, v11\n\t" \
  "v_and_b32_e32 v12, 1, v0\n\t" \
  "v_lshlrev_b32_e32 v12, 4, v12\n\t" \
  "v_or_b32_e32 v16, v11, v12\n\t" \
  "v_xor_b32_e32 v17, 128, v16\n\t" \
  "v_add_u32_e32 v18, 6144, v16\n\t" \
  "v_add_u32_e32 v19, 9216, v17\n\t" \
  "v_add_u32_e32 v17, 3072, v17\n\t" \
  "v_add_u32_e32 v24, 0, v16\n\t" \
  "v_add_u32_e32 v25, 4096, v17\n\t" \
  "v_add_u32_e32 v26, 8192, v18\n\t" \
  "v_add_u32_e32 v27, 12288, v19\n\t" \
  "s_lshl_b32 s52, s70, 2\n\t" \
  "s_sub_u32 s52, s52, 1\n\t" \
  "v_min_u32_e32 v13, s52, v0\n\t" \
  "v_lshlrev_b32_e32 v13, 3, v13\n\t" \
  "s_lshl_b32 s52, s56, 10\n\t" \
  "s_lshl_b32 s53, s71, 5\n\t" \
  "s_add_u32 s52, s52, s53\n\t" \
  "s_add_u32 s52, s52, 0x14000\n\t" \
  "v_add_u32_e32 v13, s52, v13\n\t" \
  "s_lshl_b32 s52, s56, 2\n\t" \
  "s_add_u32 s52, s52, s57\n\t" \
  "s_lshl_b32 s52, s52, 18\n\t" \
  "s_lshl_b32 s53, s58, 15\n\t" \
  "s_add_u32 s52, s52, s53\n\t" \
  "s_lshl_b32 s53, s63, 13\n\t" \
  "s_add_u32 s52, s52, s53\n\t" \
  "s_add_u32 s52, s52, 0x5020000\n\t" \
  "s_mov_b64 s[54:55], %[ws]\n\t" \
  "s_add_u32 s54, s54, s52\n\t" \
  "s_addc_u32 s55, s55, 0\n\t" \
  "v_mov_b32_e32 v44, -1\n\t" \
  "v_mov_b32_e32 v45, -1\n\t" \
  "v_mov_b32_e32 v46, -1\n\t" \
  "v_mov_b32_e32 v47, -1\n\t" \
  "global_store_dwordx4 v1, v[44:47], s[54:55] offset:0 sc1\n\t" \
  "global_store_dwordx4 v1, v[44:47], s[54:55] offset:1024 sc1\n\t" \
  "global_store_dwordx4 v1, v[44:47], s[54:55] offset:2048 sc1\n\t" \
  "global_store_dwordx4 v1, v[44:47], s[54:55] offset:3072 sc1\n\t" \
  "global_store_dwordx4 v9, v[44:47], s[54:55] offset:0 sc1\n\t" \
  "global_store_dwordx4 v9, v[44:47], s[54:55] offset:1024 sc1\n\t" \
  "global_store_dwordx4 v9, v[44:47], s[54:55] offset:2048 sc1\n\t" \
  "global_store_dwordx4 v9, v[44:47], s[54:55] offset:3072 sc1\n\t" \
  "s_cmp_lg_u32 %[bid], 0\n\t" \
  "s_cbranch_scc1 NB_%=\n\t" \
  "s_cmp_lg_u32 %[w], 4\n\t" \
  "s_cbranch_scc1 NB_%=\n\t" \
  "v_add_u32_e32 v36, 0x11000, v1\n\t" \
  "global_load_dwordx4 v[40:43], v36, s[60:61] sc1\n\t" \
  "s_waitcnt vmcnt(0)\n\t" \
  "v_cmp_ne_u64_e32 vcc, s[36:37], v[40:41]\n\t" \
  "v_cmp_ne_u64_e64 s[54:55], s[36:37], v[42:43]\n\t" \
  "s_nop 0\n\t" \
  "s_or_b64 vcc, vcc, s[54:55]\n\t" \
  "s_nop 0\n\t" \
  "s_cbranch_vccnz PSe0_%=\n\t" \
  "PRe0_%=:\n\t" \
  "v_add_u32_e32 v36, 0x11400, v1\n\t" \
  "global_load_dwordx4 v[40:43], v36, s[60:61] sc1\n\t" \
  "s_waitcnt vmcnt(0)\n\t" \
  "v_cmp_ne_u64_e32 vcc, s[36:37], v[40:41]\n\t" \
  "v_cmp_ne_u64_e64 s[54:55], s[36:37], v[42:43]\n\t" \
  "s_nop 0\n\t" \
  "s_or_b64 vcc, vcc, s[54:55]\n\t" \
  "s_nop 0\n\t" \
  "s_cbranch_vccnz PSe1_%=\n\t" \
  "PRe1_%=:\n\t" \
  "s_mov_b32 s52, 0x10000\n\t" \
  "v_mov_b32_e32 v40, s36\n\t" \
  "v_mov_b32_e32 v41, s37\n\t" \
  "v_mov_b32_e32 v36, s52\n\t" \
  "s_mov_b64 exec, 1\n\t" \
  "global_store_dwordx2 v36, v[40:41], s[60:61] sc1\n\t" \
  "s_mov_b64 exec, -1\n\t" \
  "NB_%=:\n\t" \
  "s_sub_u32 s59, 15, s66\n\t" \
  "s_cmp_lt_u32 %[w], 4\n\t" \
  "s_cselect_b32 s59, s59, s66\n\t" \
  "s_lshl_b32 s52, s59, 7\n\t" \
  "s_lshl_b32 s53, s63, 5\n\t" \
  "s_add_u32 s52, s52, s53\n\t" \
  "s_lshl_b32 s52, s59, 5\n\t" \
  "s_add_u32 s52, s52, s65\n\t" \
  "s_lshl_b32 s52, s52, 5\n\t" \
  "s_lshr_b32 s53, s53, 2\n\t" \
  "s_add_u32 s52, s52, s53\n\t" \
  "s_add_u32 s52, s52, 0x18000\n\t" \
  "s_mov_b32 s80, s52\n\t" \
  "s_setprio 0\n\t" \
  "global_load_dwordx2 v[28:29], v13, s[60:61] sc1\n\t" \
  "s_cmp_ge_u32 s43, s68\n\t" \
  "s_cbranch_scc1 LX_%=\n\t" \
  "s_waitcnt vmcnt(25)\n\t" \
  "LL_%=:\n\t" \
  "s_add_u32 s43, s43, 1\n\t" \
  "s_barrier\n\t" \
  "s_cmp_ge_u32 s43, s68\n\t" \
  "s_cbranch_scc1 LX_%=\n\t" \
  "s_cmp_lt_u32 s43, s81\n\t" \
  "s_cbranch_scc1 LK_%=\n\t" \
  "s_waitcnt vmcnt(0)\n\t" \
  "global_load_dwordx2 v[28:29], v13, s[60:61] sc1\n\t" \
  "s_branch LL_%=\n\t" \
  "LK_%=:\n\t" \
  "s_mov_b32 m0, s42\n\t" \
  "s_nop 0\n\t" \
  "global_load_lds_dwordx4 v1, s[40:41] offset:0 sc1\n\t" \
  "global_load_lds_dwordx4 v1, s[40:41] offset:1024 sc1\n\t" \
  "global_load_lds_dwordx4 v1, s[40:41] offset:2048 sc1\n\t" \
  "global_load_lds_dwordx4 v1, s[40:41] offset:3072 sc1\n\t" \
  "s_add_u32 m0, s42, 0x1000\n\t" \
  "s_nop 0\n\t" \
  "global_load_lds_dwordx4 v9, s[40:41] offset:0 sc1\n\t" \
  "global_load_lds_dwordx4 v9, s[40:41] offset:1024 sc1\n\t" \
  "global_load_lds_dwordx4 v9, s[40:41] offset:2048 sc1\n\t" \
  "global_load_lds_dwordx4 v9, s[40:41] offset:3072 sc1\n\t" \
  "s_add_u32 s40, s40, 0x8000\n\t" \
  "s_addc_u32 s41, s41, 0\n\t" \
  "s_add_u32 s42, s42, 0x8000\n\t" \
  "s_and_b32 s42, s42, 0x1ffff\n\t" \
  "s_waitcnt vmcnt(8)\n\t" \
  "global_load_dwordx2 v[28:29], v13, s[60:61] sc1\n\t" \
  "s_branch LL_%=\n\t" \
  "LX_%=:\n\t" \
  "s_waitcnt vmcnt(0)\n\t" \
  "v_cmp_ne_u64_e32 vcc, s[36:37], v[28:29]\n\t" \
  "s_nop 0\n\t" \
  "s_cbranch_vccnz PSh_%=\n\t" \
  "PRh_%=:\n\t" \
  "v_mov_b32_e32 v32, s36\n\t" \
  "v_mov_b32_e32 v33, s37\n\t" \
  "v_mov_b32_e32 v14, s80\n\t" \
  "s_mov_b64 exec, 1\n\t" \
  "global_store_dwordx2 v14, v[32:33], s[60:61] sc1\n\t" \
  "s_mov_b64 exec, -1\n\t" \
  "s_add_u32 m0, s50, s62\n\t" \
  "s_nop 0\n\t" \
  "global_load_lds_dwordx4 v1, s[76:77] offset:0 sc1\n\t" \
  "global_load_lds_dwordx4 v1, s[76:77] offset:1024 sc1\n\t" \
  "global_load_lds_dwordx4 v1, s[76:77] offset:2048 sc1\n\t" \
  "global_load_lds_dwordx4 v1, s[76:77] offset:3072 sc1\n\t" \
  "s_add_u32 m0, m0, 0x4000\n\t" \
  "s_nop 0\n\t" \
  "global_load_lds_dwordx4 v24, s[78:79] offset:0\n\t" \
  "global_load_lds_dwordx4 v25, s[78:79] offset:1024\n\t" \
  "global_load_lds_dwordx4 v26, s[78:79] offset:2048\n\t" \
  "global_load_lds_dwordx4 v27, s[78:79] offset:3072\n\t" \
  "s_add_u32 s76, s76, 0x4000\n\t" \
  "s_addc_u32 s77, s77, 0\n\t" \
  "s_add_u32 s78, s78, 0x10000\n\t" \
  "s_addc_u32 s79, s79, 0\n\t" \
  "s_add_u32 s48, s48, 0x8000\n\t" \
  "s_cmp_eq_u32 s48, 0x28000\n\t" \
  "s_cselect_b32 s48, 0, s48\n\t" \
  "s_xor_b32 s50, s48, s49\n\t" \
  "s_cmp_eq_u32 s48, 0x20000\n\t" \
  "s_cselect_b32 s50, s48, s50\n\t" \
  "s_add_u32 m0, s50, s62\n\t" \
  "s_nop 0\n\t" \
  "global_load_lds_dwordx4 v1, s[76:77] offset:0 sc1\n\t" \
  "global_load_lds_dwordx4 v1, s[76:77] offset:1024 sc1\n\t" \
  "global_load_lds_dwordx4 v1, s[76:77] offset:2048 sc1\n\t" \
  "global_load_lds_dwordx4 v1, s[76:77] offset:3072 sc1\n\t" \
  "s_add_u32 m0, m0, 0x4000\n\t" \
  "s_nop 0\n\t" \
  "global_load_lds_dwordx4 v24, s[78:79] offset:0\n\t" \
  "global_load_lds_dwordx4 v25, s[78:79] offset:1024\n\t" \
  "global_load_lds_dwordx4 v26, s[78:79] offset:2048\n\t" \
  "global_load_lds_dwordx4 v27, s[78:79] offset:3072\n\t" \
  "s_add_u32 s76, s76, 0x4000\n\t" \
  "s_addc_u32 s77, s77, 0\n\t" \
  "s_add_u32 s78, s78, 0x10000\n\t" \
  "s_addc_u32 s79, s79, 0\n\t" \
  "s_add_u32 s48, s48, 0x8000\n\t" \
  "s_cmp_eq_u32 s48, 0x28000\n\t" \
  "s_cselect_b32 s48, 0, s48\n\t" \
  "s_xor_b32 s50, s48, s49\n\t" \
  "s_cmp_eq_u32 s48, 0x20000\n\t" \
  "s_cselect_b32 s50, s48, s50\n\t" \
  "s_barrier\n\t" \
  "v_and_b32_e32 v13, 31, v0\n\t" \
  "v_lshlrev_b32_e32 v13, 3, v13\n\t" \
  "s_lshl_b32 s52, s56, 10\n\t" \
  "s_lshl_b32 s53, s58, 8\n\t" \
  "s_add_u32 s52, s52, s53\n\t" \
  "s_add_u32 s52, s52, 0x18000\n\t" \
  "v_add_u32_e32 v13, s52, v13\n\t" \
  "s_waitcnt vmcnt(8)\n\t" \
  "s_barrier\n\t" \
  "s_add_u32 m0, s50, s62\n\t" \
  "s_nop 0\n\t" \
  "global_load_lds_dwordx4 v1, s[76:77] offset:0 sc1\n\t" \
  "global_load_lds_dwordx4 v1, s[76:77] offset:1024 sc1\n\t" \
  "global_load_lds_dwordx4 v1, s[76:77] offset:2048 sc1\n\t" \
  "global_load_lds_dwordx4 v1, s[76:77] offset:3072 sc1\n\t" \
  "s_add_u32 m0, m0, 0x4000\n\t" \
  "s_nop 0\n\t" \
  "global_load_lds_dwordx4 v24, s[78:79] offset:0\n\t" \
  "global_load_lds_dwordx4 v25, s[78:79] offset:1024\n\t" \
  "global_load_lds_dwordx4 v26, s[78:79] offset:2048\n\t" \
  "global_load_lds_dwordx4 v27, s[78:79] offset:3072\n\t" \
  "s_add_u32 s76, s76, 0x4000\n\t" \
  "s_addc_u32 s77, s77, 0\n\t" \
  "s_add_u32 s78, s78, 0x10000\n\t" \
  "s_addc_u32 s79, s79, 0\n\t" \
  "s_add_u32 s48, s48, 0x8000\n\t" \
  "s_cmp_eq_u32 s48, 0x28000\n\t" \
  "s_cselect_b32 s48, 0, s48\n\t" \
  "s_xor_b32 s50, s48, s49\n\t" \
  "s_cmp_eq_u32 s48, 0x20000\n\t" \
  "s_cselect_b32 s50, s48, s50\n\t" \
  "s_add_u32 m0, s50, s62\n\t" \
  "s_nop 0\n\t" \
  "global_load_lds_dwordx4 v1, s[76:77] offset:0 sc1\n\t" \
  "global_load_lds_dwordx4 v1, s[76:77] offset:1024 sc1\n\t" \
  "global_load_lds_dwordx4 v1, s[76:77] offset:2048 sc1\n\t" \
  "global_load_lds_dwordx4 v1, s[76:77] offset:3072 sc1\n\t" \
  "s_add_u32 m0, m0, 0x4000\n\t" \
  "s_nop 0\n\t" \
  "global_load_lds_dwordx4 v24, s[78:79] offset:0\n\t" \
  "global_load_lds_dwordx4 v25, s[78:79] offset:1024\n\t" \
  "global_load_lds_dwordx4 v26, s[78:79] offset:2048\n\t" \
  "global_load_lds_dwordx4 v27, s[78:79] offset:3072\n\t" \
  "s_add_u32 s76, s76, 0x4000\n\t" \
  "s_addc_u32 s77, s77, 0\n\t" \
  "s_add_u32 s78, s78, 0x10000\n\t" \
  "s_addc_u32 s79, s79, 0\n\t" \
  "s_add_u32 s48, s48, 0x8000\n\t" \
  "s_cmp_eq_u32 s48, 0x28000\n\t" \
  "s_cselect_b32 s48, 0, s48\n\t" \
  "s_xor_b32 s50, s48, s49\n\t" \
  "s_cmp_eq_u32 s48, 0x20000\n\t" \
  "s_cselect_b32 s50, s48, s50\n\t" \
  "s_add_u32 m0, s50, s62\n\t" \
  "s_nop 0\n\t" \
  "global_load_lds_dwordx4 v1, s[76:77] offset:0 sc1\n\t" \
  "global_load_lds_dwordx4 v1, s[76:77] offset:1024 sc1\n\t" \
  "global_load_lds_dwordx4 v1, s[76:77] offset:2048 sc1\n\t" \
  "global_load_lds_dwordx4 v1, s[76:77] offset:3072 sc1\n\t" \
  "s_add_u32 m0, m0, 0x4000\n\t" \
  "s_nop 0\n\t" \
  "global_load_lds_dwordx4 v24, s[78:79] offset:0\n\t" \
  "global_load_lds_dwordx4 v25, s[78:79] offset:1024\n\t" \
  "global_load_lds_dwordx4 v26, s[78:79] offset:2048\n\t" \
  "global_load_lds_dwordx4 v27, s[78:79] offset:3072\n\t" \
  "s_add_u32 s76, s76, 0x4000\n\t" \
  "s_addc_u32 s77, s77, 0\n\t" \
  "s_add_u32 s78, s78, 0x10000\n\t" \
  "s_addc_u32 s79, s79, 0\n\t" \
  "s_add_u32 s48, s48, 0x8000\n\t" \
  "s_cmp_eq_u32 s48, 0x28000\n\t" \
  "s_cselect_b32 s48, 0, s48\n\t" \
  "s_xor_b32 s50, s48, s49\n\t" \
  "s_cmp_eq_u32 s48, 0x20000\n\t" \
  "s_cselect_b32 s50, s48, s50\n\t" \
  "s_sub_u32 s51, s70, 5\n\t" \
  "s_cmp_eq_u32 s51, 0\n\t" \
  "s_cbranch_scc1 XQD1_%=\n\t" \
  "QD1_%=:\n\t" \
  "s_waitcnt vmcnt(24)\n\t" \
  "s_barrier\n\t" \
  "s_add_u32 m0, s50, s62\n\t" \
  "s_nop 0\n\t" \
  "global_load_lds_dwordx4 v1, s[76:77] offset:0 sc1\n\t" \
  "global_load_lds_dwordx4 v1, s[76:77] offset:1024 sc1\n\t" \
  "global_load_lds_dwordx4 v1, s[76:77] offset:2048 sc1\n\t" \
  "global_load_lds_dwordx4 v1, s[76:77] offset:3072 sc1\n\t" \
  "s_add_u32 m0, m0, 0x4000\n\t" \
  "s_nop 0\n\t" \
  "global_load_lds_dwordx4 v24, s[78:79] offset:0\n\t" \
  "global_load_lds_dwordx4 v25, s[78:79] offset:1024\n\t" \
  "global_load_lds_dwordx4 v26, s[78:79] offset:2048\n\t" \
  "global_load_lds_dwordx4 v27, s[78:79] offset:3072\n\t" \
  "s_add_u32 s76, s76, 0x4000\n\t" \
  "s_addc_u32 s77, s77, 0\n\t" \
  "s_add_u32 s78, s78, 0x10000\n\t" \
  "s_addc_u32 s79, s79, 0\n\t" \
  "s_add_u32 s48, s48, 0x8000\n\t" \
  "s_cmp_eq_u32 s48, 0x28000\n\t" \
  "s_cselect_b32 s48, 0, s48\n\t" \
  "s_xor_b32 s50, s48, s49\n\t" \
  "s_cmp_eq_u32 s48, 0x20000\n\t" \
  "s_cselect_b32 s50, s48, s50\n\t" \
  "s_sub_u32 s51, s51, 1\n\t" \
  "s_cmp_lg_u32 s51, 0\n\t" \
  "s_cbranch_scc1 QD1_%=\n\t" \
  "XQD1_%=:\n\t" \
  "global_load_dwordx2 v[28:29], v13, s[60:61] sc1\n\t" \
  "s_waitcnt vmcnt(25)\n\t" \
  "s_barrier\n\t" \
  "s_waitcnt vmcnt(17)\n\t" \
  "s_barrier\n\t" \
  "s_waitcnt vmcnt(0)\n\t" \
  "v_cmp_ne_u64_e32 vcc, s[36:37], v[28:29]\n\t" \
  "s_nop 0\n\t" \
  "s_cbranch_vccnz PSc_%=\n\t" \
  "PRc_%=:\n\t" \
  "s_add_u32 m0, s50, s62\n\t" \
  "s_nop 0\n\t" \
  "global_load_lds_dwordx4 v1, s[72:73] offset:0 sc1\n\t" \
  "global_load_lds_dwordx4 v1, s[72:73] offset:1024 sc1\n\t" \
  "global_load_lds_dwordx4 v1, s[72:73] offset:2048 sc1\n\t" \
  "global_load_lds_dwordx4 v1, s[72:73] offset:3072 sc1\n\t" \
  "s_add_u32 m0, m0, 0x4000\n\t" \
  "s_nop 0\n\t" \
  "global_load_lds_dwordx4 v16, s[74:75] offset:0\n\t" \
  "global_load_lds_dwordx4 v17, s[74:75] offset:1024\n\t" \
  "global_load_lds_dwordx4 v18, s[74:75] offset:2048\n\t" \
  "global_load_lds_dwordx4 v19, s[74:75] offset:3072\n\t" \
  "s_add_u32 s72, s72, 0x4000\n\t" \
  "s_addc_u32 s73, s73, 0\n\t" \
  "s_add_u32 s74, s74, 0x10000\n\t" \
  "s_addc_u32 s75, s75, 0\n\t" \
  "s_add_u32 s48, s48, 0x8000\n\t" \
  "s_cmp_eq_u32 s48, 0x28000\n\t" \
  "s_cselect_b32 s48, 0, s48\n\t" \
  "s_xor_b32 s50, s48, s49\n\t" \
  "s_cmp_eq_u32 s48, 0x20000\n\t" \
  "s_cselect_b32 s50, s48, s50\n\t" \
  "s_add_u32 m0, s50, s62\n\t" \
  "s_nop 0\n\t" \
  "global_load_lds_dwordx4 v1, s[72:73] offset:0 sc1\n\t" \
  "global_load_lds_dwordx4 v1, s[72:73] offset:1024 sc1\n\t" \
  "global_load_lds_dwordx4 v1, s[72:73] offset:2048 sc1\n\t" \
  "global_load_lds_dwordx4 v1, s[72:73] offset:3072 sc1\n\t" \
  "s_add_u32 m0, m0, 0x4000\n\t" \
  "s_nop 0\n\t" \
  "global_load_lds_dwordx4 v16, s[74:75] offset:0\n\t" \
  "global_load_lds_dwordx4 v17, s[74:75] offset:1024\n\t" \
  "global_load_lds_dwordx4 v18, s[74:75] offset:2048\n\t" \
  "global_load_lds_dwordx4 v19, s[74:75] offset:3072\n\t" \
  "s_add_u32 s72, s72, 0x4000\n\t" \
  "s_addc_u32 s73, s73, 0\n\t" \
  "s_add_u32 s74, s74, 0x10000\n\t" \
  "s_addc_u32 s75, s75, 0\n\t" \
  "s_add_u32 s48, s48, 0x8000\n\t" \
  "s_cmp_eq_u32 s48, 0x28000\n\t" \
  "s_cselect_b32 s48, 0, s48\n\t" \
  "s_xor_b32 s50, s48, s49\n\t" \
  "s_cmp_eq_u32 s48, 0x20000\n\t" \
  "s_cselect_b32 s50, s48, s50\n\t" \
  "s_barrier\n\t" \
  "s_add_u32 m0, s50, s62\n\t" \
  "s_nop 0\n\t" \
  "global_load_lds_dwordx4 v1, s[72:73] offset:0 sc1\n\t" \
  "global_load_lds_dwordx4 v1, s[72:73] offset:1024 sc1\n\t" \
  "global_load_lds_dwordx4 v1, s[72:73] offset:2048 sc1\n\t" \
  "global_load_lds_dwordx4 v1, s[72:73] offset:3072 sc1\n\t" \
  "s_add_u32 m0, m0, 0x4000\n\t" \
  "s_nop 0\n\t" \
  "global_load_lds_dwordx4 v16, s[74:75] offset:0\n\t" \
  "global_load_lds_dwordx4 v17, s[74:75] offset:1024\n\t" \
  "global_load_lds_dwordx4 v18, s[74:75] offset:2048\n\t" \
  "global_load_lds_dwordx4 v19, s[74:75] offset:3072\n\t" \
  "s_add_u32 s72, s72, 0x4000\n\t" \
  "s_addc_u32 s73, s73, 0\n\t" \
  "s_add_u32 s74, s74, 0x10000\n\t" \
  "s_addc_u32 s75, s75, 0\n\t" \
  "s_add_u32 s48, s48, 0x8000\n\t" \
  "s_cmp_eq_u32 s48, 0x28000\n\t" \
  "s_cselect_b32 s48, 0, s48\n\t" \
  "s_xor_b32 s50, s48, s49\n\t" \
  "s_cmp_eq_u32 s48, 0x20000\n\t" \
  "s_cselect_b32 s50, s48, s50\n\t" \
  "s_barrier\n\t" \
  "s_add_u32 m0, s50, s62\n\t" \
  "s_nop 0\n\t" \
  "global_load_lds_dwordx4 v1, s[72:73] offset:0 sc1\n\t" \
  "global_load_lds_dwordx4 v1, s[72:73] offset:1024 sc1\n\t" \
  "global_load_lds_dwordx4 v1, s[72:73] offset:2048 sc1\n\t" \
  "global_load_lds_dwordx4 v1, s[72:73] offset:3072 sc1\n\t" \
  "s_add_u32 m0, m0, 0x4000\n\t" \
  "s_nop 0\n\t" \
  "global_load_lds_dwordx4 v16, s[74:75] offset:0\n\t" \
  "global_load_lds_dwordx4 v17, s[74:75] offset:1024\n\t" \
  "global_load_lds_dwordx4 v18, s[74:75] offset:2048\n\t" \
  "global_load_lds_dwordx4 v19, s[74:75] offset:3072\n\t" \
  "s_add_u32 s72, s72, 0x4000\n\t" \
  "s_addc_u32 s73, s73, 0\n\t" \
  "s_add_u32 s74, s74, 0x10000\n\t" \
  "s_addc_u32 s75, s75, 0\n\t" \
  "s_add_u32 s48, s48, 0x8000\n\t" \
  "s_cmp_eq_u32 s48, 0x28000\n\t" \
  "s_cselect_b32 s48, 0, s48\n\t" \
  "s_xor_b32 s50, s48, s49\n\t" \
  "s_cmp_eq_u32 s48, 0x20000\n\t" \
  "s_cselect_b32 s50, s48, s50\n\t" \
  "s_mov_b32 s51, 12\n\t" \
  "QS_%=:\n\t" \
  "s_waitcnt vmcnt(24)\n\t" \
  "s_barrier\n\t" \
  "s_add_u32 m0, s50, s62\n\t" \
  "s_nop 0\n\t" \
  "global_load_lds_dwordx4 v1, s[72:73] offset:0 sc1\n\t" \
  "global_load_lds_dwordx4 v1, s[72:73] offset:1024 sc1\n\t" \
  "global_load_lds_dwordx4 v1, s[72:73] offset:2048 sc1\n\t" \
  "global_load_lds_dwordx4 v1, s[72:73] offset:3072 sc1\n\t" \
  "s_add_u32 m0, m0, 0x4000\n\t" \
  "s_nop 0\n\t" \
  "global_load_lds_dwordx4 v16, s[74:75] offset:0\n\t" \
  "global_load_lds_dwordx4 v17, s[74:75] offset:1024\n\t" \
  "global_load_lds_dwordx4 v18, s[74:75] offset:2048\n\t" \
  "global_load_lds_dwordx4 v19, s[74:75] offset:3072\n\t" \
  "s_add_u32 s72, s72, 0x4000\n\t" \
  "s_addc_u32 s73, s73, 0\n\t" \
  "s_add_u32 s74, s74, 0x10000\n\t" \
  "s_addc_u32 s75, s75, 0\n\t" \
  "s_add_u32 s48, s48, 0x8000\n\t" \
  "s_cmp_eq_u32 s48, 0x28000\n\t" \
  "s_cselect_b32 s48, 0, s48\n\t" \
  "s_xor_b32 s50, s48, s49\n\t" \
  "s_cmp_eq_u32 s48, 0x20000\n\t" \
  "s_cselect_b32 s50, s48, s50\n\t" \
  "s_sub_u32 s51, s51, 1\n\t" \
  "s_cmp_lg_u32 s51, 0\n\t" \
  "s_cbranch_scc1 QS_%=\n\t" \
  "s_waitcnt vmcnt(24)\n\t" \
  "s_barrier\n\t" \
  "s_waitcnt vmcnt(16)\n\t" \
  "s_barrier\n\t" \
  "s_waitcnt vmcnt(8)\n\t" \
  "s_barrier\n\t" \
  "s_waitcnt vmcnt(0)\n\t" \
  "s_barrier\n\t" \
  "s_barrier\n\t" \
  "s_branch END_%=\n\t" \
  "RALa_%=:\n\t" \
  "s_getpc_b64 s[46:47]\n\t" \
  "s_branch RAX1_%=\n\t" \
  "s_branch RArLa_%=\n\t" \
  "RBLa_%=:\n\t" \
  "s_getpc_b64 s[46:47]\n\t" \
  "s_branch RBX_%=\n\t" \
  "s_branch RBrLa_%=\n\t" \
  "RALb_%=:\n\t" \
  "s_getpc_b64 s[46:47]\n\t" \
  "s_branch RAX0_%=\n\t" \
  "s_branch RArLb_%=\n\t" \
  "RBLb_%=:\n\t" \
  "s_getpc_b64 s[46:47]\n\t" \
  "s_branch RBX_%=\n\t" \
  "s_branch RBrLb_%=\n\t" \
  "RALc_%=:\n\t" \
  "s_getpc_b64 s[46:47]\n\t" \
  "s_branch RAX0_%=\n\t" \
  "s_branch RArLc_%=\n\t" \
  "RBLc_%=:\n\t" \
  "s_getpc_b64 s[46:47]\n\t" \
  "s_branch RBX_%=\n\t" \
  "s_branch RBrLc_%=\n\t" \
  "RALd_%=:\n\t" \
  "s_getpc_b64 s[46:47]\n\t" \
  "s_branch RAX1_%=\n\t" \
  "s_branch RArLd_%=\n\t" \
  "RBLd_%=:\n\t" \
  "s_getpc_b64 s[46:47]\n\t" \
  "s_branch RBX_%=\n\t" \
  "s_branch RBrLd_%=\n\t" \
  "RASa_%=:\n\t" \
  "s_getpc_b64 s[46:47]\n\t" \
  "s_branch RAX1_%=\n\t" \
  "s_branch RArSa_%=\n\t" \
  "RBSa_%=:\n\t" \
  "s_getpc_b64 s[46:47]\n\t" \
  "s_branch RBX_%=\n\t" \
  "s_branch RBrSa_%=\n\t" \
  "RASb_%=:\n\t" \
  "s_getpc_b64 s[46:47]\n\t" \
  "s_branch RAX0_%=\n\t" \
  "s_branch RArSb_%=\n\t" \
  "RBSb_%=:\n\t" \
  "s_getpc_b64 s[46:47]\n\t" \
  "s_branch RBX_%=\n\t" \
  "s_branch RBrSb_%=\n\t" \
  "RASc_%=:\n\t" \
  "s_getpc_b64 s[46:47]\n\t" \
  "s_branch RAX0_%=\n\t" \
  "s_branch RArSc_%=\n\t" \
  "RBSc_%=:\n\t" \
  "s_getpc_b64 s[46:47]\n\t" \
  "s_branch RBX_%=\n\t" \
  "s_branch RBrSc_%=\n\t" \
  "RASd_%=:\n\t" \
  "s_getpc_b64 s[46:47]\n\t" \
  "s_branch RAX1_%=\n\t" \
  "s_branch RArSd_%=\n\t" \
  "RBSd_%=:\n\t" \
  "s_getpc_b64 s[46:47]\n\t" \
  "s_branch RBX_%=\n\t" \
  "s_branch RBrSd_%=\n\t" \
  "RAX0_%=:\n\t" \
  "v_max3_f32 v10, v112, v113, v114\n\t" \
  "v_max3_f32 v11, v120, v121, v122\n\t" \
  "v_max3_f32 v10, v10, v115, v116\n\t" \
  "v_max3_f32 v11, v11, v123, v124\n\t" \
  "v_max3_f32 v10, v10, v117, v118\n\t" \
  "v_max3_f32 v11, v11, v125, v126\n\t" \
  "v_max3_f32 v10, v10, v119, v128\n\t" \
  "v_max3_f32 v11, v11, v127, v136\n\t" \
  "v_max3_f32 v10, v10, v129, v130\n\t" \
  "v_max3_f32 v11, v11, v137, v138\n\t" \
  "v_max3_f32 v10, v10, v131, v132\n\t" \
  "v_max3_f32 v11, v11, v139, v140\n\t" \
  "v_max3_f32 v10, v10, v133, v134\n\t" \
  "v_max3_f32 v11, v11, v141, v142\n\t" \
  "v_max_f32_e32 v10, v10, v135\n\t" \
  "v_max_f32_e32 v11, v11, v143\n\t" \
  "v_mov_b32_e32 v186, v10\n\t" \
  "v_mov_b32_e32 v187, v11\n\t" \
  "s_nop 0\n\t" \
  "v_permlane16_swap_b32_e32 v10, v186\n\t" \
  "v_permlane16_swap_b32_e32 v11, v187\n\t" \
  "s_nop 0\n\t" \
  "v_max_f32_e32 v10, v10, v186\n\t" \
  "v_max_f32_e32 v11, v11, v187\n\t" \
  "v_mov_b32_e32 v186, v10\n\t" \
  "v_mov_b32_e32 v187, v11\n\t" \
  "s_nop 0\n\t" \
  "v_permlane32_swap_b32_e32 v10, v186\n\t" \
  "v_permlane32_swap_b32_e32 v11, v187\n\t" \
  "s_nop 0\n\t" \
  "v_max_f32_e32 v10, v10, v186\n\t" \
  "v_max_f32_e32 v11, v11, v187\n\t" \
  "v_max_f32_e32 v10, 0, v10\n\t" \
  "v_max_f32_e32 v11, 0, v11\n\t" \
  "v_sub_f32_e32 v112, v112, v10\n\t" \
  "v_sub_f32_e32 v113, v113, v10\n\t" \
  "v_sub_f32_e32 v114, v114, v10\n\t" \
  "v_sub_f32_e32 v115, v115, v10\n\t" \
  "v_sub_f32_e32 v116, v116, v10\n\t" \
  "v_sub_f32_e32 v117, v117, v10\n\t" \
  "v_sub_f32_e32 v118, v118, v10\n\t" \
  "v_sub_f32_e32 v119, v119, v10\n\t" \
  "v_sub_f32_e32 v120, v120, v11\n\t" \
  "v_sub_f32_e32 v121, v121, v11\n\t" \
  "v_sub_f32_e32 v122, v122, v11\n\t" \
  "v_sub_f32_e32 v123, v123, v11\n\t" \
  "v_sub_f32_e32 v124, v124, v11\n\t" \
  "v_sub_f32_e32 v125, v125, v11\n\t" \
  "v_sub_f32_e32 v126, v126, v11\n\t" \
  "v_sub_f32_e32 v127, v127, v11\n\t" \
  "v_sub_f32_e32 v128, v128, v10\n\t" \
  "v_sub_f32_e32 v129, v129, v10\n\t" \
  "v_sub_f32_e32 v130, v130, v10\n\t" \
  "v_sub_f32_e32 v131, v131, v10\n\t" \
  "v_sub_f32_e32 v132, v132, v10\n\t" \
  "v_sub_f32_e32 v133, v133, v10\n\t" \
  "v_sub_f32_e32 v134, v134, v10\n\t" \
  "v_sub_f32_e32 v135, v135, v10\n\t" \
  "v_sub_f32_e32 v136, v136, v11\n\t" \
  "v_sub_f32_e32 v137, v137, v11\n\t" \
  "v_sub_f32_e32 v138, v138, v11\n\t" \
  "v_sub_f32_e32 v139, v139, v11\n\t" \
  "v_sub_f32_e32 v140, v140, v11\n\t" \
  "v_sub_f32_e32 v141, v141, v11\n\t" \
  "v_sub_f32_e32 v142, v142, v11\n\t" \
  "v_sub_f32_e32 v143, v143, v11\n\t" \
  "v_sub_f32_e32 v176, v176, v10\n\t" \
  "v_sub_f32_e32 v177, v177, v10\n\t" \
  "v_sub_f32_e32 v178, v178, v10\n\t" \
  "v_sub_f32_e32 v179, v179, v10\n\t" \
  "v_sub_f32_e32 v180, v180, v11\n\t" \
  "v_sub_f32_e32 v181, v181, v11\n\t" \
  "v_sub_f32_e32 v182, v182, v11\n\t" \
  "v_sub_f32_e32 v183, v183, v11\n\t" \
  "v_exp_f32_e64 v186, -v10\n\t" \
  "v_exp_f32_e64 v187, -v11\n\t" \
  "s_mov_b32 s44, 1\n\t" \
  "s_mov_b32 s81, s43\n\t" \
  "s_add_u32 s46, s46, 4\n\t" \
  "s_addc_u32 s47, s47, 0\n\t" \
  "s_setpc_b64 s[46:47]\n\t" \
  "RAX1_%=:\n\t" \
  "v_max3_f32 v10, v144, v145, v146\n\t" \
  "v_max3_f32 v11, v152, v153, v154\n\t" \
  "v_max3_f32 v10, v10, v147, v148\n\t" \
  "v_max3_f32 v11, v11, v155, v156\n\t" \
  "v_max3_f32 v10, v10, v149, v150\n\t" \
  "v_max3_f32 v11, v11, v157, v158\n\t" \
  "v_max3_f32 v10, v10, v151, v160\n\t" \
  "v_max3_f32 v11, v11, v159, v168\n\t" \
  "v_max3_f32 v10, v10, v161, v162\n\t" \
  "v_max3_f32 v11, v11, v169, v170\n\t" \
  "v_max3_f32 v10, v10, v163, v164\n\t" \
  "v_max3_f32 v11, v11, v171, v172\n\t" \
  "v_max3_f32 v10, v10, v165, v166\n\t" \
  "v_max3_f32 v11, v11, v173, v174\n\t" \
  "v_max_f32_e32 v10, v10, v167\n\t" \
  "v_max_f32_e32 v11, v11, v175\n\t" \
  "v_mov_b32_e32 v186, v10\n\t" \
  "v_mov_b32_e32 v187, v11\n\t" \
  "s_nop 0\n\t" \
  "v_permlane16_swap_b32_e32 v10, v186\n\t" \
  "v_permlane16_swap_b32_e32 v11, v187\n\t" \
  "s_nop 0\n\t" \
  "v_max_f32_e32 v10, v10, v186\n\t" \
  "v_max_f32_e32 v11, v11, v187\n\t" \
  "v_mov_b32_e32 v186, v10\n\t" \
  "v_mov_b32_e32 v187, v11\n\t" \
  "s_nop 0\n\t" \
  "v_permlane32_swap_b32_e32 v10, v186\n\t" \
  "v_permlane32_swap_b32_e32 v11, v187\n\t" \
  "s_nop 0\n\t" \
  "v_max_f32_e32 v10, v10, v186\n\t" \
  "v_max_f32_e32 v11, v11, v187\n\t" \
  "v_max_f32_e32 v10, 0, v10\n\t" \
  "v_max_f32_e32 v11, 0, v11\n\t" \
  "v_sub_f32_e32 v144, v144, v10\n\t" \
  "v_sub_f32_e32 v145, v145, v10\n\t" \
  "v_sub_f32_e32 v146, v146, v10\n\t" \
  "v_sub_f32_e32 v147, v147, v10\n\t" \
  "v_sub_f32_e32 v148, v148, v10\n\t" \
  "v_sub_f32_e32 v149, v149, v10\n\t" \
  "v_sub_f32_e32 v150, v150, v10\n\t" \
  "v_sub_f32_e32 v151, v151, v10\n\t" \
  "v_sub_f32_e32 v152, v152, v11\n\t" \
  "v_sub_f32_e32 v153, v153, v11\n\t" \
  "v_sub_f32_e32 v154, v154, v11\n\t" \
  "v_sub_f32_e32 v155, v155, v11\n\t" \
  "v_sub_f32_e32 v156, v156, v11\n\t" \
  "v_sub_f32_e32 v157, v157, v11\n\t" \
  "v_sub_f32_e32 v158, v158, v11\n\t" \
  "v_sub_f32_e32 v159, v159, v11\n\t" \
  "v_sub_f32_e32 v160, v160, v10\n\t" \
  "v_sub_f32_e32 v161, v161, v10\n\t" \
  "v_sub_f32_e32 v162, v162, v10\n\t" \
  "v_sub_f32_e32 v163, v163, v10\n\t" \
  "v_sub_f32_e32 v164, v164, v10\n\t" \
  "v_sub_f32_e32 v165, v165, v10\n\t" \
  "v_sub_f32_e32 v166, v166, v10\n\t" \
  "v_sub_f32_e32 v167, v167, v10\n\t" \
  "v_sub_f32_e32 v168, v168, v11\n\t" \
  "v_sub_f32_e32 v169, v169, v11\n\t" \
  "v_sub_f32_e32 v170, v170, v11\n\t" \
  "v_sub_f32_e32 v171, v171, v11\n\t" \
  "v_sub_f32_e32 v172, v172, v11\n\t" \
  "v_sub_f32_e32 v173, v173, v11\n\t" \
  "v_sub_f32_e32 v174, v174, v11\n\t" \
  "v_sub_f32_e32 v175, v175, v11\n\t" \
  "v_sub_f32_e32 v176, v176, v10\n\t" \
  "v_sub_f32_e32 v177, v177, v10\n\t" \
  "v_sub_f32_e32 v178, v178, v10\n\t" \
  "v_sub_f32_e32 v179, v179, v10\n\t" \
  "v_sub_f32_e32 v180, v180, v11\n\t" \
  "v_sub_f32_e32 v181, v181, v11\n\t" \
  "v_sub_f32_e32 v182, v182, v11\n\t" \
  "v_sub_f32_e32 v183, v183, v11\n\t" \
  "v_exp_f32_e64 v186, -v10\n\t" \
  "v_exp_f32_e64 v187, -v11\n\t" \
  "s_mov_b32 s44, 1\n\t" \
  "s_mov_b32 s81, s43\n\t" \
  "s_add_u32 s46, s46, 4\n\t" \
  "s_addc_u32 s47, s47, 0\n\t" \
  "s_setpc_b64 s[46:47]\n\t" \
  "RBX_%=:\n\t" \
  "s_nop 9\n\t" \
  "v_mul_f32_e32 v16, v16, v186\n\t" \
  "v_mul_f32_e32 v17, v17, v186\n\t" \
  "v_mul_f32_e32 v18, v18, v186\n\t" \
  "v_mul_f32_e32 v19, v19, v186\n\t" \
  "v_mul_f32_e32 v20, v20, v186\n\t" \
  "v_mul_f32_e32 v21, v21, v186\n\t" \
  "v_mul_f32_e32 v22, v22, v186\n\t" \
  "v_mul_f32_e32 v23, v23, v186\n\t" \
  "v_mul_f32_e32 v24, v24, v187\n\t" \
  "v_mul_f32_e32 v25, v25, v187\n\t" \
  "v_mul_f32_e32 v26, v26, v187\n\t" \
  "v_mul_f32_e32 v27, v27, v187\n\t" \
  "v_mul_f32_e32 v28, v28, v187\n\t" \
  "v_mul_f32_e32 v29, v29, v187\n\t" \
  "v_mul_f32_e32 v30, v30, v187\n\t" \
  "v_mul_f32_e32 v31, v31, v187\n\t" \
  "v_mul_f32_e32 v32, v32, v186\n\t" \
  "v_mul_f32_e32 v33, v33, v186\n\t" \
  "v_mul_f32_e32 v34, v34, v186\n\t" \
  "v_mul_f32_e32 v35, v35, v186\n\t" \
  "v_mul_f32_e32 v36, v36, v186\n\t" \
  "v_mul_f32_e32 v37, v37, v186\n\t" \
  "v_mul_f32_e32 v38, v38, v186\n\t" \
  "v_mul_f32_e32 v39, v39, v186\n\t" \
  "v_mul_f32_e32 v40, v40, v187\n\t" \
  "v_mul_f32_e32 v41, v41, v187\n\t" \
  "v_mul_f32_e32 v42, v42, v187\n\t" \
  "v_mul_f32_e32 v43, v43, v187\n\t" \
  "v_mul_f32_e32 v44, v44, v187\n\t" \
  "v_mul_f32_e32 v45, v45, v187\n\t" \
  "v_mul_f32_e32 v46, v46, v187\n\t" \
  "v_mul_f32_e32 v47, v47, v187\n\t" \
  "v_mul_f32_e32 v48, v48, v186\n\t" \
  "v_mul_f32_e32 v49, v49, v186\n\t" \
  "v_mul_f32_e32 v50, v50, v186\n\t" \
  "v_mul_f32_e32 v51, v51, v186\n\t" \
  "v_mul_f32_e32 v52, v52, v186\n\t" \
  "v_mul_f32_e32 v53, v53, v186\n\t" \
  "v_mul_f32_e32 v54, v54, v186\n\t" \
  "v_mul_f32_e32 v55, v55, v186\n\t" \
  "v_mul_f32_e32 v56, v56, v187\n\t" \
  "v_mul_f32_e32 v57, v57, v187\n\t" \
  "v_mul_f32_e32 v58, v58, v187\n\t" \
  "v_mul_f32_e32 v59, v59, v187\n\t" \
  "v_mul_f32_e32 v60, v60, v187\n\t" \
  "v_mul_f32_e32 v61, v61, v187\n\t" \
  "v_mul_f32_e32 v62, v62, v187\n\t" \
  "v_mul_f32_e32 v63, v63, v187\n\t" \
  "v_mul_f32_e32 v64, v64, v186\n\t" \
  "v_mul_f32_e32 v65, v65, v186\n\t" \
  "v_mul_f32_e32 v66, v66, v186\n\t" \
  "v_mul_f32_e32 v67, v67, v186\n\t" \
  "v_mul_f32_e32 v68, v68, v186\n\t" \
  "v_mul_f32_e32 v69, v69, v186\n\t" \
  "v_mul_f32_e32 v70, v70, v186\n\t" \
  "v_mul_f32_e32 v71, v71, v186\n\t" \
  "v_mul_f32_e32 v72, v72, v187\n\t" \
  "v_mul_f32_e32 v73, v73, v187\n\t" \
  "v_mul_f32_e32 v74, v74, v187\n\t" \
  "v_mul_f32_e32 v75, v75, v187\n\t" \
  "v_mul_f32_e32 v76, v76, v187\n\t" \
  "v_mul_f32_e32 v77, v77, v187\n\t" \
  "v_mul_f32_e32 v78, v78, v187\n\t" \
  "v_mul_f32_e32 v79, v79, v187\n\t" \
  "v_mul_f32_e32 v4, v4, v186\n\t" \
  "v_mul_f32_e32 v5, v5, v186\n\t" \
  "v_mul_f32_e32 v6, v6, v186\n\t" \
  "v_mul_f32_e32 v7, v7, v186\n\t" \
  "v_mul_f32_e32 v12, v12, v187\n\t" \
  "v_mul_f32_e32 v13, v13, v187\n\t" \
  "v_mul_f32_e32 v14, v14, v187\n\t" \
  "v_mul_f32_e32 v15, v15, v187\n\t" \
  "s_mov_b32 s44, 0\n\t" \
  "s_add_u32 s46, s46, 4\n\t" \
  "s_addc_u32 s47, s47, 0\n\t" \
  "s_setpc_b64 s[46:47]\n\t" \
  "DPLa_%=:\n\t" \
  "s_cmp_lg_u32 s44, 0\n\t" \
  "s_cbranch_scc1 RBLa_%=\n\t" \
  "RBrLa_%=:\n\t" \
  "s_cmp_eq_u32 s43, 1\n\t" \
  "s_cbranch_scc1 HHL_%=\n\t" \
  "HBL_%=:\n\t" \
  "s_cmp_eq_u32 s43, s58\n\t" \
  "s_cbranch_scc1 TEL_%=\n\t" \
  "s_mov_b32 s81, s58\n\t" \
  "s_branch DBLa_%=\n\t" \
  "SKL_%=:\n\t" \
  "v_xor_b32_e32 v8, 0x10000, v8\n\t" \
  "s_barrier\n\t" \
  "ds_read_b128 v[192:195], v8 offset:16384\n\t" \
  "v_exp_f32_e32 v123, v123\n\t" \
  "ds_read_b128 v[196:199], v8 offset:17408\n\t" \
  "v_cvt_pk_bf16_f32 v114, v116, v117\n\t" \
  "ds_read_b128 v[200:203], v8 offset:18432\n\t" \
  "v_exp_f32_e32 v124, v124\n\t" \
  "ds_read_b128 v[204:207], v8 offset:19456\n\t" \
  "v_cvt_pk_bf16_f32 v115, v118, v119\n\t" \
  "ds_read_b128 v[208:211], v8 offset:20480\n\t" \
  "v_exp_f32_e32 v125, v125\n\t" \
  "ds_read_b128 v[212:215], v8 offset:21504\n\t" \
  "v_cvt_pk_bf16_f32 v120, v120, v121\n\t" \
  "ds_read_b128 v[216:219], v8 offset:22528\n\t" \
  "v_exp_f32_e32 v126, v126\n\t" \
  "ds_read_b128 v[220:223], v8 offset:23552\n\t" \
  "v_cvt_pk_bf16_f32 v121, v122, v123\n\t" \
  "ds_read_b128 v[224:227], v8 offset:24576\n\t" \
  "v_exp_f32_e32 v127, v127\n\t" \
  "ds_read_b128 v[228:231], v8 offset:25600\n\t" \
  "v_cvt_pk_bf16_f32 v122, v124, v125\n\t" \
  "ds_read_b128 v[232:235], v8 offset:26624\n\t" \
  "v_exp_f32_e32 v128, v128\n\t" \
  "ds_read_b128 v[236:239], v8 offset:27648\n\t" \
  "v_cvt_pk_bf16_f32 v123, v126, v127\n\t" \
  "ds_read_b128 v[240:243], v8 offset:28672\n\t" \
  "v_exp_f32_e32 v129, v129\n\t" \
  "ds_read_b128 v[244:247], v8 offset:29696\n\t" \
  "v_exp_f32_e32 v130, v130\n\t" \
  "ds_read_b128 v[248:251], v8 offset:30720\n\t" \
  "v_cvt_pk_bf16_f32 v128, v128, v129\n\t" \
  "ds_read_b128 v[252:255], v8 offset:31744\n\t" \
  "v_exp_f32_e32 v131, v131\n\t" \
  "s_waitcnt lgkmcnt(8)\n\t" \
  "v_mfma_f32_16x16x32_bf16 v[16:19], v[192:195], v[112:115], v[16:19]\n\t" \
  "v_exp_f32_e32 v132, v132\n\t" \
  "v_cvt_pk_bf16_f32 v129, v130, v131\n\t" \
  "v_mfma_f32_16x16x32_bf16 v[24:27], v[192:195], v[120:123], v[24:27]\n\t" \
  "v_exp_f32_e32 v133, v133\n\t" \
  "v_mfma_f32_16x16x32_bf16 v[28:31], v[196:199], v[120:123], v[28:31]\n\t" \
  "v_exp_f32_e32 v134, v134\n\t" \
  "v_mfma_f32_16x16x32_bf16 v[20:23], v[196:199], v[112:115], v[20:23]\n\t" \
  "v_cvt_pk_bf16_f32 v130, v132, v133\n\t" \
  "v_mfma_f32_16x16x32_bf16 v[32:35], v[200:203], v[112:115], v[32:35]\n\t" \
  "v_exp_f32_e32 v135, v135\n\t" \
  "v_exp_f32_e32 v136, v136\n\t" \
  "v_mfma_f32_16x16x32_bf16 v[40:43], v[200:203], v[120:123], v[40:43]\n\t" \
  "v_cvt_pk_bf16_f32 v131, v134, v135\n\t" \
  "v_mfma_f32_16x16x32_bf16 v[44:47], v[204:207], v[120:123], v[44:47]\n\t" \
  "v_exp_f32_e32 v137, v137\n\t" \
  "v_mfma_f32_16x16x32_bf16 v[36:39], v[204:207], v[112:115], v[36:39]\n\t" \
  "v_exp_f32_e32 v138, v138\n\t" \
  "v_mfma_f32_16x16x32_bf16 v[48:51], v[208:211], v[112:115], v[48:51]\n\t" \
  "v_cvt_pk_bf16_f32 v136, v136, v137\n\t" \
  "v_exp_f32_e32 v139, v139\n\t" \
  "v_mfma_f32_16x16x32_bf16 v[56:59], v[208:211], v[120:123], v[56:59]\n\t" \
  "v_exp_f32_e32 v140, v140\n\t" \
  "v_mfma_f32_16x16x32_bf16 v[60:63], v[212:215], v[120:123], v[60:63]\n\t" \
  "v_cvt_pk_bf16_f32 v137, v138, v139\n\t" \
  "v_mfma_f32_16x16x32_bf16 v[52:55], v[212:215], v[112:115], v[52:55]\n\t" \
  "v_exp_f32_e32 v141, v141\n\t" \
  "v_mfma_f32_16x16x32_bf16 v[64:67], v[216:219], v[112:115], v[64:67]\n\t" \
  "v_exp_f32_e32 v142, v142\n\t" \
  "v_cvt_pk_bf16_f32 v138, v140, v141\n\t" \
  "v_mfma_f32_16x16x32_bf16 v[72:75], v[216:219], v[120:123], v[72:75]\n\t" \
  "v_exp_f32_e32 v143, v143\n\t" \
  "v_mfma_f32_16x16x32_bf16 v[76:79], v[220:223], v[120:123], v[76:79]\n\t" \
  "s_nop 0\n\t" \
  "v_mfma_f32_16x16x32_bf16 v[68:71], v[220:223], v[112:115], v[68:71]\n\t" \
  "v_cvt_pk_bf16_f32 v139, v142, v143\n\t" \
  "v_mfma_f32_16x16x32_bf16 v[4:7], v[188:191], v[112:115], v[4:7]\n\t" \
  "v_mfma_f32_16x16x32_bf16 v[12:15], v[188:191], v[120:123], v[12:15]\n\t" \
  "s_waitcnt lgkmcnt(0)\n\t" \
  "v_mfma_f32_16x16x32_bf16 v[4:7], v[188:191], v[128:131], v[4:7]\n\t" \
  "v_mfma_f32_16x16x32_bf16 v[12:15], v[188:191], v[136:139], v[12:15]\n\t" \
  "v_mfma_f32_16x16x32_bf16 v[16:19], v[224:227], v[128:131], v[16:19]\n\t" \
  "v_mfma_f32_16x16x32_bf16 v[24:27], v[224:227], v[136:139], v[24:27]\n\t" \
  "v_mfma_f32_16x16x32_bf16 v[28:31], v[228:231], v[136:139], v[28:31]\n\t" \
  "v_mfma_f32_16x16x32_bf16 v[20:23], v[228:231], v[128:131], v[20:23]\n\t" \
  "v_mfma_f32_16x16x32_bf16 v[32:35], v[232:235], v[128:131], v[32:35]\n\t" \
  "v_mfma_f32_16x16x32_bf16 v[40:43], v[232:235], v[136:139], v[40:43]\n\t" \
  "v_mfma_f32_16x16x32_bf16 v[44:47], v[236:239], v[136:139], v[44:47]\n\t" \
  "v_mfma_f32_16x16x32_bf16 v[36:39], v[236:239], v[128:131], v[36:39]\n\t" \
  "v_mfma_f32_16x16x32_bf16 v[48:51], v[240:243], v[128:131], v[48:51]\n\t" \
  "v_mfma_f32_16x16x32_bf16 v[56:59], v[240:243], v[136:139], v[56:59]\n\t" \
  "v_mfma_f32_16x16x32_bf16 v[60:63], v[244:247], v[136:139], v[60:63]\n\t" \
  "v_mfma_f32_16x16x32_bf16 v[52:55], v[244:247], v[128:131], v[52:55]\n\t" \
  "v_rcp_f32_e32 v4, v4\n\t" \
  "v_rcp_f32_e32 v5, v5\n\t" \
  "v_rcp_f32_e32 v12, v12\n\t" \
  "v_rcp_f32_e32 v13, v13\n\t" \
  "v_mfma_f32_16x16x32_bf16 v[64:67], v[248:251], v[128:131], v[64:67]\n\t" \
  "v_mfma_f32_16x16x32_bf16 v[72:75], v[248:251], v[136:139], v[72:75]\n\t" \
  "v_mfma_f32_16x16x32_bf16 v[76:79], v[252:255], v[136:139], v[76:79]\n\t" \
  "v_mfma_f32_16x16x32_bf16 v[68:71], v[252:255], v[128:131], v[68:71]\n\t" \
  "s_barrier\n\t" \
  "s_branch EPL_%=\n\t" \
  "SKS_%=:\n\t" \
  "v_xor_b32_e32 v8, 0x10000, v8\n\t" \
  "s_waitcnt vmcnt(8)\n\t" \
  "s_barrier\n\t" \
  "ds_read_b128 v[192:195], v8 offset:16384\n\t" \
  "v_exp_f32_e32 v123, v123\n\t" \
  "ds_read_b128 v[196:199], v8 offset:17408\n\t" \
  "v_cvt_pk_bf16_f32 v114, v116, v117\n\t" \
  "ds_read_b128 v[200:203], v8 offset:18432\n\t" \
  "v_exp_f32_e32 v124, v124\n\t" \
  "ds_read_b128 v[204:207], v8 offset:19456\n\t" \
  "v_cvt_pk_bf16_f32 v115, v118, v119\n\t" \
  "ds_read_b128 v[208:211], v8 offset:20480\n\t" \
  "v_exp_f32_e32 v125, v125\n\t" \
  "ds_read_b128 v[212:215], v8 offset:21504\n\t" \
  "v_cvt_pk_bf16_f32 v120, v120, v121\n\t" \
  "ds_read_b128 v[216:219], v8 offset:22528\n\t" \
  "v_exp_f32_e32 v126, v126\n\t" \
  "ds_read_b128 v[220:223], v8 offset:23552\n\t" \
  "v_cvt_pk_bf16_f32 v121, v122, v123\n\t" \
  "ds_read_b128 v[224:227], v8 offset:24576\n\t" \
  "v_exp_f32_e32 v127, v127\n\t" \
  "ds_read_b128 v[228:231], v8 offset:25600\n\t" \
  "v_cvt_pk_bf16_f32 v122, v124, v125\n\t" \
  "ds_read_b128 v[232:235], v8 offset:26624\n\t" \
  "v_exp_f32_e32 v128, v128\n\t" \
  "ds_read_b128 v[236:239], v8 offset:27648\n\t" \
  "v_cvt_pk_bf16_f32 v123, v126, v127\n\t" \
  "ds_read_b128 v[240:243], v8 offset:28672\n\t" \
  "v_exp_f32_e32 v129, v129\n\t" \
  "ds_read_b128 v[244:247], v8 offset:29696\n\t" \
  "v_exp_f32_e32 v130, v130\n\t" \
  "ds_read_b128 v[248:251], v8 offset:30720\n\t" \
  "v_cvt_pk_bf16_f32 v128, v128, v129\n\t" \
  "ds_read_b128 v[252:255], v8 offset:31744\n\t" \
  "v_exp_f32_e32 v131, v131\n\t" \
  "s_mov_b32 m0, s42\n\t" \
  "s_nop 0\n\t" \
  "global_load_lds_dwordx4 v1, s[40:41] offset:0 sc1\n\t" \
  "global_load_lds_dwordx4 v1, s[40:41] offset:1024 sc1\n\t" \
  "global_load_lds_dwordx4 v1, s[40:41] offset:2048 sc1\n\t" \
  "global_load_lds_dwordx4 v1, s[40:41] offset:3072 sc1\n\t" \
  "s_add_u32 m0, s42, 0x1000\n\t" \
  "s_nop 0\n\t" \
  "global_load_lds_dwordx4 v9, s[40:41] offset:0 sc1\n\t" \
  "global_load_lds_dwordx4 v9, s[40:41] offset:1024 sc1\n\t" \
  "global_load_lds_dwordx4 v9, s[40:41] offset:2048 sc1\n\t" \
  "global_load_lds_dwordx4 v9, s[40:41] offset:3072 sc1\n\t" \
  "s_add_u32 s40, s40, 0x8000\n\t" \
  "s_addc_u32 s41, s41, 0\n\t" \
  "s_add_u32 s42, s42, 0x8000\n\t" \
  "s_and_b32 s42, s42, 0x1ffff\n\t" \
  "s_waitcnt lgkmcnt(8)\n\t" \
  "v_mfma_f32_16x16x32_bf16 v[16:19], v[192:195], v[112:115], v[16:19]\n\t" \
  "v_exp_f32_e32 v132, v132\n\t" \
  "v_cvt_pk_bf16_f32 v129, v130, v131\n\t" \
  "v_mfma_f32_16x16x32_bf16 v[24:27], v[192:195], v[120:123], v[24:27]\n\t" \
  "v_exp_f32_e32 v133, v133\n\t" \
  "v_mfma_f32_16x16x32_bf16 v[28:31], v[196:199], v[120:123], v[28:31]\n\t" \
  "v_exp_f32_e32 v134, v134\n\t" \
  "v_mfma_f32_16x16x32_bf16 v[20:23], v[196:199], v[112:115], v[20:23]\n\t" \
  "v_cvt_pk_bf16_f32 v130, v132, v133\n\t" \
  "v_mfma_f32_16x16x32_bf16 v[32:35], v[200:203], v[112:115], v[32:35]\n\t" \
  "v_exp_f32_e32 v135, v135\n\t" \
  "v_exp_f32_e32 v136, v136\n\t" \
  "v_mfma_f32_16x16x32_bf16 v[40:43], v[200:203], v[120:123], v[40:43]\n\t" \
  "v_cvt_pk_bf16_f32 v131, v134, v135\n\t" \
  "v_mfma_f32_16x16x32_bf16 v[44:47], v[204:207], v[120:123], v[44:47]\n\t" \
  "v_exp_f32_e32 v137, v137\n\t" \
  "v_mfma_f32_16x16x32_bf16 v[36:39], v[204:207], v[112:115], v[36:39]\n\t" \
  "v_exp_f32_e32 v138, v138\n\t" \
  "v_mfma_f32_16x16x32_bf16 v[48:51], v[208:211], v[112:115], v[48:51]\n\t" \
  "v_cvt_pk_bf16_f32 v136, v136, v137\n\t" \
  "v_exp_f32_e32 v139, v139\n\t" \
  "v_mfma_f32_16x16x32_bf16 v[56:59], v[208:211], v[120:123], v[56:59]\n\t" \
  "v_exp_f32_e32 v140, v140\n\t" \
  "v_mfma_f32_16x16x32_bf16 v[60:63], v[212:215], v[120:123], v[60:63]\n\t" \
  "v_cvt_pk_bf16_f32 v137, v138, v139\n\t" \
  "v_mfma_f32_16x16x32_bf16 v[52:55], v[212:215], v[112:115], v[52:55]\n\t" \
  "v_exp_f32_e32 v141, v141\n\t" \
  "v_mfma_f32_16x16x32_bf16 v[64:67], v[216:219], v[112:115], v[64:67]\n\t" \
  "v_exp_f32_e32 v142, v142\n\t" \
  "v_cvt_pk_bf16_f32 v138, v140, v141\n\t" \
  "v_mfma_f32_16x16x32_bf16 v[72:75], v[216:219], v[120:123], v[72:75]\n\t" \
  "v_exp_f32_e32 v143, v143\n\t" \
  "v_mfma_f32_16x16x32_bf16 v[76:79], v[220:223], v[120:123], v[76:79]\n\t" \
  "s_nop 0\n\t" \
  "v_mfma_f32_16x16x32_bf16 v[68:71], v[220:223], v[112:115], v[68:71]\n\t" \
  "v_cvt_pk_bf16_f32 v139, v142, v143\n\t" \
  "v_mfma_f32_16x16x32_bf16 v[4:7], v[188:191], v[112:115], v[4:7]\n\t" \
  "v_mfma_f32_16x16x32_bf16 v[12:15], v[188:191], v[120:123], v[12:15]\n\t" \
  "s_waitcnt lgkmcnt(0)\n\t" \
  "v_mfma_f32_16x16x32_bf16 v[4:7], v[188:191], v[128:131], v[4:7]\n\t" \
  "v_mfma_f32_16x16x32_bf16 v[12:15], v[188:191], v[136:139], v[12:15]\n\t" \
  "v_mfma_f32_16x16x32_bf16 v[16:19], v[224:227], v[128:131], v[16:19]\n\t" \
  "v_mfma_f32_16x16x32_bf16 v[24:27], v[224:227], v[136:139], v[24:27]\n\t" \
  "v_mfma_f32_16x16x32_bf16 v[28:31], v[228:231], v[136:139], v[28:31]\n\t" \
  "v_mfma_f32_16x16x32_bf16 v[20:23], v[228:231], v[128:131], v[20:23]\n\t" \
  "v_mfma_f32_16x16x32_bf16 v[32:35], v[232:235], v[128:131], v[32:35]\n\t" \
  "v_mfma_f32_16x16x32_bf16 v[40:43], v[232:235], v[136:139], v[40:43]\n\t" \
  "v_mfma_f32_16x16x32_bf16 v[44:47], v[236:239], v[136:139], v[44:47]\n\t" \
  "v_mfma_f32_16x16x32_bf16 v[36:39], v[236:239], v[128:131], v[36:39]\n\t" \
  "v_mfma_f32_16x16x32_bf16 v[48:51], v[240:243], v[128:131], v[48:51]\n\t" \
  "v_mfma_f32_16x16x32_bf16 v[56:59], v[240:243], v[136:139], v[56:59]\n\t" \
  "v_mfma_f32_16x16x32_bf16 v[60:63], v[244:247], v[136:139], v[60:63]\n\t" \
  "v_mfma_f32_16x16x32_bf16 v[52:55], v[244:247], v[128:131], v[52:55]\n\t" \
  "v_rcp_f32_e32 v4, v4\n\t" \
  "v_rcp_f32_e32 v5, v5\n\t" \
  "v_rcp_f32_e32 v12, v12\n\t" \
  "v_rcp_f32_e32 v13, v13\n\t" \
  "v_mfma_f32_16x16x32_bf16 v[64:67], v[248:251], v[128:131], v[64:67]\n\t" \
  "v_mfma_f32_16x16x32_bf16 v[72:75], v[248:251], v[136:139], v[72:75]\n\t" \
  "v_mfma_f32_16x16x32_bf16 v[76:79], v[252:255], v[136:139], v[76:79]\n\t" \
  "v_mfma_f32_16x16x32_bf16 v[68:71], v[252:255], v[128:131], v[68:71]\n\t" \
  "s_waitcnt vmcnt(8)\n\t" \
  "s_barrier\n\t" \
  "s_mov_b32 m0, s42\n\t" \
  "s_nop 0\n\t" \
  "global_load_lds_dwordx4 v1, s[40:41] offset:0 sc1\n\t" \
  "global_load_lds_dwordx4 v1, s[40:41] offset:1024 sc1\n\t" \
  "global_load_lds_dwordx4 v1, s[40:41] offset:2048 sc1\n\t" \
  "global_load_lds_dwordx4 v1, s[40:41] offset:3072 sc1\n\t" \
  "s_add_u32 m0, s42, 0x1000\n\t" \
  "s_nop 0\n\t" \
  "global_load_lds_dwordx4 v9, s[40:41] offset:0 sc1\n\t" \
  "global_load_lds_dwordx4 v9, s[40:41] offset:1024 sc1\n\t" \
  "global_load_lds_dwordx4 v9, s[40:41] offset:2048 sc1\n\t" \
  "global_load_lds_dwordx4 v9, s[40:41] offset:3072 sc1\n\t" \
  "s_add_u32 s40, s40, 0x8000\n\t" \
  "s_addc_u32 s41, s41, 0\n\t" \
  "s_add_u32 s42, s42, 0x8000\n\t" \
  "s_and_b32 s42, s42, 0x1ffff\n\t" \
  "s_branch EPS_%=\n\t" \
  "HTX_%=:\n\t" \
  "s_cmp_lt_u32 s66, 4\n\t" \
  "s_cbranch_scc1 T4_%=\n\t" \
  "s_lshl_b32 s52, s53, 5\n\t" \
  "s_and_b32 s53, %[bid], 31\n\t" \
  "s_add_u32 s52, s52, s53\n\t" \
  "s_lshl_b32 s53, s52, 2\n\t" \
  "s_add_u32 s53, s53, %[w]\n\t" \
  "s_lshl_b32 s52, s52, 1\n\t" \
  "s_and_b32 s59, %[w], 1\n\t" \
  "s_add_u32 s52, s52, s59\n\t" \
  "s_mul_i32 s52, s52, 7\n\t" \
  "s_lshr_b32 s52, s52, 3\n\t" \
  "s_cmp_gt_u32 %[w], 5\n\t" \
  "s_cselect_b64 s[54:55], %[wu], %[wg]\n\t" \
  "s_cmp_lt_u32 %[w], 4\n\t" \
  "s_cselect_b64 s[54:55], %[wq], s[54:55]\n\t" \
  "s_cselect_b32 s52, s53, s52\n\t" \
  "s_lshl_b32 s52, s52, 13\n\t" \
  "s_add_u32 s54, s54, s52\n\t" \
  "s_addc_u32 s55, s55, 0\n\t" \
  "v_lshlrev_b32_e32 v225, 7, v0\n\t" \
  "global_load_dword v224, v225, s[54:55]\n\t" \
  "T4_%=:\n\t" \
  "s_add_u32 s70, s70, 4\n\t" \
  "s_addc_u32 s71, s71, 0\n\t" \
  "s_setpc_b64 s[70:71]\n\t" \
  "PSxT_%=:\n\t" \
  "s_mov_b32 s69, 0x40000\n\t" \
  "PLxT_%=:\n\t" \
  "s_sleep 1\n\t" \
  "global_load_dwordx2 v[14:15], v13, s[60:61] sc1\n\t" \
  "s_waitcnt vmcnt(0)\n\t" \
  "v_cmp_ne_u64_e32 vcc, s[36:37], v[14:15]\n\t" \
  "s_nop 0\n\t" \
  "s_cbranch_vccz PRxT_%=\n\t" \
  "s_sub_u32 s69, s69, 1\n\t" \
  "s_cbranch_scc0 PLxT_%=\n\t" \
  "s_branch PRxT_%=\n\t" \
  "HT0T_%=:\n\t" \
  "s_getpc_b64 s[70:71]\n\t" \
  "s_branch HTX_%=\n\t" \
  "s_branch HR0T_%=\n\t" \
  "HT1T_%=:\n\t" \
  "s_getpc_b64 s[70:71]\n\t" \
  "s_branch HTX_%=\n\t" \
  "s_branch HR1T_%=\n\t" \
  "HT2T_%=:\n\t" \
  "s_getpc_b64 s[70:71]\n\t" \
  "s_branch HTX_%=\n\t" \
  "s_branch HR2T_%=\n\t" \
  "HT3T_%=:\n\t" \
  "s_getpc_b64 s[70:71]\n\t" \
  "s_branch HTX_%=\n\t" \
  "s_branch HR3T_%=\n\t" \
  "PSxV_%=:\n\t" \
  "s_mov_b32 s69, 0x40000\n\t" \
  "PLxV_%=:\n\t" \
  "s_sleep 1\n\t" \
  "global_load_dwordx2 v[14:15], v13, s[60:61] sc1\n\t" \
  "s_waitcnt vmcnt(0)\n\t" \
  "v_cmp_ne_u64_e32 vcc, s[36:37], v[14:15]\n\t" \
  "s_nop 0\n\t" \
  "s_cbranch_vccz PRxV_%=\n\t" \
  "s_sub_u32 s69, s69, 1\n\t" \
  "s_cbranch_scc0 PLxV_%=\n\t" \
  "s_branch PRxV_%=\n\t" \
  "HT0V_%=:\n\t" \
  "s_getpc_b64 s[70:71]\n\t" \
  "s_branch HTX_%=\n\t" \
  "s_branch HR0V_%=\n\t" \
  "HT1V_%=:\n\t" \
  "s_getpc_b64 s[70:71]\n\t" \
  "s_branch HTX_%=\n\t" \
  "s_branch HR1V_%=\n\t" \
  "HT2V_%=:\n\t" \
  "s_getpc_b64 s[70:71]\n\t" \
  "s_branch HTX_%=\n\t" \
  "s_branch HR2V_%=\n\t" \
  "HT3V_%=:\n\t" \
  "s_getpc_b64 s[70:71]\n\t" \
  "s_branch HTX_%=\n\t" \
  "s_branch HR3V_%=\n\t" \
  "PSk_%=:\n\t" \
  "s_mov_b32 s69, 0x40000\n\t" \
  "PLk_%=:\n\t" \
  "s_sleep 1\n\t" \
  "global_load_dwordx2 v[14:15], v2, s[60:61] sc1\n\t" \
  "s_waitcnt vmcnt(0)\n\t" \
  "v_cmp_ne_u64_e32 vcc, s[36:37], v[14:15]\n\t" \
  "s_nop 0\n\t" \
  "s_cbranch_vccz PRk_%=\n\t" \
  "s_sub_u32 s69, s69, 1\n\t" \
  "s_cbranch_scc0 PLk_%=\n\t" \
  "s_branch PRk_%=\n\t" \
  "PSe0_%=:\n\t" \
  "s_mov_b32 s69, 0x40000\n\t" \
  "PLe0_%=:\n\t" \
  "s_sleep 1\n\t" \
  "global_load_dwordx4 v[40:43], v36, s[60:61] sc1\n\t" \
  "s_waitcnt vmcnt(0)\n\t" \
  "v_cmp_ne_u64_e32 vcc, s[36:37], v[40:41]\n\t" \
  "v_cmp_ne_u64_e64 s[54:55], s[36:37], v[42:43]\n\t" \
  "s_nop 0\n\t" \
  "s_or_b64 vcc, vcc, s[54:55]\n\t" \
  "s_nop 0\n\t" \
  "s_cbranch_vccz PRe0_%=\n\t" \
  "s_sub_u32 s69, s69, 1\n\t" \
  "s_cbranch_scc0 PLe0_%=\n\t" \
  "s_branch PRe0_%=\n\t" \
  "PSe1_%=:\n\t" \
  "s_mov_b32 s69, 0x40000\n\t" \
  "PLe1_%=:\n\t" \
  "s_sleep 1\n\t" \
  "global_load_dwordx4 v[40:43], v36, s[60:61] sc1\n\t" \
  "s_waitcnt vmcnt(0)\n\t" \
  "v_cmp_ne_u64_e32 vcc, s[36:37], v[40:41]\n\t" \
  "v_cmp_ne_u64_e64 s[54:55], s[36:37], v[42:43]\n\t" \
  "s_nop 0\n\t" \
  "s_or_b64 vcc, vcc, s[54:55]\n\t" \
  "s_nop 0\n\t" \
  "s_cbranch_vccz PRe1_%=\n\t" \
  "s_sub_u32 s69, s69, 1\n\t" \
  "s_cbranch_scc0 PLe1_%=\n\t" \
  "s_branch PRe1_%=\n\t" \
  "PSk2_%=:\n\t" \
  "s_mov_b32 s69, 0x40000\n\t" \
  "PLk2_%=:\n\t" \
  "s_sleep 1\n\t" \
  "global_load_dwordx2 v[144:145], v3, s[60:61] sc1\n\t" \
  "s_waitcnt vmcnt(0)\n\t" \
  "v_cmp_ne_u64_e32 vcc, s[36:37], v[144:145]\n\t" \
  "s_nop 0\n\t" \
  "s_cbranch_vccz PRk2_%=\n\t" \
  "s_sub_u32 s69, s69, 1\n\t" \
  "s_cbranch_scc0 PLk2_%=\n\t" \
  "s_branch PRk2_%=\n\t" \
  "PSxd_%=:\n\t" \
  "s_mov_b32 s69, 0x40000\n\t" \
  "PLxd_%=:\n\t" \
  "s_sleep 1\n\t" \
  "global_load_dwordx2 v[6:7], v13, s[60:61] sc1\n\t" \
  "s_waitcnt vmcnt(0)\n\t" \
  "v_cmp_ne_u64_e32 vcc, s[36:37], v[6:7]\n\t" \
  "s_nop 0\n\t" \
  "s_cbranch_vccz PRxd_%=\n\t" \
  "s_sub_u32 s69, s69, 1\n\t" \
  "s_cbranch_scc0 PLxd_%=\n\t" \
  "s_branch PRxd_%=\n\t" \
  "PSz_%=:\n\t" \
  "s_mov_b32 s69, 0x40000\n\t" \
  "PLz_%=:\n\t" \
  "s_add_u32 s52, s58, 1\n\t" \
  "s_and_b32 s52, s52, 3\n\t" \
  "s_lshl_b32 s52, s52, 13\n\t" \
  "v_add_u32_e32 v3, s52, v1\n\t" \
  "global_load_dwordx4 v[16:19], v3, s[78:79] offset:0 sc1\n\t" \
  "global_load_dwordx4 v[20:23], v3, s[78:79] offset:2048 sc1\n\t" \
  "s_add_u32 s52, s58, 2\n\t" \
  "s_and_b32 s52, s52, 3\n\t" \
  "s_lshl_b32 s52, s52, 13\n\t" \
  "v_add_u32_e32 v3, s52, v1\n\t" \
  "global_load_dwordx4 v[24:27], v3, s[78:79] offset:0 sc1\n\t" \
  "global_load_dwordx4 v[28:31], v3, s[78:79] offset:2048 sc1\n\t" \
  "s_add_u32 s52, s58, 3\n\t" \
  "s_and_b32 s52, s52, 3\n\t" \
  "s_lshl_b32 s52, s52, 13\n\t" \
  "v_add_u32_e32 v3, s52, v1\n\t" \
  "global_load_dwordx4 v[32:35], v3, s[78:79] offset:0 sc1\n\t" \
  "global_load_dwordx4 v[36:39], v3, s[78:79] offset:2048 sc1\n\t" \
  "s_waitcnt vmcnt(0)\n\t" \
  "v_max3_u32 v10, v16, v17, v18\n\t" \
  "v_max3_u32 v11, v19, v20, v21\n\t" \
  "v_max3_u32 v10, v10, v22, v23\n\t" \
  "v_max3_u32 v11, v11, v24, v25\n\t" \
  "v_max3_u32 v10, v10, v26, v27\n\t" \
  "v_max3_u32 v11, v11, v28, v29\n\t" \
  "v_max3_u32 v10, v10, v30, v31\n\t" \
  "v_max3_u32 v11, v11, v32, v33\n\t" \
  "v_max3_u32 v10, v10, v34, v35\n\t" \
  "v_max3_u32 v11, v11, v36, v37\n\t" \
  "v_max3_u32 v10, v10, v38, v39\n\t" \
  "v_max_u32_e32 v10, v10, v11\n\t" \
  "v_cmp_eq_u32_e32 vcc, -1, v10\n\t" \
  "s_nop 0\n\t" \
  "s_cbranch_vccz PRz_%=\n\t" \
  "s_sub_u32 s69, s69, 1\n\t" \
  "s_cbranch_scc0 PLz_%=\n\t" \
  "s_branch PRz_%=\n\t" \
  "PSh_%=:\n\t" \
  "s_mov_b32 s69, 0x40000\n\t" \
  "PLh_%=:\n\t" \
  "s_sleep 1\n\t" \
  "global_load_dwordx2 v[28:29], v13, s[60:61] sc1\n\t" \
  "s_waitcnt vmcnt(0)\n\t" \
  "v_cmp_ne_u64_e32 vcc, s[36:37], v[28:29]\n\t" \
  "s_nop 0\n\t" \
  "s_cbranch_vccz PRh_%=\n\t" \
  "s_sub_u32 s69, s69, 1\n\t" \
  "s_cbranch_scc0 PLh_%=\n\t" \
  "s_branch PRh_%=\n\t" \
  "PSc_%=:\n\t" \
  "s_mov_b32 s69, 0x40000\n\t" \
  "PLc_%=:\n\t" \
  "s_sleep 1\n\t" \
  "global_load_dwordx2 v[28:29], v13, s[60:61] sc1\n\t" \
  "s_waitcnt vmcnt(0)\n\t" \
  "v_cmp_ne_u64_e32 vcc, s[36:37], v[28:29]\n\t" \
  "s_nop 0\n\t" \
  "s_cbranch_vccz PRc_%=\n\t" \
  "s_sub_u32 s69, s69, 1\n\t" \
  "s_cbranch_scc0 PLc_%=\n\t" \
  "s_branch PRc_%=\n\t" \
  "HHL_%=:\n\t" \
  "s_waitcnt vmcnt(0)\n\t" \
  "s_cmp_ge_u32 s64, 28\n\t" \
  "s_cbranch_scc1 HPL_%=\n\t" \
  "s_sub_u32 s59, 15, s66\n\t" \
  "s_cmp_lt_u32 %[w], 4\n\t" \
  "s_cselect_b32 s59, s59, s66\n\t" \
  "s_lshl_b32 s52, s59, 7\n\t" \
  "s_lshl_b32 s53, s63, 5\n\t" \
  "s_add_u32 s52, s52, s53\n\t" \
  "s_lshl_b32 s52, s59, 5\n\t" \
  "s_add_u32 s52, s52, s64\n\t" \
  "s_lshl_b32 s52, s52, 5\n\t" \
  "s_lshr_b32 s53, s53, 2\n\t" \
  "s_add_u32 s52, s52, s53\n\t" \
  "s_add_u32 s52, s52, 0x14000\n\t" \
  "v_mov_b32_e32 v186, s36\n\t" \
  "v_mov_b32_e32 v187, s37\n\t" \
  "v_mov_b32_e32 v3, s52\n\t" \
  "s_mov_b64 exec, 1\n\t" \
  "global_store_dwordx2 v3, v[186:187], s[60:61] sc1\n\t" \
  "s_mov_b64 exec, -1\n\t" \
  "HPL_%=:\n\t" \
  "s_cmp_lt_u32 s66, 4\n\t" \
  "s_cbranch_scc1 T3_%=\n\t" \
  "s_lshr_b32 s52, %[bid], 6\n\t" \
  "s_lshl_b32 s52, s52, 5\n\t" \
  "s_and_b32 s53, %[bid], 31\n\t" \
  "s_add_u32 s52, s52, s53\n\t" \
  "s_lshl_b32 s52, s52, 2\n\t" \
  "s_add_u32 s52, s52, s63\n\t" \
  "s_lshl_b32 s53, s52, 13\n\t" \
  "s_mov_b64 s[54:55], %[wo]\n\t" \
  "s_add_u32 s54, s54, s53\n\t" \
  "s_addc_u32 s55, s55, 0\n\t" \
  "v_lshlrev_b32_e32 v185, 3, v1\n\t" \
  "global_load_dword v3, v185, s[54:55]\n\t" \
  "s_mul_i32 s53, s52, 3584\n\t" \
  "s_mov_b64 s[54:55], %[wd]\n\t" \
  "s_add_u32 s54, s54, s53\n\t" \
  "s_addc_u32 s55, s55, 0\n\t" \
  "v_min_u32_e32 v185, 27, v0\n\t" \
  "v_lshlrev_b32_e32 v185, 7, v185\n\t" \
  "global_load_dword v3, v185, s[54:55]\n\t" \
  "T3_%=:\n\t" \
  "s_branch HBL_%=\n\t" \
  "N2_%=:\n\t" \
  "s_waitcnt vmcnt(0)\n\t" \
  "s_cmp_ge_u32 s64, 28\n\t" \
  "s_cbranch_scc1 HPN_%=\n\t" \
  "s_sub_u32 s59, 15, s66\n\t" \
  "s_cmp_lt_u32 %[w], 4\n\t" \
  "s_cselect_b32 s59, s59, s66\n\t" \
  "s_lshl_b32 s52, s59, 7\n\t" \
  "s_lshl_b32 s53, s63, 5\n\t" \
  "s_add_u32 s52, s52, s53\n\t" \
  "s_lshl_b32 s52, s59, 5\n\t" \
  "s_add_u32 s52, s52, s64\n\t" \
  "s_lshl_b32 s52, s52, 5\n\t" \
  "s_lshr_b32 s53, s53, 2\n\t" \
  "s_add_u32 s52, s52, s53\n\t" \
  "s_add_u32 s52, s52, 0x14000\n\t" \
  "v_mov_b32_e32 v186, s36\n\t" \
  "v_mov_b32_e32 v187, s37\n\t" \
  "v_mov_b32_e32 v3, s52\n\t" \
  "s_mov_b64 exec, 1\n\t" \
  "global_store_dwordx2 v3, v[186:187], s[60:61] sc1\n\t" \
  "s_mov_b64 exec, -1\n\t" \
  "HPN_%=:\n\t" \
  "s_branch TOS_%=\n\t" \
  "HHS_%=:\n\t" \
  "s_waitcnt vmcnt(8)\n\t" \
  "s_cmp_ge_u32 s64, 28\n\t" \
  "s_cbranch_scc1 HPS_%=\n\t" \
  "s_sub_u32 s59, 15, s66\n\t" \
  "s_cmp_lt_u32 %[w], 4\n\t" \
  "s_cselect_b32 s59, s59, s66\n\t" \
  "s_lshl_b32 s52, s59, 7\n\t" \
  "s_lshl_b32 s53, s63, 5\n\t" \
  "s_add_u32 s52, s52, s53\n\t" \
  "s_lshl_b32 s52, s59, 5\n\t" \
  "s_add_u32 s52, s52, s64\n\t" \
  "s_lshl_b32 s52, s52, 5\n\t" \
  "s_lshr_b32 s53, s53, 2\n\t" \
  "s_add_u32 s52, s52, s53\n\t" \
  "s_add_u32 s52, s52, 0x14000\n\t" \
  "v_mov_b32_e32 v186, s36\n\t" \
  "v_mov_b32_e32 v187, s37\n\t" \
  "v_mov_b32_e32 v3, s52\n\t" \
  "s_mov_b64 exec, 1\n\t" \
  "global_store_dwordx2 v3, v[186:187], s[60:61] sc1\n\t" \
  "s_mov_b64 exec, -1\n\t" \
  "HPS_%=:\n\t" \
  "s_branch HBS_%=\n\t" \
  "END_%=:\n\t" \
  "FIN_%=:\n\t" \
  "s_waitcnt vmcnt(0) lgkmcnt(0)\n\t"
#define MEGA_CLOB "v0","v1","v2","v3","v4","v5","v6","v7","v8","v9","v10","v11","v12","v13","v14","v15","v16","v17","v18","v19","v20","v21","v22","v23","v24","v25","v26","v27","v28","v29","v30","v31","v32","v33","v34","v35","v36","v37","v38","v39","v40","v41","v42","v43","v44","v45","v46","v47","v48","v49","v50","v51","v52","v53","v54","v55","v56","v57","v58","v59","v60","v61","v62","v63","v64","v65","v66","v67","v68","v69","v70","v71","v72","v73","v74","v75","v76","v77","v78","v79","v80","v81","v82","v83","v84","v85","v86","v87","v88","v89","v90","v91","v92","v93","v94","v95","v96","v97","v98","v99","v100","v101","v102","v103","v104","v105","v106","v107","v108","v109","v110","v111","v112","v113","v114","v115","v116","v117","v118","v119","v120","v121","v122","v123","v124","v125","v126","v127","v128","v129","v130","v131","v132","v133","v134","v135","v136","v137","v138","v139","v140","v141","v142","v143","v144","v145","v146","v147","v148","v149","v150","v151","v152","v153","v154","v155","v156","v157","v158","v159","v160","v161","v162","v163","v164","v165","v166","v167","v168","v169","v170","v171","v172","v173","v174","v175","v176","v177","v178","v179","v180","v181","v182","v183","v184","v185","v186","v187","v188","v189","v190","v191","v192","v193","v194","v195","v196","v197","v198","v199","v200","v201","v202","v203","v204","v205","v206","v207","v208","v209","v210","v211","v212","v213","v214","v215","v216","v217","v218","v219","v220","v221","v222","v223","v224","v225","v226","v227","v228","v229","v230","v231","v232","v233","v234","v235","v236","v237","v238","v239","v240","v241","v242","v243","v244","v245","v246","v247","v248","v249","v250","v251","v252","v253","v254","v255","s36","s37","s38","s39","s40","s41","s42","s43","s44","s45","s46","s47","s48","s49","s50","s51","s52","s53","s54","s55","s56","s57","s58","s59","s60","s61","s62","s63","s64","s65","s66","s67","s68","s69","s70","s71","s72","s73","s74","s75","s76","s77","s78","s79","s80","s81","s82","s83","s84","s85","s86","s87","s88","s89","s90","s91","s92","s93","s94","s95","s96","s97","s98","s99","vcc","scc","memory"

__global__ __launch_bounds__(512, 2) void mega(const u16* __restrict__ x, const u16* __restrict__ ckv, const u16* __restrict__ wq, const u16* __restrict__ wuk,
                                               const u16* __restrict__ wuv, const u16* __restrict__ wo, const u16* __restrict__ wg, const u16* __restrict__ wu,
                                               const u16* __restrict__ wd, unsigned char* __restrict__ ws, u16* __restrict__ out) {
  const int w = __builtin_amdgcn_readfirstlane(threadIdx.x >> 6), bid = blockIdx.x;
  asm volatile(MEGA_ASM
    :: [x] "s"(x), [ckv] "s"(ckv), [wq] "s"(wq), [wuk] "s"(wuk), [wuv] "s"(wuv), [wo] "s"(wo), [wg] "s"(wg), [wu] "s"(wu), [wd] "s"(wd), [ws] "s"(ws), [out] "s"(out),
       [bid] "s"(bid), [w] "s"(w)
    : MEGA_CLOB);
}

extern "C" void kernel_launch(void* const* d_in, const int* in_sizes, int n_in, void* d_out, int out_size, void* d_ws, size_t ws_size, hipStream_t stream) {
  (void)in_sizes; (void)n_in; (void)out_size; (void)ws_size;
  (void)hipFuncSetAttribute((const void*)mega, hipFuncAttributeMaxDynamicSharedMemorySize, 163840);
  mega<<<256, 512, 163840, stream>>>((const u16*)d_in[0], (const u16*)d_in[1], (const u16*)d_in[2], (const u16*)d_in[3], (const u16*)d_in[4], (const u16*)d_in[5],
                                     (const u16*)d_in[7], (const u16*)d_in[8], (const u16*)d_in[9], (unsigned char*)d_ws, (u16*)d_out);
}
```
